```python
import torch

def _softmax(x, axis=-1):
    return torch.softmax(x, dim=axis)
def _gelu(x):
    return torch.nn.functional.gelu(x, approximate="tanh")
def _silu(x):
    return torch.nn.functional.silu(x)
def _sigmoid(x):
    return torch.sigmoid(x)
def _layernorm(x, axis=-1, eps=1e-5):
    m = x.mean(dim=axis, keepdim=True)
    v = x.var(dim=axis, keepdim=True, unbiased=False)
    return (x - m) / torch.sqrt(v + eps)
def _scatter_add(tgt, idx, src, axis=0):
    return tgt.clone().index_add_(axis, idx, src)
def _segment_sum(src, seg, n):
    out = torch.zeros((n,) + src.shape[1:], dtype=src.dtype)
    return out.index_add_(0, seg, src)
def _pad(x, pw):
    flat = tuple(v for pair in reversed(pw) for v in pair)
    return torch.nn.functional.pad(x, flat)

kernel_name = 'standalone__mha_block_cached__near_ridge'

def setup_inputs():

    _w = (lambda a: (lambda t: t.to(torch.bfloat16) if t.is_floating_point() else t)(torch.as_tensor(a)))
    B, Sq, D = 16, 208, 1024
    H, Sk, Dh = 32, 1024, 128
    return {
        "x": _w((lambda *s: torch.randn(*s, dtype=torch.bfloat16) * 0.8)(B, Sq, D)),
        "k_cache": _w((lambda *s: torch.randn(*s, dtype=torch.bfloat16) * 0.8)(B, H, Sk, Dh)),
        "v_cache": _w((lambda *s: torch.randn(*s, dtype=torch.bfloat16) * 0.05)(B, H, Sk, Dh)),
        "wq": _w((lambda *s: torch.randn(*s, dtype=torch.bfloat16) * 0.8)(D, H * Dh) * (D ** -0.5)),
        "wo": _w((lambda *s: torch.randn(*s, dtype=torch.bfloat16) * 0.05)(H * Dh, D) * ((H * Dh) ** -0.5)),
    }

def reference(x, k_cache, v_cache, wq, wo):
    B, Sq, D = x.shape
    H, Dh = 32, 128
    q = (lambda x, *d: x.permute(*d))((x @ wq).reshape(B, Sq, H, Dh), 0, 2, 1, 3)
    scores = torch.einsum("bhqd,bhkd->bhqk", q, k_cache) * (Dh ** -0.5)
    p = _softmax(scores, axis=-1)
    o = torch.einsum("bhqk,bhkd->bhqd", p, v_cache)
    return (lambda x, *d: x.permute(*d))(o, 0, 2, 1, 3).reshape(B, Sq, H * Dh) @ wo
```

```cpp
#include <hip/hip_runtime.h>
#include <hip/hip_bf16.h>
typedef unsigned short u16;
__device__ __forceinline__ float bf2f(u16 v){ return __uint_as_float(((unsigned)v)<<16); }
__device__ __forceinline__ u16 f2bf(float f){ unsigned u=__float_as_uint(f); u += 0x7fff + ((u>>16)&1); return (u16)(u>>16);}

__global__ void gemm_naive(const u16* A, const u16* B, u16* C, int M, int N, int K){
  __shared__ float As[16][65], Bs[16][65];
  int tx=threadIdx.x&15, ty=threadIdx.x>>4;
  int m0=blockIdx.y*64, n0=blockIdx.x*64;
  float acc[4][4]={};
  for(int k0=0;k0<K;k0+=16){
    for(int i=threadIdx.x;i<16*64;i+=256){ int kk=i&15, mm=i>>4; As[kk][mm]=bf2f(A[(size_t)(m0+mm)*K+k0+kk]); }
    for(int i=threadIdx.x;i<16*64;i+=256){ int nn=i&63, kk=i>>6; Bs[kk][nn]=bf2f(B[(size_t)(k0+kk)*N+n0+nn]); }
    __syncthreads();
    for(int kk=0;kk<16;kk++){
      float a[4],b[4];
      for(int i=0;i<4;i++){a[i]=As[kk][ty*4+i]; b[i]=Bs[kk][tx*4+i];}
      for(int i=0;i<4;i++)for(int j=0;j<4;j++)acc[i][j]+=a[i]*b[j];
    }
    __syncthreads();
  }
  for(int i=0;i<4;i++)for(int j=0;j<4;j++) C[(size_t)(m0+ty*4+i)*N+n0+tx*4+j]=f2bf(acc[i][j]);
}
__global__ void attn_naive(const u16* q, const u16* k, const u16* v, u16* o){
  const int Sq=208,H=32,Sk=1024,Dh=128;
  int qt=blockIdx.x, h=blockIdx.y, b=blockIdx.z;
  __shared__ float qs[16][128];
  __shared__ float s[16][1024];
  int t=threadIdx.x;
  for(int i=t;i<16*128;i+=256){int r=i>>7,d=i&127; qs[r][d]=bf2f(q[((size_t)(b*Sq+qt*16+r))*(H*Dh)+h*Dh+d]);}
  __syncthreads();
  const u16* kb=k+((size_t)(b*H+h))*Sk*Dh; const u16* vb=v+((size_t)(b*H+h))*Sk*Dh;
  for(int j=t;j<Sk;j+=256){
    float acc[16]={};
    for(int d=0;d<128;d++){ float kv=bf2f(kb[(size_t)j*Dh+d]); for(int r=0;r<16;r++) acc[r]+=qs[r][d]*kv; }
    for(int r=0;r<16;r++) s[r][j]=acc[r]*0.08838834764831845f;
  }
  __syncthreads();
  { int r=t>>4, l=t&15; float m=-1e30f; for(int j=l;j<Sk;j+=16) m=fmaxf(m,s[r][j]);
    for(int o2=8;o2>0;o2>>=1) m=fmaxf(m,__shfl_xor(m,o2,16));
    float sum=0; for(int j=l;j<Sk;j+=16){ float e=__expf(s[r][j]-m); s[r][j]=e; sum+=e; }
    for(int o2=8;o2>0;o2>>=1) sum+=__shfl_xor(sum,o2,16);
    float inv=1.f/sum; for(int j=l;j<Sk;j+=16) s[r][j]*=inv; }
  __syncthreads();
  { int d=t&127, rh=t>>7; float acc[8]={};
    for(int j=0;j<Sk;j++){ float vv=bf2f(vb[(size_t)j*Dh+d]); for(int r=0;r<8;r++) acc[r]+=s[rh*8+r][j]*vv; }
    for(int r=0;r<8;r++) o[((size_t)(b*Sq+qt*16+rh*8+r))*(H*Dh)+h*Dh+d]=f2bf(acc[r]); }
}
extern "C" void kernel_launch(void* const* d_in, const int* in_sizes, int n_in, void* d_out, int out_size, void* d_ws, size_t ws_size, hipStream_t stream){
  const u16* x=(const u16*)d_in[0]; const u16* kc=(const u16*)d_in[1]; const u16* vc=(const u16*)d_in[2];
  const u16* wq=(const u16*)d_in[3]; const u16* wo=(const u16*)d_in[4];
  u16* q=(u16*)d_ws; u16* o=q+(size_t)3328*4096;
  gemm_naive<<<dim3(4096/64,3328/64),256,0,stream>>>(x,wq,q,3328,4096,1024);
  attn_naive<<<dim3(13,32,16),256,0,stream>>>(q,kc,vc,o);
  gemm_naive<<<dim3(1024/64,3328/64),256,0,stream>>>(o,wo,(u16*)d_out,3328,1024,4096);
}
```

```cpp
#include <hip/hip_runtime.h>
#include <hip/hip_bf16.h>
#define WS_O 0
#define RBS_BYTES 132096
#define KCS_BYTES 1024

typedef unsigned short k1u16;
__device__ __forceinline__ float k1b2f(k1u16 v){ return __uint_as_float(((unsigned)v)<<16); }
__device__ __forceinline__ k1u16 k1f2b(float f){ unsigned u=__float_as_uint(f); if((u&0x7fffffffu)>0x7f800000u) return (k1u16)0x7fc0; u += 0x7fff + ((u>>16)&1); return (k1u16)(u>>16);}
__device__ __forceinline__ void slow_wave(const k1u16* x, const k1u16* kc, const k1u16* vc, const k1u16* wq, k1u16* of, unsigned g, unsigned w, unsigned lane){
  float* qs = (float*)((char*)of + 50331648) + (g*4+w)*4096;
  float* ss = qs + 2048;
  unsigned j=g&7, i=g>>3, b=(j&1)*8+(i&7), hp=(j>>1)*4+(i>>3), h=2*hp+(w>>1), par=w&1;
  const k1u16* kb=kc+(size_t)(b*32+h)*131072; const k1u16* vb=vc+(size_t)(b*32+h)*131072;
  for(int n=0;n<7;n++){
    int blk = n<6 ? 6*par+n : 12;
    const k1u16* xb = x + (size_t)(b*208+blk*16)*1024;
    for(int r=0;r<16;r++){
      float a0=0,a1=0;
      for(int k=0;k<1024;k++){ float xv=k1b2f(xb[r*1024+k]); unsigned wv=*(const unsigned*)(wq+(size_t)k*4096+h*128+2*lane); a0+=xv*__uint_as_float(wv<<16); a1+=xv*__uint_as_float(wv&0xffff0000u); }
      qs[r*128+2*lane]=k1b2f(k1f2b(a0)); qs[r*128+2*lane+1]=k1b2f(k1f2b(a1));
    }
    __builtin_amdgcn_fence(__ATOMIC_SEQ_CST,"workgroup"); __builtin_amdgcn_wave_barrier();
    for(int r=0;r<16;r++){
      float mx=-INFINITY;
      for(int t=0;t<16;t++){
        int key=lane+64*t; float a=0;
        for(int d=0;d<128;d+=2){ unsigned kv=*(const unsigned*)(kb+key*128+d); a+=qs[r*128+d]*__uint_as_float(kv<<16)+qs[r*128+d+1]*__uint_as_float(kv&0xffff0000u); }
        a*=0.08838834764831845f; ss[key]=a; mx=fmaxf(mx,a);
      }
      for(int o=32;o>0;o>>=1) mx=fmaxf(mx,__int_as_float(__builtin_amdgcn_ds_bpermute((lane^o)<<2,__float_as_int(mx))));
      float sum=0;
      for(int t=0;t<16;t++){ int key=lane+64*t; float e=__expf(ss[key]-mx); ss[key]=e; sum+=e; }
      for(int o=32;o>0;o>>=1) sum+=__int_as_float(__builtin_amdgcn_ds_bpermute((lane^o)<<2,__float_as_int(sum)));
      __builtin_amdgcn_fence(__ATOMIC_SEQ_CST,"workgroup"); __builtin_amdgcn_wave_barrier();
      float a0=0,a1=0;
      for(int key=0;key<1024;key++){ float p=ss[key]; unsigned vv=*(const unsigned*)(vb+key*128+2*lane); a0+=p*__uint_as_float(vv<<16); a1+=p*__uint_as_float(vv&0xffff0000u); }
      float inv=1.f/sum;
      int d=2*lane;
      if(n<6 || (unsigned)(d>>6)==par){
        int c=d>>5, jj=((d>>4)&1)*4+(d&3), gg=(d>>2)&3;
        k1u16* dst=of+((size_t)(b*13+blk)*RBS_BYTES+(size_t)(4*h+c)*1024)/2+(gg*16+r)*8+jj;
        __hip_atomic_store((unsigned*)dst,(unsigned)k1f2b(a0*inv)|((unsigned)k1f2b(a1*inv)<<16),__ATOMIC_RELAXED,__HIP_MEMORY_SCOPE_AGENT);
      }
      __builtin_amdgcn_fence(__ATOMIC_SEQ_CST,"workgroup"); __builtin_amdgcn_wave_barrier();
    }
  }
}

__global__ __launch_bounds__(256,1) void mha_kernel(const void* x, const void* kc, const void* vc, const void* wq, void* of, const void* wo, void* out){
  unsigned w = __builtin_amdgcn_readfirstlane(threadIdx.x>>6);
  unsigned g = blockIdx.x;
  asm volatile(
"Lbeg%=:\n"
"s_mov_b32 s76, %7\n"
"s_mov_b32 s99, %6\n"
"s_and_b32 s80, s99, 7\n"
"s_lshr_b32 s81, s99, 3\n"
"s_and_b32 s77, s80, 1\n"
"s_lshl_b32 s77, s77, 3\n"
"s_and_b32 s82, s81, 7\n"
"s_add_u32 s77, s77, s82\n"
"s_lshr_b32 s78, s80, 1\n"
"s_lshl_b32 s78, s78, 2\n"
"s_lshr_b32 s82, s81, 3\n"
"s_add_u32 s78, s78, s82\n"
"s_lshr_b32 s83, s76, 1\n"
"s_and_b32 s84, s76, 1\n"
"s_lshl_b32 s79, s78, 1\n"
"s_add_u32 s79, s79, s83\n"
"s_mov_b64 s[48:49], %4\n"
"s_lshl_b32 s80, s78, 9\n"
"s_add_u32 s48, s48, s80\n"
"s_addc_u32 s49, s49, 0\n"
"s_sub_u32 s48, s48, 2688\n"
"s_subb_u32 s49, s49, 0\n"
"s_sub_u32 s50, 8391296, s80\n"
"s_mov_b32 s51, 0x00020000\n"
"s_movk_i32 s63, 2688\n"
"s_movk_i32 s64, 1792\n"
"s_movk_i32 s65, 896\n"
"s_mov_b64 s[52:53], %1\n"
"s_mul_i32 s80, s77, 425984\n"
"s_add_u32 s52, s52, s80\n"
"s_addc_u32 s53, s53, 0\n"
"s_mov_b32 s54, 425984\n"
"s_mov_b32 s55, 0x00020000\n"
"s_mul_i32 s66, s76, 98304\n"
"s_add_u32 s67, s66, 31744\n"
"s_add_u32 s68, s67, 31744\n"
"s_add_u32 s69, s68, 31744\n"
"s_cmp_eq_u32 s76, 3\n"
"s_cselect_b32 s69, s69, 0x40000000\n"
"s_mul_i32 s80, s84, 65544\n"
"s_lshl_b32 s81, s83, 12\n"
"s_add_u32 s71, s80, s81\n"
"s_mul_i32 s80, s84, 16392\n"
"s_add_u32 s80, s80, s81\n"
"s_add_u32 s72, s80, 131072\n"
"s_mul_i32 s80, s84, 3072\n"
"s_add_u32 s80, s80, 8192\n"
"s_mul_i32 s81, s83, 65552\n"
"s_add_u32 s73, s80, s81\n"
"s_mul_i32 s81, s83, 16400\n"
"s_add_u32 s81, s81, s80\n"
"s_add_u32 s74, s81, 131072\n"
"v_mbcnt_lo_u32_b32 v0, -1, 0\n"
"v_mbcnt_hi_u32_b32 v0, -1, v0\n"
"s_movk_i32 s85, 0x78\n"
"v_lshrrev_b32 v1, 3, v0\n"
"s_lshl_b32 s80, s76, 16\n"
"v_lshlrev_b32 v1, 13, v1\n"
"v_add_u32 v1, s80, v1\n"
"v_lshrrev_b32 v2, 2, v0\n"
"v_and_b32 v3, 4, v2\n"
"v_and_b32 v4, 7, v0\n"
"v_xor_b32 v3, v3, v4\n"
"v_lshlrev_b32 v3, 4, v3\n"
"v_add_u32 v253, v1, v3\n"
"v_lshrrev_b32 v5, 4, v0\n"
"v_lshlrev_b32 v3, 1, v5\n"
"v_lshrrev_b32 v3, v3, s85\n"
"v_and_b32 v6, 3, v3\n"
"v_and_b32 v7, 3, v0\n"
"v_xor_b32 v3, v6, v7\n"
"v_lshlrev_b32 v3, 4, v3\n"
"v_lshlrev_b32 v254, 11, v2\n"
"v_add_u32 v254, v254, v3\n"
"v_lshlrev_b32 v223, 8, v2\n"
"v_add_u32 v223, v223, v3\n"
"s_lshl_b32 s80, s84, 12\n"
"v_add_u32 v223, s80, v223\n"
"s_lshl_b32 s81, s83, 16\n"
"s_add_u32 s70, s81, s80\n"
"s_cmp_eq_u32 s84, 1\n"
"s_cbranch_scc1 Lp1%=\n"
"s_add_u32 m0, s71, 16384\n"
"s_nop 0\n"
"buffer_load_dwordx4 v253, s[48:51], s63 offen offset:0  lds\n"
"buffer_load_dwordx4 v253, s[48:51], s64 offen offset:1024  lds\n"
"buffer_load_dwordx4 v253, s[48:51], s65 offen offset:2048  lds\n"
"buffer_load_dwordx4 v253, s[48:51], 0 offen offset:3072  lds\n"
"v_add_u32 v253, 0x40000, v253\n"
"s_add_u32 m0, s73, 16384\n"
"s_nop 0\n"
"buffer_load_dwordx4 v254, s[52:55], s66 offen offset:0  lds\n"
"buffer_load_dwordx4 v254, s[52:55], s67 offen offset:1024  lds\n"
"buffer_load_dwordx4 v254, s[52:55], s68 offen offset:2048  lds\n"
"v_add_u32 v254, 64, v254\n"
"s_add_u32 m0, s71, 32768\n"
"s_nop 0\n"
"buffer_load_dwordx4 v253, s[48:51], s63 offen offset:0  lds\n"
"buffer_load_dwordx4 v253, s[48:51], s64 offen offset:1024  lds\n"
"buffer_load_dwordx4 v253, s[48:51], s65 offen offset:2048  lds\n"
"buffer_load_dwordx4 v253, s[48:51], 0 offen offset:3072  lds\n"
"v_add_u32 v253, 0x40000, v253\n"
"s_add_u32 m0, s73, 32768\n"
"s_nop 0\n"
"buffer_load_dwordx4 v254, s[52:55], s66 offen offset:0  lds\n"
"buffer_load_dwordx4 v254, s[52:55], s67 offen offset:1024  lds\n"
"buffer_load_dwordx4 v254, s[52:55], s68 offen offset:2048  lds\n"
"v_add_u32 v254, 64, v254\n"
"v_and_b32 v8, 15, v0\n"
"v_lshrrev_b32 v3, 2, v8\n"
"v_lshlrev_b32 v3, 1, v3\n"
"v_lshrrev_b32 v3, v3, s85\n"
"v_and_b32 v3, 3, v3\n"
"v_xor_b32 v3, v3, v5\n"
"v_lshlrev_b32 v3, 4, v3\n"
"v_lshlrev_b32 v4, 6, v8\n"
"v_add_u32 v246, v3, v4\n"
"v_add_u32 v247, 0x10000, v246\n"
"v_add_u32 v248, 0x20000, v246\n"
"v_and_b32 v3, 3, v2\n"
"v_lshlrev_b32 v4, 7, v3\n"
"v_lshrrev_b32 v3, 1, v3\n"
"v_lshlrev_b32 v3, 6, v3\n"
"v_add_u32 v4, v4, v3\n"
"v_lshlrev_b32 v3, 4, v7\n"
"v_add_u32 v4, v4, v3\n"
"v_lshrrev_b32 v3, 1, v5\n"
"v_lshlrev_b32 v3, 12, v3\n"
"v_add_u32 v4, v4, v3\n"
"s_lshl_b32 s80, s83, 11\n"
"v_add_u32 v4, s80, v4\n"
"v_and_b32 v3, 1, v5\n"
"v_mul_u32_u24 v9, 65544, v3\n"
"v_add_u32 v249, v4, v9\n"
"v_xor_b32 v250, 64, v249\n"
"v_mul_u32_u24 v9, 16392, v3\n"
"v_add_u32 v9, v4, v9\n"
"v_add_u32 v251, 0x20000, v9\n"
"v_xor_b32 v252, 64, v251\n"
"s_add_u32 m0, s71, 49152\n"
"s_nop 0\n"
"buffer_load_dwordx4 v253, s[48:51], s63 offen offset:0  lds\n"
"buffer_load_dwordx4 v253, s[48:51], s65 offen offset:2048  lds\n"
"v_add_u32 v253, 0x40000, v253\n"
"s_mov_b32 m0, s72\n"
"s_nop 0\n"
"buffer_load_dwordx4 v253, s[48:51], s63 offen offset:0  lds\n"
"buffer_load_dwordx4 v253, s[48:51], s65 offen offset:2048  lds\n"
"v_subrev_u32 v253, 0x40000, v253\n"
"s_add_u32 m0, s73, 49152\n"
"s_nop 0\n"
"buffer_load_dwordx4 v254, s[52:55], s66 offen offset:0  lds\n"
"buffer_load_dwordx4 v254, s[52:55], s67 offen offset:1024  lds\n"
"buffer_load_dwordx4 v254, s[52:55], s68 offen offset:2048  lds\n"
"v_add_u32 v254, 64, v254\n"
"s_and_b32 s80, s78, 3\n"
"s_lshl_b32 s80, s80, 2\n"
"s_add_u32 s80, s80, s76\n"
"s_mul_i32 s80, s80, 26624\n"
"s_add_u32 s80, s80, 256\n"
"v_lshrrev_b32 v1, 1, v0\n"
"v_lshlrev_b32 v1, 11, v1\n"
"v_and_b32 v3, 1, v0\n"
"v_lshlrev_b32 v3, 7, v3\n"
"v_add3_u32 v1, v1, v3, s80\n"
"v_cmp_gt_u32 vcc, 26, v0\n"
"v_mov_b32 v3, 0x40000000\n"
"s_nop 0\n"
"v_cndmask_b32 v209, v3, v1, vcc\n"
"s_and_b32 s80, s77, 7\n"
"s_lshl_b32 s80, s80, 2\n"
"s_add_u32 s80, s80, s76\n"
"s_lshl_b32 s80, s80, 15\n"
"s_add_u32 s80, s80, 1051264\n"
"v_lshrrev_b32 v1, 2, v0\n"
"v_lshlrev_b32 v1, 13, v1\n"
"v_and_b32 v4, 3, v0\n"
"v_lshlrev_b32 v4, 7, v4\n"
"v_add3_u32 v1, v1, v4, s80\n"
"v_cmp_gt_u32 vcc, 16, v0\n"
"s_nop 1\n"
"v_cndmask_b32 v210, v3, v1, vcc\n"
"buffer_load_dword v208, v209, s[52:55], 0 offen \n"
"v_add_u32 v209, 0x100, v209\n"
"buffer_load_dword v208, v210, s[48:51], 0 offen \n"
"v_add_u32 v210, 0x100000, v210\n"
"s_lshl_b32 s80, s77, 5\n"
"s_add_u32 s80, s80, s79\n"
"s_lshl_b32 s80, s80, 18\n"
"s_mov_b64 s[40:41], %2\n"
"s_add_u32 s40, s40, s80\n"
"s_addc_u32 s41, s41, 0\n"
"s_sub_u32 s40, s40, 2880\n"
"s_subb_u32 s41, s41, 0\n"
"s_mov_b32 s42, 265024\n"
"s_mov_b32 s43, 0x00020000\n"
"s_mov_b64 s[44:45], %3\n"
"s_add_u32 s44, s44, s80\n"
"s_addc_u32 s45, s45, 0\n"
"s_sub_u32 s44, s44, 2880\n"
"s_subb_u32 s45, s45, 0\n"
"s_mov_b32 s46, 265024\n"
"s_mov_b32 s47, 0x00020000\n"
"s_movk_i32 s60, 2880\n"
"s_movk_i32 s61, 1920\n"
"s_movk_i32 s62, 960\n"
"s_mov_b64 s[56:57], %5\n"
"s_mov_b32 s58, 50331648\n"
"s_mov_b32 s59, 0x00020000\n"
"v_and_b32 v214, 3, v0\n"
"v_lshlrev_b32 v215, 3, v214\n"
"v_lshlrev_b32 v214, 7, v214\n"
"s_lshl_b32 s80, s77, 2\n"
"s_add_u32 s80, s80, s76\n"
"s_lshl_b32 s80, s80, 9\n"
"s_add_u32 s80, s80, 33554432\n"
"buffer_load_dwordx2 v[212:213], v214, s[56:59], s80 offen sc1\n"
"s_lshl_b32 s80, s76, 5\n"
"s_add_u32 s80, s80, 163328\n"
"v_add_u32 v215, s80, v215\n"
"v_cmp_gt_u32 vcc, 4, v0\n"
"v_mov_b32 v217, 0x40000000\n"
"s_nop 0\n"
"v_cndmask_b32 v216, v217, v214, vcc\n"
"s_lshl_b32 s81, s83, 16\n"
"s_mov_b32 s98, 1040356334\n"
"v_add_u32 v220, s81, v246\n"
"v_lshrrev_b32 v3, 1, v7\n"
"v_xor_b32 v3, v3, v6\n"
"v_lshlrev_b32 v3, 4, v3\n"
"v_and_b32 v4, 1, v7\n"
"v_lshlrev_b32 v4, 3, v4\n"
"v_add_u32 v3, v3, v4\n"
"v_lshlrev_b32 v4, 6, v2\n"
"v_add_u32 v3, v3, v4\n"
"s_add_u32 s80, s81, 8192\n"
"v_add_u32 v221, s80, v3\n"
"v_xor_b32 v222, 32, v221\n"
"s_lshl_b32 s80, s83, 12\n"
"s_add_u32 s80, s80, 131072\n"
"v_lshlrev_b32 v255, 4, v0\n"
"v_add_u32 v245, s80, v255\n"
"v_lshrrev_b32 v3, 1, v8\n"
"v_and_b32 v3, 1, v3\n"
"v_lshrrev_b32 v4, 1, v5\n"
"v_cmp_eq_u32 vcc, v3, v4\n"
"v_mov_b32 v9, 0x3f803f80\n"
"s_nop 0\n"
"v_cndmask_b32 v240, 0, v9, vcc\n"
"v_cndmask_b32 v241, 0, v9, vcc\n"
"v_cndmask_b32 v242, 0, v9, vcc\n"
"v_cndmask_b32 v243, 0, v9, vcc\n"
"v_and_b32 v3, 1, v8\n"
"v_mul_u32_u24 v3, 0xAAAA, v3\n"
"v_add_u32 v244, 0x4444, v3\n"
"s_lshl_b32 s80, s99, 2\n"
"s_add_u32 s80, s80, s76\n"
"s_lshl_b32 s80, s80, 11\n"
"v_lshrrev_b32 v211, 2, v0\n"
"v_lshlrev_b32 v211, 7, v211\n"
"v_add_u32 v211, s80, v211\n"
"s_getpc_b64 s[80:81]\n"
"Lcpa0%=:\n"
"s_sub_u32 s80, s80, Lcpa0%=-Lbeg%=\n"
"s_subb_u32 s81, s81, 0\n"
"s_and_b32 s80, s80, 0xffffff80\n"
"s_mov_b32 s82, Lx%=-Lbeg%=\n"
"s_mov_b32 s83, 0x00020000\n"
"s_lshr_b32 s84, s99, 3\n"
"s_lshl_b32 s84, s84, 2\n"
"s_add_u32 s84, s84, s76\n"
"s_lshl_b32 s84, s84, 10\n"
"v_cmp_gt_u32 vcc, 128, v255\n"
"v_lshl_add_u32 v1, v255, 3, s84\n"
"v_mov_b32 v3, 0x40000000\n"
"v_cndmask_b32 v1, v3, v1, vcc\n"
"buffer_load_dword v208, v1, s[80:83], 0 offen \n"
"s_waitcnt vmcnt(18)\n"
"s_barrier\n"
"ds_read_b128 a[160:163], v246 offset:24576\n"
"ds_read_b64_tr_b16 a[96:97], v249 offset:16384\n"
"ds_read_b64_tr_b16 a[98:99], v249 offset:16896\n"
"ds_read_b64_tr_b16 a[100:101], v249 offset:16392\n"
"ds_read_b64_tr_b16 a[102:103], v249 offset:16904\n"
"ds_read_b64_tr_b16 a[104:105], v250 offset:16384\n"
"ds_read_b64_tr_b16 a[106:107], v250 offset:16896\n"
"ds_read_b64_tr_b16 a[108:109], v250 offset:16392\n"
"ds_read_b64_tr_b16 a[110:111], v250 offset:16904\n"
"ds_read_b64_tr_b16 a[112:113], v249 offset:17408\n"
"ds_read_b64_tr_b16 a[114:115], v249 offset:17920\n"
"ds_read_b64_tr_b16 a[116:117], v249 offset:17416\n"
"ds_read_b64_tr_b16 a[118:119], v249 offset:17928\n"
"ds_read_b64_tr_b16 a[120:121], v250 offset:17408\n"
"ds_read_b64_tr_b16 a[122:123], v250 offset:17920\n"
"ds_read_b64_tr_b16 a[124:125], v250 offset:17416\n"
"ds_read_b64_tr_b16 a[126:127], v250 offset:17928\n"
"ds_read_b128 a[164:167], v246 offset:25600\n"
"ds_read_b128 a[168:171], v246 offset:26624\n"
"ds_read_b128 a[172:175], v246 offset:27648\n"
"ds_read_b128 a[176:179], v246 offset:28672\n"
"ds_read_b128 a[180:183], v246 offset:29696\n"
"ds_read_b128 a[184:187], v247 offset:30736\n"
"s_mov_b32 s75, 3\n"
"s_waitcnt vmcnt(11)\n"
"s_barrier\n"
"ds_read_b128 a[192:195], v246 offset:40960\n"
"ds_read_b64_tr_b16 a[128:129], v249 offset:32768\n"
"ds_read_b64_tr_b16 a[130:131], v249 offset:33280\n"
"ds_read_b128 a[196:199], v246 offset:41984\n"
"ds_read_b128 a[200:203], v246 offset:43008\n"
"ds_read_b128 a[204:207], v246 offset:44032\n"
"ds_read_b128 a[208:211], v246 offset:45056\n"
"ds_read_b128 a[212:215], v246 offset:46080\n"
"ds_read_b128 a[216:219], v247 offset:47120\n"
"s_waitcnt lgkmcnt(0)\n"
"v_mfma_f32_16x16x32_bf16 v[0:3], a[96:99], a[160:163], 0\n"
"s_add_u32 m0, s71, 49152\n"
"ds_read_b64_tr_b16 a[132:133], v249 offset:32776\n"
"ds_read_b64_tr_b16 a[134:135], v249 offset:33288\n"
"v_mfma_f32_16x16x32_bf16 v[0:3], a[128:131], a[192:195], v[0:3]\n"
"buffer_load_dwordx4 v253, s[48:51], s64 offen offset:1024  lds\n"
"ds_read_b64_tr_b16 a[136:137], v250 offset:32768\n"
"ds_read_b64_tr_b16 a[138:139], v250 offset:33280\n"
"v_mfma_f32_16x16x32_bf16 v[32:35], a[96:99], a[164:167], 0\n"
"buffer_load_dwordx4 v253, s[48:51], 0 offen offset:3072  lds\n"
"ds_read_b64_tr_b16 a[140:141], v250 offset:32776\n"
"ds_read_b64_tr_b16 a[142:143], v250 offset:33288\n"
"v_mfma_f32_16x16x32_bf16 v[32:35], a[128:131], a[196:199], v[32:35]\n"
"v_add_u32 v253, 0x40000, v253\n"
"s_mov_b32 m0, s72\n"
"ds_read_b64_tr_b16 a[144:145], v249 offset:33792\n"
"v_mfma_f32_16x16x32_bf16 v[64:67], a[96:99], a[168:171], 0\n"
"buffer_load_dwordx4 v253, s[48:51], s64 offen offset:1024  lds\n"
"ds_read_b64_tr_b16 a[146:147], v249 offset:34304\n"
"ds_read_b64_tr_b16 a[148:149], v249 offset:33800\n"
"v_mfma_f32_16x16x32_bf16 v[64:67], a[128:131], a[200:203], v[64:67]\n"
"buffer_load_dwordx4 v253, s[48:51], 0 offen offset:3072  lds\n"
"ds_read_b64_tr_b16 a[150:151], v249 offset:34312\n"
"ds_read_b64_tr_b16 a[152:153], v250 offset:33792\n"
"v_mfma_f32_16x16x32_bf16 v[96:99], a[96:99], a[172:175], 0\n"
"v_add_u32 v253, 0x40000, v253\n"
"s_mov_b32 m0, s74\n"
"ds_read_b64_tr_b16 a[154:155], v250 offset:34304\n"
"v_mfma_f32_16x16x32_bf16 v[96:99], a[128:131], a[204:207], v[96:99]\n"
"buffer_load_dwordx4 v254, s[52:55], s66 offen offset:0  lds\n"
"ds_read_b64_tr_b16 a[156:157], v250 offset:33800\n"
"ds_read_b64_tr_b16 a[158:159], v250 offset:34312\n"
"v_mfma_f32_16x16x32_bf16 v[128:131], a[96:99], a[176:179], 0\n"
"buffer_load_dwordx4 v254, s[52:55], s67 offen offset:1024  lds\n"
"v_mfma_f32_16x16x32_bf16 v[128:131], a[128:131], a[208:211], v[128:131]\n"
"buffer_load_dwordx4 v254, s[52:55], s68 offen offset:2048  lds\n"
"v_mfma_f32_16x16x32_bf16 v[160:163], a[96:99], a[180:183], 0\n"
"v_add_u32 v254, 64, v254\n"
"v_mfma_f32_16x16x32_bf16 v[160:163], a[128:131], a[212:215], v[160:163]\n"
"v_mfma_f32_16x16x32_bf16 v[192:195], a[96:99], a[184:187], 0\n"
"v_mfma_f32_16x16x32_bf16 v[192:195], a[128:131], a[216:219], v[192:195]\n"
"s_waitcnt vmcnt(11) lgkmcnt(0)\n"
"s_barrier\n"
"v_mfma_f32_16x16x32_bf16 v[4:7], a[100:103], a[160:163], 0\n"
"s_add_u32 m0, s71, 16384\n"
"ds_read_b64_tr_b16 a[96:97], v249 offset:49152\n"
"v_mfma_f32_16x16x32_bf16 v[4:7], a[132:135], a[192:195], v[4:7]\n"
"buffer_load_dwordx4 v253, s[48:51], s63 offen offset:0  lds\n"
"v_mfma_f32_16x16x32_bf16 v[36:39], a[100:103], a[164:167], 0\n"
"ds_read_b64_tr_b16 a[98:99], v249 offset:49664\n"
"ds_read_b64_tr_b16 a[128:129], v251 offset:0\n"
"v_mfma_f32_16x16x32_bf16 v[36:39], a[132:135], a[196:199], v[36:39]\n"
"buffer_load_dwordx4 v253, s[48:51], s65 offen offset:2048  lds\n"
"v_mfma_f32_16x16x32_bf16 v[68:71], a[100:103], a[168:171], 0\n"
"ds_read_b64_tr_b16 a[130:131], v251 offset:512\n"
"ds_read_b128 a[0:3], v246 offset:57344\n"
"v_mfma_f32_16x16x32_bf16 v[68:71], a[132:135], a[200:203], v[68:71]\n"
"v_add_u32 v253, 0x40000, v253\n"
"s_add_u32 m0, s71, 32768\n"
"v_mfma_f32_16x16x32_bf16 v[100:103], a[100:103], a[172:175], 0\n"
"buffer_load_dwordx4 v253, s[48:51], s63 offen offset:0  lds\n"
"v_mfma_f32_16x16x32_bf16 v[100:103], a[132:135], a[204:207], v[100:103]\n"
"ds_read_b128 a[4:7], v246 offset:58368\n"
"ds_read_b128 a[8:11], v246 offset:59392\n"
"v_mfma_f32_16x16x32_bf16 v[132:135], a[100:103], a[176:179], 0\n"
"buffer_load_dwordx4 v253, s[48:51], s65 offen offset:2048  lds\n"
"v_mfma_f32_16x16x32_bf16 v[132:135], a[132:135], a[208:211], v[132:135]\n"
"ds_read_b128 a[12:15], v246 offset:60416\n"
"ds_read_b128 a[16:19], v246 offset:61440\n"
"v_mfma_f32_16x16x32_bf16 v[164:167], a[100:103], a[180:183], 0\n"
"v_subrev_u32 v253, 0x40000, v253\n"
"s_add_u32 m0, s73, 16384\n"
"v_mfma_f32_16x16x32_bf16 v[164:167], a[132:135], a[212:215], v[164:167]\n"
"buffer_load_dwordx4 v254, s[52:55], s66 offen offset:0  lds\n"
"v_mfma_f32_16x16x32_bf16 v[196:199], a[100:103], a[184:187], 0\n"
"ds_read_b128 a[20:23], v246 offset:62464\n"
"ds_read_b128 a[24:27], v247 offset:63504\n"
"v_mfma_f32_16x16x32_bf16 v[196:199], a[132:135], a[216:219], v[196:199]\n"
"buffer_load_dwordx4 v254, s[52:55], s67 offen offset:1024  lds\n"
"v_mfma_f32_16x16x32_bf16 v[8:11], a[104:107], a[160:163], 0\n"
"ds_read_b64_tr_b16 a[100:101], v249 offset:49160\n"
"ds_read_b64_tr_b16 a[102:103], v249 offset:49672\n"
"v_mfma_f32_16x16x32_bf16 v[8:11], a[136:139], a[192:195], v[8:11]\n"
"buffer_load_dwordx4 v254, s[52:55], s68 offen offset:2048  lds\n"
"v_mfma_f32_16x16x32_bf16 v[40:43], a[104:107], a[164:167], 0\n"
"ds_read_b64_tr_b16 a[132:133], v251 offset:8\n"
"ds_read_b64_tr_b16 a[134:135], v251 offset:520\n"
"v_mfma_f32_16x16x32_bf16 v[40:43], a[136:139], a[196:199], v[40:43]\n"
"v_add_u32 v254, 64, v254\n"
"s_add_u32 m0, s71, 16384\n"
"v_mfma_f32_16x16x32_bf16 v[72:75], a[104:107], a[168:171], 0\n"
"buffer_load_dwordx4 v253, s[48:51], s64 offen offset:1024  lds\n"
"v_mfma_f32_16x16x32_bf16 v[72:75], a[136:139], a[200:203], v[72:75]\n"
"v_mfma_f32_16x16x32_bf16 v[104:107], a[104:107], a[172:175], 0\n"
"buffer_load_dwordx4 v253, s[48:51], 0 offen offset:3072  lds\n"
"v_mfma_f32_16x16x32_bf16 v[104:107], a[136:139], a[204:207], v[104:107]\n"
"v_mfma_f32_16x16x32_bf16 v[136:139], a[104:107], a[176:179], 0\n"
"v_add_u32 v253, 0x40000, v253\n"
"s_add_u32 m0, s71, 32768\n"
"v_mfma_f32_16x16x32_bf16 v[136:139], a[136:139], a[208:211], v[136:139]\n"
"buffer_load_dwordx4 v253, s[48:51], s64 offen offset:1024  lds\n"
"v_mfma_f32_16x16x32_bf16 v[168:171], a[104:107], a[180:183], 0\n"
"v_mfma_f32_16x16x32_bf16 v[168:171], a[136:139], a[212:215], v[168:171]\n"
"buffer_load_dwordx4 v253, s[48:51], 0 offen offset:3072  lds\n"
"v_mfma_f32_16x16x32_bf16 v[200:203], a[104:107], a[184:187], 0\n"
"v_mfma_f32_16x16x32_bf16 v[200:203], a[136:139], a[216:219], v[200:203]\n"
"v_add_u32 v253, 0x40000, v253\n"
"s_add_u32 m0, s73, 32768\n"
"v_mfma_f32_16x16x32_bf16 v[12:15], a[108:111], a[160:163], 0\n"
"buffer_load_dwordx4 v254, s[52:55], s66 offen offset:0  lds\n"
"v_mfma_f32_16x16x32_bf16 v[12:15], a[140:143], a[192:195], v[12:15]\n"
"ds_read_b64_tr_b16 a[104:105], v250 offset:49152\n"
"ds_read_b64_tr_b16 a[106:107], v250 offset:49664\n"
"v_mfma_f32_16x16x32_bf16 v[44:47], a[108:111], a[164:167], 0\n"
"buffer_load_dwordx4 v254, s[52:55], s67 offen offset:1024  lds\n"
"v_mfma_f32_16x16x32_bf16 v[44:47], a[140:143], a[196:199], v[44:47]\n"
"ds_read_b64_tr_b16 a[136:137], v252 offset:0\n"
"ds_read_b64_tr_b16 a[138:139], v252 offset:512\n"
"v_mfma_f32_16x16x32_bf16 v[76:79], a[108:111], a[168:171], 0\n"
"buffer_load_dwordx4 v254, s[52:55], s68 offen offset:2048  lds\n"
"v_mfma_f32_16x16x32_bf16 v[76:79], a[140:143], a[200:203], v[76:79]\n"
"v_mfma_f32_16x16x32_bf16 v[108:111], a[108:111], a[172:175], 0\n"
"v_add_u32 v254, 64, v254\n"
"v_mfma_f32_16x16x32_bf16 v[108:111], a[140:143], a[204:207], v[108:111]\n"
"buffer_load_dword v208, v209, s[52:55], 0 offen \n"
"v_mfma_f32_16x16x32_bf16 v[140:143], a[108:111], a[176:179], 0\n"
"v_mfma_f32_16x16x32_bf16 v[140:143], a[140:143], a[208:211], v[140:143]\n"
"v_add_u32 v209, 0x100, v209\n"
"v_mfma_f32_16x16x32_bf16 v[172:175], a[108:111], a[180:183], 0\n"
"buffer_load_dword v208, v210, s[48:51], 0 offen \n"
"v_mfma_f32_16x16x32_bf16 v[172:175], a[140:143], a[212:215], v[172:175]\n"
"v_mfma_f32_16x16x32_bf16 v[204:207], a[108:111], a[184:187], 0\n"
"v_add_u32 v210, 0x100000, v210\n"
"v_mfma_f32_16x16x32_bf16 v[204:207], a[140:143], a[216:219], v[204:207]\n"
"ds_read_b64_tr_b16 a[108:109], v250 offset:49160\n"
"ds_read_b64_tr_b16 a[110:111], v250 offset:49672\n"
"v_mfma_f32_16x16x32_bf16 v[16:19], a[112:115], a[160:163], 0\n"
"ds_read_b64_tr_b16 a[140:141], v252 offset:8\n"
"ds_read_b64_tr_b16 a[142:143], v252 offset:520\n"
"v_mfma_f32_16x16x32_bf16 v[16:19], a[144:147], a[192:195], v[16:19]\n"
"v_mfma_f32_16x16x32_bf16 v[48:51], a[112:115], a[164:167], 0\n"
"v_mfma_f32_16x16x32_bf16 v[48:51], a[144:147], a[196:199], v[48:51]\n"
"v_mfma_f32_16x16x32_bf16 v[80:83], a[112:115], a[168:171], 0\n"
"v_mfma_f32_16x16x32_bf16 v[80:83], a[144:147], a[200:203], v[80:83]\n"
"v_mfma_f32_16x16x32_bf16 v[112:115], a[112:115], a[172:175], 0\n"
"v_mfma_f32_16x16x32_bf16 v[112:115], a[144:147], a[204:207], v[112:115]\n"
"v_mfma_f32_16x16x32_bf16 v[144:147], a[112:115], a[176:179], 0\n"
"v_mfma_f32_16x16x32_bf16 v[144:147], a[144:147], a[208:211], v[144:147]\n"
"v_mfma_f32_16x16x32_bf16 v[176:179], a[112:115], a[180:183], 0\n"
"v_mfma_f32_16x16x32_bf16 v[176:179], a[144:147], a[212:215], v[176:179]\n"
"s_waitcnt vmcnt(16) lgkmcnt(0)\n"
"s_barrier\n"
"v_mfma_f32_16x16x32_bf16 v[20:23], a[116:119], a[160:163], 0\n"
"s_add_u32 m0, s71, 49152\n"
"ds_read_b128 a[28:31], v248 offset:8192\n"
"v_mfma_f32_16x16x32_bf16 v[20:23], a[148:151], a[192:195], v[20:23]\n"
"buffer_load_dwordx4 v253, s[48:51], s63 offen offset:0  lds\n"
"v_mfma_f32_16x16x32_bf16 v[52:55], a[116:119], a[164:167], 0\n"
"ds_read_b128 a[32:35], v248 offset:9216\n"
"ds_read_b128 a[36:39], v248 offset:10240\n"
"v_mfma_f32_16x16x32_bf16 v[52:55], a[148:151], a[196:199], v[52:55]\n"
"ds_read_b128 a[40:43], v248 offset:11264\n"
"ds_read_b128 a[44:47], v248 offset:12288\n"
"v_mfma_f32_16x16x32_bf16 v[84:87], a[116:119], a[168:171], 0\n"
"buffer_load_dwordx4 v253, s[48:51], s65 offen offset:2048  lds\n"
"v_mfma_f32_16x16x32_bf16 v[84:87], a[148:151], a[200:203], v[84:87]\n"
"ds_read_b128 a[48:51], v248 offset:13312\n"
"ds_read_b128 a[52:55], v248 offset:30736\n"
"v_mfma_f32_16x16x32_bf16 v[116:119], a[116:119], a[172:175], 0\n"
"ds_read_b64_tr_b16 a[112:113], v249 offset:50176\n"
"ds_read_b64_tr_b16 a[114:115], v249 offset:50688\n"
"v_mfma_f32_16x16x32_bf16 v[116:119], a[148:151], a[204:207], v[116:119]\n"
"v_add_u32 v253, 0x40000, v253\n"
"s_mov_b32 m0, s72\n"
"v_mfma_f32_16x16x32_bf16 v[148:151], a[116:119], a[176:179], 0\n"
"buffer_load_dwordx4 v253, s[48:51], s63 offen offset:0  lds\n"
"v_mfma_f32_16x16x32_bf16 v[148:151], a[148:151], a[208:211], v[148:151]\n"
"ds_read_b64_tr_b16 a[144:145], v251 offset:1024\n"
"ds_read_b64_tr_b16 a[146:147], v251 offset:1536\n"
"v_mfma_f32_16x16x32_bf16 v[180:183], a[116:119], a[180:183], 0\n"
"v_mfma_f32_16x16x32_bf16 v[180:183], a[148:151], a[212:215], v[180:183]\n"
"buffer_load_dwordx4 v253, s[48:51], s65 offen offset:2048  lds\n"
"v_mfma_f32_16x16x32_bf16 v[24:27], a[120:123], a[160:163], 0\n"
"ds_read_b64_tr_b16 a[116:117], v249 offset:50184\n"
"ds_read_b64_tr_b16 a[118:119], v249 offset:50696\n"
"v_mfma_f32_16x16x32_bf16 v[24:27], a[152:155], a[192:195], v[24:27]\n"
"ds_read_b64_tr_b16 a[148:149], v251 offset:1032\n"
"ds_read_b64_tr_b16 a[150:151], v251 offset:1544\n"
"v_mfma_f32_16x16x32_bf16 v[56:59], a[120:123], a[164:167], 0\n"
"v_subrev_u32 v253, 0x40000, v253\n"
"s_add_u32 m0, s73, 49152\n"
"v_mfma_f32_16x16x32_bf16 v[56:59], a[152:155], a[196:199], v[56:59]\n"
"buffer_load_dwordx4 v254, s[52:55], s66 offen offset:0  lds\n"
"v_mfma_f32_16x16x32_bf16 v[88:91], a[120:123], a[168:171], 0\n"
"v_mfma_f32_16x16x32_bf16 v[88:91], a[152:155], a[200:203], v[88:91]\n"
"v_mfma_f32_16x16x32_bf16 v[120:123], a[120:123], a[172:175], 0\n"
"buffer_load_dwordx4 v254, s[52:55], s67 offen offset:1024  lds\n"
"v_mfma_f32_16x16x32_bf16 v[120:123], a[152:155], a[204:207], v[120:123]\n"
"v_mfma_f32_16x16x32_bf16 v[152:155], a[120:123], a[176:179], 0\n"
"v_mfma_f32_16x16x32_bf16 v[152:155], a[152:155], a[208:211], v[152:155]\n"
"buffer_load_dwordx4 v254, s[52:55], s68 offen offset:2048  lds\n"
"v_mfma_f32_16x16x32_bf16 v[184:187], a[120:123], a[180:183], 0\n"
"v_mfma_f32_16x16x32_bf16 v[184:187], a[152:155], a[212:215], v[184:187]\n"
"ds_read_b64_tr_b16 a[120:121], v250 offset:50176\n"
"ds_read_b64_tr_b16 a[122:123], v250 offset:50688\n"
"v_mfma_f32_16x16x32_bf16 v[28:31], a[124:127], a[160:163], 0\n"
"v_add_u32 v254, 64, v254\n"
"ds_read_b64_tr_b16 a[152:153], v252 offset:1024\n"
"v_mfma_f32_16x16x32_bf16 v[28:31], a[156:159], a[192:195], v[28:31]\n"
"ds_read_b64_tr_b16 a[154:155], v252 offset:1536\n"
"v_mfma_f32_16x16x32_bf16 v[60:63], a[124:127], a[164:167], 0\n"
"v_mfma_f32_16x16x32_bf16 v[60:63], a[156:159], a[196:199], v[60:63]\n"
"v_mfma_f32_16x16x32_bf16 v[92:95], a[124:127], a[168:171], 0\n"
"v_mfma_f32_16x16x32_bf16 v[92:95], a[156:159], a[200:203], v[92:95]\n"
"v_mfma_f32_16x16x32_bf16 v[124:127], a[124:127], a[172:175], 0\n"
"v_mfma_f32_16x16x32_bf16 v[124:127], a[156:159], a[204:207], v[124:127]\n"
"v_mfma_f32_16x16x32_bf16 v[156:159], a[124:127], a[176:179], 0\n"
"v_mfma_f32_16x16x32_bf16 v[156:159], a[156:159], a[208:211], v[156:159]\n"
"v_mfma_f32_16x16x32_bf16 v[188:191], a[124:127], a[180:183], 0\n"
"v_mfma_f32_16x16x32_bf16 v[188:191], a[156:159], a[212:215], v[188:191]\n"
"s_waitcnt lgkmcnt(12)\n"
"v_mfma_f32_16x16x32_bf16 v[0:3], a[96:99], a[0:3], v[0:3]\n"
"ds_read_b64_tr_b16 a[124:125], v250 offset:50184\n"
"v_mfma_f32_16x16x32_bf16 v[0:3], a[128:131], a[28:31], v[0:3]\n"
"ds_read_b64_tr_b16 a[126:127], v250 offset:50696\n"
"v_mfma_f32_16x16x32_bf16 v[32:35], a[96:99], a[4:7], v[32:35]\n"
"ds_read_b64_tr_b16 a[156:157], v252 offset:1032\n"
"v_mfma_f32_16x16x32_bf16 v[32:35], a[128:131], a[32:35], v[32:35]\n"
"ds_read_b64_tr_b16 a[158:159], v252 offset:1544\n"
"v_mfma_f32_16x16x32_bf16 v[64:67], a[96:99], a[8:11], v[64:67]\n"
"v_mfma_f32_16x16x32_bf16 v[64:67], a[128:131], a[36:39], v[64:67]\n"
"v_mfma_f32_16x16x32_bf16 v[96:99], a[96:99], a[12:15], v[96:99]\n"
"v_mfma_f32_16x16x32_bf16 v[96:99], a[128:131], a[40:43], v[96:99]\n"
"v_mfma_f32_16x16x32_bf16 v[128:131], a[96:99], a[16:19], v[128:131]\n"
"v_mfma_f32_16x16x32_bf16 v[128:131], a[128:131], a[44:47], v[128:131]\n"
"v_mfma_f32_16x16x32_bf16 v[160:163], a[96:99], a[20:23], v[160:163]\n"
"v_mfma_f32_16x16x32_bf16 v[160:163], a[128:131], a[48:51], v[160:163]\n"
"v_mfma_f32_16x16x32_bf16 v[192:195], a[96:99], a[24:27], v[192:195]\n"
"v_mfma_f32_16x16x32_bf16 v[192:195], a[128:131], a[52:55], v[192:195]\n"
"s_waitcnt vmcnt(16) lgkmcnt(0)\n"
"s_barrier\n"
"v_mfma_f32_16x16x32_bf16 v[4:7], a[100:103], a[0:3], v[4:7]\n"
"s_add_u32 m0, s71, 49152\n"
"v_mfma_f32_16x16x32_bf16 v[4:7], a[132:135], a[28:31], v[4:7]\n"
"buffer_load_dwordx4 v253, s[48:51], s64 offen offset:1024  lds\n"
"v_mfma_f32_16x16x32_bf16 v[36:39], a[100:103], a[4:7], v[36:39]\n"
"ds_read_b64_tr_b16 a[96:97], v249 offset:16384\n"
"v_mfma_f32_16x16x32_bf16 v[36:39], a[132:135], a[32:35], v[36:39]\n"
"ds_read_b64_tr_b16 a[98:99], v249 offset:16896\n"
"v_mfma_f32_16x16x32_bf16 v[68:71], a[100:103], a[8:11], v[68:71]\n"
"ds_read_b64_tr_b16 a[128:129], v249 offset:32768\n"
"v_mfma_f32_16x16x32_bf16 v[68:71], a[132:135], a[36:39], v[68:71]\n"
"buffer_load_dwordx4 v253, s[48:51], 0 offen offset:3072  lds\n"
"v_mfma_f32_16x16x32_bf16 v[100:103], a[100:103], a[12:15], v[100:103]\n"
"ds_read_b64_tr_b16 a[130:131], v249 offset:33280\n"
"v_mfma_f32_16x16x32_bf16 v[100:103], a[132:135], a[40:43], v[100:103]\n"
"ds_read_b128 a[160:163], v246 offset:24576\n"
"v_mfma_f32_16x16x32_bf16 v[132:135], a[100:103], a[16:19], v[132:135]\n"
"ds_read_b128 a[164:167], v246 offset:25600\n"
"v_mfma_f32_16x16x32_bf16 v[132:135], a[132:135], a[44:47], v[132:135]\n"
"v_add_u32 v253, 0x40000, v253\n"
"s_mov_b32 m0, s72\n"
"v_mfma_f32_16x16x32_bf16 v[164:167], a[100:103], a[20:23], v[164:167]\n"
"buffer_load_dwordx4 v253, s[48:51], s64 offen offset:1024  lds\n"
"v_mfma_f32_16x16x32_bf16 v[164:167], a[132:135], a[48:51], v[164:167]\n"
"ds_read_b128 a[168:171], v246 offset:26624\n"
"v_mfma_f32_16x16x32_bf16 v[196:199], a[100:103], a[24:27], v[196:199]\n"
"ds_read_b128 a[172:175], v246 offset:27648\n"
"v_mfma_f32_16x16x32_bf16 v[196:199], a[132:135], a[52:55], v[196:199]\n"
"ds_read_b64_tr_b16 a[100:101], v249 offset:16392\n"
"v_mfma_f32_16x16x32_bf16 v[8:11], a[104:107], a[0:3], v[8:11]\n"
"buffer_load_dwordx4 v253, s[48:51], 0 offen offset:3072  lds\n"
"v_mfma_f32_16x16x32_bf16 v[8:11], a[136:139], a[28:31], v[8:11]\n"
"ds_read_b64_tr_b16 a[102:103], v249 offset:16904\n"
"v_mfma_f32_16x16x32_bf16 v[40:43], a[104:107], a[4:7], v[40:43]\n"
"ds_read_b64_tr_b16 a[132:133], v249 offset:32776\n"
"v_add_u32 v212, 1, v212\n"
"v_mfma_f32_16x16x32_bf16 v[40:43], a[136:139], a[32:35], v[40:43]\n"
"ds_read_b64_tr_b16 a[134:135], v249 offset:33288\n"
"s_lshl_b32 s80, s77, 2\n"
"v_mfma_f32_16x16x32_bf16 v[72:75], a[104:107], a[8:11], v[72:75]\n"
"v_add_u32 v253, 0x40000, v253\n"
"s_mov_b32 m0, s74\n"
"v_mfma_f32_16x16x32_bf16 v[72:75], a[136:139], a[36:39], v[72:75]\n"
"buffer_load_dwordx4 v254, s[52:55], s66 offen offset:0  lds\n"
"v_mfma_f32_16x16x32_bf16 v[104:107], a[104:107], a[12:15], v[104:107]\n"
"ds_read_b128 a[176:179], v246 offset:28672\n"
"s_add_u32 s80, s80, s76\n"
"v_mfma_f32_16x16x32_bf16 v[104:107], a[136:139], a[40:43], v[104:107]\n"
"ds_read_b128 a[180:183], v246 offset:29696\n"
"s_lshl_b32 s80, s80, 9\n"
"v_mfma_f32_16x16x32_bf16 v[136:139], a[104:107], a[16:19], v[136:139]\n"
"ds_read_b128 a[184:187], v247 offset:30736\n"
"s_lshl_b32 s81, s78, 3\n"
"v_mfma_f32_16x16x32_bf16 v[136:139], a[136:139], a[44:47], v[136:139]\n"
"buffer_load_dwordx4 v254, s[52:55], s67 offen offset:1024  lds\n"
"v_mfma_f32_16x16x32_bf16 v[168:171], a[104:107], a[20:23], v[168:171]\n"
"s_add_u32 s80, s80, s81\n"
"s_add_u32 s80, s80, 33685504\n"
"v_mfma_f32_16x16x32_bf16 v[168:171], a[136:139], a[48:51], v[168:171]\n"
"ds_write_b64 v215, v[212:213]\n"
"v_mfma_f32_16x16x32_bf16 v[200:203], a[104:107], a[24:27], v[200:203]\n"
"buffer_store_dwordx2 v[212:213], v216, s[56:59], s80 offen sc1\n"
"v_mfma_f32_16x16x32_bf16 v[200:203], a[136:139], a[52:55], v[200:203]\n"
"buffer_load_dwordx4 v254, s[52:55], s68 offen offset:2048  lds\n"
"v_mfma_f32_16x16x32_bf16 v[12:15], a[108:111], a[0:3], v[12:15]\n"
"ds_read_b64_tr_b16 a[104:105], v250 offset:16384\n"
"v_mfma_f32_16x16x32_bf16 v[12:15], a[140:143], a[28:31], v[12:15]\n"
"ds_read_b64_tr_b16 a[106:107], v250 offset:16896\n"
"v_mfma_f32_16x16x32_bf16 v[44:47], a[108:111], a[4:7], v[44:47]\n"
"ds_read_b64_tr_b16 a[136:137], v250 offset:32768\n"
"v_mfma_f32_16x16x32_bf16 v[44:47], a[140:143], a[32:35], v[44:47]\n"
"v_add_u32 v254, 64, v254\n"
"v_mfma_f32_16x16x32_bf16 v[76:79], a[108:111], a[8:11], v[76:79]\n"
"ds_read_b64_tr_b16 a[138:139], v250 offset:33280\n"
"v_mfma_f32_16x16x32_bf16 v[76:79], a[140:143], a[36:39], v[76:79]\n"
"v_mfma_f32_16x16x32_bf16 v[108:111], a[108:111], a[12:15], v[108:111]\n"
"v_mfma_f32_16x16x32_bf16 v[108:111], a[140:143], a[40:43], v[108:111]\n"
"v_mfma_f32_16x16x32_bf16 v[140:143], a[108:111], a[16:19], v[140:143]\n"
"v_mfma_f32_16x16x32_bf16 v[140:143], a[140:143], a[44:47], v[140:143]\n"
"v_mfma_f32_16x16x32_bf16 v[172:175], a[108:111], a[20:23], v[172:175]\n"
"v_mov_b32 v224, 0\n"
"v_mov_b32 v225, 0\n"
"v_mfma_f32_16x16x32_bf16 v[172:175], a[140:143], a[48:51], v[172:175]\n"
"v_mov_b32 v226, 0\n"
"v_mov_b32 v227, 0\n"
"v_mfma_f32_16x16x32_bf16 v[204:207], a[108:111], a[24:27], v[204:207]\n"
"v_mov_b32 v228, 0\n"
"v_mov_b32 v229, 0\n"
"v_mfma_f32_16x16x32_bf16 v[204:207], a[140:143], a[52:55], v[204:207]\n"
"ds_read_b64_tr_b16 a[108:109], v250 offset:16392\n"
"v_mov_b32 v230, 0\n"
"v_mfma_f32_16x16x32_bf16 v[16:19], a[112:115], a[0:3], v[16:19]\n"
"ds_read_b64_tr_b16 a[110:111], v250 offset:16904\n"
"v_mov_b32 v231, 0\n"
"v_mfma_f32_16x16x32_bf16 v[16:19], a[144:147], a[28:31], v[16:19]\n"
"ds_read_b64_tr_b16 a[140:141], v250 offset:32776\n"
"v_mov_b32 v232, 0\n"
"v_mfma_f32_16x16x32_bf16 v[48:51], a[112:115], a[4:7], v[48:51]\n"
"ds_read_b64_tr_b16 a[142:143], v250 offset:33288\n"
"v_mov_b32 v233, 0\n"
"v_mfma_f32_16x16x32_bf16 v[48:51], a[144:147], a[32:35], v[48:51]\n"
"v_mov_b32 v234, 0\n"
"v_mov_b32 v235, 0\n"
"v_mfma_f32_16x16x32_bf16 v[80:83], a[112:115], a[8:11], v[80:83]\n"
"v_mov_b32 v236, 0\n"
"v_mov_b32 v237, 0\n"
"v_mfma_f32_16x16x32_bf16 v[80:83], a[144:147], a[36:39], v[80:83]\n"
"v_mov_b32 v238, 0\n"
"v_mov_b32 v239, 0\n"
"v_mfma_f32_16x16x32_bf16 v[112:115], a[112:115], a[12:15], v[112:115]\n"
"v_accvgpr_write_b32 a224, 0\n"
"v_accvgpr_write_b32 a225, 0\n"
"v_mfma_f32_16x16x32_bf16 v[112:115], a[144:147], a[40:43], v[112:115]\n"
"v_accvgpr_write_b32 a226, 0\n"
"v_accvgpr_write_b32 a227, 0\n"
"v_mfma_f32_16x16x32_bf16 v[144:147], a[112:115], a[16:19], v[144:147]\n"
"v_accvgpr_write_b32 a228, 0\n"
"v_accvgpr_write_b32 a229, 0\n"
"v_mfma_f32_16x16x32_bf16 v[144:147], a[144:147], a[44:47], v[144:147]\n"
"v_accvgpr_write_b32 a230, 0\n"
"v_accvgpr_write_b32 a231, 0\n"
"v_mfma_f32_16x16x32_bf16 v[176:179], a[112:115], a[20:23], v[176:179]\n"
"v_accvgpr_write_b32 a232, 0\n"
"v_accvgpr_write_b32 a233, 0\n"
"v_mfma_f32_16x16x32_bf16 v[176:179], a[144:147], a[48:51], v[176:179]\n"
"s_waitcnt vmcnt(17) lgkmcnt(0)\n"
"s_barrier\n"
"v_accvgpr_write_b32 a234, 0\n"
"v_mfma_f32_16x16x32_bf16 v[20:23], a[116:119], a[0:3], v[20:23]\n"
"s_add_u32 m0, s71, 16384\n"
"v_accvgpr_write_b32 a235, 0\n"
"v_mfma_f32_16x16x32_bf16 v[20:23], a[148:151], a[28:31], v[20:23]\n"
"buffer_load_dwordx4 v253, s[48:51], s63 offen offset:0  lds\n"
"v_mfma_f32_16x16x32_bf16 v[52:55], a[116:119], a[4:7], v[52:55]\n"
"ds_read_b128 a[192:195], v246 offset:40960\n"
"v_accvgpr_write_b32 a236, 0\n"
"v_mfma_f32_16x16x32_bf16 v[52:55], a[148:151], a[32:35], v[52:55]\n"
"ds_read_b128 a[196:199], v246 offset:41984\n"
"v_accvgpr_write_b32 a237, 0\n"
"v_mfma_f32_16x16x32_bf16 v[84:87], a[116:119], a[8:11], v[84:87]\n"
"buffer_load_dwordx4 v253, s[48:51], s65 offen offset:2048  lds\n"
"v_mfma_f32_16x16x32_bf16 v[84:87], a[148:151], a[36:39], v[84:87]\n"
"ds_read_b128 a[200:203], v246 offset:43008\n"
"v_accvgpr_write_b32 a238, 0\n"
"v_mfma_f32_16x16x32_bf16 v[116:119], a[116:119], a[12:15], v[116:119]\n"
"ds_read_b128 a[204:207], v246 offset:44032\n"
"v_accvgpr_write_b32 a239, 0\n"
"v_mfma_f32_16x16x32_bf16 v[116:119], a[148:151], a[40:43], v[116:119]\n"
"v_add_u32 v253, 0x40000, v253\n"
"s_add_u32 m0, s71, 32768\n"
"v_mfma_f32_16x16x32_bf16 v[148:151], a[116:119], a[16:19], v[148:151]\n"
"buffer_load_dwordx4 v253, s[48:51], s63 offen offset:0  lds\n"
"v_mfma_f32_16x16x32_bf16 v[148:151], a[148:151], a[44:47], v[148:151]\n"
"ds_read_b128 a[208:211], v246 offset:45056\n"
"v_accvgpr_write_b32 a240, 0\n"
"v_mfma_f32_16x16x32_bf16 v[180:183], a[116:119], a[20:23], v[180:183]\n"
"ds_read_b128 a[212:215], v246 offset:46080\n"
"v_accvgpr_write_b32 a241, 0\n"
"v_mfma_f32_16x16x32_bf16 v[180:183], a[148:151], a[48:51], v[180:183]\n"
"buffer_load_dwordx4 v253, s[48:51], s65 offen offset:2048  lds\n"
"v_mfma_f32_16x16x32_bf16 v[24:27], a[120:123], a[0:3], v[24:27]\n"
"ds_read_b128 a[216:219], v247 offset:47120\n"
"v_accvgpr_write_b32 a242, 0\n"
"v_mfma_f32_16x16x32_bf16 v[24:27], a[152:155], a[28:31], v[24:27]\n"
"ds_read_b64_tr_b16 a[112:113], v249 offset:17408\n"
"v_accvgpr_write_b32 a243, 0\n"
"v_mfma_f32_16x16x32_bf16 v[56:59], a[120:123], a[4:7], v[56:59]\n"
"v_subrev_u32 v253, 0x40000, v253\n"
"s_add_u32 m0, s73, 16384\n"
"v_mfma_f32_16x16x32_bf16 v[56:59], a[152:155], a[32:35], v[56:59]\n"
"buffer_load_dwordx4 v254, s[52:55], s66 offen offset:0  lds\n"
"v_mfma_f32_16x16x32_bf16 v[88:91], a[120:123], a[8:11], v[88:91]\n"
"ds_read_b64_tr_b16 a[114:115], v249 offset:17920\n"
"v_accvgpr_write_b32 a244, 0\n"
"v_mfma_f32_16x16x32_bf16 v[88:91], a[152:155], a[36:39], v[88:91]\n"
"ds_read_b64_tr_b16 a[144:145], v249 offset:33792\n"
"v_accvgpr_write_b32 a245, 0\n"
"v_mfma_f32_16x16x32_bf16 v[120:123], a[120:123], a[12:15], v[120:123]\n"
"buffer_load_dwordx4 v254, s[52:55], s67 offen offset:1024  lds\n"
"v_mfma_f32_16x16x32_bf16 v[120:123], a[152:155], a[40:43], v[120:123]\n"
"ds_read_b64_tr_b16 a[146:147], v249 offset:34304\n"
"v_accvgpr_write_b32 a246, 0\n"
"v_mfma_f32_16x16x32_bf16 v[152:155], a[120:123], a[16:19], v[152:155]\n"
"ds_read_b64_tr_b16 a[116:117], v249 offset:17416\n"
"v_accvgpr_write_b32 a247, 0\n"
"v_mfma_f32_16x16x32_bf16 v[152:155], a[152:155], a[44:47], v[152:155]\n"
"buffer_load_dwordx4 v254, s[52:55], s68 offen offset:2048  lds\n"
"v_mfma_f32_16x16x32_bf16 v[184:187], a[120:123], a[20:23], v[184:187]\n"
"ds_read_b64_tr_b16 a[118:119], v249 offset:17928\n"
"v_accvgpr_write_b32 a248, 0\n"
"v_mfma_f32_16x16x32_bf16 v[184:187], a[152:155], a[48:51], v[184:187]\n"
"ds_read_b64_tr_b16 a[148:149], v249 offset:33800\n"
"v_accvgpr_write_b32 a249, 0\n"
"v_mfma_f32_16x16x32_bf16 v[28:31], a[124:127], a[0:3], v[28:31]\n"
"v_add_u32 v254, 64, v254\n"
"v_accvgpr_write_b32 a250, 0\n"
"v_mfma_f32_16x16x32_bf16 v[28:31], a[156:159], a[28:31], v[28:31]\n"
"ds_read_b64_tr_b16 a[150:151], v249 offset:34312\n"
"v_accvgpr_write_b32 a251, 0\n"
"v_mfma_f32_16x16x32_bf16 v[60:63], a[124:127], a[4:7], v[60:63]\n"
"ds_read_b64_tr_b16 a[120:121], v250 offset:17408\n"
"v_accvgpr_write_b32 a252, 0\n"
"v_mfma_f32_16x16x32_bf16 v[60:63], a[156:159], a[32:35], v[60:63]\n"
"ds_read_b64_tr_b16 a[122:123], v250 offset:17920\n"
"v_accvgpr_write_b32 a253, 0\n"
"v_mfma_f32_16x16x32_bf16 v[92:95], a[124:127], a[8:11], v[92:95]\n"
"ds_read_b64_tr_b16 a[152:153], v250 offset:33792\n"
"v_accvgpr_write_b32 a254, 0\n"
"v_mfma_f32_16x16x32_bf16 v[92:95], a[156:159], a[36:39], v[92:95]\n"
"ds_read_b64_tr_b16 a[154:155], v250 offset:34304\n"
"v_accvgpr_write_b32 a255, 0\n"
"v_mfma_f32_16x16x32_bf16 v[124:127], a[124:127], a[12:15], v[124:127]\n"
"v_mfma_f32_16x16x32_bf16 v[124:127], a[156:159], a[40:43], v[124:127]\n"
"v_mfma_f32_16x16x32_bf16 v[156:159], a[124:127], a[16:19], v[156:159]\n"
"v_mfma_f32_16x16x32_bf16 v[156:159], a[156:159], a[44:47], v[156:159]\n"
"v_mfma_f32_16x16x32_bf16 v[188:191], a[124:127], a[20:23], v[188:191]\n"
"v_mfma_f32_16x16x32_bf16 v[188:191], a[156:159], a[48:51], v[188:191]\n"
"s_waitcnt lgkmcnt(12)\n"
".p2align 6\n"
"Lq0%=:\n"
"v_mfma_f32_16x16x32_bf16 v[0:3], a[96:99], a[160:163], v[0:3]\n"
"ds_read_b64_tr_b16 a[124:125], v250 offset:17416\n"
"v_mfma_f32_16x16x32_bf16 v[0:3], a[128:131], a[192:195], v[0:3]\n"
"ds_read_b64_tr_b16 a[126:127], v250 offset:17928\n"
"v_mfma_f32_16x16x32_bf16 v[32:35], a[96:99], a[164:167], v[32:35]\n"
"ds_read_b64_tr_b16 a[156:157], v250 offset:33800\n"
"v_mfma_f32_16x16x32_bf16 v[32:35], a[128:131], a[196:199], v[32:35]\n"
"ds_read_b64_tr_b16 a[158:159], v250 offset:34312\n"
"v_mfma_f32_16x16x32_bf16 v[64:67], a[96:99], a[168:171], v[64:67]\n"
"v_mfma_f32_16x16x32_bf16 v[64:67], a[128:131], a[200:203], v[64:67]\n"
"v_mfma_f32_16x16x32_bf16 v[96:99], a[96:99], a[172:175], v[96:99]\n"
"v_mfma_f32_16x16x32_bf16 v[96:99], a[128:131], a[204:207], v[96:99]\n"
"v_mfma_f32_16x16x32_bf16 v[128:131], a[96:99], a[176:179], v[128:131]\n"
"v_mfma_f32_16x16x32_bf16 v[128:131], a[128:131], a[208:211], v[128:131]\n"
"v_mfma_f32_16x16x32_bf16 v[160:163], a[96:99], a[180:183], v[160:163]\n"
"v_mfma_f32_16x16x32_bf16 v[160:163], a[128:131], a[212:215], v[160:163]\n"
"v_mfma_f32_16x16x32_bf16 v[192:195], a[96:99], a[184:187], v[192:195]\n"
"v_mfma_f32_16x16x32_bf16 v[192:195], a[128:131], a[216:219], v[192:195]\n"
"s_waitcnt vmcnt(14) lgkmcnt(0)\n"
"s_barrier\n"
"v_mfma_f32_16x16x32_bf16 v[4:7], a[100:103], a[160:163], v[4:7]\n"
"s_add_u32 m0, s71, 16384\n"
"v_mfma_f32_16x16x32_bf16 v[4:7], a[132:135], a[192:195], v[4:7]\n"
"buffer_load_dwordx4 v253, s[48:51], s64 offen offset:1024  lds\n"
"v_mfma_f32_16x16x32_bf16 v[36:39], a[100:103], a[164:167], v[36:39]\n"
"ds_read_b64_tr_b16 a[96:97], v249 offset:49152\n"
"v_mfma_f32_16x16x32_bf16 v[36:39], a[132:135], a[196:199], v[36:39]\n"
"ds_read_b64_tr_b16 a[98:99], v249 offset:49664\n"
"v_mfma_f32_16x16x32_bf16 v[68:71], a[100:103], a[168:171], v[68:71]\n"
"ds_read_b64_tr_b16 a[128:129], v251 offset:0\n"
"v_mfma_f32_16x16x32_bf16 v[68:71], a[132:135], a[200:203], v[68:71]\n"
"buffer_load_dwordx4 v253, s[48:51], 0 offen offset:3072  lds\n"
"v_mfma_f32_16x16x32_bf16 v[100:103], a[100:103], a[172:175], v[100:103]\n"
"ds_read_b64_tr_b16 a[130:131], v251 offset:512\n"
"v_mfma_f32_16x16x32_bf16 v[100:103], a[132:135], a[204:207], v[100:103]\n"
"ds_read_b128 a[0:3], v246 offset:57344\n"
"v_mfma_f32_16x16x32_bf16 v[132:135], a[100:103], a[176:179], v[132:135]\n"
"ds_read_b128 a[4:7], v246 offset:58368\n"
"v_mfma_f32_16x16x32_bf16 v[132:135], a[132:135], a[208:211], v[132:135]\n"
"v_add_u32 v253, 0x40000, v253\n"
"s_add_u32 m0, s71, 32768\n"
"v_mfma_f32_16x16x32_bf16 v[164:167], a[100:103], a[180:183], v[164:167]\n"
"buffer_load_dwordx4 v253, s[48:51], s64 offen offset:1024  lds\n"
"v_mfma_f32_16x16x32_bf16 v[164:167], a[132:135], a[212:215], v[164:167]\n"
"ds_read_b128 a[8:11], v246 offset:59392\n"
"v_mfma_f32_16x16x32_bf16 v[196:199], a[100:103], a[184:187], v[196:199]\n"
"ds_read_b128 a[12:15], v246 offset:60416\n"
"v_mfma_f32_16x16x32_bf16 v[196:199], a[132:135], a[216:219], v[196:199]\n"
"ds_read_b64_tr_b16 a[100:101], v249 offset:49160\n"
"v_mfma_f32_16x16x32_bf16 v[8:11], a[104:107], a[160:163], v[8:11]\n"
"buffer_load_dwordx4 v253, s[48:51], 0 offen offset:3072  lds\n"
"v_mfma_f32_16x16x32_bf16 v[8:11], a[136:139], a[192:195], v[8:11]\n"
"ds_read_b64_tr_b16 a[102:103], v249 offset:49672\n"
"v_mfma_f32_16x16x32_bf16 v[40:43], a[104:107], a[164:167], v[40:43]\n"
"ds_read_b64_tr_b16 a[132:133], v251 offset:8\n"
"v_mfma_f32_16x16x32_bf16 v[40:43], a[136:139], a[196:199], v[40:43]\n"
"ds_read_b64_tr_b16 a[134:135], v251 offset:520\n"
"v_mfma_f32_16x16x32_bf16 v[72:75], a[104:107], a[168:171], v[72:75]\n"
"v_add_u32 v253, 0x40000, v253\n"
"s_add_u32 m0, s73, 32768\n"
"v_mfma_f32_16x16x32_bf16 v[72:75], a[136:139], a[200:203], v[72:75]\n"
"buffer_load_dwordx4 v254, s[52:55], s66 offen offset:0  lds\n"
"v_mfma_f32_16x16x32_bf16 v[104:107], a[104:107], a[172:175], v[104:107]\n"
"ds_read_b128 a[16:19], v246 offset:61440\n"
"v_mfma_f32_16x16x32_bf16 v[104:107], a[136:139], a[204:207], v[104:107]\n"
"ds_read_b128 a[20:23], v246 offset:62464\n"
"v_mfma_f32_16x16x32_bf16 v[136:139], a[104:107], a[176:179], v[136:139]\n"
"ds_read_b128 a[24:27], v247 offset:63504\n"
"v_mfma_f32_16x16x32_bf16 v[136:139], a[136:139], a[208:211], v[136:139]\n"
"buffer_load_dwordx4 v254, s[52:55], s67 offen offset:1024  lds\n"
"v_mfma_f32_16x16x32_bf16 v[168:171], a[104:107], a[180:183], v[168:171]\n"
"v_mfma_f32_16x16x32_bf16 v[168:171], a[136:139], a[212:215], v[168:171]\n"
"v_mfma_f32_16x16x32_bf16 v[200:203], a[104:107], a[184:187], v[200:203]\n"
"v_mfma_f32_16x16x32_bf16 v[200:203], a[136:139], a[216:219], v[200:203]\n"
"buffer_load_dwordx4 v254, s[52:55], s68 offen offset:2048  lds\n"
"v_mfma_f32_16x16x32_bf16 v[12:15], a[108:111], a[160:163], v[12:15]\n"
"ds_read_b64_tr_b16 a[104:105], v250 offset:49152\n"
"v_mfma_f32_16x16x32_bf16 v[12:15], a[140:143], a[192:195], v[12:15]\n"
"ds_read_b64_tr_b16 a[106:107], v250 offset:49664\n"
"v_mfma_f32_16x16x32_bf16 v[44:47], a[108:111], a[164:167], v[44:47]\n"
"ds_read_b64_tr_b16 a[136:137], v252 offset:0\n"
"v_mfma_f32_16x16x32_bf16 v[44:47], a[140:143], a[196:199], v[44:47]\n"
"v_add_u32 v254, 64, v254\n"
"v_mfma_f32_16x16x32_bf16 v[76:79], a[108:111], a[168:171], v[76:79]\n"
"buffer_load_dword v208, v209, s[52:55], 0 offen \n"
"v_mfma_f32_16x16x32_bf16 v[76:79], a[140:143], a[200:203], v[76:79]\n"
"ds_read_b64_tr_b16 a[138:139], v252 offset:512\n"
"v_mfma_f32_16x16x32_bf16 v[108:111], a[108:111], a[172:175], v[108:111]\n"
"v_mfma_f32_16x16x32_bf16 v[108:111], a[140:143], a[204:207], v[108:111]\n"
"v_mfma_f32_16x16x32_bf16 v[140:143], a[108:111], a[176:179], v[140:143]\n"
"v_add_u32 v209, 0x100, v209\n"
"v_mfma_f32_16x16x32_bf16 v[140:143], a[140:143], a[208:211], v[140:143]\n"
"buffer_load_dword v208, v210, s[48:51], 0 offen \n"
"v_mfma_f32_16x16x32_bf16 v[172:175], a[108:111], a[180:183], v[172:175]\n"
"v_mfma_f32_16x16x32_bf16 v[172:175], a[140:143], a[212:215], v[172:175]\n"
"v_mfma_f32_16x16x32_bf16 v[204:207], a[108:111], a[184:187], v[204:207]\n"
"v_mfma_f32_16x16x32_bf16 v[204:207], a[140:143], a[216:219], v[204:207]\n"
"v_add_u32 v210, 0x100000, v210\n"
"v_mfma_f32_16x16x32_bf16 v[16:19], a[112:115], a[160:163], v[16:19]\n"
"global_load_dword v208, v211, %8\n"
"v_mfma_f32_16x16x32_bf16 v[16:19], a[144:147], a[192:195], v[16:19]\n"
"ds_read_b64_tr_b16 a[108:109], v250 offset:49160\n"
"v_mfma_f32_16x16x32_bf16 v[48:51], a[112:115], a[164:167], v[48:51]\n"
"ds_read_b64_tr_b16 a[110:111], v250 offset:49672\n"
"v_mfma_f32_16x16x32_bf16 v[48:51], a[144:147], a[196:199], v[48:51]\n"
"ds_read_b64_tr_b16 a[140:141], v252 offset:8\n"
"v_mfma_f32_16x16x32_bf16 v[80:83], a[112:115], a[168:171], v[80:83]\n"
"v_add_u32 v211, 2097152, v211\n"
"v_mfma_f32_16x16x32_bf16 v[80:83], a[144:147], a[200:203], v[80:83]\n"
"ds_read_b64_tr_b16 a[142:143], v252 offset:520\n"
"v_mfma_f32_16x16x32_bf16 v[112:115], a[112:115], a[172:175], v[112:115]\n"
"v_mfma_f32_16x16x32_bf16 v[112:115], a[144:147], a[204:207], v[112:115]\n"
"v_mfma_f32_16x16x32_bf16 v[144:147], a[112:115], a[176:179], v[144:147]\n"
"v_mfma_f32_16x16x32_bf16 v[144:147], a[144:147], a[208:211], v[144:147]\n"
"v_mfma_f32_16x16x32_bf16 v[176:179], a[112:115], a[180:183], v[176:179]\n"
"v_mfma_f32_16x16x32_bf16 v[176:179], a[144:147], a[212:215], v[176:179]\n"
"s_waitcnt vmcnt(17) lgkmcnt(0)\n"
"s_barrier\n"
"v_mfma_f32_16x16x32_bf16 v[20:23], a[116:119], a[160:163], v[20:23]\n"
"s_add_u32 m0, s71, 49152\n"
"v_mfma_f32_16x16x32_bf16 v[20:23], a[148:151], a[192:195], v[20:23]\n"
"buffer_load_dwordx4 v253, s[48:51], s63 offen offset:0  lds\n"
"v_mfma_f32_16x16x32_bf16 v[52:55], a[116:119], a[164:167], v[52:55]\n"
"ds_read_b128 a[28:31], v248 offset:8192\n"
"v_mfma_f32_16x16x32_bf16 v[52:55], a[148:151], a[196:199], v[52:55]\n"
"ds_read_b128 a[32:35], v248 offset:9216\n"
"v_mfma_f32_16x16x32_bf16 v[84:87], a[116:119], a[168:171], v[84:87]\n"
"buffer_load_dwordx4 v253, s[48:51], s65 offen offset:2048  lds\n"
"v_mfma_f32_16x16x32_bf16 v[84:87], a[148:151], a[200:203], v[84:87]\n"
"ds_read_b128 a[36:39], v248 offset:10240\n"
"v_mfma_f32_16x16x32_bf16 v[116:119], a[116:119], a[172:175], v[116:119]\n"
"ds_read_b128 a[40:43], v248 offset:11264\n"
"v_mfma_f32_16x16x32_bf16 v[116:119], a[148:151], a[204:207], v[116:119]\n"
"v_add_u32 v253, 0x40000, v253\n"
"s_mov_b32 m0, s72\n"
"v_mfma_f32_16x16x32_bf16 v[148:151], a[116:119], a[176:179], v[148:151]\n"
"buffer_load_dwordx4 v253, s[48:51], s63 offen offset:0  lds\n"
"v_mfma_f32_16x16x32_bf16 v[148:151], a[148:151], a[208:211], v[148:151]\n"
"ds_read_b128 a[44:47], v248 offset:12288\n"
"v_mfma_f32_16x16x32_bf16 v[180:183], a[116:119], a[180:183], v[180:183]\n"
"ds_read_b128 a[48:51], v248 offset:13312\n"
"v_mfma_f32_16x16x32_bf16 v[180:183], a[148:151], a[212:215], v[180:183]\n"
"buffer_load_dwordx4 v253, s[48:51], s65 offen offset:2048  lds\n"
"v_mfma_f32_16x16x32_bf16 v[24:27], a[120:123], a[160:163], v[24:27]\n"
"ds_read_b128 a[52:55], v248 offset:30736\n"
"v_mfma_f32_16x16x32_bf16 v[24:27], a[152:155], a[192:195], v[24:27]\n"
"ds_read_b64_tr_b16 a[112:113], v249 offset:50176\n"
"v_mfma_f32_16x16x32_bf16 v[56:59], a[120:123], a[164:167], v[56:59]\n"
"v_subrev_u32 v253, 0x40000, v253\n"
"s_add_u32 m0, s73, 49152\n"
"v_mfma_f32_16x16x32_bf16 v[56:59], a[152:155], a[196:199], v[56:59]\n"
"buffer_load_dwordx4 v254, s[52:55], s66 offen offset:0  lds\n"
"v_mfma_f32_16x16x32_bf16 v[88:91], a[120:123], a[168:171], v[88:91]\n"
"ds_read_b64_tr_b16 a[114:115], v249 offset:50688\n"
"v_mfma_f32_16x16x32_bf16 v[88:91], a[152:155], a[200:203], v[88:91]\n"
"ds_read_b64_tr_b16 a[144:145], v251 offset:1024\n"
"v_mfma_f32_16x16x32_bf16 v[120:123], a[120:123], a[172:175], v[120:123]\n"
"buffer_load_dwordx4 v254, s[52:55], s67 offen offset:1024  lds\n"
"v_mfma_f32_16x16x32_bf16 v[120:123], a[152:155], a[204:207], v[120:123]\n"
"ds_read_b64_tr_b16 a[146:147], v251 offset:1536\n"
"v_mfma_f32_16x16x32_bf16 v[152:155], a[120:123], a[176:179], v[152:155]\n"
"ds_read_b64_tr_b16 a[116:117], v249 offset:50184\n"
"v_mfma_f32_16x16x32_bf16 v[152:155], a[152:155], a[208:211], v[152:155]\n"
"buffer_load_dwordx4 v254, s[52:55], s68 offen offset:2048  lds\n"
"v_mfma_f32_16x16x32_bf16 v[184:187], a[120:123], a[180:183], v[184:187]\n"
"ds_read_b64_tr_b16 a[118:119], v249 offset:50696\n"
"v_mfma_f32_16x16x32_bf16 v[184:187], a[152:155], a[212:215], v[184:187]\n"
"ds_read_b64_tr_b16 a[148:149], v251 offset:1032\n"
"v_mfma_f32_16x16x32_bf16 v[28:31], a[124:127], a[160:163], v[28:31]\n"
"v_add_u32 v254, 64, v254\n"
"v_mfma_f32_16x16x32_bf16 v[28:31], a[156:159], a[192:195], v[28:31]\n"
"ds_read_b64_tr_b16 a[150:151], v251 offset:1544\n"
"v_mfma_f32_16x16x32_bf16 v[60:63], a[124:127], a[164:167], v[60:63]\n"
"ds_read_b64_tr_b16 a[120:121], v250 offset:50176\n"
"v_mfma_f32_16x16x32_bf16 v[60:63], a[156:159], a[196:199], v[60:63]\n"
"ds_read_b64_tr_b16 a[122:123], v250 offset:50688\n"
"v_mfma_f32_16x16x32_bf16 v[92:95], a[124:127], a[168:171], v[92:95]\n"
"ds_read_b64_tr_b16 a[152:153], v252 offset:1024\n"
"v_mfma_f32_16x16x32_bf16 v[92:95], a[156:159], a[200:203], v[92:95]\n"
"ds_read_b64_tr_b16 a[154:155], v252 offset:1536\n"
"v_mfma_f32_16x16x32_bf16 v[124:127], a[124:127], a[172:175], v[124:127]\n"
"v_mfma_f32_16x16x32_bf16 v[124:127], a[156:159], a[204:207], v[124:127]\n"
"v_mfma_f32_16x16x32_bf16 v[156:159], a[124:127], a[176:179], v[156:159]\n"
"v_mfma_f32_16x16x32_bf16 v[156:159], a[156:159], a[208:211], v[156:159]\n"
"v_mfma_f32_16x16x32_bf16 v[188:191], a[124:127], a[180:183], v[188:191]\n"
"v_mfma_f32_16x16x32_bf16 v[188:191], a[156:159], a[212:215], v[188:191]\n"
"s_waitcnt lgkmcnt(12)\n"
"v_mfma_f32_16x16x32_bf16 v[0:3], a[96:99], a[0:3], v[0:3]\n"
"ds_read_b64_tr_b16 a[124:125], v250 offset:50184\n"
"v_mfma_f32_16x16x32_bf16 v[0:3], a[128:131], a[28:31], v[0:3]\n"
"ds_read_b64_tr_b16 a[126:127], v250 offset:50696\n"
"v_mfma_f32_16x16x32_bf16 v[32:35], a[96:99], a[4:7], v[32:35]\n"
"ds_read_b64_tr_b16 a[156:157], v252 offset:1032\n"
"v_mfma_f32_16x16x32_bf16 v[32:35], a[128:131], a[32:35], v[32:35]\n"
"ds_read_b64_tr_b16 a[158:159], v252 offset:1544\n"
"v_mfma_f32_16x16x32_bf16 v[64:67], a[96:99], a[8:11], v[64:67]\n"
"v_mfma_f32_16x16x32_bf16 v[64:67], a[128:131], a[36:39], v[64:67]\n"
"v_mfma_f32_16x16x32_bf16 v[96:99], a[96:99], a[12:15], v[96:99]\n"
"v_mfma_f32_16x16x32_bf16 v[96:99], a[128:131], a[40:43], v[96:99]\n"
"v_mfma_f32_16x16x32_bf16 v[128:131], a[96:99], a[16:19], v[128:131]\n"
"v_mfma_f32_16x16x32_bf16 v[128:131], a[128:131], a[44:47], v[128:131]\n"
"v_mfma_f32_16x16x32_bf16 v[160:163], a[96:99], a[20:23], v[160:163]\n"
"v_mfma_f32_16x16x32_bf16 v[160:163], a[128:131], a[48:51], v[160:163]\n"
"v_mfma_f32_16x16x32_bf16 v[192:195], a[96:99], a[24:27], v[192:195]\n"
"v_mfma_f32_16x16x32_bf16 v[192:195], a[128:131], a[52:55], v[192:195]\n"
"s_waitcnt vmcnt(17) lgkmcnt(0)\n"
"s_barrier\n"
"v_mfma_f32_16x16x32_bf16 v[4:7], a[100:103], a[0:3], v[4:7]\n"
"s_add_u32 m0, s71, 49152\n"
"v_mfma_f32_16x16x32_bf16 v[4:7], a[132:135], a[28:31], v[4:7]\n"
"buffer_load_dwordx4 v253, s[48:51], s64 offen offset:1024  lds\n"
"v_mfma_f32_16x16x32_bf16 v[36:39], a[100:103], a[4:7], v[36:39]\n"
"ds_read_b64_tr_b16 a[96:97], v249 offset:16384\n"
"v_mfma_f32_16x16x32_bf16 v[36:39], a[132:135], a[32:35], v[36:39]\n"
"ds_read_b64_tr_b16 a[98:99], v249 offset:16896\n"
"v_mfma_f32_16x16x32_bf16 v[68:71], a[100:103], a[8:11], v[68:71]\n"
"ds_read_b64_tr_b16 a[128:129], v249 offset:32768\n"
"v_mfma_f32_16x16x32_bf16 v[68:71], a[132:135], a[36:39], v[68:71]\n"
"buffer_load_dwordx4 v253, s[48:51], 0 offen offset:3072  lds\n"
"v_mfma_f32_16x16x32_bf16 v[100:103], a[100:103], a[12:15], v[100:103]\n"
"ds_read_b64_tr_b16 a[130:131], v249 offset:33280\n"
"v_mfma_f32_16x16x32_bf16 v[100:103], a[132:135], a[40:43], v[100:103]\n"
"ds_read_b128 a[160:163], v246 offset:24576\n"
"v_mfma_f32_16x16x32_bf16 v[132:135], a[100:103], a[16:19], v[132:135]\n"
"ds_read_b128 a[164:167], v246 offset:25600\n"
"v_mfma_f32_16x16x32_bf16 v[132:135], a[132:135], a[44:47], v[132:135]\n"
"v_add_u32 v253, 0x40000, v253\n"
"s_mov_b32 m0, s72\n"
"v_mfma_f32_16x16x32_bf16 v[164:167], a[100:103], a[20:23], v[164:167]\n"
"buffer_load_dwordx4 v253, s[48:51], s64 offen offset:1024  lds\n"
"v_mfma_f32_16x16x32_bf16 v[164:167], a[132:135], a[48:51], v[164:167]\n"
"ds_read_b128 a[168:171], v246 offset:26624\n"
"v_mfma_f32_16x16x32_bf16 v[196:199], a[100:103], a[24:27], v[196:199]\n"
"ds_read_b128 a[172:175], v246 offset:27648\n"
"v_mfma_f32_16x16x32_bf16 v[196:199], a[132:135], a[52:55], v[196:199]\n"
"ds_read_b64_tr_b16 a[100:101], v249 offset:16392\n"
"v_mfma_f32_16x16x32_bf16 v[8:11], a[104:107], a[0:3], v[8:11]\n"
"buffer_load_dwordx4 v253, s[48:51], 0 offen offset:3072  lds\n"
"v_mfma_f32_16x16x32_bf16 v[8:11], a[136:139], a[28:31], v[8:11]\n"
"ds_read_b64_tr_b16 a[102:103], v249 offset:16904\n"
"v_mfma_f32_16x16x32_bf16 v[40:43], a[104:107], a[4:7], v[40:43]\n"
"ds_read_b64_tr_b16 a[132:133], v249 offset:32776\n"
"v_mfma_f32_16x16x32_bf16 v[40:43], a[136:139], a[32:35], v[40:43]\n"
"ds_read_b64_tr_b16 a[134:135], v249 offset:33288\n"
"v_mfma_f32_16x16x32_bf16 v[72:75], a[104:107], a[8:11], v[72:75]\n"
"v_add_u32 v253, 0x40000, v253\n"
"s_mov_b32 m0, s74\n"
"v_mfma_f32_16x16x32_bf16 v[72:75], a[136:139], a[36:39], v[72:75]\n"
"buffer_load_dwordx4 v254, s[52:55], s66 offen offset:0  lds\n"
"v_mfma_f32_16x16x32_bf16 v[104:107], a[104:107], a[12:15], v[104:107]\n"
"ds_read_b128 a[176:179], v246 offset:28672\n"
"v_mfma_f32_16x16x32_bf16 v[104:107], a[136:139], a[40:43], v[104:107]\n"
"ds_read_b128 a[180:183], v246 offset:29696\n"
"v_mfma_f32_16x16x32_bf16 v[136:139], a[104:107], a[16:19], v[136:139]\n"
"ds_read_b128 a[184:187], v247 offset:30736\n"
"v_mfma_f32_16x16x32_bf16 v[136:139], a[136:139], a[44:47], v[136:139]\n"
"buffer_load_dwordx4 v254, s[52:55], s67 offen offset:1024  lds\n"
"v_mfma_f32_16x16x32_bf16 v[168:171], a[104:107], a[20:23], v[168:171]\n"
"v_mfma_f32_16x16x32_bf16 v[168:171], a[136:139], a[48:51], v[168:171]\n"
"v_mfma_f32_16x16x32_bf16 v[200:203], a[104:107], a[24:27], v[200:203]\n"
"v_mfma_f32_16x16x32_bf16 v[200:203], a[136:139], a[52:55], v[200:203]\n"
"buffer_load_dwordx4 v254, s[52:55], s68 offen offset:2048  lds\n"
"v_mfma_f32_16x16x32_bf16 v[12:15], a[108:111], a[0:3], v[12:15]\n"
"ds_read_b64_tr_b16 a[104:105], v250 offset:16384\n"
"v_mfma_f32_16x16x32_bf16 v[12:15], a[140:143], a[28:31], v[12:15]\n"
"ds_read_b64_tr_b16 a[106:107], v250 offset:16896\n"
"v_mfma_f32_16x16x32_bf16 v[44:47], a[108:111], a[4:7], v[44:47]\n"
"ds_read_b64_tr_b16 a[136:137], v250 offset:32768\n"
"v_mfma_f32_16x16x32_bf16 v[44:47], a[140:143], a[32:35], v[44:47]\n"
"v_add_u32 v254, 64, v254\n"
"v_mfma_f32_16x16x32_bf16 v[76:79], a[108:111], a[8:11], v[76:79]\n"
"ds_read_b64_tr_b16 a[138:139], v250 offset:33280\n"
"v_mfma_f32_16x16x32_bf16 v[76:79], a[140:143], a[36:39], v[76:79]\n"
"v_mfma_f32_16x16x32_bf16 v[108:111], a[108:111], a[12:15], v[108:111]\n"
"v_mfma_f32_16x16x32_bf16 v[108:111], a[140:143], a[40:43], v[108:111]\n"
"v_mfma_f32_16x16x32_bf16 v[140:143], a[108:111], a[16:19], v[140:143]\n"
"v_mfma_f32_16x16x32_bf16 v[140:143], a[140:143], a[44:47], v[140:143]\n"
"v_mfma_f32_16x16x32_bf16 v[172:175], a[108:111], a[20:23], v[172:175]\n"
"v_mfma_f32_16x16x32_bf16 v[172:175], a[140:143], a[48:51], v[172:175]\n"
"v_mfma_f32_16x16x32_bf16 v[204:207], a[108:111], a[24:27], v[204:207]\n"
"v_mfma_f32_16x16x32_bf16 v[204:207], a[140:143], a[52:55], v[204:207]\n"
"ds_read_b64_tr_b16 a[108:109], v250 offset:16392\n"
"v_mfma_f32_16x16x32_bf16 v[16:19], a[112:115], a[0:3], v[16:19]\n"
"ds_read_b64_tr_b16 a[110:111], v250 offset:16904\n"
"v_mfma_f32_16x16x32_bf16 v[16:19], a[144:147], a[28:31], v[16:19]\n"
"ds_read_b64_tr_b16 a[140:141], v250 offset:32776\n"
"v_mfma_f32_16x16x32_bf16 v[48:51], a[112:115], a[4:7], v[48:51]\n"
"ds_read_b64_tr_b16 a[142:143], v250 offset:33288\n"
"v_mfma_f32_16x16x32_bf16 v[48:51], a[144:147], a[32:35], v[48:51]\n"
"v_mfma_f32_16x16x32_bf16 v[80:83], a[112:115], a[8:11], v[80:83]\n"
"v_mfma_f32_16x16x32_bf16 v[80:83], a[144:147], a[36:39], v[80:83]\n"
"v_mfma_f32_16x16x32_bf16 v[112:115], a[112:115], a[12:15], v[112:115]\n"
"v_mfma_f32_16x16x32_bf16 v[112:115], a[144:147], a[40:43], v[112:115]\n"
"v_mfma_f32_16x16x32_bf16 v[144:147], a[112:115], a[16:19], v[144:147]\n"
"v_mfma_f32_16x16x32_bf16 v[144:147], a[144:147], a[44:47], v[144:147]\n"
"v_mfma_f32_16x16x32_bf16 v[176:179], a[112:115], a[20:23], v[176:179]\n"
"v_mfma_f32_16x16x32_bf16 v[176:179], a[144:147], a[48:51], v[176:179]\n"
"s_waitcnt vmcnt(17) lgkmcnt(0)\n"
"s_barrier\n"
"v_mfma_f32_16x16x32_bf16 v[20:23], a[116:119], a[0:3], v[20:23]\n"
"s_add_u32 m0, s71, 16384\n"
"v_mfma_f32_16x16x32_bf16 v[20:23], a[148:151], a[28:31], v[20:23]\n"
"buffer_load_dwordx4 v253, s[48:51], s63 offen offset:0  lds\n"
"v_mfma_f32_16x16x32_bf16 v[52:55], a[116:119], a[4:7], v[52:55]\n"
"ds_read_b128 a[192:195], v246 offset:40960\n"
"v_mfma_f32_16x16x32_bf16 v[52:55], a[148:151], a[32:35], v[52:55]\n"
"ds_read_b128 a[196:199], v246 offset:41984\n"
"v_mfma_f32_16x16x32_bf16 v[84:87], a[116:119], a[8:11], v[84:87]\n"
"buffer_load_dwordx4 v253, s[48:51], s65 offen offset:2048  lds\n"
"v_mfma_f32_16x16x32_bf16 v[84:87], a[148:151], a[36:39], v[84:87]\n"
"ds_read_b128 a[200:203], v246 offset:43008\n"
"v_mfma_f32_16x16x32_bf16 v[116:119], a[116:119], a[12:15], v[116:119]\n"
"ds_read_b128 a[204:207], v246 offset:44032\n"
"v_mfma_f32_16x16x32_bf16 v[116:119], a[148:151], a[40:43], v[116:119]\n"
"v_add_u32 v253, 0x40000, v253\n"
"s_add_u32 m0, s71, 32768\n"
"v_mfma_f32_16x16x32_bf16 v[148:151], a[116:119], a[16:19], v[148:151]\n"
"buffer_load_dwordx4 v253, s[48:51], s63 offen offset:0  lds\n"
"v_mfma_f32_16x16x32_bf16 v[148:151], a[148:151], a[44:47], v[148:151]\n"
"ds_read_b128 a[208:211], v246 offset:45056\n"
"v_mfma_f32_16x16x32_bf16 v[180:183], a[116:119], a[20:23], v[180:183]\n"
"ds_read_b128 a[212:215], v246 offset:46080\n"
"v_mfma_f32_16x16x32_bf16 v[180:183], a[148:151], a[48:51], v[180:183]\n"
"buffer_load_dwordx4 v253, s[48:51], s65 offen offset:2048  lds\n"
"v_mfma_f32_16x16x32_bf16 v[24:27], a[120:123], a[0:3], v[24:27]\n"
"ds_read_b128 a[216:219], v247 offset:47120\n"
"v_mfma_f32_16x16x32_bf16 v[24:27], a[152:155], a[28:31], v[24:27]\n"
"ds_read_b64_tr_b16 a[112:113], v249 offset:17408\n"
"v_mfma_f32_16x16x32_bf16 v[56:59], a[120:123], a[4:7], v[56:59]\n"
"v_subrev_u32 v253, 0x40000, v253\n"
"s_add_u32 m0, s73, 16384\n"
"v_mfma_f32_16x16x32_bf16 v[56:59], a[152:155], a[32:35], v[56:59]\n"
"buffer_load_dwordx4 v254, s[52:55], s66 offen offset:0  lds\n"
"v_mfma_f32_16x16x32_bf16 v[88:91], a[120:123], a[8:11], v[88:91]\n"
"ds_read_b64_tr_b16 a[114:115], v249 offset:17920\n"
"v_mfma_f32_16x16x32_bf16 v[88:91], a[152:155], a[36:39], v[88:91]\n"
"ds_read_b64_tr_b16 a[144:145], v249 offset:33792\n"
"v_mfma_f32_16x16x32_bf16 v[120:123], a[120:123], a[12:15], v[120:123]\n"
"buffer_load_dwordx4 v254, s[52:55], s67 offen offset:1024  lds\n"
"v_mfma_f32_16x16x32_bf16 v[120:123], a[152:155], a[40:43], v[120:123]\n"
"ds_read_b64_tr_b16 a[146:147], v249 offset:34304\n"
"v_mfma_f32_16x16x32_bf16 v[152:155], a[120:123], a[16:19], v[152:155]\n"
"ds_read_b64_tr_b16 a[116:117], v249 offset:17416\n"
"v_mfma_f32_16x16x32_bf16 v[152:155], a[152:155], a[44:47], v[152:155]\n"
"buffer_load_dwordx4 v254, s[52:55], s68 offen offset:2048  lds\n"
"v_mfma_f32_16x16x32_bf16 v[184:187], a[120:123], a[20:23], v[184:187]\n"
"ds_read_b64_tr_b16 a[118:119], v249 offset:17928\n"
"v_mfma_f32_16x16x32_bf16 v[184:187], a[152:155], a[48:51], v[184:187]\n"
"ds_read_b64_tr_b16 a[148:149], v249 offset:33800\n"
"v_mfma_f32_16x16x32_bf16 v[28:31], a[124:127], a[0:3], v[28:31]\n"
"v_add_u32 v254, 64, v254\n"
"v_mfma_f32_16x16x32_bf16 v[28:31], a[156:159], a[28:31], v[28:31]\n"
"ds_read_b64_tr_b16 a[150:151], v249 offset:34312\n"
"v_mfma_f32_16x16x32_bf16 v[60:63], a[124:127], a[4:7], v[60:63]\n"
"ds_read_b64_tr_b16 a[120:121], v250 offset:17408\n"
"v_mfma_f32_16x16x32_bf16 v[60:63], a[156:159], a[32:35], v[60:63]\n"
"ds_read_b64_tr_b16 a[122:123], v250 offset:17920\n"
"v_mfma_f32_16x16x32_bf16 v[92:95], a[124:127], a[8:11], v[92:95]\n"
"ds_read_b64_tr_b16 a[152:153], v250 offset:33792\n"
"v_mfma_f32_16x16x32_bf16 v[92:95], a[156:159], a[36:39], v[92:95]\n"
"ds_read_b64_tr_b16 a[154:155], v250 offset:34304\n"
"v_mfma_f32_16x16x32_bf16 v[124:127], a[124:127], a[12:15], v[124:127]\n"
"v_mfma_f32_16x16x32_bf16 v[124:127], a[156:159], a[40:43], v[124:127]\n"
"v_mfma_f32_16x16x32_bf16 v[156:159], a[124:127], a[16:19], v[156:159]\n"
"v_mfma_f32_16x16x32_bf16 v[156:159], a[156:159], a[44:47], v[156:159]\n"
"v_mfma_f32_16x16x32_bf16 v[188:191], a[124:127], a[20:23], v[188:191]\n"
"v_mfma_f32_16x16x32_bf16 v[188:191], a[156:159], a[48:51], v[188:191]\n"
"s_waitcnt lgkmcnt(12)\n"
"s_sub_u32 s75, s75, 1\n"
"s_cbranch_scc0 Lq0%=\n"
"v_mfma_f32_16x16x32_bf16 v[0:3], a[96:99], a[160:163], v[0:3]\n"
"ds_read_b64_tr_b16 a[124:125], v250 offset:17416\n"
"v_mfma_f32_16x16x32_bf16 v[0:3], a[128:131], a[192:195], v[0:3]\n"
"ds_read_b64_tr_b16 a[126:127], v250 offset:17928\n"
"v_mfma_f32_16x16x32_bf16 v[32:35], a[96:99], a[164:167], v[32:35]\n"
"ds_read_b64_tr_b16 a[156:157], v250 offset:33800\n"
"v_mfma_f32_16x16x32_bf16 v[32:35], a[128:131], a[196:199], v[32:35]\n"
"ds_read_b64_tr_b16 a[158:159], v250 offset:34312\n"
"v_mfma_f32_16x16x32_bf16 v[64:67], a[96:99], a[168:171], v[64:67]\n"
"s_add_u32 m0, s70, 0\n"
"v_mfma_f32_16x16x32_bf16 v[64:67], a[128:131], a[200:203], v[64:67]\n"
"buffer_load_dwordx4 v223, s[40:43], s60 offen offset:0 nt lds\n"
"v_mfma_f32_16x16x32_bf16 v[96:99], a[96:99], a[172:175], v[96:99]\n"
"buffer_load_dwordx4 v223, s[40:43], s61 offen offset:1024 nt lds\n"
"v_mfma_f32_16x16x32_bf16 v[96:99], a[128:131], a[204:207], v[96:99]\n"
"buffer_load_dwordx4 v223, s[40:43], s62 offen offset:2048 nt lds\n"
"v_mfma_f32_16x16x32_bf16 v[128:131], a[96:99], a[176:179], v[128:131]\n"
"buffer_load_dwordx4 v223, s[40:43], 0 offen offset:3072 nt lds\n"
"v_mfma_f32_16x16x32_bf16 v[128:131], a[128:131], a[208:211], v[128:131]\n"
"v_mfma_f32_16x16x32_bf16 v[160:163], a[96:99], a[180:183], v[160:163]\n"
"v_mfma_f32_16x16x32_bf16 v[160:163], a[128:131], a[212:215], v[160:163]\n"
"v_mfma_f32_16x16x32_bf16 v[192:195], a[96:99], a[184:187], v[192:195]\n"
"v_mfma_f32_16x16x32_bf16 v[192:195], a[128:131], a[216:219], v[192:195]\n"
"s_waitcnt vmcnt(18) lgkmcnt(0)\n"
"s_barrier\n"
"v_mfma_f32_16x16x32_bf16 v[4:7], a[100:103], a[160:163], v[4:7]\n"
"s_add_u32 m0, s71, 16384\n"
"v_mfma_f32_16x16x32_bf16 v[4:7], a[132:135], a[192:195], v[4:7]\n"
"buffer_load_dwordx4 v253, s[48:51], s64 offen offset:1024  lds\n"
"v_mfma_f32_16x16x32_bf16 v[36:39], a[100:103], a[164:167], v[36:39]\n"
"ds_read_b64_tr_b16 a[96:97], v249 offset:49152\n"
"v_mfma_f32_16x16x32_bf16 v[36:39], a[132:135], a[196:199], v[36:39]\n"
"ds_read_b64_tr_b16 a[98:99], v249 offset:49664\n"
"v_mfma_f32_16x16x32_bf16 v[68:71], a[100:103], a[168:171], v[68:71]\n"
"ds_read_b64_tr_b16 a[128:129], v251 offset:0\n"
"v_mfma_f32_16x16x32_bf16 v[68:71], a[132:135], a[200:203], v[68:71]\n"
"buffer_load_dwordx4 v253, s[48:51], 0 offen offset:3072  lds\n"
"v_mfma_f32_16x16x32_bf16 v[100:103], a[100:103], a[172:175], v[100:103]\n"
"ds_read_b64_tr_b16 a[130:131], v251 offset:512\n"
"v_mfma_f32_16x16x32_bf16 v[100:103], a[132:135], a[204:207], v[100:103]\n"
"ds_read_b128 a[0:3], v246 offset:57344\n"
"v_mfma_f32_16x16x32_bf16 v[132:135], a[100:103], a[176:179], v[132:135]\n"
"ds_read_b128 a[4:7], v246 offset:58368\n"
"v_mfma_f32_16x16x32_bf16 v[132:135], a[132:135], a[208:211], v[132:135]\n"
"v_add_u32 v253, 0x40000, v253\n"
"s_add_u32 m0, s71, 32768\n"
"v_mfma_f32_16x16x32_bf16 v[164:167], a[100:103], a[180:183], v[164:167]\n"
"buffer_load_dwordx4 v253, s[48:51], s64 offen offset:1024  lds\n"
"v_mfma_f32_16x16x32_bf16 v[164:167], a[132:135], a[212:215], v[164:167]\n"
"ds_read_b128 a[8:11], v246 offset:59392\n"
"v_mfma_f32_16x16x32_bf16 v[196:199], a[100:103], a[184:187], v[196:199]\n"
"ds_read_b128 a[12:15], v246 offset:60416\n"
"v_mfma_f32_16x16x32_bf16 v[196:199], a[132:135], a[216:219], v[196:199]\n"
"ds_read_b64_tr_b16 a[100:101], v249 offset:49160\n"
"v_mfma_f32_16x16x32_bf16 v[8:11], a[104:107], a[160:163], v[8:11]\n"
"buffer_load_dwordx4 v253, s[48:51], 0 offen offset:3072  lds\n"
"v_mfma_f32_16x16x32_bf16 v[8:11], a[136:139], a[192:195], v[8:11]\n"
"ds_read_b64_tr_b16 a[102:103], v249 offset:49672\n"
"v_mfma_f32_16x16x32_bf16 v[40:43], a[104:107], a[164:167], v[40:43]\n"
"ds_read_b64_tr_b16 a[132:133], v251 offset:8\n"
"v_mfma_f32_16x16x32_bf16 v[40:43], a[136:139], a[196:199], v[40:43]\n"
"ds_read_b64_tr_b16 a[134:135], v251 offset:520\n"
"v_mfma_f32_16x16x32_bf16 v[72:75], a[104:107], a[168:171], v[72:75]\n"
"v_add_u32 v253, 0x40000, v253\n"
"s_add_u32 m0, s73, 32768\n"
"v_mfma_f32_16x16x32_bf16 v[72:75], a[136:139], a[200:203], v[72:75]\n"
"buffer_load_dwordx4 v254, s[52:55], s66 offen offset:0  lds\n"
"v_mfma_f32_16x16x32_bf16 v[104:107], a[104:107], a[172:175], v[104:107]\n"
"ds_read_b128 a[16:19], v246 offset:61440\n"
"v_mfma_f32_16x16x32_bf16 v[104:107], a[136:139], a[204:207], v[104:107]\n"
"ds_read_b128 a[20:23], v246 offset:62464\n"
"v_mfma_f32_16x16x32_bf16 v[136:139], a[104:107], a[176:179], v[136:139]\n"
"ds_read_b128 a[24:27], v247 offset:63504\n"
"v_mfma_f32_16x16x32_bf16 v[136:139], a[136:139], a[208:211], v[136:139]\n"
"buffer_load_dwordx4 v254, s[52:55], s67 offen offset:1024  lds\n"
"v_mfma_f32_16x16x32_bf16 v[168:171], a[104:107], a[180:183], v[168:171]\n"
"v_mfma_f32_16x16x32_bf16 v[168:171], a[136:139], a[212:215], v[168:171]\n"
"v_mfma_f32_16x16x32_bf16 v[200:203], a[104:107], a[184:187], v[200:203]\n"
"v_mfma_f32_16x16x32_bf16 v[200:203], a[136:139], a[216:219], v[200:203]\n"
"buffer_load_dwordx4 v254, s[52:55], s68 offen offset:2048  lds\n"
"v_mfma_f32_16x16x32_bf16 v[12:15], a[108:111], a[160:163], v[12:15]\n"
"ds_read_b64_tr_b16 a[104:105], v250 offset:49152\n"
"v_mfma_f32_16x16x32_bf16 v[12:15], a[140:143], a[192:195], v[12:15]\n"
"ds_read_b64_tr_b16 a[106:107], v250 offset:49664\n"
"v_mfma_f32_16x16x32_bf16 v[44:47], a[108:111], a[164:167], v[44:47]\n"
"ds_read_b64_tr_b16 a[136:137], v252 offset:0\n"
"v_mfma_f32_16x16x32_bf16 v[44:47], a[140:143], a[196:199], v[44:47]\n"
"v_add_u32 v254, 64, v254\n"
"v_mfma_f32_16x16x32_bf16 v[76:79], a[108:111], a[168:171], v[76:79]\n"
"buffer_load_dword v208, v209, s[52:55], 0 offen \n"
"v_mfma_f32_16x16x32_bf16 v[76:79], a[140:143], a[200:203], v[76:79]\n"
"ds_read_b64_tr_b16 a[138:139], v252 offset:512\n"
"v_mfma_f32_16x16x32_bf16 v[108:111], a[108:111], a[172:175], v[108:111]\n"
"v_mfma_f32_16x16x32_bf16 v[108:111], a[140:143], a[204:207], v[108:111]\n"
"v_mfma_f32_16x16x32_bf16 v[140:143], a[108:111], a[176:179], v[140:143]\n"
"v_add_u32 v209, 0x100, v209\n"
"v_mfma_f32_16x16x32_bf16 v[140:143], a[140:143], a[208:211], v[140:143]\n"
"buffer_load_dword v208, v210, s[48:51], 0 offen \n"
"v_mfma_f32_16x16x32_bf16 v[172:175], a[108:111], a[180:183], v[172:175]\n"
"v_mfma_f32_16x16x32_bf16 v[172:175], a[140:143], a[212:215], v[172:175]\n"
"v_mfma_f32_16x16x32_bf16 v[204:207], a[108:111], a[184:187], v[204:207]\n"
"v_mfma_f32_16x16x32_bf16 v[204:207], a[140:143], a[216:219], v[204:207]\n"
"v_add_u32 v210, 0x100000, v210\n"
"v_mfma_f32_16x16x32_bf16 v[16:19], a[112:115], a[160:163], v[16:19]\n"
"ds_read_b64_tr_b16 a[108:109], v250 offset:49160\n"
"v_mfma_f32_16x16x32_bf16 v[16:19], a[144:147], a[192:195], v[16:19]\n"
"ds_read_b64_tr_b16 a[110:111], v250 offset:49672\n"
"v_mfma_f32_16x16x32_bf16 v[48:51], a[112:115], a[164:167], v[48:51]\n"
"ds_read_b64_tr_b16 a[140:141], v252 offset:8\n"
"v_mfma_f32_16x16x32_bf16 v[48:51], a[144:147], a[196:199], v[48:51]\n"
"ds_read_b64_tr_b16 a[142:143], v252 offset:520\n"
"v_mfma_f32_16x16x32_bf16 v[80:83], a[112:115], a[168:171], v[80:83]\n"
"v_mfma_f32_16x16x32_bf16 v[80:83], a[144:147], a[200:203], v[80:83]\n"
"v_mfma_f32_16x16x32_bf16 v[112:115], a[112:115], a[172:175], v[112:115]\n"
"v_mfma_f32_16x16x32_bf16 v[112:115], a[144:147], a[204:207], v[112:115]\n"
"v_mfma_f32_16x16x32_bf16 v[144:147], a[112:115], a[176:179], v[144:147]\n"
"v_mfma_f32_16x16x32_bf16 v[144:147], a[144:147], a[208:211], v[144:147]\n"
"v_mfma_f32_16x16x32_bf16 v[176:179], a[112:115], a[180:183], v[176:179]\n"
"v_mfma_f32_16x16x32_bf16 v[176:179], a[144:147], a[212:215], v[176:179]\n"
"s_waitcnt vmcnt(20) lgkmcnt(0)\n"
"s_barrier\n"
"v_mfma_f32_16x16x32_bf16 v[20:23], a[116:119], a[160:163], v[20:23]\n"
"s_add_u32 m0, s71, 49152\n"
"v_mfma_f32_16x16x32_bf16 v[20:23], a[148:151], a[192:195], v[20:23]\n"
"buffer_load_dwordx4 v253, s[48:51], s63 offen offset:0  lds\n"
"v_mfma_f32_16x16x32_bf16 v[52:55], a[116:119], a[164:167], v[52:55]\n"
"ds_read_b128 a[28:31], v248 offset:8192\n"
"v_mfma_f32_16x16x32_bf16 v[52:55], a[148:151], a[196:199], v[52:55]\n"
"ds_read_b128 a[32:35], v248 offset:9216\n"
"v_mfma_f32_16x16x32_bf16 v[84:87], a[116:119], a[168:171], v[84:87]\n"
"buffer_load_dwordx4 v253, s[48:51], s65 offen offset:2048  lds\n"
"v_mfma_f32_16x16x32_bf16 v[84:87], a[148:151], a[200:203], v[84:87]\n"
"ds_read_b128 a[36:39], v248 offset:10240\n"
"v_mfma_f32_16x16x32_bf16 v[116:119], a[116:119], a[172:175], v[116:119]\n"
"ds_read_b128 a[40:43], v248 offset:11264\n"
"v_mfma_f32_16x16x32_bf16 v[116:119], a[148:151], a[204:207], v[116:119]\n"
"v_add_u32 v253, 0x40000, v253\n"
"s_mov_b32 m0, s72\n"
"v_mfma_f32_16x16x32_bf16 v[148:151], a[116:119], a[176:179], v[148:151]\n"
"buffer_load_dwordx4 v253, s[48:51], s63 offen offset:0  lds\n"
"v_mfma_f32_16x16x32_bf16 v[148:151], a[148:151], a[208:211], v[148:151]\n"
"ds_read_b128 a[44:47], v248 offset:12288\n"
"v_mfma_f32_16x16x32_bf16 v[180:183], a[116:119], a[180:183], v[180:183]\n"
"ds_read_b128 a[48:51], v248 offset:13312\n"
"v_mfma_f32_16x16x32_bf16 v[180:183], a[148:151], a[212:215], v[180:183]\n"
"buffer_load_dwordx4 v253, s[48:51], s65 offen offset:2048  lds\n"
"v_mfma_f32_16x16x32_bf16 v[24:27], a[120:123], a[160:163], v[24:27]\n"
"ds_read_b128 a[52:55], v248 offset:30736\n"
"v_mfma_f32_16x16x32_bf16 v[24:27], a[152:155], a[192:195], v[24:27]\n"
"ds_read_b64_tr_b16 a[112:113], v249 offset:50176\n"
"v_mfma_f32_16x16x32_bf16 v[56:59], a[120:123], a[164:167], v[56:59]\n"
"v_subrev_u32 v253, 0x40000, v253\n"
"s_add_u32 m0, s73, 49152\n"
"v_mfma_f32_16x16x32_bf16 v[56:59], a[152:155], a[196:199], v[56:59]\n"
"buffer_load_dwordx4 v254, s[52:55], s66 offen offset:0  lds\n"
"v_mfma_f32_16x16x32_bf16 v[88:91], a[120:123], a[168:171], v[88:91]\n"
"ds_read_b64_tr_b16 a[114:115], v249 offset:50688\n"
"v_mfma_f32_16x16x32_bf16 v[88:91], a[152:155], a[200:203], v[88:91]\n"
"ds_read_b64_tr_b16 a[144:145], v251 offset:1024\n"
"v_mfma_f32_16x16x32_bf16 v[120:123], a[120:123], a[172:175], v[120:123]\n"
"buffer_load_dwordx4 v254, s[52:55], s67 offen offset:1024  lds\n"
"v_mfma_f32_16x16x32_bf16 v[120:123], a[152:155], a[204:207], v[120:123]\n"
"ds_read_b64_tr_b16 a[146:147], v251 offset:1536\n"
"v_mfma_f32_16x16x32_bf16 v[152:155], a[120:123], a[176:179], v[152:155]\n"
"ds_read_b64_tr_b16 a[116:117], v249 offset:50184\n"
"v_mfma_f32_16x16x32_bf16 v[152:155], a[152:155], a[208:211], v[152:155]\n"
"buffer_load_dwordx4 v254, s[52:55], s68 offen offset:2048  lds\n"
"v_mfma_f32_16x16x32_bf16 v[184:187], a[120:123], a[180:183], v[184:187]\n"
"ds_read_b64_tr_b16 a[118:119], v249 offset:50696\n"
"v_mfma_f32_16x16x32_bf16 v[184:187], a[152:155], a[212:215], v[184:187]\n"
"ds_read_b64_tr_b16 a[148:149], v251 offset:1032\n"
"v_mfma_f32_16x16x32_bf16 v[28:31], a[124:127], a[160:163], v[28:31]\n"
"v_add_u32 v254, 64, v254\n"
"v_mfma_f32_16x16x32_bf16 v[28:31], a[156:159], a[192:195], v[28:31]\n"
"ds_read_b64_tr_b16 a[150:151], v251 offset:1544\n"
"v_mfma_f32_16x16x32_bf16 v[60:63], a[124:127], a[164:167], v[60:63]\n"
"ds_read_b64_tr_b16 a[120:121], v250 offset:50176\n"
"v_mfma_f32_16x16x32_bf16 v[60:63], a[156:159], a[196:199], v[60:63]\n"
"ds_read_b64_tr_b16 a[122:123], v250 offset:50688\n"
"v_mfma_f32_16x16x32_bf16 v[92:95], a[124:127], a[168:171], v[92:95]\n"
"ds_read_b64_tr_b16 a[152:153], v252 offset:1024\n"
"v_mfma_f32_16x16x32_bf16 v[92:95], a[156:159], a[200:203], v[92:95]\n"
"ds_read_b64_tr_b16 a[154:155], v252 offset:1536\n"
"v_mfma_f32_16x16x32_bf16 v[124:127], a[124:127], a[172:175], v[124:127]\n"
"v_mfma_f32_16x16x32_bf16 v[124:127], a[156:159], a[204:207], v[124:127]\n"
"v_mfma_f32_16x16x32_bf16 v[156:159], a[124:127], a[176:179], v[156:159]\n"
"v_mfma_f32_16x16x32_bf16 v[156:159], a[156:159], a[208:211], v[156:159]\n"
"v_mfma_f32_16x16x32_bf16 v[188:191], a[124:127], a[180:183], v[188:191]\n"
"v_mfma_f32_16x16x32_bf16 v[188:191], a[156:159], a[212:215], v[188:191]\n"
"s_waitcnt lgkmcnt(12)\n"
"v_mfma_f32_16x16x32_bf16 v[0:3], a[96:99], a[0:3], v[0:3]\n"
"ds_read_b64_tr_b16 a[124:125], v250 offset:50184\n"
"v_mfma_f32_16x16x32_bf16 v[0:3], a[128:131], a[28:31], v[0:3]\n"
"ds_read_b64_tr_b16 a[126:127], v250 offset:50696\n"
"v_mfma_f32_16x16x32_bf16 v[32:35], a[96:99], a[4:7], v[32:35]\n"
"ds_read_b64_tr_b16 a[156:157], v252 offset:1032\n"
"v_mfma_f32_16x16x32_bf16 v[32:35], a[128:131], a[32:35], v[32:35]\n"
"ds_read_b64_tr_b16 a[158:159], v252 offset:1544\n"
"v_mfma_f32_16x16x32_bf16 v[64:67], a[96:99], a[8:11], v[64:67]\n"
"v_mfma_f32_16x16x32_bf16 v[64:67], a[128:131], a[36:39], v[64:67]\n"
"v_mfma_f32_16x16x32_bf16 v[96:99], a[96:99], a[12:15], v[96:99]\n"
"v_mfma_f32_16x16x32_bf16 v[96:99], a[128:131], a[40:43], v[96:99]\n"
"v_mfma_f32_16x16x32_bf16 v[128:131], a[96:99], a[16:19], v[128:131]\n"
"v_mfma_f32_16x16x32_bf16 v[128:131], a[128:131], a[44:47], v[128:131]\n"
"v_mfma_f32_16x16x32_bf16 v[160:163], a[96:99], a[20:23], v[160:163]\n"
"v_mfma_f32_16x16x32_bf16 v[160:163], a[128:131], a[48:51], v[160:163]\n"
"v_mfma_f32_16x16x32_bf16 v[192:195], a[96:99], a[24:27], v[192:195]\n"
"v_mfma_f32_16x16x32_bf16 v[192:195], a[128:131], a[52:55], v[192:195]\n"
"s_waitcnt vmcnt(20) lgkmcnt(0)\n"
"s_barrier\n"
"v_mfma_f32_16x16x32_bf16 v[4:7], a[100:103], a[0:3], v[4:7]\n"
"s_add_u32 m0, s71, 49152\n"
"v_mfma_f32_16x16x32_bf16 v[4:7], a[132:135], a[28:31], v[4:7]\n"
"buffer_load_dwordx4 v253, s[48:51], s64 offen offset:1024  lds\n"
"v_mfma_f32_16x16x32_bf16 v[36:39], a[100:103], a[4:7], v[36:39]\n"
"ds_read_b64_tr_b16 a[96:97], v249 offset:16384\n"
"v_mfma_f32_16x16x32_bf16 v[36:39], a[132:135], a[32:35], v[36:39]\n"
"ds_read_b64_tr_b16 a[98:99], v249 offset:16896\n"
"v_mfma_f32_16x16x32_bf16 v[68:71], a[100:103], a[8:11], v[68:71]\n"
"ds_read_b64_tr_b16 a[128:129], v249 offset:32768\n"
"v_mfma_f32_16x16x32_bf16 v[68:71], a[132:135], a[36:39], v[68:71]\n"
"buffer_load_dwordx4 v253, s[48:51], 0 offen offset:3072  lds\n"
"v_mfma_f32_16x16x32_bf16 v[100:103], a[100:103], a[12:15], v[100:103]\n"
"ds_read_b64_tr_b16 a[130:131], v249 offset:33280\n"
"v_mfma_f32_16x16x32_bf16 v[100:103], a[132:135], a[40:43], v[100:103]\n"
"ds_read_b128 a[160:163], v246 offset:24576\n"
"v_mfma_f32_16x16x32_bf16 v[132:135], a[100:103], a[16:19], v[132:135]\n"
"ds_read_b128 a[164:167], v246 offset:25600\n"
"v_mfma_f32_16x16x32_bf16 v[132:135], a[132:135], a[44:47], v[132:135]\n"
"v_add_u32 v253, 0x40000, v253\n"
"s_mov_b32 m0, s72\n"
"v_mfma_f32_16x16x32_bf16 v[164:167], a[100:103], a[20:23], v[164:167]\n"
"buffer_load_dwordx4 v253, s[48:51], s64 offen offset:1024  lds\n"
"v_mfma_f32_16x16x32_bf16 v[164:167], a[132:135], a[48:51], v[164:167]\n"
"ds_read_b128 a[168:171], v246 offset:26624\n"
"v_mfma_f32_16x16x32_bf16 v[196:199], a[100:103], a[24:27], v[196:199]\n"
"ds_read_b128 a[172:175], v246 offset:27648\n"
"v_mfma_f32_16x16x32_bf16 v[196:199], a[132:135], a[52:55], v[196:199]\n"
"ds_read_b64_tr_b16 a[100:101], v249 offset:16392\n"
"v_mfma_f32_16x16x32_bf16 v[8:11], a[104:107], a[0:3], v[8:11]\n"
"buffer_load_dwordx4 v253, s[48:51], 0 offen offset:3072  lds\n"
"v_mfma_f32_16x16x32_bf16 v[8:11], a[136:139], a[28:31], v[8:11]\n"
"ds_read_b64_tr_b16 a[102:103], v249 offset:16904\n"
"v_mfma_f32_16x16x32_bf16 v[40:43], a[104:107], a[4:7], v[40:43]\n"
"ds_read_b64_tr_b16 a[132:133], v249 offset:32776\n"
"v_mfma_f32_16x16x32_bf16 v[40:43], a[136:139], a[32:35], v[40:43]\n"
"ds_read_b64_tr_b16 a[134:135], v249 offset:33288\n"
"v_mfma_f32_16x16x32_bf16 v[72:75], a[104:107], a[8:11], v[72:75]\n"
"v_add_u32 v253, 0x40000, v253\n"
"s_mov_b32 m0, s74\n"
"v_mfma_f32_16x16x32_bf16 v[72:75], a[136:139], a[36:39], v[72:75]\n"
"buffer_load_dwordx4 v254, s[52:55], s66 offen offset:0  lds\n"
"v_mfma_f32_16x16x32_bf16 v[104:107], a[104:107], a[12:15], v[104:107]\n"
"ds_read_b128 a[176:179], v246 offset:28672\n"
"v_mfma_f32_16x16x32_bf16 v[104:107], a[136:139], a[40:43], v[104:107]\n"
"ds_read_b128 a[180:183], v246 offset:29696\n"
"v_mfma_f32_16x16x32_bf16 v[136:139], a[104:107], a[16:19], v[136:139]\n"
"ds_read_b128 a[184:187], v247 offset:30736\n"
"v_mfma_f32_16x16x32_bf16 v[136:139], a[136:139], a[44:47], v[136:139]\n"
"buffer_load_dwordx4 v254, s[52:55], s67 offen offset:1024  lds\n"
"v_mfma_f32_16x16x32_bf16 v[168:171], a[104:107], a[20:23], v[168:171]\n"
"v_mfma_f32_16x16x32_bf16 v[168:171], a[136:139], a[48:51], v[168:171]\n"
"v_mfma_f32_16x16x32_bf16 v[200:203], a[104:107], a[24:27], v[200:203]\n"
"v_mfma_f32_16x16x32_bf16 v[200:203], a[136:139], a[52:55], v[200:203]\n"
"buffer_load_dwordx4 v254, s[52:55], s68 offen offset:2048  lds\n"
"v_mfma_f32_16x16x32_bf16 v[12:15], a[108:111], a[0:3], v[12:15]\n"
"ds_read_b64_tr_b16 a[104:105], v250 offset:16384\n"
"v_mfma_f32_16x16x32_bf16 v[12:15], a[140:143], a[28:31], v[12:15]\n"
"ds_read_b64_tr_b16 a[106:107], v250 offset:16896\n"
"v_mfma_f32_16x16x32_bf16 v[44:47], a[108:111], a[4:7], v[44:47]\n"
"ds_read_b64_tr_b16 a[136:137], v250 offset:32768\n"
"v_mfma_f32_16x16x32_bf16 v[44:47], a[140:143], a[32:35], v[44:47]\n"
"v_add_u32 v254, 64, v254\n"
"v_mfma_f32_16x16x32_bf16 v[76:79], a[108:111], a[8:11], v[76:79]\n"
"ds_read_b64_tr_b16 a[138:139], v250 offset:33280\n"
"v_mfma_f32_16x16x32_bf16 v[76:79], a[140:143], a[36:39], v[76:79]\n"
"v_mfma_f32_16x16x32_bf16 v[108:111], a[108:111], a[12:15], v[108:111]\n"
"v_mfma_f32_16x16x32_bf16 v[108:111], a[140:143], a[40:43], v[108:111]\n"
"v_mfma_f32_16x16x32_bf16 v[140:143], a[108:111], a[16:19], v[140:143]\n"
"v_mfma_f32_16x16x32_bf16 v[140:143], a[140:143], a[44:47], v[140:143]\n"
"v_mfma_f32_16x16x32_bf16 v[172:175], a[108:111], a[20:23], v[172:175]\n"
"v_mfma_f32_16x16x32_bf16 v[172:175], a[140:143], a[48:51], v[172:175]\n"
"v_mfma_f32_16x16x32_bf16 v[204:207], a[108:111], a[24:27], v[204:207]\n"
"v_mfma_f32_16x16x32_bf16 v[204:207], a[140:143], a[52:55], v[204:207]\n"
"ds_read_b64_tr_b16 a[108:109], v250 offset:16392\n"
"v_mfma_f32_16x16x32_bf16 v[16:19], a[112:115], a[0:3], v[16:19]\n"
"ds_read_b64_tr_b16 a[110:111], v250 offset:16904\n"
"v_mfma_f32_16x16x32_bf16 v[16:19], a[144:147], a[28:31], v[16:19]\n"
"ds_read_b64_tr_b16 a[140:141], v250 offset:32776\n"
"v_mfma_f32_16x16x32_bf16 v[48:51], a[112:115], a[4:7], v[48:51]\n"
"ds_read_b64_tr_b16 a[142:143], v250 offset:33288\n"
"v_mfma_f32_16x16x32_bf16 v[48:51], a[144:147], a[32:35], v[48:51]\n"
"v_mfma_f32_16x16x32_bf16 v[80:83], a[112:115], a[8:11], v[80:83]\n"
"v_mfma_f32_16x16x32_bf16 v[80:83], a[144:147], a[36:39], v[80:83]\n"
"v_mfma_f32_16x16x32_bf16 v[112:115], a[112:115], a[12:15], v[112:115]\n"
"v_mfma_f32_16x16x32_bf16 v[112:115], a[144:147], a[40:43], v[112:115]\n"
"v_mfma_f32_16x16x32_bf16 v[144:147], a[112:115], a[16:19], v[144:147]\n"
"v_mfma_f32_16x16x32_bf16 v[144:147], a[144:147], a[44:47], v[144:147]\n"
"v_mfma_f32_16x16x32_bf16 v[176:179], a[112:115], a[20:23], v[176:179]\n"
"v_mfma_f32_16x16x32_bf16 v[176:179], a[144:147], a[48:51], v[176:179]\n"
"s_waitcnt vmcnt(16) lgkmcnt(0)\n"
"s_barrier\n"
"v_mfma_f32_16x16x32_bf16 v[20:23], a[116:119], a[0:3], v[20:23]\n"
"s_add_u32 m0, s71, 16384\n"
"v_mfma_f32_16x16x32_bf16 v[20:23], a[148:151], a[28:31], v[20:23]\n"
"buffer_load_dwordx4 v253, s[48:51], s63 offen offset:0  lds\n"
"v_mfma_f32_16x16x32_bf16 v[52:55], a[116:119], a[4:7], v[52:55]\n"
"ds_read_b128 a[192:195], v246 offset:40960\n"
"v_mfma_f32_16x16x32_bf16 v[52:55], a[148:151], a[32:35], v[52:55]\n"
"ds_read_b128 a[196:199], v246 offset:41984\n"
"v_mfma_f32_16x16x32_bf16 v[84:87], a[116:119], a[8:11], v[84:87]\n"
"buffer_load_dwordx4 v253, s[48:51], s65 offen offset:2048  lds\n"
"v_mfma_f32_16x16x32_bf16 v[84:87], a[148:151], a[36:39], v[84:87]\n"
"ds_read_b128 a[200:203], v246 offset:43008\n"
"v_mfma_f32_16x16x32_bf16 v[116:119], a[116:119], a[12:15], v[116:119]\n"
"ds_read_b128 a[204:207], v246 offset:44032\n"
"v_mfma_f32_16x16x32_bf16 v[116:119], a[148:151], a[40:43], v[116:119]\n"
"v_add_u32 v253, 0x40000, v253\n"
"s_add_u32 m0, s71, 32768\n"
"v_mfma_f32_16x16x32_bf16 v[148:151], a[116:119], a[16:19], v[148:151]\n"
"buffer_load_dwordx4 v253, s[48:51], s63 offen offset:0  lds\n"
"v_mfma_f32_16x16x32_bf16 v[148:151], a[148:151], a[44:47], v[148:151]\n"
"ds_read_b128 a[208:211], v246 offset:45056\n"
"v_mfma_f32_16x16x32_bf16 v[180:183], a[116:119], a[20:23], v[180:183]\n"
"ds_read_b128 a[212:215], v246 offset:46080\n"
"v_mfma_f32_16x16x32_bf16 v[180:183], a[148:151], a[48:51], v[180:183]\n"
"buffer_load_dwordx4 v253, s[48:51], s65 offen offset:2048  lds\n"
"v_mfma_f32_16x16x32_bf16 v[24:27], a[120:123], a[0:3], v[24:27]\n"
"ds_read_b128 a[216:219], v247 offset:47120\n"
"v_mfma_f32_16x16x32_bf16 v[24:27], a[152:155], a[28:31], v[24:27]\n"
"ds_read_b64_tr_b16 a[112:113], v249 offset:17408\n"
"v_mfma_f32_16x16x32_bf16 v[56:59], a[120:123], a[4:7], v[56:59]\n"
"v_subrev_u32 v253, 0x40000, v253\n"
"s_add_u32 m0, s73, 16384\n"
"v_mfma_f32_16x16x32_bf16 v[56:59], a[152:155], a[32:35], v[56:59]\n"
"buffer_load_dwordx4 v254, s[52:55], s66 offen offset:0  lds\n"
"v_mfma_f32_16x16x32_bf16 v[88:91], a[120:123], a[8:11], v[88:91]\n"
"ds_read_b64_tr_b16 a[114:115], v249 offset:17920\n"
"v_mfma_f32_16x16x32_bf16 v[88:91], a[152:155], a[36:39], v[88:91]\n"
"ds_read_b64_tr_b16 a[144:145], v249 offset:33792\n"
"v_mfma_f32_16x16x32_bf16 v[120:123], a[120:123], a[12:15], v[120:123]\n"
"buffer_load_dwordx4 v254, s[52:55], s67 offen offset:1024  lds\n"
"v_mfma_f32_16x16x32_bf16 v[120:123], a[152:155], a[40:43], v[120:123]\n"
"ds_read_b64_tr_b16 a[146:147], v249 offset:34304\n"
"v_mfma_f32_16x16x32_bf16 v[152:155], a[120:123], a[16:19], v[152:155]\n"
"ds_read_b64_tr_b16 a[116:117], v249 offset:17416\n"
"v_mfma_f32_16x16x32_bf16 v[152:155], a[152:155], a[44:47], v[152:155]\n"
"buffer_load_dwordx4 v254, s[52:55], s68 offen offset:2048  lds\n"
"v_mfma_f32_16x16x32_bf16 v[184:187], a[120:123], a[20:23], v[184:187]\n"
"ds_read_b64_tr_b16 a[118:119], v249 offset:17928\n"
"v_mfma_f32_16x16x32_bf16 v[184:187], a[152:155], a[48:51], v[184:187]\n"
"ds_read_b64_tr_b16 a[148:149], v249 offset:33800\n"
"v_mfma_f32_16x16x32_bf16 v[28:31], a[124:127], a[0:3], v[28:31]\n"
"v_add_u32 v254, 64, v254\n"
"v_mfma_f32_16x16x32_bf16 v[28:31], a[156:159], a[28:31], v[28:31]\n"
"ds_read_b64_tr_b16 a[150:151], v249 offset:34312\n"
"v_mfma_f32_16x16x32_bf16 v[60:63], a[124:127], a[4:7], v[60:63]\n"
"ds_read_b64_tr_b16 a[120:121], v250 offset:17408\n"
"v_mfma_f32_16x16x32_bf16 v[60:63], a[156:159], a[32:35], v[60:63]\n"
"ds_read_b64_tr_b16 a[122:123], v250 offset:17920\n"
"v_mfma_f32_16x16x32_bf16 v[92:95], a[124:127], a[8:11], v[92:95]\n"
"ds_read_b64_tr_b16 a[152:153], v250 offset:33792\n"
"v_mfma_f32_16x16x32_bf16 v[92:95], a[156:159], a[36:39], v[92:95]\n"
"ds_read_b64_tr_b16 a[154:155], v250 offset:34304\n"
"v_mfma_f32_16x16x32_bf16 v[124:127], a[124:127], a[12:15], v[124:127]\n"
"v_mfma_f32_16x16x32_bf16 v[124:127], a[156:159], a[40:43], v[124:127]\n"
"v_mfma_f32_16x16x32_bf16 v[156:159], a[124:127], a[16:19], v[156:159]\n"
"v_mfma_f32_16x16x32_bf16 v[156:159], a[156:159], a[44:47], v[156:159]\n"
"v_mfma_f32_16x16x32_bf16 v[188:191], a[124:127], a[20:23], v[188:191]\n"
"v_mfma_f32_16x16x32_bf16 v[188:191], a[156:159], a[48:51], v[188:191]\n"
"s_waitcnt lgkmcnt(12)\n"
"v_mfma_f32_16x16x32_bf16 v[0:3], a[96:99], a[160:163], v[0:3]\n"
"ds_read_b64_tr_b16 a[124:125], v250 offset:17416\n"
"v_mfma_f32_16x16x32_bf16 v[0:3], a[128:131], a[192:195], v[0:3]\n"
"ds_read_b64_tr_b16 a[126:127], v250 offset:17928\n"
"v_mfma_f32_16x16x32_bf16 v[32:35], a[96:99], a[164:167], v[32:35]\n"
"ds_read_b64_tr_b16 a[156:157], v250 offset:33800\n"
"v_mfma_f32_16x16x32_bf16 v[32:35], a[128:131], a[196:199], v[32:35]\n"
"ds_read_b64_tr_b16 a[158:159], v250 offset:34312\n"
"v_mfma_f32_16x16x32_bf16 v[64:67], a[96:99], a[168:171], v[64:67]\n"
"s_add_u32 m0, s70, 8192\n"
"v_mfma_f32_16x16x32_bf16 v[64:67], a[128:131], a[200:203], v[64:67]\n"
"buffer_load_dwordx4 v223, s[44:47], s60 offen offset:0 nt lds\n"
"v_mfma_f32_16x16x32_bf16 v[96:99], a[96:99], a[172:175], v[96:99]\n"
"buffer_load_dwordx4 v223, s[44:47], s61 offen offset:1024 nt lds\n"
"v_mfma_f32_16x16x32_bf16 v[96:99], a[128:131], a[204:207], v[96:99]\n"
"buffer_load_dwordx4 v223, s[44:47], s62 offen offset:2048 nt lds\n"
"v_mfma_f32_16x16x32_bf16 v[128:131], a[96:99], a[176:179], v[128:131]\n"
"buffer_load_dwordx4 v223, s[44:47], 0 offen offset:3072 nt lds\n"
"v_mfma_f32_16x16x32_bf16 v[128:131], a[128:131], a[208:211], v[128:131]\n"
"v_add_u32 v223, 0x2000, v223\n"
"v_mfma_f32_16x16x32_bf16 v[160:163], a[96:99], a[180:183], v[160:163]\n"
"v_mfma_f32_16x16x32_bf16 v[160:163], a[128:131], a[212:215], v[160:163]\n"
"v_mfma_f32_16x16x32_bf16 v[192:195], a[96:99], a[184:187], v[192:195]\n"
"v_mfma_f32_16x16x32_bf16 v[192:195], a[128:131], a[216:219], v[192:195]\n"
"s_waitcnt vmcnt(18) lgkmcnt(0)\n"
"s_barrier\n"
"v_mfma_f32_16x16x32_bf16 v[4:7], a[100:103], a[160:163], v[4:7]\n"
"s_add_u32 m0, s71, 16384\n"
"v_mfma_f32_16x16x32_bf16 v[4:7], a[132:135], a[192:195], v[4:7]\n"
"buffer_load_dwordx4 v253, s[48:51], s64 offen offset:1024  lds\n"
"v_mfma_f32_16x16x32_bf16 v[36:39], a[100:103], a[164:167], v[36:39]\n"
"ds_read_b64_tr_b16 a[96:97], v249 offset:49152\n"
"v_mfma_f32_16x16x32_bf16 v[36:39], a[132:135], a[196:199], v[36:39]\n"
"ds_read_b64_tr_b16 a[98:99], v249 offset:49664\n"
"v_mfma_f32_16x16x32_bf16 v[68:71], a[100:103], a[168:171], v[68:71]\n"
"ds_read_b64_tr_b16 a[128:129], v251 offset:0\n"
"v_mfma_f32_16x16x32_bf16 v[68:71], a[132:135], a[200:203], v[68:71]\n"
"buffer_load_dwordx4 v253, s[48:51], 0 offen offset:3072  lds\n"
"v_mfma_f32_16x16x32_bf16 v[100:103], a[100:103], a[172:175], v[100:103]\n"
"ds_read_b64_tr_b16 a[130:131], v251 offset:512\n"
"v_mfma_f32_16x16x32_bf16 v[100:103], a[132:135], a[204:207], v[100:103]\n"
"ds_read_b128 a[0:3], v246 offset:57344\n"
"v_mfma_f32_16x16x32_bf16 v[132:135], a[100:103], a[176:179], v[132:135]\n"
"ds_read_b128 a[4:7], v246 offset:58368\n"
"v_mfma_f32_16x16x32_bf16 v[132:135], a[132:135], a[208:211], v[132:135]\n"
"v_add_u32 v253, 0x40000, v253\n"
"s_add_u32 m0, s71, 32768\n"
"v_mfma_f32_16x16x32_bf16 v[164:167], a[100:103], a[180:183], v[164:167]\n"
"buffer_load_dwordx4 v253, s[48:51], s64 offen offset:1024  lds\n"
"v_mfma_f32_16x16x32_bf16 v[164:167], a[132:135], a[212:215], v[164:167]\n"
"ds_read_b128 a[8:11], v246 offset:59392\n"
"v_mfma_f32_16x16x32_bf16 v[196:199], a[100:103], a[184:187], v[196:199]\n"
"ds_read_b128 a[12:15], v246 offset:60416\n"
"v_mfma_f32_16x16x32_bf16 v[196:199], a[132:135], a[216:219], v[196:199]\n"
"ds_read_b64_tr_b16 a[100:101], v249 offset:49160\n"
"v_mfma_f32_16x16x32_bf16 v[8:11], a[104:107], a[160:163], v[8:11]\n"
"buffer_load_dwordx4 v253, s[48:51], 0 offen offset:3072  lds\n"
"v_mfma_f32_16x16x32_bf16 v[8:11], a[136:139], a[192:195], v[8:11]\n"
"ds_read_b64_tr_b16 a[102:103], v249 offset:49672\n"
"v_mfma_f32_16x16x32_bf16 v[40:43], a[104:107], a[164:167], v[40:43]\n"
"ds_read_b64_tr_b16 a[132:133], v251 offset:8\n"
"v_mfma_f32_16x16x32_bf16 v[40:43], a[136:139], a[196:199], v[40:43]\n"
"ds_read_b64_tr_b16 a[134:135], v251 offset:520\n"
"v_mfma_f32_16x16x32_bf16 v[72:75], a[104:107], a[168:171], v[72:75]\n"
"v_add_u32 v253, 0x40000, v253\n"
"s_add_u32 m0, s73, 32768\n"
"v_mfma_f32_16x16x32_bf16 v[72:75], a[136:139], a[200:203], v[72:75]\n"
"buffer_load_dwordx4 v254, s[52:55], s66 offen offset:0  lds\n"
"v_mfma_f32_16x16x32_bf16 v[104:107], a[104:107], a[172:175], v[104:107]\n"
"ds_read_b128 a[16:19], v246 offset:61440\n"
"v_mfma_f32_16x16x32_bf16 v[104:107], a[136:139], a[204:207], v[104:107]\n"
"ds_read_b128 a[20:23], v246 offset:62464\n"
"v_mfma_f32_16x16x32_bf16 v[136:139], a[104:107], a[176:179], v[136:139]\n"
"ds_read_b128 a[24:27], v247 offset:63504\n"
"v_mfma_f32_16x16x32_bf16 v[136:139], a[136:139], a[208:211], v[136:139]\n"
"buffer_load_dwordx4 v254, s[52:55], s67 offen offset:1024  lds\n"
"v_mfma_f32_16x16x32_bf16 v[168:171], a[104:107], a[180:183], v[168:171]\n"
"v_mfma_f32_16x16x32_bf16 v[168:171], a[136:139], a[212:215], v[168:171]\n"
"v_mfma_f32_16x16x32_bf16 v[200:203], a[104:107], a[184:187], v[200:203]\n"
"v_mfma_f32_16x16x32_bf16 v[200:203], a[136:139], a[216:219], v[200:203]\n"
"buffer_load_dwordx4 v254, s[52:55], s68 offen offset:2048  lds\n"
"v_mfma_f32_16x16x32_bf16 v[12:15], a[108:111], a[160:163], v[12:15]\n"
"ds_read_b64_tr_b16 a[104:105], v250 offset:49152\n"
"v_mfma_f32_16x16x32_bf16 v[12:15], a[140:143], a[192:195], v[12:15]\n"
"ds_read_b64_tr_b16 a[106:107], v250 offset:49664\n"
"v_mfma_f32_16x16x32_bf16 v[44:47], a[108:111], a[164:167], v[44:47]\n"
"ds_read_b64_tr_b16 a[136:137], v252 offset:0\n"
"v_mfma_f32_16x16x32_bf16 v[44:47], a[140:143], a[196:199], v[44:47]\n"
"v_add_u32 v254, 64, v254\n"
"v_mfma_f32_16x16x32_bf16 v[76:79], a[108:111], a[168:171], v[76:79]\n"
"ds_read_b64_tr_b16 a[138:139], v252 offset:512\n"
"v_mfma_f32_16x16x32_bf16 v[76:79], a[140:143], a[200:203], v[76:79]\n"
"v_mfma_f32_16x16x32_bf16 v[108:111], a[108:111], a[172:175], v[108:111]\n"
"v_mfma_f32_16x16x32_bf16 v[108:111], a[140:143], a[204:207], v[108:111]\n"
"v_mfma_f32_16x16x32_bf16 v[140:143], a[108:111], a[176:179], v[140:143]\n"
"v_mfma_f32_16x16x32_bf16 v[140:143], a[140:143], a[208:211], v[140:143]\n"
"v_mfma_f32_16x16x32_bf16 v[172:175], a[108:111], a[180:183], v[172:175]\n"
"v_mfma_f32_16x16x32_bf16 v[172:175], a[140:143], a[212:215], v[172:175]\n"
"v_mfma_f32_16x16x32_bf16 v[204:207], a[108:111], a[184:187], v[204:207]\n"
"v_mfma_f32_16x16x32_bf16 v[204:207], a[140:143], a[216:219], v[204:207]\n"
"ds_read_b64_tr_b16 a[108:109], v250 offset:49160\n"
"v_mfma_f32_16x16x32_bf16 v[16:19], a[112:115], a[160:163], v[16:19]\n"
"ds_read_b64_tr_b16 a[110:111], v250 offset:49672\n"
"v_mfma_f32_16x16x32_bf16 v[16:19], a[144:147], a[192:195], v[16:19]\n"
"ds_read_b64_tr_b16 a[140:141], v252 offset:8\n"
"v_mfma_f32_16x16x32_bf16 v[48:51], a[112:115], a[164:167], v[48:51]\n"
"ds_read_b64_tr_b16 a[142:143], v252 offset:520\n"
"v_mfma_f32_16x16x32_bf16 v[48:51], a[144:147], a[196:199], v[48:51]\n"
"v_mfma_f32_16x16x32_bf16 v[80:83], a[112:115], a[168:171], v[80:83]\n"
"v_mfma_f32_16x16x32_bf16 v[80:83], a[144:147], a[200:203], v[80:83]\n"
"v_mfma_f32_16x16x32_bf16 v[112:115], a[112:115], a[172:175], v[112:115]\n"
"v_mfma_f32_16x16x32_bf16 v[112:115], a[144:147], a[204:207], v[112:115]\n"
"v_mfma_f32_16x16x32_bf16 v[144:147], a[112:115], a[176:179], v[144:147]\n"
"v_mfma_f32_16x16x32_bf16 v[144:147], a[144:147], a[208:211], v[144:147]\n"
"v_mfma_f32_16x16x32_bf16 v[176:179], a[112:115], a[180:183], v[176:179]\n"
"v_mfma_f32_16x16x32_bf16 v[176:179], a[144:147], a[212:215], v[176:179]\n"
"s_waitcnt vmcnt(18) lgkmcnt(0)\n"
"s_barrier\n"
"v_mfma_f32_16x16x32_bf16 v[20:23], a[116:119], a[160:163], v[20:23]\n"
"s_add_u32 m0, s71, 49152\n"
"v_mfma_f32_16x16x32_bf16 v[20:23], a[148:151], a[192:195], v[20:23]\n"
"buffer_load_dwordx4 v253, s[48:51], s63 offen offset:0  lds\n"
"v_mfma_f32_16x16x32_bf16 v[52:55], a[116:119], a[164:167], v[52:55]\n"
"ds_read_b128 a[28:31], v248 offset:8192\n"
"v_mfma_f32_16x16x32_bf16 v[52:55], a[148:151], a[196:199], v[52:55]\n"
"ds_read_b128 a[32:35], v248 offset:9216\n"
"v_mfma_f32_16x16x32_bf16 v[84:87], a[116:119], a[168:171], v[84:87]\n"
"buffer_load_dwordx4 v253, s[48:51], s65 offen offset:2048  lds\n"
"v_mfma_f32_16x16x32_bf16 v[84:87], a[148:151], a[200:203], v[84:87]\n"
"ds_read_b128 a[36:39], v248 offset:10240\n"
"v_mfma_f32_16x16x32_bf16 v[116:119], a[116:119], a[172:175], v[116:119]\n"
"ds_read_b128 a[40:43], v248 offset:11264\n"
"v_mfma_f32_16x16x32_bf16 v[116:119], a[148:151], a[204:207], v[116:119]\n"
"v_add_u32 v253, 0x40000, v253\n"
"s_mov_b32 m0, s72\n"
"v_mfma_f32_16x16x32_bf16 v[148:151], a[116:119], a[176:179], v[148:151]\n"
"buffer_load_dwordx4 v253, s[48:51], s63 offen offset:0  lds\n"
"v_mfma_f32_16x16x32_bf16 v[148:151], a[148:151], a[208:211], v[148:151]\n"
"ds_read_b128 a[44:47], v248 offset:12288\n"
"v_mfma_f32_16x16x32_bf16 v[180:183], a[116:119], a[180:183], v[180:183]\n"
"ds_read_b128 a[48:51], v248 offset:13312\n"
"v_mfma_f32_16x16x32_bf16 v[180:183], a[148:151], a[212:215], v[180:183]\n"
"buffer_load_dwordx4 v253, s[48:51], s65 offen offset:2048  lds\n"
"v_mfma_f32_16x16x32_bf16 v[24:27], a[120:123], a[160:163], v[24:27]\n"
"ds_read_b128 a[52:55], v248 offset:30736\n"
"v_mfma_f32_16x16x32_bf16 v[24:27], a[152:155], a[192:195], v[24:27]\n"
"ds_read_b64_tr_b16 a[112:113], v249 offset:50176\n"
"v_mfma_f32_16x16x32_bf16 v[56:59], a[120:123], a[164:167], v[56:59]\n"
"v_subrev_u32 v253, 0x40000, v253\n"
"s_add_u32 m0, s73, 49152\n"
"v_mfma_f32_16x16x32_bf16 v[56:59], a[152:155], a[196:199], v[56:59]\n"
"buffer_load_dwordx4 v254, s[52:55], s66 offen offset:0  lds\n"
"v_mfma_f32_16x16x32_bf16 v[88:91], a[120:123], a[168:171], v[88:91]\n"
"ds_read_b64_tr_b16 a[114:115], v249 offset:50688\n"
"v_mfma_f32_16x16x32_bf16 v[88:91], a[152:155], a[200:203], v[88:91]\n"
"ds_read_b64_tr_b16 a[144:145], v251 offset:1024\n"
"v_mfma_f32_16x16x32_bf16 v[120:123], a[120:123], a[172:175], v[120:123]\n"
"buffer_load_dwordx4 v254, s[52:55], s67 offen offset:1024  lds\n"
"v_mfma_f32_16x16x32_bf16 v[120:123], a[152:155], a[204:207], v[120:123]\n"
"ds_read_b64_tr_b16 a[146:147], v251 offset:1536\n"
"v_mfma_f32_16x16x32_bf16 v[152:155], a[120:123], a[176:179], v[152:155]\n"
"ds_read_b64_tr_b16 a[116:117], v249 offset:50184\n"
"v_mfma_f32_16x16x32_bf16 v[152:155], a[152:155], a[208:211], v[152:155]\n"
"buffer_load_dwordx4 v254, s[52:55], s68 offen offset:2048  lds\n"
"v_mfma_f32_16x16x32_bf16 v[184:187], a[120:123], a[180:183], v[184:187]\n"
"ds_read_b64_tr_b16 a[118:119], v249 offset:50696\n"
"v_mfma_f32_16x16x32_bf16 v[184:187], a[152:155], a[212:215], v[184:187]\n"
"ds_read_b64_tr_b16 a[148:149], v251 offset:1032\n"
"v_mfma_f32_16x16x32_bf16 v[28:31], a[124:127], a[160:163], v[28:31]\n"
"v_add_u32 v254, 64, v254\n"
"v_mfma_f32_16x16x32_bf16 v[28:31], a[156:159], a[192:195], v[28:31]\n"
"ds_read_b64_tr_b16 a[150:151], v251 offset:1544\n"
"v_mfma_f32_16x16x32_bf16 v[60:63], a[124:127], a[164:167], v[60:63]\n"
"ds_read_b64_tr_b16 a[120:121], v250 offset:50176\n"
"v_mfma_f32_16x16x32_bf16 v[60:63], a[156:159], a[196:199], v[60:63]\n"
"ds_read_b64_tr_b16 a[122:123], v250 offset:50688\n"
"v_mfma_f32_16x16x32_bf16 v[92:95], a[124:127], a[168:171], v[92:95]\n"
"ds_read_b64_tr_b16 a[152:153], v252 offset:1024\n"
"v_mfma_f32_16x16x32_bf16 v[92:95], a[156:159], a[200:203], v[92:95]\n"
"ds_read_b64_tr_b16 a[154:155], v252 offset:1536\n"
"v_mfma_f32_16x16x32_bf16 v[124:127], a[124:127], a[172:175], v[124:127]\n"
"v_mfma_f32_16x16x32_bf16 v[124:127], a[156:159], a[204:207], v[124:127]\n"
"v_mfma_f32_16x16x32_bf16 v[156:159], a[124:127], a[176:179], v[156:159]\n"
"v_mfma_f32_16x16x32_bf16 v[156:159], a[156:159], a[208:211], v[156:159]\n"
"v_mfma_f32_16x16x32_bf16 v[188:191], a[124:127], a[180:183], v[188:191]\n"
"v_mfma_f32_16x16x32_bf16 v[188:191], a[156:159], a[212:215], v[188:191]\n"
"s_waitcnt lgkmcnt(12)\n"
"v_mfma_f32_16x16x32_bf16 v[0:3], a[96:99], a[0:3], v[0:3]\n"
"ds_read_b64_tr_b16 a[124:125], v250 offset:50184\n"
"v_mfma_f32_16x16x32_bf16 v[0:3], a[128:131], a[28:31], v[0:3]\n"
"ds_read_b64_tr_b16 a[126:127], v250 offset:50696\n"
"v_mfma_f32_16x16x32_bf16 v[32:35], a[96:99], a[4:7], v[32:35]\n"
"ds_read_b64_tr_b16 a[156:157], v252 offset:1032\n"
"v_mfma_f32_16x16x32_bf16 v[32:35], a[128:131], a[32:35], v[32:35]\n"
"ds_read_b64_tr_b16 a[158:159], v252 offset:1544\n"
"v_mfma_f32_16x16x32_bf16 v[64:67], a[96:99], a[8:11], v[64:67]\n"
"v_mfma_f32_16x16x32_bf16 v[64:67], a[128:131], a[36:39], v[64:67]\n"
"v_mfma_f32_16x16x32_bf16 v[96:99], a[96:99], a[12:15], v[96:99]\n"
"v_mfma_f32_16x16x32_bf16 v[96:99], a[128:131], a[40:43], v[96:99]\n"
"v_mfma_f32_16x16x32_bf16 v[128:131], a[96:99], a[16:19], v[128:131]\n"
"v_mfma_f32_16x16x32_bf16 v[128:131], a[128:131], a[44:47], v[128:131]\n"
"v_mfma_f32_16x16x32_bf16 v[160:163], a[96:99], a[20:23], v[160:163]\n"
"v_mfma_f32_16x16x32_bf16 v[160:163], a[128:131], a[48:51], v[160:163]\n"
"v_mfma_f32_16x16x32_bf16 v[192:195], a[96:99], a[24:27], v[192:195]\n"
"v_mfma_f32_16x16x32_bf16 v[192:195], a[128:131], a[52:55], v[192:195]\n"
"s_waitcnt vmcnt(18) lgkmcnt(0)\n"
"s_barrier\n"
"v_mfma_f32_16x16x32_bf16 v[4:7], a[100:103], a[0:3], v[4:7]\n"
"s_add_u32 m0, s71, 49152\n"
"v_mfma_f32_16x16x32_bf16 v[4:7], a[132:135], a[28:31], v[4:7]\n"
"buffer_load_dwordx4 v253, s[48:51], s64 offen offset:1024  lds\n"
"v_mfma_f32_16x16x32_bf16 v[36:39], a[100:103], a[4:7], v[36:39]\n"
"ds_read_b128 a[160:163], v246 offset:24576\n"
"v_mfma_f32_16x16x32_bf16 v[36:39], a[132:135], a[32:35], v[36:39]\n"
"ds_read_b128 a[164:167], v246 offset:25600\n"
"v_mfma_f32_16x16x32_bf16 v[68:71], a[100:103], a[8:11], v[68:71]\n"
"ds_read_b128 a[168:171], v246 offset:26624\n"
"v_mfma_f32_16x16x32_bf16 v[68:71], a[132:135], a[36:39], v[68:71]\n"
"buffer_load_dwordx4 v253, s[48:51], 0 offen offset:3072  lds\n"
"v_mfma_f32_16x16x32_bf16 v[100:103], a[100:103], a[12:15], v[100:103]\n"
"ds_read_b128 a[172:175], v246 offset:27648\n"
"v_mfma_f32_16x16x32_bf16 v[100:103], a[132:135], a[40:43], v[100:103]\n"
"ds_read_b128 a[176:179], v246 offset:28672\n"
"v_mfma_f32_16x16x32_bf16 v[132:135], a[100:103], a[16:19], v[132:135]\n"
"ds_read_b128 a[180:183], v246 offset:29696\n"
"v_mfma_f32_16x16x32_bf16 v[132:135], a[132:135], a[44:47], v[132:135]\n"
"v_add_u32 v253, 0x40000, v253\n"
"s_mov_b32 m0, s72\n"
"v_mfma_f32_16x16x32_bf16 v[164:167], a[100:103], a[20:23], v[164:167]\n"
"buffer_load_dwordx4 v253, s[48:51], s64 offen offset:1024  lds\n"
"v_mfma_f32_16x16x32_bf16 v[164:167], a[132:135], a[48:51], v[164:167]\n"
"ds_read_b128 a[184:187], v247 offset:30736\n"
"v_mfma_f32_16x16x32_bf16 v[196:199], a[100:103], a[24:27], v[196:199]\n"
"ds_read_b64_tr_b16 a[56:57], v249 offset:16384\n"
"v_mfma_f32_16x16x32_bf16 v[196:199], a[132:135], a[52:55], v[196:199]\n"
"ds_read_b64_tr_b16 a[58:59], v249 offset:16896\n"
"v_mfma_f32_16x16x32_bf16 v[8:11], a[104:107], a[0:3], v[8:11]\n"
"buffer_load_dwordx4 v253, s[48:51], 0 offen offset:3072  lds\n"
"v_mfma_f32_16x16x32_bf16 v[8:11], a[136:139], a[28:31], v[8:11]\n"
"ds_read_b64_tr_b16 a[60:61], v249 offset:16392\n"
"v_mfma_f32_16x16x32_bf16 v[40:43], a[104:107], a[4:7], v[40:43]\n"
"ds_read_b64_tr_b16 a[62:63], v249 offset:16904\n"
"v_mfma_f32_16x16x32_bf16 v[40:43], a[136:139], a[32:35], v[40:43]\n"
"ds_read_b64_tr_b16 a[64:65], v250 offset:16384\n"
"v_mfma_f32_16x16x32_bf16 v[72:75], a[104:107], a[8:11], v[72:75]\n"
"v_add_u32 v253, 0x40000, v253\n"
"s_mov_b32 m0, s74\n"
"v_mfma_f32_16x16x32_bf16 v[72:75], a[136:139], a[36:39], v[72:75]\n"
"buffer_load_dwordx4 v254, s[52:55], s66 offen offset:0  lds\n"
"v_mfma_f32_16x16x32_bf16 v[104:107], a[104:107], a[12:15], v[104:107]\n"
"ds_read_b64_tr_b16 a[66:67], v250 offset:16896\n"
"v_mfma_f32_16x16x32_bf16 v[104:107], a[136:139], a[40:43], v[104:107]\n"
"ds_read_b64_tr_b16 a[68:69], v250 offset:16392\n"
"v_mfma_f32_16x16x32_bf16 v[136:139], a[104:107], a[16:19], v[136:139]\n"
"ds_read_b64_tr_b16 a[70:71], v250 offset:16904\n"
"v_mfma_f32_16x16x32_bf16 v[136:139], a[136:139], a[44:47], v[136:139]\n"
"buffer_load_dwordx4 v254, s[52:55], s67 offen offset:1024  lds\n"
"v_mfma_f32_16x16x32_bf16 v[168:171], a[104:107], a[20:23], v[168:171]\n"
"v_mfma_f32_16x16x32_bf16 v[168:171], a[136:139], a[48:51], v[168:171]\n"
"v_mfma_f32_16x16x32_bf16 v[200:203], a[104:107], a[24:27], v[200:203]\n"
"v_mfma_f32_16x16x32_bf16 v[200:203], a[136:139], a[52:55], v[200:203]\n"
"buffer_load_dwordx4 v254, s[52:55], s68 offen offset:2048  lds\n"
"v_mfma_f32_16x16x32_bf16 v[12:15], a[108:111], a[0:3], v[12:15]\n"
"v_mfma_f32_16x16x32_bf16 v[12:15], a[140:143], a[28:31], v[12:15]\n"
"v_mfma_f32_16x16x32_bf16 v[44:47], a[108:111], a[4:7], v[44:47]\n"
"v_mfma_f32_16x16x32_bf16 v[44:47], a[140:143], a[32:35], v[44:47]\n"
"v_add_u32 v254, 64, v254\n"
"v_mfma_f32_16x16x32_bf16 v[76:79], a[108:111], a[8:11], v[76:79]\n"
"v_mfma_f32_16x16x32_bf16 v[76:79], a[140:143], a[36:39], v[76:79]\n"
"v_mfma_f32_16x16x32_bf16 v[108:111], a[108:111], a[12:15], v[108:111]\n"
"v_mfma_f32_16x16x32_bf16 v[108:111], a[140:143], a[40:43], v[108:111]\n"
"v_mfma_f32_16x16x32_bf16 v[140:143], a[108:111], a[16:19], v[140:143]\n"
"v_mfma_f32_16x16x32_bf16 v[140:143], a[140:143], a[44:47], v[140:143]\n"
"v_mfma_f32_16x16x32_bf16 v[172:175], a[108:111], a[20:23], v[172:175]\n"
"v_mfma_f32_16x16x32_bf16 v[172:175], a[140:143], a[48:51], v[172:175]\n"
"v_mfma_f32_16x16x32_bf16 v[204:207], a[108:111], a[24:27], v[204:207]\n"
"v_mfma_f32_16x16x32_bf16 v[204:207], a[140:143], a[52:55], v[204:207]\n"
"v_mfma_f32_16x16x32_bf16 v[16:19], a[112:115], a[0:3], v[16:19]\n"
"v_mfma_f32_16x16x32_bf16 v[16:19], a[144:147], a[28:31], v[16:19]\n"
"v_mfma_f32_16x16x32_bf16 v[48:51], a[112:115], a[4:7], v[48:51]\n"
"v_mfma_f32_16x16x32_bf16 v[48:51], a[144:147], a[32:35], v[48:51]\n"
"v_mfma_f32_16x16x32_bf16 v[80:83], a[112:115], a[8:11], v[80:83]\n"
"v_mfma_f32_16x16x32_bf16 v[80:83], a[144:147], a[36:39], v[80:83]\n"
"v_mfma_f32_16x16x32_bf16 v[112:115], a[112:115], a[12:15], v[112:115]\n"
"v_mfma_f32_16x16x32_bf16 v[112:115], a[144:147], a[40:43], v[112:115]\n"
"v_mfma_f32_16x16x32_bf16 v[144:147], a[112:115], a[16:19], v[144:147]\n"
"v_mfma_f32_16x16x32_bf16 v[144:147], a[144:147], a[44:47], v[144:147]\n"
"v_mfma_f32_16x16x32_bf16 v[176:179], a[112:115], a[20:23], v[176:179]\n"
"v_mfma_f32_16x16x32_bf16 v[176:179], a[144:147], a[48:51], v[176:179]\n"
"s_waitcnt vmcnt(14) lgkmcnt(0)\n"
"s_barrier\n"
"v_mfma_f32_16x16x32_bf16 v[20:23], a[116:119], a[0:3], v[20:23]\n"
"ds_read_b64_tr_b16 a[72:73], v249 offset:17408\n"
"v_mfma_f32_16x16x32_bf16 v[20:23], a[148:151], a[28:31], v[20:23]\n"
"ds_read_b64_tr_b16 a[74:75], v249 offset:17920\n"
"v_mfma_f32_16x16x32_bf16 v[52:55], a[116:119], a[4:7], v[52:55]\n"
"ds_read_b64_tr_b16 a[76:77], v249 offset:17416\n"
"v_mfma_f32_16x16x32_bf16 v[52:55], a[148:151], a[32:35], v[52:55]\n"
"ds_read_b64_tr_b16 a[78:79], v249 offset:17928\n"
"v_mfma_f32_16x16x32_bf16 v[84:87], a[116:119], a[8:11], v[84:87]\n"
"ds_read_b64_tr_b16 a[80:81], v250 offset:17408\n"
"v_mfma_f32_16x16x32_bf16 v[84:87], a[148:151], a[36:39], v[84:87]\n"
"ds_read_b64_tr_b16 a[82:83], v250 offset:17920\n"
"v_mfma_f32_16x16x32_bf16 v[116:119], a[116:119], a[12:15], v[116:119]\n"
"ds_read_b64_tr_b16 a[84:85], v250 offset:17416\n"
"v_mfma_f32_16x16x32_bf16 v[116:119], a[148:151], a[40:43], v[116:119]\n"
"ds_read_b64_tr_b16 a[86:87], v250 offset:17928\n"
"v_mfma_f32_16x16x32_bf16 v[148:151], a[116:119], a[16:19], v[148:151]\n"
"v_mfma_f32_16x16x32_bf16 v[148:151], a[148:151], a[44:47], v[148:151]\n"
"v_mfma_f32_16x16x32_bf16 v[180:183], a[116:119], a[20:23], v[180:183]\n"
"v_mfma_f32_16x16x32_bf16 v[180:183], a[148:151], a[48:51], v[180:183]\n"
"v_mfma_f32_16x16x32_bf16 v[24:27], a[120:123], a[0:3], v[24:27]\n"
"v_mfma_f32_16x16x32_bf16 v[24:27], a[152:155], a[28:31], v[24:27]\n"
"v_mfma_f32_16x16x32_bf16 v[56:59], a[120:123], a[4:7], v[56:59]\n"
"v_mfma_f32_16x16x32_bf16 v[56:59], a[152:155], a[32:35], v[56:59]\n"
"v_mfma_f32_16x16x32_bf16 v[88:91], a[120:123], a[8:11], v[88:91]\n"
"v_mfma_f32_16x16x32_bf16 v[88:91], a[152:155], a[36:39], v[88:91]\n"
"v_mfma_f32_16x16x32_bf16 v[120:123], a[120:123], a[12:15], v[120:123]\n"
"v_mfma_f32_16x16x32_bf16 v[120:123], a[152:155], a[40:43], v[120:123]\n"
"v_mfma_f32_16x16x32_bf16 v[152:155], a[120:123], a[16:19], v[152:155]\n"
"v_mfma_f32_16x16x32_bf16 v[152:155], a[152:155], a[44:47], v[152:155]\n"
"v_mfma_f32_16x16x32_bf16 v[184:187], a[120:123], a[20:23], v[184:187]\n"
"v_mfma_f32_16x16x32_bf16 v[184:187], a[152:155], a[48:51], v[184:187]\n"
"v_mfma_f32_16x16x32_bf16 v[28:31], a[124:127], a[0:3], v[28:31]\n"
"v_mfma_f32_16x16x32_bf16 v[28:31], a[156:159], a[28:31], v[28:31]\n"
"v_mfma_f32_16x16x32_bf16 v[60:63], a[124:127], a[4:7], v[60:63]\n"
"v_mfma_f32_16x16x32_bf16 v[60:63], a[156:159], a[32:35], v[60:63]\n"
"v_mfma_f32_16x16x32_bf16 v[92:95], a[124:127], a[8:11], v[92:95]\n"
"v_mfma_f32_16x16x32_bf16 v[92:95], a[156:159], a[36:39], v[92:95]\n"
"v_mfma_f32_16x16x32_bf16 v[124:127], a[124:127], a[12:15], v[124:127]\n"
"v_mfma_f32_16x16x32_bf16 v[124:127], a[156:159], a[40:43], v[124:127]\n"
"v_mfma_f32_16x16x32_bf16 v[156:159], a[124:127], a[16:19], v[156:159]\n"
"v_mfma_f32_16x16x32_bf16 v[156:159], a[156:159], a[44:47], v[156:159]\n"
"v_mfma_f32_16x16x32_bf16 v[188:191], a[124:127], a[20:23], v[188:191]\n"
"v_mfma_f32_16x16x32_bf16 v[188:191], a[156:159], a[48:51], v[188:191]\n"
"s_waitcnt lgkmcnt(0)\n"
"s_barrier\n"
"v_mfma_f32_16x16x32_bf16 v[0:3], a[56:59], a[160:163], v[0:3]\n"
"s_add_u32 m0, s70, 16384\n"
"v_mfma_f32_16x16x32_bf16 v[4:7], a[60:63], a[160:163], v[4:7]\n"
"buffer_load_dwordx4 v223, s[40:43], s60 offen offset:0 nt lds\n"
"v_mfma_f32_16x16x32_bf16 v[8:11], a[64:67], a[160:163], v[8:11]\n"
"ds_read_b128 a[192:195], v246 offset:40960\n"
"v_mfma_f32_16x16x32_bf16 v[12:15], a[68:71], a[160:163], v[12:15]\n"
"ds_read_b64_tr_b16 a[128:129], v249 offset:32768\n"
"v_mfma_f32_16x16x32_bf16 v[16:19], a[72:75], a[160:163], v[16:19]\n"
"ds_read_b64_tr_b16 a[130:131], v249 offset:33280\n"
"v_mfma_f32_16x16x32_bf16 v[20:23], a[76:79], a[160:163], v[20:23]\n"
"buffer_load_dwordx4 v223, s[40:43], s61 offen offset:1024 nt lds\n"
"v_mfma_f32_16x16x32_bf16 v[24:27], a[80:83], a[160:163], v[24:27]\n"
"ds_read_b64_tr_b16 a[132:133], v249 offset:32776\n"
"v_mfma_f32_16x16x32_bf16 v[28:31], a[84:87], a[160:163], v[28:31]\n"
"ds_read_b64_tr_b16 a[134:135], v249 offset:33288\n"
"v_mfma_f32_16x16x32_bf16 v[32:35], a[56:59], a[164:167], v[32:35]\n"
"ds_read_b64_tr_b16 a[136:137], v250 offset:32768\n"
"v_mfma_f32_16x16x32_bf16 v[36:39], a[60:63], a[164:167], v[36:39]\n"
"buffer_load_dwordx4 v223, s[40:43], s62 offen offset:2048 nt lds\n"
"v_mfma_f32_16x16x32_bf16 v[40:43], a[64:67], a[164:167], v[40:43]\n"
"ds_read_b64_tr_b16 a[138:139], v250 offset:33280\n"
"v_mfma_f32_16x16x32_bf16 v[44:47], a[68:71], a[164:167], v[44:47]\n"
"ds_read_b64_tr_b16 a[140:141], v250 offset:32776\n"
"v_mfma_f32_16x16x32_bf16 v[48:51], a[72:75], a[164:167], v[48:51]\n"
"ds_read_b64_tr_b16 a[142:143], v250 offset:33288\n"
"v_mfma_f32_16x16x32_bf16 v[52:55], a[76:79], a[164:167], v[52:55]\n"
"buffer_load_dwordx4 v223, s[40:43], 0 offen offset:3072 nt lds\n"
"v_mfma_f32_16x16x32_bf16 v[56:59], a[80:83], a[164:167], v[56:59]\n"
"ds_read_b64_tr_b16 a[144:145], v249 offset:33792\n"
"v_mfma_f32_16x16x32_bf16 v[60:63], a[84:87], a[164:167], v[60:63]\n"
"ds_read_b64_tr_b16 a[146:147], v249 offset:34304\n"
"v_mfma_f32_16x16x32_bf16 v[64:67], a[56:59], a[168:171], v[64:67]\n"
"ds_read_b64_tr_b16 a[148:149], v249 offset:33800\n"
"v_mfma_f32_16x16x32_bf16 v[68:71], a[60:63], a[168:171], v[68:71]\n"
"s_add_u32 m0, s70, 24576\n"
"v_mfma_f32_16x16x32_bf16 v[72:75], a[64:67], a[168:171], v[72:75]\n"
"buffer_load_dwordx4 v223, s[44:47], s60 offen offset:0 nt lds\n"
"v_mfma_f32_16x16x32_bf16 v[76:79], a[68:71], a[168:171], v[76:79]\n"
"ds_read_b64_tr_b16 a[150:151], v249 offset:34312\n"
"v_mfma_f32_16x16x32_bf16 v[80:83], a[72:75], a[168:171], v[80:83]\n"
"ds_read_b64_tr_b16 a[152:153], v250 offset:33792\n"
"v_mfma_f32_16x16x32_bf16 v[84:87], a[76:79], a[168:171], v[84:87]\n"
"ds_read_b64_tr_b16 a[154:155], v250 offset:34304\n"
"v_mfma_f32_16x16x32_bf16 v[88:91], a[80:83], a[168:171], v[88:91]\n"
"buffer_load_dwordx4 v223, s[44:47], s61 offen offset:1024 nt lds\n"
"v_mfma_f32_16x16x32_bf16 v[92:95], a[84:87], a[168:171], v[92:95]\n"
"ds_read_b64_tr_b16 a[156:157], v250 offset:33800\n"
"v_mfma_f32_16x16x32_bf16 v[96:99], a[56:59], a[172:175], v[96:99]\n"
"ds_read_b64_tr_b16 a[158:159], v250 offset:34312\n"
"v_mfma_f32_16x16x32_bf16 v[100:103], a[60:63], a[172:175], v[100:103]\n"
"ds_read_b128 a[196:199], v246 offset:41984\n"
"v_mfma_f32_16x16x32_bf16 v[104:107], a[64:67], a[172:175], v[104:107]\n"
"buffer_load_dwordx4 v223, s[44:47], s62 offen offset:2048 nt lds\n"
"v_mfma_f32_16x16x32_bf16 v[108:111], a[68:71], a[172:175], v[108:111]\n"
"ds_read_b128 a[200:203], v246 offset:43008\n"
"v_mfma_f32_16x16x32_bf16 v[112:115], a[72:75], a[172:175], v[112:115]\n"
"ds_read_b128 a[204:207], v246 offset:44032\n"
"v_mfma_f32_16x16x32_bf16 v[116:119], a[76:79], a[172:175], v[116:119]\n"
"ds_read_b128 a[208:211], v246 offset:45056\n"
"v_mfma_f32_16x16x32_bf16 v[120:123], a[80:83], a[172:175], v[120:123]\n"
"buffer_load_dwordx4 v223, s[44:47], 0 offen offset:3072 nt lds\n"
"v_mfma_f32_16x16x32_bf16 v[124:127], a[84:87], a[172:175], v[124:127]\n"
"ds_read_b128 a[212:215], v246 offset:46080\n"
"v_mfma_f32_16x16x32_bf16 v[128:131], a[56:59], a[176:179], v[128:131]\n"
"ds_read_b128 a[216:219], v247 offset:47120\n"
"v_mfma_f32_16x16x32_bf16 v[132:135], a[60:63], a[176:179], v[132:135]\n"
"v_mfma_f32_16x16x32_bf16 v[136:139], a[64:67], a[176:179], v[136:139]\n"
"v_add_u32 v223, 0x2000, v223\n"
"v_mfma_f32_16x16x32_bf16 v[140:143], a[68:71], a[176:179], v[140:143]\n"
"v_mfma_f32_16x16x32_bf16 v[144:147], a[72:75], a[176:179], v[144:147]\n"
"v_mfma_f32_16x16x32_bf16 v[148:151], a[76:79], a[176:179], v[148:151]\n"
"v_mfma_f32_16x16x32_bf16 v[152:155], a[80:83], a[176:179], v[152:155]\n"
"v_mfma_f32_16x16x32_bf16 v[156:159], a[84:87], a[176:179], v[156:159]\n"
"v_mfma_f32_16x16x32_bf16 v[160:163], a[56:59], a[180:183], v[160:163]\n"
"v_mfma_f32_16x16x32_bf16 v[164:167], a[60:63], a[180:183], v[164:167]\n"
"v_mfma_f32_16x16x32_bf16 v[168:171], a[64:67], a[180:183], v[168:171]\n"
"v_mfma_f32_16x16x32_bf16 v[172:175], a[68:71], a[180:183], v[172:175]\n"
"v_mfma_f32_16x16x32_bf16 v[176:179], a[72:75], a[180:183], v[176:179]\n"
"v_mfma_f32_16x16x32_bf16 v[180:183], a[76:79], a[180:183], v[180:183]\n"
"v_mfma_f32_16x16x32_bf16 v[184:187], a[80:83], a[180:183], v[184:187]\n"
"v_mfma_f32_16x16x32_bf16 v[188:191], a[84:87], a[180:183], v[188:191]\n"
"v_mfma_f32_16x16x32_bf16 v[192:195], a[56:59], a[184:187], v[192:195]\n"
"v_mfma_f32_16x16x32_bf16 v[196:199], a[60:63], a[184:187], v[196:199]\n"
"v_mfma_f32_16x16x32_bf16 v[200:203], a[64:67], a[184:187], v[200:203]\n"
"v_mfma_f32_16x16x32_bf16 v[204:207], a[68:71], a[184:187], v[204:207]\n"
"s_waitcnt vmcnt(13) lgkmcnt(0)\n"
"s_barrier\n"
"v_mfma_f32_16x16x32_bf16 v[0:3], a[128:131], a[192:195], v[0:3]\n"
"v_mfma_f32_16x16x32_bf16 v[4:7], a[132:135], a[192:195], v[4:7]\n"
"ds_read_b128 a[160:163], v246 offset:57344\n"
"v_mfma_f32_16x16x32_bf16 v[8:11], a[136:139], a[192:195], v[8:11]\n"
"ds_read_b64_tr_b16 a[96:97], v249 offset:49152\n"
"v_mfma_f32_16x16x32_bf16 v[12:15], a[140:143], a[192:195], v[12:15]\n"
"ds_read_b64_tr_b16 a[98:99], v249 offset:49664\n"
"v_mfma_f32_16x16x32_bf16 v[16:19], a[144:147], a[192:195], v[16:19]\n"
"ds_read_b64_tr_b16 a[100:101], v249 offset:49160\n"
"v_mfma_f32_16x16x32_bf16 v[20:23], a[148:151], a[192:195], v[20:23]\n"
"ds_read_b64_tr_b16 a[102:103], v249 offset:49672\n"
"v_mfma_f32_16x16x32_bf16 v[24:27], a[152:155], a[192:195], v[24:27]\n"
"ds_read_b64_tr_b16 a[104:105], v250 offset:49152\n"
"v_mfma_f32_16x16x32_bf16 v[28:31], a[156:159], a[192:195], v[28:31]\n"
"ds_read_b64_tr_b16 a[106:107], v250 offset:49664\n"
"v_mfma_f32_16x16x32_bf16 v[32:35], a[128:131], a[196:199], v[32:35]\n"
"ds_read_b64_tr_b16 a[108:109], v250 offset:49160\n"
"v_mfma_f32_16x16x32_bf16 v[36:39], a[132:135], a[196:199], v[36:39]\n"
"ds_read_b64_tr_b16 a[110:111], v250 offset:49672\n"
"v_mfma_f32_16x16x32_bf16 v[40:43], a[136:139], a[196:199], v[40:43]\n"
"ds_read_b64_tr_b16 a[112:113], v249 offset:50176\n"
"v_mfma_f32_16x16x32_bf16 v[44:47], a[140:143], a[196:199], v[44:47]\n"
"ds_read_b64_tr_b16 a[114:115], v249 offset:50688\n"
"v_mfma_f32_16x16x32_bf16 v[48:51], a[144:147], a[196:199], v[48:51]\n"
"ds_read_b64_tr_b16 a[116:117], v249 offset:50184\n"
"v_mfma_f32_16x16x32_bf16 v[52:55], a[148:151], a[196:199], v[52:55]\n"
"ds_read_b64_tr_b16 a[118:119], v249 offset:50696\n"
"v_mfma_f32_16x16x32_bf16 v[56:59], a[152:155], a[196:199], v[56:59]\n"
"ds_read_b64_tr_b16 a[120:121], v250 offset:50176\n"
"v_mfma_f32_16x16x32_bf16 v[60:63], a[156:159], a[196:199], v[60:63]\n"
"ds_read_b64_tr_b16 a[122:123], v250 offset:50688\n"
"v_mfma_f32_16x16x32_bf16 v[64:67], a[128:131], a[200:203], v[64:67]\n"
"ds_read_b64_tr_b16 a[124:125], v250 offset:50184\n"
"v_mfma_f32_16x16x32_bf16 v[68:71], a[132:135], a[200:203], v[68:71]\n"
"ds_read_b64_tr_b16 a[126:127], v250 offset:50696\n"
"v_mfma_f32_16x16x32_bf16 v[72:75], a[136:139], a[200:203], v[72:75]\n"
"ds_read_b128 a[164:167], v246 offset:58368\n"
"v_mfma_f32_16x16x32_bf16 v[76:79], a[140:143], a[200:203], v[76:79]\n"
"ds_read_b128 a[168:171], v246 offset:59392\n"
"v_mfma_f32_16x16x32_bf16 v[80:83], a[144:147], a[200:203], v[80:83]\n"
"s_add_u32 m0, s70, 32768\n"
"v_mfma_f32_16x16x32_bf16 v[84:87], a[148:151], a[200:203], v[84:87]\n"
"buffer_load_dwordx4 v223, s[40:43], s60 offen offset:0 nt lds\n"
"v_mfma_f32_16x16x32_bf16 v[88:91], a[152:155], a[200:203], v[88:91]\n"
"ds_read_b128 a[172:175], v246 offset:60416\n"
"v_mfma_f32_16x16x32_bf16 v[92:95], a[156:159], a[200:203], v[92:95]\n"
"ds_read_b128 a[176:179], v246 offset:61440\n"
"v_mfma_f32_16x16x32_bf16 v[96:99], a[128:131], a[204:207], v[96:99]\n"
"ds_read_b128 a[180:183], v246 offset:62464\n"
"v_mfma_f32_16x16x32_bf16 v[100:103], a[132:135], a[204:207], v[100:103]\n"
"ds_read_b128 a[184:187], v247 offset:63504\n"
"v_mfma_f32_16x16x32_bf16 v[104:107], a[136:139], a[204:207], v[104:107]\n"
"v_mfma_f32_16x16x32_bf16 v[108:111], a[140:143], a[204:207], v[108:111]\n"
"v_mfma_f32_16x16x32_bf16 v[112:115], a[144:147], a[204:207], v[112:115]\n"
"v_mfma_f32_16x16x32_bf16 v[116:119], a[148:151], a[204:207], v[116:119]\n"
"v_mfma_f32_16x16x32_bf16 v[120:123], a[152:155], a[204:207], v[120:123]\n"
"v_mfma_f32_16x16x32_bf16 v[124:127], a[156:159], a[204:207], v[124:127]\n"
"v_mfma_f32_16x16x32_bf16 v[128:131], a[128:131], a[208:211], v[128:131]\n"
"v_mfma_f32_16x16x32_bf16 v[132:135], a[132:135], a[208:211], v[132:135]\n"
"v_mfma_f32_16x16x32_bf16 v[136:139], a[136:139], a[208:211], v[136:139]\n"
"v_mfma_f32_16x16x32_bf16 v[140:143], a[140:143], a[208:211], v[140:143]\n"
"buffer_load_dwordx4 v223, s[40:43], s61 offen offset:1024 nt lds\n"
"v_mfma_f32_16x16x32_bf16 v[144:147], a[144:147], a[208:211], v[144:147]\n"
"v_mfma_f32_16x16x32_bf16 v[148:151], a[148:151], a[208:211], v[148:151]\n"
"v_mfma_f32_16x16x32_bf16 v[152:155], a[152:155], a[208:211], v[152:155]\n"
"v_mfma_f32_16x16x32_bf16 v[156:159], a[156:159], a[208:211], v[156:159]\n"
"v_mfma_f32_16x16x32_bf16 v[160:163], a[128:131], a[212:215], v[160:163]\n"
"v_mfma_f32_16x16x32_bf16 v[164:167], a[132:135], a[212:215], v[164:167]\n"
"v_mfma_f32_16x16x32_bf16 v[168:171], a[136:139], a[212:215], v[168:171]\n"
"v_mfma_f32_16x16x32_bf16 v[172:175], a[140:143], a[212:215], v[172:175]\n"
"v_mfma_f32_16x16x32_bf16 v[176:179], a[144:147], a[212:215], v[176:179]\n"
"v_mfma_f32_16x16x32_bf16 v[180:183], a[148:151], a[212:215], v[180:183]\n"
"v_mfma_f32_16x16x32_bf16 v[184:187], a[152:155], a[212:215], v[184:187]\n"
"v_mfma_f32_16x16x32_bf16 v[188:191], a[156:159], a[212:215], v[188:191]\n"
"v_mfma_f32_16x16x32_bf16 v[192:195], a[128:131], a[216:219], v[192:195]\n"
"v_mfma_f32_16x16x32_bf16 v[196:199], a[132:135], a[216:219], v[196:199]\n"
"v_mfma_f32_16x16x32_bf16 v[200:203], a[136:139], a[216:219], v[200:203]\n"
"v_mfma_f32_16x16x32_bf16 v[204:207], a[140:143], a[216:219], v[204:207]\n"
"s_waitcnt vmcnt(10) lgkmcnt(0)\n"
"s_barrier\n"
"v_mfma_f32_16x16x32_bf16 v[0:3], a[96:99], a[160:163], v[0:3]\n"
"v_mfma_f32_16x16x32_bf16 v[4:7], a[100:103], a[160:163], v[4:7]\n"
"ds_read_b128 a[192:195], v248 offset:8192\n"
"v_mfma_f32_16x16x32_bf16 v[8:11], a[104:107], a[160:163], v[8:11]\n"
"s_add_u32 m0, s70, 32768\n"
"v_mfma_f32_16x16x32_bf16 v[12:15], a[108:111], a[160:163], v[12:15]\n"
"buffer_load_dwordx4 v223, s[40:43], s62 offen offset:2048 nt lds\n"
"v_mfma_f32_16x16x32_bf16 v[16:19], a[112:115], a[160:163], v[16:19]\n"
"ds_read_b64_tr_b16 a[128:129], v251 offset:0\n"
"v_mfma_f32_16x16x32_bf16 v[20:23], a[116:119], a[160:163], v[20:23]\n"
"ds_read_b64_tr_b16 a[130:131], v251 offset:512\n"
"v_mfma_f32_16x16x32_bf16 v[24:27], a[120:123], a[160:163], v[24:27]\n"
"ds_read_b64_tr_b16 a[132:133], v251 offset:8\n"
"v_mfma_f32_16x16x32_bf16 v[28:31], a[124:127], a[160:163], v[28:31]\n"
"ds_read_b64_tr_b16 a[134:135], v251 offset:520\n"
"v_mfma_f32_16x16x32_bf16 v[32:35], a[96:99], a[164:167], v[32:35]\n"
"ds_read_b64_tr_b16 a[136:137], v252 offset:0\n"
"v_mfma_f32_16x16x32_bf16 v[36:39], a[100:103], a[164:167], v[36:39]\n"
"ds_read_b64_tr_b16 a[138:139], v252 offset:512\n"
"v_mfma_f32_16x16x32_bf16 v[40:43], a[104:107], a[164:167], v[40:43]\n"
"ds_read_b64_tr_b16 a[140:141], v252 offset:8\n"
"v_mfma_f32_16x16x32_bf16 v[44:47], a[108:111], a[164:167], v[44:47]\n"
"ds_read_b64_tr_b16 a[142:143], v252 offset:520\n"
"v_mfma_f32_16x16x32_bf16 v[48:51], a[112:115], a[164:167], v[48:51]\n"
"ds_read_b64_tr_b16 a[144:145], v251 offset:1024\n"
"v_mfma_f32_16x16x32_bf16 v[52:55], a[116:119], a[164:167], v[52:55]\n"
"ds_read_b64_tr_b16 a[146:147], v251 offset:1536\n"
"v_mfma_f32_16x16x32_bf16 v[56:59], a[120:123], a[164:167], v[56:59]\n"
"ds_read_b64_tr_b16 a[148:149], v251 offset:1032\n"
"v_mfma_f32_16x16x32_bf16 v[60:63], a[124:127], a[164:167], v[60:63]\n"
"ds_read_b64_tr_b16 a[150:151], v251 offset:1544\n"
"v_mfma_f32_16x16x32_bf16 v[64:67], a[96:99], a[168:171], v[64:67]\n"
"ds_read_b64_tr_b16 a[152:153], v252 offset:1024\n"
"v_mfma_f32_16x16x32_bf16 v[68:71], a[100:103], a[168:171], v[68:71]\n"
"ds_read_b64_tr_b16 a[154:155], v252 offset:1536\n"
"v_mfma_f32_16x16x32_bf16 v[72:75], a[104:107], a[168:171], v[72:75]\n"
"buffer_load_dwordx4 v223, s[40:43], 0 offen offset:3072 nt lds\n"
"v_mfma_f32_16x16x32_bf16 v[76:79], a[108:111], a[168:171], v[76:79]\n"
"ds_read_b64_tr_b16 a[156:157], v252 offset:1032\n"
"v_mfma_f32_16x16x32_bf16 v[80:83], a[112:115], a[168:171], v[80:83]\n"
"ds_read_b64_tr_b16 a[158:159], v252 offset:1544\n"
"v_mfma_f32_16x16x32_bf16 v[84:87], a[116:119], a[168:171], v[84:87]\n"
"ds_read_b128 a[196:199], v248 offset:9216\n"
"v_mfma_f32_16x16x32_bf16 v[88:91], a[120:123], a[168:171], v[88:91]\n"
"ds_read_b128 a[200:203], v248 offset:10240\n"
"v_mfma_f32_16x16x32_bf16 v[92:95], a[124:127], a[168:171], v[92:95]\n"
"ds_read_b128 a[204:207], v248 offset:11264\n"
"v_mfma_f32_16x16x32_bf16 v[96:99], a[96:99], a[172:175], v[96:99]\n"
"ds_read_b128 a[208:211], v248 offset:12288\n"
"v_mfma_f32_16x16x32_bf16 v[100:103], a[100:103], a[172:175], v[100:103]\n"
"ds_read_b128 a[212:215], v248 offset:13312\n"
"v_mfma_f32_16x16x32_bf16 v[104:107], a[104:107], a[172:175], v[104:107]\n"
"ds_read_b128 a[216:219], v248 offset:30736\n"
"v_mfma_f32_16x16x32_bf16 v[108:111], a[108:111], a[172:175], v[108:111]\n"
"v_mfma_f32_16x16x32_bf16 v[112:115], a[112:115], a[172:175], v[112:115]\n"
"v_mfma_f32_16x16x32_bf16 v[116:119], a[116:119], a[172:175], v[116:119]\n"
"v_mfma_f32_16x16x32_bf16 v[120:123], a[120:123], a[172:175], v[120:123]\n"
"v_mfma_f32_16x16x32_bf16 v[124:127], a[124:127], a[172:175], v[124:127]\n"
"v_mfma_f32_16x16x32_bf16 v[128:131], a[96:99], a[176:179], v[128:131]\n"
"v_mfma_f32_16x16x32_bf16 v[132:135], a[100:103], a[176:179], v[132:135]\n"
"s_add_u32 m0, s70, 40960\n"
"v_mfma_f32_16x16x32_bf16 v[136:139], a[104:107], a[176:179], v[136:139]\n"
"buffer_load_dwordx4 v223, s[44:47], s60 offen offset:0 nt lds\n"
"v_mfma_f32_16x16x32_bf16 v[140:143], a[108:111], a[176:179], v[140:143]\n"
"v_mfma_f32_16x16x32_bf16 v[144:147], a[112:115], a[176:179], v[144:147]\n"
"v_mfma_f32_16x16x32_bf16 v[148:151], a[116:119], a[176:179], v[148:151]\n"
"v_mfma_f32_16x16x32_bf16 v[152:155], a[120:123], a[176:179], v[152:155]\n"
"v_mfma_f32_16x16x32_bf16 v[156:159], a[124:127], a[176:179], v[156:159]\n"
"v_mfma_f32_16x16x32_bf16 v[160:163], a[96:99], a[180:183], v[160:163]\n"
"v_mfma_f32_16x16x32_bf16 v[164:167], a[100:103], a[180:183], v[164:167]\n"
"v_mfma_f32_16x16x32_bf16 v[168:171], a[104:107], a[180:183], v[168:171]\n"
"v_mfma_f32_16x16x32_bf16 v[172:175], a[108:111], a[180:183], v[172:175]\n"
"v_mfma_f32_16x16x32_bf16 v[176:179], a[112:115], a[180:183], v[176:179]\n"
"v_mfma_f32_16x16x32_bf16 v[180:183], a[116:119], a[180:183], v[180:183]\n"
"v_mfma_f32_16x16x32_bf16 v[184:187], a[120:123], a[180:183], v[184:187]\n"
"v_mfma_f32_16x16x32_bf16 v[188:191], a[124:127], a[180:183], v[188:191]\n"
"v_mfma_f32_16x16x32_bf16 v[192:195], a[96:99], a[184:187], v[192:195]\n"
"v_mfma_f32_16x16x32_bf16 v[196:199], a[100:103], a[184:187], v[196:199]\n"
"v_mfma_f32_16x16x32_bf16 v[200:203], a[104:107], a[184:187], v[200:203]\n"
"v_mfma_f32_16x16x32_bf16 v[204:207], a[108:111], a[184:187], v[204:207]\n"
"s_waitcnt lgkmcnt(0)\n"
"s_barrier\n"
"v_mfma_f32_16x16x32_bf16 v[192:195], a[128:131], a[216:219], v[192:195]\n"
"ds_read_b128 a[96:99], v220 offset:0\n"
"v_mfma_f32_16x16x32_bf16 v[196:199], a[132:135], a[216:219], v[196:199]\n"
"ds_read_b128 a[100:103], v220 offset:1024\n"
"v_mfma_f32_16x16x32_bf16 v[200:203], a[136:139], a[216:219], v[200:203]\n"
"s_add_u32 m0, s70, 40960\n"
"v_mfma_f32_16x16x32_bf16 v[204:207], a[140:143], a[216:219], v[204:207]\n"
"buffer_load_dwordx4 v223, s[44:47], s61 offen offset:1024 nt lds\n"
"v_mfma_f32_16x16x32_bf16 v[0:3], a[128:131], a[192:195], v[0:3]\n"
"ds_read_b128 a[104:107], v220 offset:2048\n"
"v_mfma_f32_16x16x32_bf16 v[4:7], a[132:135], a[192:195], v[4:7]\n"
"ds_read_b128 a[108:111], v220 offset:3072\n"
"v_mfma_f32_16x16x32_bf16 v[8:11], a[136:139], a[192:195], v[8:11]\n"
"ds_read_b128 a[112:115], v220 offset:4096\n"
"v_mul_f32 v192, s98, v192\n"
"v_mfma_f32_16x16x32_bf16 v[12:15], a[140:143], a[192:195], v[12:15]\n"
"ds_read_b128 a[116:119], v220 offset:5120\n"
"v_mul_f32 v193, s98, v193\n"
"v_mfma_f32_16x16x32_bf16 v[16:19], a[144:147], a[192:195], v[16:19]\n"
"ds_read_b128 a[120:123], v220 offset:6144\n"
"v_mul_f32 v194, s98, v194\n"
"v_mfma_f32_16x16x32_bf16 v[20:23], a[148:151], a[192:195], v[20:23]\n"
"ds_read_b128 a[124:127], v220 offset:7168\n"
"v_mul_f32 v195, s98, v195\n"
"v_mfma_f32_16x16x32_bf16 v[24:27], a[152:155], a[192:195], v[24:27]\n"
"ds_read_b64_tr_b16 a[160:161], v221 offset:0\n"
"v_mul_f32 v196, s98, v196\n"
"v_mfma_f32_16x16x32_bf16 v[28:31], a[156:159], a[192:195], v[28:31]\n"
"ds_read_b64_tr_b16 a[162:163], v221 offset:4096\n"
"v_mul_f32 v197, s98, v197\n"
"v_mfma_f32_16x16x32_bf16 v[32:35], a[128:131], a[196:199], v[32:35]\n"
"ds_read_b64_tr_b16 a[164:165], v222 offset:0\n"
"v_mul_f32 v198, s98, v198\n"
"v_mfma_f32_16x16x32_bf16 v[36:39], a[132:135], a[196:199], v[36:39]\n"
"ds_read_b64_tr_b16 a[166:167], v222 offset:4096\n"
"v_mul_f32 v199, s98, v199\n"
"v_mfma_f32_16x16x32_bf16 v[40:43], a[136:139], a[196:199], v[40:43]\n"
"ds_read_b64_tr_b16 a[168:169], v221 offset:1024\n"
"v_cvt_pk_bf16_f32 v192, v192, v193\n"
"v_mfma_f32_16x16x32_bf16 v[44:47], a[140:143], a[196:199], v[44:47]\n"
"ds_read_b64_tr_b16 a[170:171], v221 offset:5120\n"
"v_cvt_pk_bf16_f32 v193, v194, v195\n"
"v_mfma_f32_16x16x32_bf16 v[48:51], a[144:147], a[196:199], v[48:51]\n"
"ds_read_b64_tr_b16 a[172:173], v222 offset:1024\n"
"v_cvt_pk_bf16_f32 v194, v196, v197\n"
"v_mfma_f32_16x16x32_bf16 v[52:55], a[148:151], a[196:199], v[52:55]\n"
"ds_read_b64_tr_b16 a[174:175], v222 offset:5120\n"
"v_cvt_pk_bf16_f32 v195, v198, v199\n"
"v_mfma_f32_16x16x32_bf16 v[56:59], a[152:155], a[196:199], v[56:59]\n"
"buffer_load_dwordx4 v223, s[44:47], s62 offen offset:2048 nt lds\n"
"v_mfma_f32_16x16x32_bf16 v[60:63], a[156:159], a[196:199], v[60:63]\n"
"ds_read_b64_tr_b16 a[176:177], v221 offset:2048\n"
"ds_write_b128 v245, v[192:195] offset:0\n"
"v_mfma_f32_16x16x32_bf16 v[64:67], a[128:131], a[200:203], v[64:67]\n"
"ds_read_b64_tr_b16 a[178:179], v221 offset:6144\n"
"v_mul_f32 v200, s98, v200\n"
"v_mfma_f32_16x16x32_bf16 v[68:71], a[132:135], a[200:203], v[68:71]\n"
"ds_read_b64_tr_b16 a[180:181], v222 offset:2048\n"
"v_mul_f32 v201, s98, v201\n"
"v_mfma_f32_16x16x32_bf16 v[72:75], a[136:139], a[200:203], v[72:75]\n"
"ds_read_b64_tr_b16 a[182:183], v222 offset:6144\n"
"v_mul_f32 v202, s98, v202\n"
"v_mfma_f32_16x16x32_bf16 v[76:79], a[140:143], a[200:203], v[76:79]\n"
"ds_read_b64_tr_b16 a[184:185], v221 offset:3072\n"
"v_mul_f32 v203, s98, v203\n"
"v_mfma_f32_16x16x32_bf16 v[80:83], a[144:147], a[200:203], v[80:83]\n"
"ds_read_b64_tr_b16 a[186:187], v221 offset:7168\n"
"v_mul_f32 v204, s98, v204\n"
"v_mfma_f32_16x16x32_bf16 v[84:87], a[148:151], a[200:203], v[84:87]\n"
"ds_read_b64_tr_b16 a[188:189], v222 offset:3072\n"
"v_mul_f32 v205, s98, v205\n"
"v_mfma_f32_16x16x32_bf16 v[88:91], a[152:155], a[200:203], v[88:91]\n"
"ds_read_b64_tr_b16 a[190:191], v222 offset:7168\n"
"v_mul_f32 v206, s98, v206\n"
"v_mfma_f32_16x16x32_bf16 v[92:95], a[156:159], a[200:203], v[92:95]\n"
"v_mul_f32 v207, s98, v207\n"
"v_cvt_pk_bf16_f32 v200, v200, v201\n"
"v_mfma_f32_16x16x32_bf16 v[96:99], a[128:131], a[204:207], v[96:99]\n"
"v_cvt_pk_bf16_f32 v201, v202, v203\n"
"v_cvt_pk_bf16_f32 v202, v204, v205\n"
"v_mfma_f32_16x16x32_bf16 v[100:103], a[132:135], a[204:207], v[100:103]\n"
"v_cvt_pk_bf16_f32 v203, v206, v207\n"
"ds_write_b128 v245, v[200:203] offset:1024\n"
"v_mfma_f32_16x16x32_bf16 v[104:107], a[136:139], a[204:207], v[104:107]\n"
"v_mul_f32 v0, s98, v0\n"
"v_mul_f32 v1, s98, v1\n"
"v_mfma_f32_16x16x32_bf16 v[108:111], a[140:143], a[204:207], v[108:111]\n"
"v_mul_f32 v2, s98, v2\n"
"v_mul_f32 v3, s98, v3\n"
"v_mfma_f32_16x16x32_bf16 v[112:115], a[144:147], a[204:207], v[112:115]\n"
"v_mul_f32 v4, s98, v4\n"
"v_mul_f32 v5, s98, v5\n"
"v_mfma_f32_16x16x32_bf16 v[116:119], a[148:151], a[204:207], v[116:119]\n"
"buffer_load_dwordx4 v223, s[44:47], 0 offen offset:3072 nt lds\n"
"v_mfma_f32_16x16x32_bf16 v[120:123], a[152:155], a[204:207], v[120:123]\n"
"v_mul_f32 v6, s98, v6\n"
"v_mul_f32 v7, s98, v7\n"
"v_mfma_f32_16x16x32_bf16 v[124:127], a[156:159], a[204:207], v[124:127]\n"
"v_cvt_pk_bf16_f32 v0, v0, v1\n"
"v_cvt_pk_bf16_f32 v1, v2, v3\n"
"v_mfma_f32_16x16x32_bf16 v[128:131], a[128:131], a[208:211], v[128:131]\n"
"v_cvt_pk_bf16_f32 v2, v4, v5\n"
"v_cvt_pk_bf16_f32 v3, v6, v7\n"
"v_mfma_f32_16x16x32_bf16 v[132:135], a[132:135], a[208:211], v[132:135]\n"
"v_accvgpr_write_b32 a0, v0\n"
"v_accvgpr_write_b32 a1, v1\n"
"v_mfma_f32_16x16x32_bf16 v[136:139], a[136:139], a[208:211], v[136:139]\n"
"v_accvgpr_write_b32 a2, v2\n"
"v_accvgpr_write_b32 a3, v3\n"
"v_mfma_f32_16x16x32_bf16 v[140:143], a[140:143], a[208:211], v[140:143]\n"
"v_mul_f32 v8, s98, v8\n"
"v_mul_f32 v9, s98, v9\n"
"v_mfma_f32_16x16x32_bf16 v[144:147], a[144:147], a[208:211], v[144:147]\n"
"v_mul_f32 v10, s98, v10\n"
"v_mul_f32 v11, s98, v11\n"
"v_mfma_f32_16x16x32_bf16 v[148:151], a[148:151], a[208:211], v[148:151]\n"
"v_mul_f32 v12, s98, v12\n"
"v_mul_f32 v13, s98, v13\n"
"v_mfma_f32_16x16x32_bf16 v[152:155], a[152:155], a[208:211], v[152:155]\n"
"v_mul_f32 v14, s98, v14\n"
"v_mul_f32 v15, s98, v15\n"
"v_mfma_f32_16x16x32_bf16 v[156:159], a[156:159], a[208:211], v[156:159]\n"
"v_cvt_pk_bf16_f32 v8, v8, v9\n"
"v_cvt_pk_bf16_f32 v9, v10, v11\n"
"v_mfma_f32_16x16x32_bf16 v[160:163], a[128:131], a[212:215], v[160:163]\n"
"v_cvt_pk_bf16_f32 v10, v12, v13\n"
"v_cvt_pk_bf16_f32 v11, v14, v15\n"
"v_mfma_f32_16x16x32_bf16 v[164:167], a[132:135], a[212:215], v[164:167]\n"
"v_accvgpr_write_b32 a4, v8\n"
"v_accvgpr_write_b32 a5, v9\n"
"v_mfma_f32_16x16x32_bf16 v[168:171], a[136:139], a[212:215], v[168:171]\n"
"v_accvgpr_write_b32 a6, v10\n"
"v_accvgpr_write_b32 a7, v11\n"
"v_mfma_f32_16x16x32_bf16 v[172:175], a[140:143], a[212:215], v[172:175]\n"
"v_mfma_f32_16x16x32_bf16 v[176:179], a[144:147], a[212:215], v[176:179]\n"
"v_add_u32 v223, 0x2000, v223\n"
"v_mfma_f32_16x16x32_bf16 v[180:183], a[148:151], a[212:215], v[180:183]\n"
"v_mfma_f32_16x16x32_bf16 v[184:187], a[152:155], a[212:215], v[184:187]\n"
"v_mfma_f32_16x16x32_bf16 v[188:191], a[156:159], a[212:215], v[188:191]\n"
"s_waitcnt lgkmcnt(0)\n"
"s_barrier\n"
"ds_read_b128 a[128:131], v245 offset:0\n"
"ds_read_b128 a[132:135], v245 offset:1024\n"
"ds_read_b128 a[136:139], v245 offset:2048\n"
"ds_read_b128 a[140:143], v245 offset:3072\n"
"s_add_u32 m0, s70, 49152\n"
"v_pk_mul_f32 v[16:17], v[16:17], s[98:99] op_sel_hi:[1,0]\n"
"buffer_load_dwordx4 v223, s[40:43], s60 offen offset:0 nt lds\n"
"v_pk_mul_f32 v[18:19], v[18:19], s[98:99] op_sel_hi:[1,0]\n"
"v_pk_mul_f32 v[20:21], v[20:21], s[98:99] op_sel_hi:[1,0]\n"
"v_pk_mul_f32 v[22:23], v[22:23], s[98:99] op_sel_hi:[1,0]\n"
"v_cvt_pk_bf16_f32 v16, v16, v17\n"
"v_cvt_pk_bf16_f32 v17, v18, v19\n"
"v_cvt_pk_bf16_f32 v18, v20, v21\n"
"v_cvt_pk_bf16_f32 v19, v22, v23\n"
"v_accvgpr_write_b32 a8, v16\n"
"v_accvgpr_write_b32 a9, v17\n"
"v_accvgpr_write_b32 a10, v18\n"
"v_accvgpr_write_b32 a11, v19\n"
"v_pk_mul_f32 v[24:25], v[24:25], s[98:99] op_sel_hi:[1,0]\n"
"v_pk_mul_f32 v[26:27], v[26:27], s[98:99] op_sel_hi:[1,0]\n"
"v_pk_mul_f32 v[28:29], v[28:29], s[98:99] op_sel_hi:[1,0]\n"
"v_pk_mul_f32 v[30:31], v[30:31], s[98:99] op_sel_hi:[1,0]\n"
"v_cvt_pk_bf16_f32 v24, v24, v25\n"
"v_cvt_pk_bf16_f32 v25, v26, v27\n"
"v_cvt_pk_bf16_f32 v26, v28, v29\n"
"v_cvt_pk_bf16_f32 v27, v30, v31\n"
"v_accvgpr_write_b32 a12, v24\n"
"buffer_load_dwordx4 v223, s[40:43], s61 offen offset:1024 nt lds\n"
"v_accvgpr_write_b32 a13, v25\n"
"v_accvgpr_write_b32 a14, v26\n"
"v_accvgpr_write_b32 a15, v27\n"
"s_waitcnt lgkmcnt(0)\n"
"v_pk_mul_f32 v[32:33], v[32:33], s[98:99] op_sel_hi:[1,0]\n"
"v_pk_mul_f32 v[34:35], v[34:35], s[98:99] op_sel_hi:[1,0]\n"
"v_pk_mul_f32 v[36:37], v[36:37], s[98:99] op_sel_hi:[1,0]\n"
"v_pk_mul_f32 v[38:39], v[38:39], s[98:99] op_sel_hi:[1,0]\n"
"v_mfma_f32_16x16x32_bf16 v[192:195], a[96:99], a[128:131], 0\n"
"v_cvt_pk_bf16_f32 v32, v32, v33\n"
"v_cvt_pk_bf16_f32 v33, v34, v35\n"
"v_cvt_pk_bf16_f32 v34, v36, v37\n"
"v_cvt_pk_bf16_f32 v35, v38, v39\n"
"v_mfma_f32_16x16x32_bf16 v[192:195], a[100:103], a[132:135], v[192:195]\n"
"v_accvgpr_write_b32 a16, v32\n"
"v_accvgpr_write_b32 a17, v33\n"
"v_accvgpr_write_b32 a18, v34\n"
"v_accvgpr_write_b32 a19, v35\n"
"v_mfma_f32_16x16x32_bf16 v[192:195], a[104:107], a[136:139], v[192:195]\n"
"v_pk_mul_f32 v[40:41], v[40:41], s[98:99] op_sel_hi:[1,0]\n"
"v_pk_mul_f32 v[42:43], v[42:43], s[98:99] op_sel_hi:[1,0]\n"
"v_pk_mul_f32 v[44:45], v[44:45], s[98:99] op_sel_hi:[1,0]\n"
"v_pk_mul_f32 v[46:47], v[46:47], s[98:99] op_sel_hi:[1,0]\n"
"v_mfma_f32_16x16x32_bf16 v[192:195], a[108:111], a[140:143], v[192:195]\n"
"v_cvt_pk_bf16_f32 v40, v40, v41\n"
"buffer_load_dwordx4 v223, s[40:43], s62 offen offset:2048 nt lds\n"
"v_cvt_pk_bf16_f32 v41, v42, v43\n"
"v_cvt_pk_bf16_f32 v42, v44, v45\n"
"v_cvt_pk_bf16_f32 v43, v46, v47\n"
"v_accvgpr_write_b32 a20, v40\n"
"v_accvgpr_write_b32 a21, v41\n"
"v_accvgpr_write_b32 a22, v42\n"
"v_accvgpr_write_b32 a23, v43\n"
"v_pk_mul_f32 v[48:49], v[48:49], s[98:99] op_sel_hi:[1,0]\n"
"v_pk_mul_f32 v[50:51], v[50:51], s[98:99] op_sel_hi:[1,0]\n"
"v_pk_mul_f32 v[52:53], v[52:53], s[98:99] op_sel_hi:[1,0]\n"
"v_pk_mul_f32 v[54:55], v[54:55], s[98:99] op_sel_hi:[1,0]\n"
"v_cvt_pk_bf16_f32 v48, v48, v49\n"
"v_cvt_pk_bf16_f32 v49, v50, v51\n"
"v_max3_f32 v192, v192, v193, v194\n"
"v_cvt_pk_bf16_f32 v50, v52, v53\n"
"v_cvt_pk_bf16_f32 v51, v54, v55\n"
"v_max_f32 v192, v192, v195\n"
"v_accvgpr_write_b32 a24, v48\n"
"v_accvgpr_write_b32 a25, v49\n"
"v_mov_b32 v193, v192\n"
"v_accvgpr_write_b32 a26, v50\n"
"v_accvgpr_write_b32 a27, v51\n"
"v_permlane32_swap_b32 v193, v192\n"
"v_pk_mul_f32 v[56:57], v[56:57], s[98:99] op_sel_hi:[1,0]\n"
"buffer_load_dwordx4 v223, s[40:43], 0 offen offset:3072 nt lds\n"
"v_pk_mul_f32 v[58:59], v[58:59], s[98:99] op_sel_hi:[1,0]\n"
"v_max_f32 v192, v192, v193\n"
"v_pk_mul_f32 v[60:61], v[60:61], s[98:99] op_sel_hi:[1,0]\n"
"v_pk_mul_f32 v[62:63], v[62:63], s[98:99] op_sel_hi:[1,0]\n"
"v_mov_b32 v193, v192\n"
"v_cvt_pk_bf16_f32 v56, v56, v57\n"
"v_cvt_pk_bf16_f32 v57, v58, v59\n"
"v_permlane16_swap_b32 v193, v192\n"
"v_cvt_pk_bf16_f32 v58, v60, v61\n"
"v_cvt_pk_bf16_f32 v59, v62, v63\n"
"v_max_f32 v192, v192, v193\n"
"v_accvgpr_write_b32 a28, v56\n"
"v_accvgpr_write_b32 a29, v57\n"
"v_max_f32_dpp v192, v192, v192 row_ror:8 row_mask:0xf bank_mask:0xf\n"
"v_accvgpr_write_b32 a30, v58\n"
"v_accvgpr_write_b32 a31, v59\n"
"v_max_f32_dpp v192, v192, v192 row_ror:4 row_mask:0xf bank_mask:0xf\n"
"v_pk_mul_f32 v[64:65], v[64:65], s[98:99] op_sel_hi:[1,0]\n"
"v_pk_mul_f32 v[66:67], v[66:67], s[98:99] op_sel_hi:[1,0]\n"
"v_max_f32_dpp v192, v192, v192 row_ror:2 row_mask:0xf bank_mask:0xf\n"
"v_pk_mul_f32 v[68:69], v[68:69], s[98:99] op_sel_hi:[1,0]\n"
"v_pk_mul_f32 v[70:71], v[70:71], s[98:99] op_sel_hi:[1,0]\n"
"v_max_f32_dpp v192, v192, v192 row_ror:1 row_mask:0xf bank_mask:0xf\n"
"v_cvt_pk_bf16_f32 v64, v64, v65\n"
"v_cvt_pk_bf16_f32 v65, v66, v67\n"
"v_xor_b32 v216, 0x80000000, v192\n"
"v_cvt_pk_bf16_f32 v66, v68, v69\n"
"v_cvt_pk_bf16_f32 v67, v70, v71\n"
"v_xor_b32 v217, 0x80000000, v192\n"
"v_accvgpr_write_b32 a32, v64\n"
"s_add_u32 m0, s70, 57344\n"
"v_accvgpr_write_b32 a33, v65\n"
"v_xor_b32 v218, 0x80000000, v192\n"
"buffer_load_dwordx4 v223, s[44:47], s60 offen offset:0 nt lds\n"
"v_accvgpr_write_b32 a34, v66\n"
"v_accvgpr_write_b32 a35, v67\n"
"v_xor_b32 v219, 0x80000000, v192\n"
"v_pk_mul_f32 v[72:73], v[72:73], s[98:99] op_sel_hi:[1,0]\n"
"v_pk_mul_f32 v[74:75], v[74:75], s[98:99] op_sel_hi:[1,0]\n"
"v_pk_mul_f32 v[76:77], v[76:77], s[98:99] op_sel_hi:[1,0]\n"
"v_pk_mul_f32 v[78:79], v[78:79], s[98:99] op_sel_hi:[1,0]\n"
"v_cvt_pk_bf16_f32 v72, v72, v73\n"
"v_cvt_pk_bf16_f32 v73, v74, v75\n"
"v_cvt_pk_bf16_f32 v74, v76, v77\n"
"v_cvt_pk_bf16_f32 v75, v78, v79\n"
"v_accvgpr_write_b32 a36, v72\n"
"v_accvgpr_write_b32 a37, v73\n"
"v_accvgpr_write_b32 a38, v74\n"
"v_accvgpr_write_b32 a39, v75\n"
"v_pk_mul_f32 v[80:81], v[80:81], s[98:99] op_sel_hi:[1,0]\n"
"v_pk_mul_f32 v[82:83], v[82:83], s[98:99] op_sel_hi:[1,0]\n"
"v_pk_mul_f32 v[84:85], v[84:85], s[98:99] op_sel_hi:[1,0]\n"
"v_pk_mul_f32 v[86:87], v[86:87], s[98:99] op_sel_hi:[1,0]\n"
"v_cvt_pk_bf16_f32 v80, v80, v81\n"
"v_cvt_pk_bf16_f32 v81, v82, v83\n"
"buffer_load_dwordx4 v223, s[44:47], s61 offen offset:1024 nt lds\n"
"v_cvt_pk_bf16_f32 v82, v84, v85\n"
"v_cvt_pk_bf16_f32 v83, v86, v87\n"
"v_accvgpr_write_b32 a40, v80\n"
"v_accvgpr_write_b32 a41, v81\n"
"v_accvgpr_write_b32 a42, v82\n"
"v_accvgpr_write_b32 a43, v83\n"
"v_pk_mul_f32 v[88:89], v[88:89], s[98:99] op_sel_hi:[1,0]\n"
"v_pk_mul_f32 v[90:91], v[90:91], s[98:99] op_sel_hi:[1,0]\n"
"v_pk_mul_f32 v[92:93], v[92:93], s[98:99] op_sel_hi:[1,0]\n"
"v_pk_mul_f32 v[94:95], v[94:95], s[98:99] op_sel_hi:[1,0]\n"
"v_cvt_pk_bf16_f32 v88, v88, v89\n"
"v_cvt_pk_bf16_f32 v89, v90, v91\n"
"v_cvt_pk_bf16_f32 v90, v92, v93\n"
"v_cvt_pk_bf16_f32 v91, v94, v95\n"
"v_accvgpr_write_b32 a44, v88\n"
"v_accvgpr_write_b32 a45, v89\n"
"v_accvgpr_write_b32 a46, v90\n"
"v_accvgpr_write_b32 a47, v91\n"
"v_pk_mul_f32 v[96:97], v[96:97], s[98:99] op_sel_hi:[1,0]\n"
"v_pk_mul_f32 v[98:99], v[98:99], s[98:99] op_sel_hi:[1,0]\n"
"buffer_load_dwordx4 v223, s[44:47], s62 offen offset:2048 nt lds\n"
"v_pk_mul_f32 v[100:101], v[100:101], s[98:99] op_sel_hi:[1,0]\n"
"v_pk_mul_f32 v[102:103], v[102:103], s[98:99] op_sel_hi:[1,0]\n"
"v_cvt_pk_bf16_f32 v96, v96, v97\n"
"v_cvt_pk_bf16_f32 v97, v98, v99\n"
"v_cvt_pk_bf16_f32 v98, v100, v101\n"
"v_cvt_pk_bf16_f32 v99, v102, v103\n"
"v_accvgpr_write_b32 a48, v96\n"
"v_accvgpr_write_b32 a49, v97\n"
"v_accvgpr_write_b32 a50, v98\n"
"v_accvgpr_write_b32 a51, v99\n"
"v_pk_mul_f32 v[104:105], v[104:105], s[98:99] op_sel_hi:[1,0]\n"
"v_pk_mul_f32 v[106:107], v[106:107], s[98:99] op_sel_hi:[1,0]\n"
"v_pk_mul_f32 v[108:109], v[108:109], s[98:99] op_sel_hi:[1,0]\n"
"v_pk_mul_f32 v[110:111], v[110:111], s[98:99] op_sel_hi:[1,0]\n"
"v_cvt_pk_bf16_f32 v104, v104, v105\n"
"v_cvt_pk_bf16_f32 v105, v106, v107\n"
"v_cvt_pk_bf16_f32 v106, v108, v109\n"
"v_cvt_pk_bf16_f32 v107, v110, v111\n"
"v_accvgpr_write_b32 a52, v104\n"
"v_accvgpr_write_b32 a53, v105\n"
"buffer_load_dwordx4 v223, s[44:47], 0 offen offset:3072 nt lds\n"
"v_accvgpr_write_b32 a54, v106\n"
"v_accvgpr_write_b32 a55, v107\n"
"v_pk_mul_f32 v[112:113], v[112:113], s[98:99] op_sel_hi:[1,0]\n"
"v_pk_mul_f32 v[114:115], v[114:115], s[98:99] op_sel_hi:[1,0]\n"
"v_pk_mul_f32 v[116:117], v[116:117], s[98:99] op_sel_hi:[1,0]\n"
"v_pk_mul_f32 v[118:119], v[118:119], s[98:99] op_sel_hi:[1,0]\n"
"v_cvt_pk_bf16_f32 v112, v112, v113\n"
"v_cvt_pk_bf16_f32 v113, v114, v115\n"
"v_cvt_pk_bf16_f32 v114, v116, v117\n"
"v_cvt_pk_bf16_f32 v115, v118, v119\n"
"v_accvgpr_write_b32 a56, v112\n"
"v_accvgpr_write_b32 a57, v113\n"
"v_accvgpr_write_b32 a58, v114\n"
"v_accvgpr_write_b32 a59, v115\n"
"v_pk_mul_f32 v[120:121], v[120:121], s[98:99] op_sel_hi:[1,0]\n"
"v_pk_mul_f32 v[122:123], v[122:123], s[98:99] op_sel_hi:[1,0]\n"
"v_pk_mul_f32 v[124:125], v[124:125], s[98:99] op_sel_hi:[1,0]\n"
"v_pk_mul_f32 v[126:127], v[126:127], s[98:99] op_sel_hi:[1,0]\n"
"v_cvt_pk_bf16_f32 v120, v120, v121\n"
"v_cvt_pk_bf16_f32 v121, v122, v123\n"
"v_add_u32 v223, 0x2000, v223\n"
"v_cvt_pk_bf16_f32 v122, v124, v125\n"
"v_cvt_pk_bf16_f32 v123, v126, v127\n"
"v_accvgpr_write_b32 a60, v120\n"
"v_accvgpr_write_b32 a61, v121\n"
"v_accvgpr_write_b32 a62, v122\n"
"v_accvgpr_write_b32 a63, v123\n"
"v_pk_mul_f32 v[128:129], v[128:129], s[98:99] op_sel_hi:[1,0]\n"
"v_pk_mul_f32 v[130:131], v[130:131], s[98:99] op_sel_hi:[1,0]\n"
"v_pk_mul_f32 v[132:133], v[132:133], s[98:99] op_sel_hi:[1,0]\n"
"v_pk_mul_f32 v[134:135], v[134:135], s[98:99] op_sel_hi:[1,0]\n"
"v_cvt_pk_bf16_f32 v128, v128, v129\n"
"v_cvt_pk_bf16_f32 v129, v130, v131\n"
"v_cvt_pk_bf16_f32 v130, v132, v133\n"
"v_cvt_pk_bf16_f32 v131, v134, v135\n"
"v_accvgpr_write_b32 a64, v128\n"
"v_accvgpr_write_b32 a65, v129\n"
"v_accvgpr_write_b32 a66, v130\n"
"v_accvgpr_write_b32 a67, v131\n"
"v_pk_mul_f32 v[136:137], v[136:137], s[98:99] op_sel_hi:[1,0]\n"
"v_pk_mul_f32 v[138:139], v[138:139], s[98:99] op_sel_hi:[1,0]\n"
"v_pk_mul_f32 v[140:141], v[140:141], s[98:99] op_sel_hi:[1,0]\n"
"v_pk_mul_f32 v[142:143], v[142:143], s[98:99] op_sel_hi:[1,0]\n"
"v_cvt_pk_bf16_f32 v136, v136, v137\n"
"v_cvt_pk_bf16_f32 v137, v138, v139\n"
"v_cvt_pk_bf16_f32 v138, v140, v141\n"
"v_cvt_pk_bf16_f32 v139, v142, v143\n"
"v_accvgpr_write_b32 a68, v136\n"
"v_accvgpr_write_b32 a69, v137\n"
"v_accvgpr_write_b32 a70, v138\n"
"v_accvgpr_write_b32 a71, v139\n"
"v_pk_mul_f32 v[144:145], v[144:145], s[98:99] op_sel_hi:[1,0]\n"
"v_pk_mul_f32 v[146:147], v[146:147], s[98:99] op_sel_hi:[1,0]\n"
"v_pk_mul_f32 v[148:149], v[148:149], s[98:99] op_sel_hi:[1,0]\n"
"v_pk_mul_f32 v[150:151], v[150:151], s[98:99] op_sel_hi:[1,0]\n"
"v_cvt_pk_bf16_f32 v144, v144, v145\n"
"v_cvt_pk_bf16_f32 v145, v146, v147\n"
"v_cvt_pk_bf16_f32 v146, v148, v149\n"
"v_cvt_pk_bf16_f32 v147, v150, v151\n"
"v_accvgpr_write_b32 a72, v144\n"
"v_accvgpr_write_b32 a73, v145\n"
"v_accvgpr_write_b32 a74, v146\n"
"v_accvgpr_write_b32 a75, v147\n"
"v_pk_mul_f32 v[152:153], v[152:153], s[98:99] op_sel_hi:[1,0]\n"
"v_pk_mul_f32 v[154:155], v[154:155], s[98:99] op_sel_hi:[1,0]\n"
"v_pk_mul_f32 v[156:157], v[156:157], s[98:99] op_sel_hi:[1,0]\n"
"v_pk_mul_f32 v[158:159], v[158:159], s[98:99] op_sel_hi:[1,0]\n"
"v_cvt_pk_bf16_f32 v152, v152, v153\n"
"v_cvt_pk_bf16_f32 v153, v154, v155\n"
"v_cvt_pk_bf16_f32 v154, v156, v157\n"
"v_cvt_pk_bf16_f32 v155, v158, v159\n"
"v_accvgpr_write_b32 a76, v152\n"
"v_accvgpr_write_b32 a77, v153\n"
"v_accvgpr_write_b32 a78, v154\n"
"v_accvgpr_write_b32 a79, v155\n"
"v_pk_mul_f32 v[160:161], v[160:161], s[98:99] op_sel_hi:[1,0]\n"
"v_pk_mul_f32 v[162:163], v[162:163], s[98:99] op_sel_hi:[1,0]\n"
"v_pk_mul_f32 v[164:165], v[164:165], s[98:99] op_sel_hi:[1,0]\n"
"v_pk_mul_f32 v[166:167], v[166:167], s[98:99] op_sel_hi:[1,0]\n"
"v_cvt_pk_bf16_f32 v160, v160, v161\n"
"v_cvt_pk_bf16_f32 v161, v162, v163\n"
"v_cvt_pk_bf16_f32 v162, v164, v165\n"
"v_cvt_pk_bf16_f32 v163, v166, v167\n"
"v_accvgpr_write_b32 a80, v160\n"
"v_accvgpr_write_b32 a81, v161\n"
"v_accvgpr_write_b32 a82, v162\n"
"v_accvgpr_write_b32 a83, v163\n"
"v_pk_mul_f32 v[168:169], v[168:169], s[98:99] op_sel_hi:[1,0]\n"
"v_pk_mul_f32 v[170:171], v[170:171], s[98:99] op_sel_hi:[1,0]\n"
"v_pk_mul_f32 v[172:173], v[172:173], s[98:99] op_sel_hi:[1,0]\n"
"v_pk_mul_f32 v[174:175], v[174:175], s[98:99] op_sel_hi:[1,0]\n"
"v_cvt_pk_bf16_f32 v168, v168, v169\n"
"v_cvt_pk_bf16_f32 v169, v170, v171\n"
"v_cvt_pk_bf16_f32 v170, v172, v173\n"
"v_cvt_pk_bf16_f32 v171, v174, v175\n"
"v_accvgpr_write_b32 a84, v168\n"
"v_accvgpr_write_b32 a85, v169\n"
"v_accvgpr_write_b32 a86, v170\n"
"v_accvgpr_write_b32 a87, v171\n"
"v_pk_mul_f32 v[176:177], v[176:177], s[98:99] op_sel_hi:[1,0]\n"
"v_pk_mul_f32 v[178:179], v[178:179], s[98:99] op_sel_hi:[1,0]\n"
"v_pk_mul_f32 v[180:181], v[180:181], s[98:99] op_sel_hi:[1,0]\n"
"v_pk_mul_f32 v[182:183], v[182:183], s[98:99] op_sel_hi:[1,0]\n"
"v_cvt_pk_bf16_f32 v176, v176, v177\n"
"v_cvt_pk_bf16_f32 v177, v178, v179\n"
"v_cvt_pk_bf16_f32 v178, v180, v181\n"
"v_cvt_pk_bf16_f32 v179, v182, v183\n"
"v_accvgpr_write_b32 a88, v176\n"
"v_accvgpr_write_b32 a89, v177\n"
"v_accvgpr_write_b32 a90, v178\n"
"v_accvgpr_write_b32 a91, v179\n"
"v_pk_mul_f32 v[184:185], v[184:185], s[98:99] op_sel_hi:[1,0]\n"
"v_pk_mul_f32 v[186:187], v[186:187], s[98:99] op_sel_hi:[1,0]\n"
"v_pk_mul_f32 v[188:189], v[188:189], s[98:99] op_sel_hi:[1,0]\n"
"v_pk_mul_f32 v[190:191], v[190:191], s[98:99] op_sel_hi:[1,0]\n"
"v_cvt_pk_bf16_f32 v184, v184, v185\n"
"v_cvt_pk_bf16_f32 v185, v186, v187\n"
"v_cvt_pk_bf16_f32 v186, v188, v189\n"
"v_cvt_pk_bf16_f32 v187, v190, v191\n"
"v_accvgpr_write_b32 a92, v184\n"
"v_accvgpr_write_b32 a93, v185\n"
"v_accvgpr_write_b32 a94, v186\n"
"v_accvgpr_write_b32 a95, v187\n"
"s_mov_b32 s75, 6\n"
"s_waitcnt vmcnt(16)\n"
"s_barrier\n"
"v_mfma_f32_16x16x32_bf16 v[192:195], a[96:99], a[0:3], v[216:219]\n"
"ds_read_b128 a[144:147], v220 offset:20480\n"
"v_mfma_f32_16x16x32_bf16 v[192:195], a[100:103], a[4:7], v[192:195]\n"
"ds_read_b128 a[148:151], v220 offset:21504\n"
"v_mfma_f32_16x16x32_bf16 v[192:195], a[104:107], a[8:11], v[192:195]\n"
"ds_read_b128 a[152:155], v220 offset:22528\n"
"v_mfma_f32_16x16x32_bf16 v[192:195], a[108:111], a[12:15], v[192:195]\n"
"ds_read_b128 a[156:159], v220 offset:23552\n"
"v_mfma_f32_16x16x32_bf16 v[196:199], a[112:115], a[0:3], v[216:219]\n"
"v_mfma_f32_16x16x32_bf16 v[196:199], a[116:119], a[4:7], v[196:199]\n"
"v_mfma_f32_16x16x32_bf16 v[196:199], a[120:123], a[8:11], v[196:199]\n"
"v_mfma_f32_16x16x32_bf16 v[196:199], a[124:127], a[12:15], v[196:199]\n"
"s_nop 4\n"
"v_exp_f32 v192, v192\n"
"v_exp_f32 v193, v193\n"
"v_exp_f32 v194, v194\n"
"v_exp_f32 v195, v195\n"
"v_exp_f32 v196, v196\n"
"v_exp_f32 v197, v197\n"
"v_exp_f32 v198, v198\n"
"v_mfma_f32_16x16x32_bf16 v[200:203], a[96:99], a[16:19], v[216:219]\n"
"v_exp_f32 v199, v199\n"
"v_mfma_f32_16x16x32_bf16 v[200:203], a[100:103], a[20:23], v[200:203]\n"
"v_cvt_pk_bf16_f32 v208, v192, v193\n"
"v_cvt_pk_bf16_f32 v209, v194, v195\n"
"v_mfma_f32_16x16x32_bf16 v[200:203], a[104:107], a[24:27], v[200:203]\n"
"v_cvt_pk_bf16_f32 v210, v196, v197\n"
"v_cvt_pk_bf16_f32 v211, v198, v199\n"
"v_mfma_f32_16x16x32_bf16 v[200:203], a[108:111], a[28:31], v[200:203]\n"
"ds_read_b64_tr_b16 a[192:193], v221 offset:16384\n"
"ds_read_b64_tr_b16 a[194:195], v221 offset:20480\n"
"v_mfma_f32_16x16x32_bf16 v[204:207], a[112:115], a[16:19], v[216:219]\n"
"ds_read_b64_tr_b16 a[196:197], v222 offset:16384\n"
"ds_read_b64_tr_b16 a[198:199], v222 offset:20480\n"
"v_mfma_f32_16x16x32_bf16 v[204:207], a[116:119], a[20:23], v[204:207]\n"
"ds_read_b64_tr_b16 a[200:201], v221 offset:17408\n"
"ds_read_b64_tr_b16 a[202:203], v221 offset:21504\n"
"v_mfma_f32_16x16x32_bf16 v[204:207], a[120:123], a[24:27], v[204:207]\n"
"ds_read_b64_tr_b16 a[204:205], v222 offset:17408\n"
"ds_read_b64_tr_b16 a[206:207], v222 offset:21504\n"
"v_mfma_f32_16x16x32_bf16 v[204:207], a[124:127], a[28:31], v[204:207]\n"
"v_mfma_f32_16x16x32_bf16 v[0:3], a[160:163], v[208:211], 0\n"
"v_mfma_f32_16x16x32_bf16 v[4:7], a[164:167], v[208:211], 0\n"
"v_exp_f32 v200, v200\n"
"v_mfma_f32_16x16x32_bf16 v[8:11], a[168:171], v[208:211], 0\n"
"v_exp_f32 v201, v201\n"
"v_mfma_f32_16x16x32_bf16 v[12:15], a[172:175], v[208:211], 0\n"
"v_exp_f32 v202, v202\n"
"v_mfma_f32_16x16x32_bf16 v[16:19], a[176:179], v[208:211], 0\n"
"v_exp_f32 v203, v203\n"
"v_mfma_f32_16x16x32_bf16 v[20:23], a[180:183], v[208:211], 0\n"
"v_exp_f32 v204, v204\n"
"v_mfma_f32_16x16x32_bf16 v[24:27], a[184:187], v[208:211], 0\n"
"v_exp_f32 v205, v205\n"
"v_mfma_f32_16x16x32_bf16 v[28:31], a[188:191], v[208:211], 0\n"
"v_exp_f32 v206, v206\n"
"v_mfma_f32_16x16x32_bf16 v[192:195], a[96:99], a[128:131], v[216:219]\n"
"v_exp_f32 v207, v207\n"
"v_mfma_f32_16x16x32_bf16 v[192:195], a[100:103], a[132:135], v[192:195]\n"
"v_cvt_pk_bf16_f32 v212, v200, v201\n"
"v_cvt_pk_bf16_f32 v213, v202, v203\n"
"v_mfma_f32_16x16x32_bf16 v[192:195], a[104:107], a[136:139], v[192:195]\n"
"v_cvt_pk_bf16_f32 v214, v204, v205\n"
"v_cvt_pk_bf16_f32 v215, v206, v207\n"
"v_mfma_f32_16x16x32_bf16 v[192:195], a[108:111], a[140:143], v[192:195]\n"
"v_mfma_f32_16x16x32_bf16 v[196:199], a[112:115], a[128:131], v[216:219]\n"
"v_mfma_f32_16x16x32_bf16 v[196:199], a[116:119], a[132:135], v[196:199]\n"
"v_mfma_f32_16x16x32_bf16 v[196:199], a[120:123], a[136:139], v[196:199]\n"
"v_mfma_f32_16x16x32_bf16 v[196:199], a[124:127], a[140:143], v[196:199]\n"
"ds_read_b128 a[128:131], v220 offset:16384\n"
"v_smfmac_f32_16x16x64_bf16 v[224:227], v[240:243], v[208:215], v244\n"
"v_mfma_f32_16x16x32_bf16 v[32:35], a[160:163], v[212:215], 0\n"
"v_exp_f32 v192, v192\n"
"v_mfma_f32_16x16x32_bf16 v[36:39], a[164:167], v[212:215], 0\n"
"v_exp_f32 v193, v193\n"
"v_mfma_f32_16x16x32_bf16 v[40:43], a[168:171], v[212:215], 0\n"
"v_exp_f32 v194, v194\n"
"v_mfma_f32_16x16x32_bf16 v[44:47], a[172:175], v[212:215], 0\n"
"v_exp_f32 v195, v195\n"
"v_mfma_f32_16x16x32_bf16 v[48:51], a[176:179], v[212:215], 0\n"
"v_exp_f32 v196, v196\n"
"v_mfma_f32_16x16x32_bf16 v[52:55], a[180:183], v[212:215], 0\n"
"v_exp_f32 v197, v197\n"
"v_mfma_f32_16x16x32_bf16 v[56:59], a[184:187], v[212:215], 0\n"
"v_exp_f32 v198, v198\n"
"v_mfma_f32_16x16x32_bf16 v[60:63], a[188:191], v[212:215], 0\n"
"v_exp_f32 v199, v199\n"
"v_mfma_f32_16x16x32_bf16 v[200:203], a[96:99], a[32:35], v[216:219]\n"
"v_cvt_pk_bf16_f32 v212, v192, v193\n"
"v_cvt_pk_bf16_f32 v213, v194, v195\n"
"v_mfma_f32_16x16x32_bf16 v[200:203], a[100:103], a[36:39], v[200:203]\n"
"v_cvt_pk_bf16_f32 v214, v196, v197\n"
"v_cvt_pk_bf16_f32 v215, v198, v199\n"
"v_mfma_f32_16x16x32_bf16 v[200:203], a[104:107], a[40:43], v[200:203]\n"
"ds_read_b128 a[132:135], v220 offset:17408\n"
"v_mfma_f32_16x16x32_bf16 v[200:203], a[108:111], a[44:47], v[200:203]\n"
"ds_read_b128 a[136:139], v220 offset:18432\n"
"v_mfma_f32_16x16x32_bf16 v[204:207], a[112:115], a[32:35], v[216:219]\n"
"ds_read_b128 a[140:143], v220 offset:19456\n"
"ds_read_b64_tr_b16 a[208:209], v221 offset:18432\n"
"v_mfma_f32_16x16x32_bf16 v[204:207], a[116:119], a[36:39], v[204:207]\n"
"ds_read_b64_tr_b16 a[210:211], v221 offset:22528\n"
"ds_read_b64_tr_b16 a[212:213], v222 offset:18432\n"
"v_mfma_f32_16x16x32_bf16 v[204:207], a[120:123], a[40:43], v[204:207]\n"
"ds_read_b64_tr_b16 a[214:215], v222 offset:22528\n"
"ds_read_b64_tr_b16 a[216:217], v221 offset:19456\n"
"v_mfma_f32_16x16x32_bf16 v[204:207], a[124:127], a[44:47], v[204:207]\n"
"ds_read_b64_tr_b16 a[218:219], v221 offset:23552\n"
"ds_read_b64_tr_b16 a[220:221], v222 offset:19456\n"
"v_smfmac_f32_16x16x64_bf16 v[236:239], v[240:243], v[208:215], v244\n"
"ds_read_b64_tr_b16 a[222:223], v222 offset:23552\n"
"v_mfma_f32_16x16x32_bf16 a[224:227], a[160:163], v[212:215], a[224:227]\n"
"v_exp_f32 v200, v200\n"
"v_mfma_f32_16x16x32_bf16 a[228:231], a[164:167], v[212:215], a[228:231]\n"
"v_exp_f32 v201, v201\n"
"v_mfma_f32_16x16x32_bf16 a[232:235], a[168:171], v[212:215], a[232:235]\n"
"v_exp_f32 v202, v202\n"
"v_mfma_f32_16x16x32_bf16 a[236:239], a[172:175], v[212:215], a[236:239]\n"
"v_exp_f32 v203, v203\n"
"v_mfma_f32_16x16x32_bf16 a[240:243], a[176:179], v[212:215], a[240:243]\n"
"v_exp_f32 v204, v204\n"
"v_mfma_f32_16x16x32_bf16 a[244:247], a[180:183], v[212:215], a[244:247]\n"
"v_exp_f32 v205, v205\n"
"v_mfma_f32_16x16x32_bf16 a[248:251], a[184:187], v[212:215], a[248:251]\n"
"v_exp_f32 v206, v206\n"
"v_mfma_f32_16x16x32_bf16 a[252:255], a[188:191], v[212:215], a[252:255]\n"
"v_exp_f32 v207, v207\n"
"v_mfma_f32_16x16x32_bf16 v[192:195], a[96:99], a[48:51], v[216:219]\n"
"v_cvt_pk_bf16_f32 v208, v200, v201\n"
"v_cvt_pk_bf16_f32 v209, v202, v203\n"
"v_mfma_f32_16x16x32_bf16 v[192:195], a[100:103], a[52:55], v[192:195]\n"
"v_cvt_pk_bf16_f32 v210, v204, v205\n"
"v_cvt_pk_bf16_f32 v211, v206, v207\n"
"v_mfma_f32_16x16x32_bf16 v[192:195], a[104:107], a[56:59], v[192:195]\n"
"v_mfma_f32_16x16x32_bf16 v[192:195], a[108:111], a[60:63], v[192:195]\n"
"v_mfma_f32_16x16x32_bf16 v[196:199], a[112:115], a[48:51], v[216:219]\n"
"v_mfma_f32_16x16x32_bf16 v[196:199], a[116:119], a[52:55], v[196:199]\n"
"v_mfma_f32_16x16x32_bf16 v[196:199], a[120:123], a[56:59], v[196:199]\n"
"v_mfma_f32_16x16x32_bf16 v[196:199], a[124:127], a[60:63], v[196:199]\n"
"v_mfma_f32_16x16x32_bf16 v[64:67], a[160:163], v[208:211], 0\n"
"v_mfma_f32_16x16x32_bf16 v[68:71], a[164:167], v[208:211], 0\n"
"v_exp_f32 v192, v192\n"
"v_mfma_f32_16x16x32_bf16 v[72:75], a[168:171], v[208:211], 0\n"
"v_exp_f32 v193, v193\n"
"v_mfma_f32_16x16x32_bf16 v[76:79], a[172:175], v[208:211], 0\n"
"v_exp_f32 v194, v194\n"
"v_mfma_f32_16x16x32_bf16 v[80:83], a[176:179], v[208:211], 0\n"
"v_exp_f32 v195, v195\n"
"v_mfma_f32_16x16x32_bf16 v[84:87], a[180:183], v[208:211], 0\n"
"v_exp_f32 v196, v196\n"
"v_mfma_f32_16x16x32_bf16 v[88:91], a[184:187], v[208:211], 0\n"
"v_exp_f32 v197, v197\n"
"v_mfma_f32_16x16x32_bf16 v[92:95], a[188:191], v[208:211], 0\n"
"v_exp_f32 v198, v198\n"
"v_mfma_f32_16x16x32_bf16 v[200:203], a[96:99], a[64:67], v[216:219]\n"
"v_exp_f32 v199, v199\n"
"v_mfma_f32_16x16x32_bf16 v[200:203], a[100:103], a[68:71], v[200:203]\n"
"v_cvt_pk_bf16_f32 v212, v192, v193\n"
"v_cvt_pk_bf16_f32 v213, v194, v195\n"
"v_mfma_f32_16x16x32_bf16 v[200:203], a[104:107], a[72:75], v[200:203]\n"
"v_cvt_pk_bf16_f32 v214, v196, v197\n"
"v_cvt_pk_bf16_f32 v215, v198, v199\n"
"v_mfma_f32_16x16x32_bf16 v[200:203], a[108:111], a[76:79], v[200:203]\n"
"v_mfma_f32_16x16x32_bf16 v[204:207], a[112:115], a[64:67], v[216:219]\n"
"v_mfma_f32_16x16x32_bf16 v[204:207], a[116:119], a[68:71], v[204:207]\n"
"v_mfma_f32_16x16x32_bf16 v[204:207], a[120:123], a[72:75], v[204:207]\n"
"v_mfma_f32_16x16x32_bf16 v[204:207], a[124:127], a[76:79], v[204:207]\n"
"v_smfmac_f32_16x16x64_bf16 v[228:231], v[240:243], v[208:215], v244\n"
"v_mfma_f32_16x16x32_bf16 v[96:99], a[160:163], v[212:215], 0\n"
"v_exp_f32 v200, v200\n"
"v_mfma_f32_16x16x32_bf16 v[100:103], a[164:167], v[212:215], 0\n"
"v_exp_f32 v201, v201\n"
"v_mfma_f32_16x16x32_bf16 v[104:107], a[168:171], v[212:215], 0\n"
"v_exp_f32 v202, v202\n"
"v_mfma_f32_16x16x32_bf16 v[108:111], a[172:175], v[212:215], 0\n"
"v_exp_f32 v203, v203\n"
"v_mfma_f32_16x16x32_bf16 v[112:115], a[176:179], v[212:215], 0\n"
"v_exp_f32 v204, v204\n"
"v_mfma_f32_16x16x32_bf16 v[116:119], a[180:183], v[212:215], 0\n"
"v_exp_f32 v205, v205\n"
"v_mfma_f32_16x16x32_bf16 v[120:123], a[184:187], v[212:215], 0\n"
"v_exp_f32 v206, v206\n"
"v_mfma_f32_16x16x32_bf16 v[124:127], a[188:191], v[212:215], 0\n"
"v_exp_f32 v207, v207\n"
"v_mfma_f32_16x16x32_bf16 v[192:195], a[96:99], a[80:83], v[216:219]\n"
"v_cvt_pk_bf16_f32 v208, v200, v201\n"
"v_cvt_pk_bf16_f32 v209, v202, v203\n"
"v_mfma_f32_16x16x32_bf16 v[192:195], a[100:103], a[84:87], v[192:195]\n"
"v_cvt_pk_bf16_f32 v210, v204, v205\n"
"v_cvt_pk_bf16_f32 v211, v206, v207\n"
"v_mfma_f32_16x16x32_bf16 v[192:195], a[104:107], a[88:91], v[192:195]\n"
"v_mfma_f32_16x16x32_bf16 v[192:195], a[108:111], a[92:95], v[192:195]\n"
"v_mfma_f32_16x16x32_bf16 v[196:199], a[112:115], a[80:83], v[216:219]\n"
"v_mfma_f32_16x16x32_bf16 v[196:199], a[116:119], a[84:87], v[196:199]\n"
"v_mfma_f32_16x16x32_bf16 v[196:199], a[120:123], a[88:91], v[196:199]\n"
"s_waitcnt vmcnt(8) lgkmcnt(0)\n"
"s_barrier\n"
"s_sleep 5\n"
"v_mfma_f32_16x16x32_bf16 v[196:199], a[124:127], a[92:95], v[196:199]\n"
"s_add_u32 m0, s70, 0\n"
"v_mfma_f32_16x16x32_bf16 v[128:131], a[160:163], v[208:211], 0\n"
"buffer_load_dwordx4 v223, s[40:43], s60 offen offset:0 nt lds\n"
"v_mfma_f32_16x16x32_bf16 v[132:135], a[164:167], v[208:211], 0\n"
"v_exp_f32 v192, v192\n"
"v_mfma_f32_16x16x32_bf16 v[136:139], a[168:171], v[208:211], 0\n"
"v_exp_f32 v193, v193\n"
"v_mfma_f32_16x16x32_bf16 v[140:143], a[172:175], v[208:211], 0\n"
"v_exp_f32 v194, v194\n"
"v_mfma_f32_16x16x32_bf16 v[144:147], a[176:179], v[208:211], 0\n"
"v_exp_f32 v195, v195\n"
"v_mfma_f32_16x16x32_bf16 v[148:151], a[180:183], v[208:211], 0\n"
"v_exp_f32 v196, v196\n"
"v_mfma_f32_16x16x32_bf16 v[152:155], a[184:187], v[208:211], 0\n"
"v_exp_f32 v197, v197\n"
"v_mfma_f32_16x16x32_bf16 v[156:159], a[188:191], v[208:211], 0\n"
"v_exp_f32 v198, v198\n"
"v_mfma_f32_16x16x32_bf16 v[200:203], a[128:131], a[0:3], v[216:219]\n"
"v_exp_f32 v199, v199\n"
"v_mfma_f32_16x16x32_bf16 v[200:203], a[132:135], a[4:7], v[200:203]\n"
"v_cvt_pk_bf16_f32 v212, v192, v193\n"
"v_cvt_pk_bf16_f32 v213, v194, v195\n"
"v_mfma_f32_16x16x32_bf16 v[200:203], a[136:139], a[8:11], v[200:203]\n"
"v_cvt_pk_bf16_f32 v214, v196, v197\n"
"v_cvt_pk_bf16_f32 v215, v198, v199\n"
"v_mfma_f32_16x16x32_bf16 v[200:203], a[140:143], a[12:15], v[200:203]\n"
"buffer_load_dwordx4 v223, s[40:43], s61 offen offset:1024 nt lds\n"
"v_mfma_f32_16x16x32_bf16 v[204:207], a[144:147], a[0:3], v[216:219]\n"
"ds_read_b128 a[112:115], v220 offset:36864\n"
"v_mfma_f32_16x16x32_bf16 v[204:207], a[148:151], a[4:7], v[204:207]\n"
"ds_read_b128 a[116:119], v220 offset:37888\n"
"v_mfma_f32_16x16x32_bf16 v[204:207], a[152:155], a[8:11], v[204:207]\n"
"ds_read_b128 a[120:123], v220 offset:38912\n"
"v_mfma_f32_16x16x32_bf16 v[204:207], a[156:159], a[12:15], v[204:207]\n"
"ds_read_b128 a[124:127], v220 offset:39936\n"
"v_smfmac_f32_16x16x64_bf16 v[232:235], v[240:243], v[208:215], v244\n"
"v_mfma_f32_16x16x32_bf16 v[160:163], a[160:163], v[212:215], 0\n"
"v_exp_f32 v200, v200\n"
"v_mfma_f32_16x16x32_bf16 v[164:167], a[164:167], v[212:215], 0\n"
"v_exp_f32 v201, v201\n"
"v_mfma_f32_16x16x32_bf16 v[168:171], a[168:171], v[212:215], 0\n"
"v_exp_f32 v202, v202\n"
"v_mfma_f32_16x16x32_bf16 v[172:175], a[172:175], v[212:215], 0\n"
"v_exp_f32 v203, v203\n"
"v_mfma_f32_16x16x32_bf16 v[176:179], a[176:179], v[212:215], 0\n"
"v_exp_f32 v204, v204\n"
"v_mfma_f32_16x16x32_bf16 v[180:183], a[180:183], v[212:215], 0\n"
"v_exp_f32 v205, v205\n"
"v_mfma_f32_16x16x32_bf16 v[184:187], a[184:187], v[212:215], 0\n"
"v_exp_f32 v206, v206\n"
"v_mfma_f32_16x16x32_bf16 v[188:191], a[188:191], v[212:215], 0\n"
"v_exp_f32 v207, v207\n"
"v_mfma_f32_16x16x32_bf16 v[192:195], a[128:131], a[16:19], v[216:219]\n"
"v_cvt_pk_bf16_f32 v208, v200, v201\n"
"v_cvt_pk_bf16_f32 v209, v202, v203\n"
"v_mfma_f32_16x16x32_bf16 v[192:195], a[132:135], a[20:23], v[192:195]\n"
"v_cvt_pk_bf16_f32 v210, v204, v205\n"
"v_cvt_pk_bf16_f32 v211, v206, v207\n"
"v_mfma_f32_16x16x32_bf16 v[192:195], a[136:139], a[24:27], v[192:195]\n"
"buffer_load_dwordx4 v223, s[40:43], s62 offen offset:2048 nt lds\n"
"v_mfma_f32_16x16x32_bf16 v[192:195], a[140:143], a[28:31], v[192:195]\n"
"ds_read_b128 a[96:99], v220 offset:32768\n"
"v_mfma_f32_16x16x32_bf16 v[196:199], a[144:147], a[16:19], v[216:219]\n"
"ds_read_b128 a[100:103], v220 offset:33792\n"
"v_mfma_f32_16x16x32_bf16 v[196:199], a[148:151], a[20:23], v[196:199]\n"
"ds_read_b128 a[104:107], v220 offset:34816\n"
"v_mfma_f32_16x16x32_bf16 v[196:199], a[152:155], a[24:27], v[196:199]\n"
"ds_read_b128 a[108:111], v220 offset:35840\n"
"ds_read_b64_tr_b16 a[160:161], v221 offset:32768\n"
"v_mfma_f32_16x16x32_bf16 v[196:199], a[156:159], a[28:31], v[196:199]\n"
"buffer_load_dwordx4 v223, s[40:43], 0 offen offset:3072 nt lds\n"
"v_mfma_f32_16x16x32_bf16 v[0:3], a[192:195], v[208:211], v[0:3]\n"
"ds_read_b64_tr_b16 a[162:163], v221 offset:36864\n"
"ds_read_b64_tr_b16 a[164:165], v222 offset:32768\n"
"v_mfma_f32_16x16x32_bf16 v[4:7], a[196:199], v[208:211], v[4:7]\n"
"v_exp_f32 v192, v192\n"
"v_mfma_f32_16x16x32_bf16 v[8:11], a[200:203], v[208:211], v[8:11]\n"
"v_exp_f32 v193, v193\n"
"v_mfma_f32_16x16x32_bf16 v[12:15], a[204:207], v[208:211], v[12:15]\n"
"v_exp_f32 v194, v194\n"
"v_mfma_f32_16x16x32_bf16 v[16:19], a[208:211], v[208:211], v[16:19]\n"
"v_exp_f32 v195, v195\n"
"v_mfma_f32_16x16x32_bf16 v[20:23], a[212:215], v[208:211], v[20:23]\n"
"v_exp_f32 v196, v196\n"
"v_mfma_f32_16x16x32_bf16 v[24:27], a[216:219], v[208:211], v[24:27]\n"
"v_exp_f32 v197, v197\n"
"v_mfma_f32_16x16x32_bf16 v[28:31], a[220:223], v[208:211], v[28:31]\n"
"v_exp_f32 v198, v198\n"
"v_mfma_f32_16x16x32_bf16 v[200:203], a[128:131], a[32:35], v[216:219]\n"
"v_exp_f32 v199, v199\n"
"v_mfma_f32_16x16x32_bf16 v[200:203], a[132:135], a[36:39], v[200:203]\n"
"v_cvt_pk_bf16_f32 v212, v192, v193\n"
"v_cvt_pk_bf16_f32 v213, v194, v195\n"
"v_mfma_f32_16x16x32_bf16 v[200:203], a[136:139], a[40:43], v[200:203]\n"
"v_cvt_pk_bf16_f32 v214, v196, v197\n"
"v_cvt_pk_bf16_f32 v215, v198, v199\n"
"v_mfma_f32_16x16x32_bf16 v[200:203], a[140:143], a[44:47], v[200:203]\n"
"s_add_u32 m0, s70, 8192\n"
"ds_read_b64_tr_b16 a[166:167], v222 offset:36864\n"
"v_mfma_f32_16x16x32_bf16 v[204:207], a[144:147], a[32:35], v[216:219]\n"
"buffer_load_dwordx4 v223, s[44:47], s60 offen offset:0 nt lds\n"
"v_mfma_f32_16x16x32_bf16 v[204:207], a[148:151], a[36:39], v[204:207]\n"
"ds_read_b64_tr_b16 a[168:169], v221 offset:33792\n"
"ds_read_b64_tr_b16 a[170:171], v221 offset:37888\n"
"v_mfma_f32_16x16x32_bf16 v[204:207], a[152:155], a[40:43], v[204:207]\n"
"ds_read_b64_tr_b16 a[172:173], v222 offset:33792\n"
"ds_read_b64_tr_b16 a[174:175], v222 offset:37888\n"
"v_mfma_f32_16x16x32_bf16 v[204:207], a[156:159], a[44:47], v[204:207]\n"
"ds_read_b64_tr_b16 a[176:177], v221 offset:34816\n"
"ds_read_b64_tr_b16 a[178:179], v221 offset:38912\n"
"v_smfmac_f32_16x16x64_bf16 v[224:227], v[240:243], v[208:215], v244\n"
"ds_read_b64_tr_b16 a[180:181], v222 offset:34816\n"
"v_mfma_f32_16x16x32_bf16 v[32:35], a[192:195], v[212:215], v[32:35]\n"
"v_exp_f32 v200, v200\n"
"v_mfma_f32_16x16x32_bf16 v[36:39], a[196:199], v[212:215], v[36:39]\n"
"v_exp_f32 v201, v201\n"
"v_mfma_f32_16x16x32_bf16 v[40:43], a[200:203], v[212:215], v[40:43]\n"
"v_exp_f32 v202, v202\n"
"v_mfma_f32_16x16x32_bf16 v[44:47], a[204:207], v[212:215], v[44:47]\n"
"v_exp_f32 v203, v203\n"
"v_mfma_f32_16x16x32_bf16 v[48:51], a[208:211], v[212:215], v[48:51]\n"
"v_exp_f32 v204, v204\n"
"v_mfma_f32_16x16x32_bf16 v[52:55], a[212:215], v[212:215], v[52:55]\n"
"v_exp_f32 v205, v205\n"
"v_mfma_f32_16x16x32_bf16 v[56:59], a[216:219], v[212:215], v[56:59]\n"
"v_exp_f32 v206, v206\n"
"v_mfma_f32_16x16x32_bf16 v[60:63], a[220:223], v[212:215], v[60:63]\n"
"v_exp_f32 v207, v207\n"
"v_mfma_f32_16x16x32_bf16 v[192:195], a[128:131], a[48:51], v[216:219]\n"
"v_cvt_pk_bf16_f32 v208, v200, v201\n"
"v_cvt_pk_bf16_f32 v209, v202, v203\n"
"v_mfma_f32_16x16x32_bf16 v[192:195], a[132:135], a[52:55], v[192:195]\n"
"v_cvt_pk_bf16_f32 v210, v204, v205\n"
"v_cvt_pk_bf16_f32 v211, v206, v207\n"
"v_mfma_f32_16x16x32_bf16 v[192:195], a[136:139], a[56:59], v[192:195]\n"
"buffer_load_dwordx4 v223, s[44:47], s61 offen offset:1024 nt lds\n"
"v_mfma_f32_16x16x32_bf16 v[192:195], a[140:143], a[60:63], v[192:195]\n"
"ds_read_b64_tr_b16 a[182:183], v222 offset:38912\n"
"ds_read_b64_tr_b16 a[184:185], v221 offset:35840\n"
"v_mfma_f32_16x16x32_bf16 v[196:199], a[144:147], a[48:51], v[216:219]\n"
"ds_read_b64_tr_b16 a[186:187], v221 offset:39936\n"
"ds_read_b64_tr_b16 a[188:189], v222 offset:35840\n"
"v_mfma_f32_16x16x32_bf16 v[196:199], a[148:151], a[52:55], v[196:199]\n"
"ds_read_b64_tr_b16 a[190:191], v222 offset:39936\n"
"v_mfma_f32_16x16x32_bf16 v[196:199], a[152:155], a[56:59], v[196:199]\n"
"v_mfma_f32_16x16x32_bf16 v[196:199], a[156:159], a[60:63], v[196:199]\n"
"buffer_load_dwordx4 v223, s[44:47], s62 offen offset:2048 nt lds\n"
"v_mfma_f32_16x16x32_bf16 v[64:67], a[192:195], v[208:211], v[64:67]\n"
"v_mfma_f32_16x16x32_bf16 v[68:71], a[196:199], v[208:211], v[68:71]\n"
"v_exp_f32 v192, v192\n"
"v_mfma_f32_16x16x32_bf16 v[72:75], a[200:203], v[208:211], v[72:75]\n"
"v_exp_f32 v193, v193\n"
"v_mfma_f32_16x16x32_bf16 v[76:79], a[204:207], v[208:211], v[76:79]\n"
"v_exp_f32 v194, v194\n"
"v_mfma_f32_16x16x32_bf16 v[80:83], a[208:211], v[208:211], v[80:83]\n"
"v_exp_f32 v195, v195\n"
"v_mfma_f32_16x16x32_bf16 v[84:87], a[212:215], v[208:211], v[84:87]\n"
"v_exp_f32 v196, v196\n"
"v_mfma_f32_16x16x32_bf16 v[88:91], a[216:219], v[208:211], v[88:91]\n"
"v_exp_f32 v197, v197\n"
"v_mfma_f32_16x16x32_bf16 v[92:95], a[220:223], v[208:211], v[92:95]\n"
"v_exp_f32 v198, v198\n"
"v_mfma_f32_16x16x32_bf16 v[200:203], a[128:131], a[64:67], v[216:219]\n"
"v_exp_f32 v199, v199\n"
"v_mfma_f32_16x16x32_bf16 v[200:203], a[132:135], a[68:71], v[200:203]\n"
"v_cvt_pk_bf16_f32 v212, v192, v193\n"
"v_cvt_pk_bf16_f32 v213, v194, v195\n"
"v_mfma_f32_16x16x32_bf16 v[200:203], a[136:139], a[72:75], v[200:203]\n"
"v_cvt_pk_bf16_f32 v214, v196, v197\n"
"v_cvt_pk_bf16_f32 v215, v198, v199\n"
"v_mfma_f32_16x16x32_bf16 v[200:203], a[140:143], a[76:79], v[200:203]\n"
"buffer_load_dwordx4 v223, s[44:47], 0 offen offset:3072 nt lds\n"
"v_mfma_f32_16x16x32_bf16 v[204:207], a[144:147], a[64:67], v[216:219]\n"
"v_mfma_f32_16x16x32_bf16 v[204:207], a[148:151], a[68:71], v[204:207]\n"
"v_mfma_f32_16x16x32_bf16 v[204:207], a[152:155], a[72:75], v[204:207]\n"
"v_mfma_f32_16x16x32_bf16 v[204:207], a[156:159], a[76:79], v[204:207]\n"
"v_smfmac_f32_16x16x64_bf16 v[228:231], v[240:243], v[208:215], v244\n"
"v_add_u32 v223, 0x2000, v223\n"
"v_mfma_f32_16x16x32_bf16 v[96:99], a[192:195], v[212:215], v[96:99]\n"
"v_exp_f32 v200, v200\n"
"v_mfma_f32_16x16x32_bf16 v[100:103], a[196:199], v[212:215], v[100:103]\n"
"v_exp_f32 v201, v201\n"
"v_mfma_f32_16x16x32_bf16 v[104:107], a[200:203], v[212:215], v[104:107]\n"
"v_exp_f32 v202, v202\n"
"v_mfma_f32_16x16x32_bf16 v[108:111], a[204:207], v[212:215], v[108:111]\n"
"v_exp_f32 v203, v203\n"
"v_mfma_f32_16x16x32_bf16 v[112:115], a[208:211], v[212:215], v[112:115]\n"
"v_exp_f32 v204, v204\n"
"v_mfma_f32_16x16x32_bf16 v[116:119], a[212:215], v[212:215], v[116:119]\n"
"v_exp_f32 v205, v205\n"
"v_mfma_f32_16x16x32_bf16 v[120:123], a[216:219], v[212:215], v[120:123]\n"
"v_exp_f32 v206, v206\n"
"v_mfma_f32_16x16x32_bf16 v[124:127], a[220:223], v[212:215], v[124:127]\n"
"v_exp_f32 v207, v207\n"
"v_mfma_f32_16x16x32_bf16 v[192:195], a[128:131], a[80:83], v[216:219]\n"
"v_cvt_pk_bf16_f32 v208, v200, v201\n"
"v_cvt_pk_bf16_f32 v209, v202, v203\n"
"v_mfma_f32_16x16x32_bf16 v[192:195], a[132:135], a[84:87], v[192:195]\n"
"v_cvt_pk_bf16_f32 v210, v204, v205\n"
"v_cvt_pk_bf16_f32 v211, v206, v207\n"
"v_mfma_f32_16x16x32_bf16 v[192:195], a[136:139], a[88:91], v[192:195]\n"
"v_mfma_f32_16x16x32_bf16 v[192:195], a[140:143], a[92:95], v[192:195]\n"
"v_mfma_f32_16x16x32_bf16 v[196:199], a[144:147], a[80:83], v[216:219]\n"
"v_mfma_f32_16x16x32_bf16 v[196:199], a[148:151], a[84:87], v[196:199]\n"
"v_mfma_f32_16x16x32_bf16 v[196:199], a[152:155], a[88:91], v[196:199]\n"
"v_mfma_f32_16x16x32_bf16 v[196:199], a[156:159], a[92:95], v[196:199]\n"
"ds_read_b128 a[128:131], v245 offset:0\n"
"v_mfma_f32_16x16x32_bf16 v[128:131], a[192:195], v[208:211], v[128:131]\n"
"ds_read_b128 a[132:135], v245 offset:1024\n"
"v_mfma_f32_16x16x32_bf16 v[132:135], a[196:199], v[208:211], v[132:135]\n"
"v_exp_f32 v192, v192\n"
"v_mfma_f32_16x16x32_bf16 v[136:139], a[200:203], v[208:211], v[136:139]\n"
"v_exp_f32 v193, v193\n"
"v_mfma_f32_16x16x32_bf16 v[140:143], a[204:207], v[208:211], v[140:143]\n"
"v_exp_f32 v194, v194\n"
"v_mfma_f32_16x16x32_bf16 v[144:147], a[208:211], v[208:211], v[144:147]\n"
"v_exp_f32 v195, v195\n"
"v_mfma_f32_16x16x32_bf16 v[148:151], a[212:215], v[208:211], v[148:151]\n"
"v_exp_f32 v196, v196\n"
"v_mfma_f32_16x16x32_bf16 v[152:155], a[216:219], v[208:211], v[152:155]\n"
"v_exp_f32 v197, v197\n"
"v_mfma_f32_16x16x32_bf16 v[156:159], a[220:223], v[208:211], v[156:159]\n"
"v_exp_f32 v198, v198\n"
"s_waitcnt vmcnt(8) lgkmcnt(0)\n"
"s_barrier\n"
"s_sleep 5\n"
".p2align 6\n"
"La0%=:\n"
"v_mfma_f32_16x16x32_bf16 v[200:203], a[96:99], a[0:3], v[216:219]\n"
"v_exp_f32 v199, v199\n"
"v_mfma_f32_16x16x32_bf16 v[200:203], a[100:103], a[4:7], v[200:203]\n"
"v_cvt_pk_bf16_f32 v212, v192, v193\n"
"v_cvt_pk_bf16_f32 v213, v194, v195\n"
"v_mfma_f32_16x16x32_bf16 v[200:203], a[104:107], a[8:11], v[200:203]\n"
"v_cvt_pk_bf16_f32 v214, v196, v197\n"
"v_cvt_pk_bf16_f32 v215, v198, v199\n"
"v_mfma_f32_16x16x32_bf16 v[200:203], a[108:111], a[12:15], v[200:203]\n"
"s_add_u32 m0, s70, 16384\n"
"v_mfma_f32_16x16x32_bf16 v[204:207], a[112:115], a[0:3], v[216:219]\n"
"buffer_load_dwordx4 v223, s[40:43], s60 offen offset:0 nt lds\n"
"v_mfma_f32_16x16x32_bf16 v[204:207], a[116:119], a[4:7], v[204:207]\n"
"ds_read_b128 a[136:139], v245 offset:2048\n"
"v_mfma_f32_16x16x32_bf16 v[204:207], a[120:123], a[8:11], v[204:207]\n"
"ds_read_b128 a[140:143], v245 offset:3072\n"
"v_mfma_f32_16x16x32_bf16 v[204:207], a[124:127], a[12:15], v[204:207]\n"
"ds_read_b128 a[144:147], v220 offset:53248\n"
"v_smfmac_f32_16x16x64_bf16 v[232:235], v[240:243], v[208:215], v244\n"
"v_mfma_f32_16x16x32_bf16 v[160:163], a[192:195], v[212:215], v[160:163]\n"
"v_exp_f32 v200, v200\n"
"v_mfma_f32_16x16x32_bf16 v[164:167], a[196:199], v[212:215], v[164:167]\n"
"v_exp_f32 v201, v201\n"
"v_mfma_f32_16x16x32_bf16 v[168:171], a[200:203], v[212:215], v[168:171]\n"
"v_exp_f32 v202, v202\n"
"v_mfma_f32_16x16x32_bf16 v[172:175], a[204:207], v[212:215], v[172:175]\n"
"v_exp_f32 v203, v203\n"
"v_mfma_f32_16x16x32_bf16 v[176:179], a[208:211], v[212:215], v[176:179]\n"
"v_exp_f32 v204, v204\n"
"v_mfma_f32_16x16x32_bf16 v[180:183], a[212:215], v[212:215], v[180:183]\n"
"v_exp_f32 v205, v205\n"
"v_mfma_f32_16x16x32_bf16 v[184:187], a[216:219], v[212:215], v[184:187]\n"
"v_exp_f32 v206, v206\n"
"v_mfma_f32_16x16x32_bf16 v[188:191], a[220:223], v[212:215], v[188:191]\n"
"v_exp_f32 v207, v207\n"
"v_mfma_f32_16x16x32_bf16 v[192:195], a[96:99], a[16:19], v[216:219]\n"
"v_cvt_pk_bf16_f32 v208, v200, v201\n"
"v_cvt_pk_bf16_f32 v209, v202, v203\n"
"v_mfma_f32_16x16x32_bf16 v[192:195], a[100:103], a[20:23], v[192:195]\n"
"v_cvt_pk_bf16_f32 v210, v204, v205\n"
"v_cvt_pk_bf16_f32 v211, v206, v207\n"
"v_mfma_f32_16x16x32_bf16 v[192:195], a[104:107], a[24:27], v[192:195]\n"
"buffer_load_dwordx4 v223, s[40:43], s61 offen offset:1024 nt lds\n"
"v_mfma_f32_16x16x32_bf16 v[192:195], a[108:111], a[28:31], v[192:195]\n"
"ds_read_b128 a[148:151], v220 offset:54272\n"
"v_mfma_f32_16x16x32_bf16 v[196:199], a[112:115], a[16:19], v[216:219]\n"
"ds_read_b128 a[152:155], v220 offset:55296\n"
"v_mfma_f32_16x16x32_bf16 v[196:199], a[116:119], a[20:23], v[196:199]\n"
"ds_read_b128 a[156:159], v220 offset:56320\n"
"ds_read_b64_tr_b16 a[192:193], v221 offset:49152\n"
"v_mfma_f32_16x16x32_bf16 v[196:199], a[120:123], a[24:27], v[196:199]\n"
"ds_read_b64_tr_b16 a[194:195], v221 offset:53248\n"
"ds_read_b64_tr_b16 a[196:197], v222 offset:49152\n"
"v_mfma_f32_16x16x32_bf16 v[196:199], a[124:127], a[28:31], v[196:199]\n"
"buffer_load_dwordx4 v223, s[40:43], s62 offen offset:2048 nt lds\n"
"v_mfma_f32_16x16x32_bf16 v[0:3], a[160:163], v[208:211], v[0:3]\n"
"ds_read_b64_tr_b16 a[198:199], v222 offset:53248\n"
"ds_read_b64_tr_b16 a[200:201], v221 offset:50176\n"
"v_mfma_f32_16x16x32_bf16 v[4:7], a[164:167], v[208:211], v[4:7]\n"
"v_exp_f32 v192, v192\n"
"v_mfma_f32_16x16x32_bf16 v[8:11], a[168:171], v[208:211], v[8:11]\n"
"v_exp_f32 v193, v193\n"
"v_mfma_f32_16x16x32_bf16 v[12:15], a[172:175], v[208:211], v[12:15]\n"
"v_exp_f32 v194, v194\n"
"v_mfma_f32_16x16x32_bf16 v[16:19], a[176:179], v[208:211], v[16:19]\n"
"v_exp_f32 v195, v195\n"
"v_mfma_f32_16x16x32_bf16 v[20:23], a[180:183], v[208:211], v[20:23]\n"
"v_exp_f32 v196, v196\n"
"v_mfma_f32_16x16x32_bf16 v[24:27], a[184:187], v[208:211], v[24:27]\n"
"v_exp_f32 v197, v197\n"
"v_mfma_f32_16x16x32_bf16 v[28:31], a[188:191], v[208:211], v[28:31]\n"
"v_exp_f32 v198, v198\n"
"s_waitcnt lgkmcnt(9)\n"
"v_mfma_f32_16x16x32_bf16 v[200:203], a[96:99], a[128:131], v[216:219]\n"
"v_exp_f32 v199, v199\n"
"v_mfma_f32_16x16x32_bf16 v[200:203], a[100:103], a[132:135], v[200:203]\n"
"v_cvt_pk_bf16_f32 v212, v192, v193\n"
"v_cvt_pk_bf16_f32 v213, v194, v195\n"
"v_mfma_f32_16x16x32_bf16 v[200:203], a[104:107], a[136:139], v[200:203]\n"
"v_cvt_pk_bf16_f32 v214, v196, v197\n"
"v_cvt_pk_bf16_f32 v215, v198, v199\n"
"v_mfma_f32_16x16x32_bf16 v[200:203], a[108:111], a[140:143], v[200:203]\n"
"buffer_load_dwordx4 v223, s[40:43], 0 offen offset:3072 nt lds\n"
"v_mfma_f32_16x16x32_bf16 v[204:207], a[112:115], a[128:131], v[216:219]\n"
"ds_read_b64_tr_b16 a[202:203], v221 offset:54272\n"
"ds_read_b64_tr_b16 a[204:205], v222 offset:50176\n"
"v_mfma_f32_16x16x32_bf16 v[204:207], a[116:119], a[132:135], v[204:207]\n"
"ds_read_b64_tr_b16 a[206:207], v222 offset:54272\n"
"v_mfma_f32_16x16x32_bf16 v[204:207], a[120:123], a[136:139], v[204:207]\n"
"v_mfma_f32_16x16x32_bf16 v[204:207], a[124:127], a[140:143], v[204:207]\n"
"ds_read_b128 a[128:131], v220 offset:49152\n"
"v_smfmac_f32_16x16x64_bf16 v[224:227], v[240:243], v[208:215], v244\n"
"s_add_u32 m0, s70, 24576\n"
"v_mfma_f32_16x16x32_bf16 v[32:35], a[160:163], v[212:215], v[32:35]\n"
"v_exp_f32 v200, v200\n"
"v_mfma_f32_16x16x32_bf16 v[36:39], a[164:167], v[212:215], v[36:39]\n"
"v_exp_f32 v201, v201\n"
"v_mfma_f32_16x16x32_bf16 v[40:43], a[168:171], v[212:215], v[40:43]\n"
"v_exp_f32 v202, v202\n"
"v_mfma_f32_16x16x32_bf16 v[44:47], a[172:175], v[212:215], v[44:47]\n"
"v_exp_f32 v203, v203\n"
"v_mfma_f32_16x16x32_bf16 v[48:51], a[176:179], v[212:215], v[48:51]\n"
"v_exp_f32 v204, v204\n"
"v_mfma_f32_16x16x32_bf16 v[52:55], a[180:183], v[212:215], v[52:55]\n"
"v_exp_f32 v205, v205\n"
"v_mfma_f32_16x16x32_bf16 v[56:59], a[184:187], v[212:215], v[56:59]\n"
"v_exp_f32 v206, v206\n"
"v_mfma_f32_16x16x32_bf16 v[60:63], a[188:191], v[212:215], v[60:63]\n"
"v_exp_f32 v207, v207\n"
"v_mfma_f32_16x16x32_bf16 v[192:195], a[96:99], a[32:35], v[216:219]\n"
"v_cvt_pk_bf16_f32 v212, v200, v201\n"
"v_cvt_pk_bf16_f32 v213, v202, v203\n"
"v_mfma_f32_16x16x32_bf16 v[192:195], a[100:103], a[36:39], v[192:195]\n"
"v_cvt_pk_bf16_f32 v214, v204, v205\n"
"v_cvt_pk_bf16_f32 v215, v206, v207\n"
"v_mfma_f32_16x16x32_bf16 v[192:195], a[104:107], a[40:43], v[192:195]\n"
"buffer_load_dwordx4 v223, s[44:47], s60 offen offset:0 nt lds\n"
"v_mfma_f32_16x16x32_bf16 v[192:195], a[108:111], a[44:47], v[192:195]\n"
"ds_read_b128 a[132:135], v220 offset:50176\n"
"v_mfma_f32_16x16x32_bf16 v[196:199], a[112:115], a[32:35], v[216:219]\n"
"ds_read_b128 a[136:139], v220 offset:51200\n"
"v_mfma_f32_16x16x32_bf16 v[196:199], a[116:119], a[36:39], v[196:199]\n"
"ds_read_b128 a[140:143], v220 offset:52224\n"
"ds_read_b64_tr_b16 a[208:209], v221 offset:51200\n"
"v_mfma_f32_16x16x32_bf16 v[196:199], a[120:123], a[40:43], v[196:199]\n"
"ds_read_b64_tr_b16 a[210:211], v221 offset:55296\n"
"ds_read_b64_tr_b16 a[212:213], v222 offset:51200\n"
"v_mfma_f32_16x16x32_bf16 v[196:199], a[124:127], a[44:47], v[196:199]\n"
"buffer_load_dwordx4 v223, s[44:47], s61 offen offset:1024 nt lds\n"
"v_smfmac_f32_16x16x64_bf16 v[236:239], v[240:243], v[208:215], v244\n"
"ds_read_b64_tr_b16 a[214:215], v222 offset:55296\n"
"v_mfma_f32_16x16x32_bf16 a[224:227], a[160:163], v[212:215], a[224:227]\n"
"v_exp_f32 v192, v192\n"
"v_mfma_f32_16x16x32_bf16 a[228:231], a[164:167], v[212:215], a[228:231]\n"
"v_exp_f32 v193, v193\n"
"v_mfma_f32_16x16x32_bf16 a[232:235], a[168:171], v[212:215], a[232:235]\n"
"v_exp_f32 v194, v194\n"
"v_mfma_f32_16x16x32_bf16 a[236:239], a[172:175], v[212:215], a[236:239]\n"
"v_exp_f32 v195, v195\n"
"v_mfma_f32_16x16x32_bf16 a[240:243], a[176:179], v[212:215], a[240:243]\n"
"v_exp_f32 v196, v196\n"
"v_mfma_f32_16x16x32_bf16 a[244:247], a[180:183], v[212:215], a[244:247]\n"
"v_exp_f32 v197, v197\n"
"v_mfma_f32_16x16x32_bf16 a[248:251], a[184:187], v[212:215], a[248:251]\n"
"v_exp_f32 v198, v198\n"
"v_mfma_f32_16x16x32_bf16 a[252:255], a[188:191], v[212:215], a[252:255]\n"
"v_exp_f32 v199, v199\n"
"v_mfma_f32_16x16x32_bf16 v[200:203], a[96:99], a[48:51], v[216:219]\n"
"v_cvt_pk_bf16_f32 v208, v192, v193\n"
"v_cvt_pk_bf16_f32 v209, v194, v195\n"
"v_mfma_f32_16x16x32_bf16 v[200:203], a[100:103], a[52:55], v[200:203]\n"
"v_cvt_pk_bf16_f32 v210, v196, v197\n"
"v_cvt_pk_bf16_f32 v211, v198, v199\n"
"v_mfma_f32_16x16x32_bf16 v[200:203], a[104:107], a[56:59], v[200:203]\n"
"buffer_load_dwordx4 v223, s[44:47], s62 offen offset:2048 nt lds\n"
"v_mfma_f32_16x16x32_bf16 v[200:203], a[108:111], a[60:63], v[200:203]\n"
"ds_read_b64_tr_b16 a[216:217], v221 offset:52224\n"
"ds_read_b64_tr_b16 a[218:219], v221 offset:56320\n"
"v_mfma_f32_16x16x32_bf16 v[204:207], a[112:115], a[48:51], v[216:219]\n"
"ds_read_b64_tr_b16 a[220:221], v222 offset:52224\n"
"ds_read_b64_tr_b16 a[222:223], v222 offset:56320\n"
"v_mfma_f32_16x16x32_bf16 v[204:207], a[116:119], a[52:55], v[204:207]\n"
"v_mfma_f32_16x16x32_bf16 v[204:207], a[120:123], a[56:59], v[204:207]\n"
"v_mfma_f32_16x16x32_bf16 v[204:207], a[124:127], a[60:63], v[204:207]\n"
"buffer_load_dwordx4 v223, s[44:47], 0 offen offset:3072 nt lds\n"
"v_mfma_f32_16x16x32_bf16 v[64:67], a[160:163], v[208:211], v[64:67]\n"
"v_mfma_f32_16x16x32_bf16 v[68:71], a[164:167], v[208:211], v[68:71]\n"
"v_exp_f32 v200, v200\n"
"v_mfma_f32_16x16x32_bf16 v[72:75], a[168:171], v[208:211], v[72:75]\n"
"v_exp_f32 v201, v201\n"
"v_mfma_f32_16x16x32_bf16 v[76:79], a[172:175], v[208:211], v[76:79]\n"
"v_exp_f32 v202, v202\n"
"v_mfma_f32_16x16x32_bf16 v[80:83], a[176:179], v[208:211], v[80:83]\n"
"v_exp_f32 v203, v203\n"
"v_mfma_f32_16x16x32_bf16 v[84:87], a[180:183], v[208:211], v[84:87]\n"
"v_exp_f32 v204, v204\n"
"v_mfma_f32_16x16x32_bf16 v[88:91], a[184:187], v[208:211], v[88:91]\n"
"v_exp_f32 v205, v205\n"
"v_mfma_f32_16x16x32_bf16 v[92:95], a[188:191], v[208:211], v[92:95]\n"
"v_exp_f32 v206, v206\n"
"v_mfma_f32_16x16x32_bf16 v[192:195], a[96:99], a[64:67], v[216:219]\n"
"v_exp_f32 v207, v207\n"
"v_mfma_f32_16x16x32_bf16 v[192:195], a[100:103], a[68:71], v[192:195]\n"
"v_cvt_pk_bf16_f32 v212, v200, v201\n"
"v_cvt_pk_bf16_f32 v213, v202, v203\n"
"v_mfma_f32_16x16x32_bf16 v[192:195], a[104:107], a[72:75], v[192:195]\n"
"v_cvt_pk_bf16_f32 v214, v204, v205\n"
"v_cvt_pk_bf16_f32 v215, v206, v207\n"
"v_mfma_f32_16x16x32_bf16 v[192:195], a[108:111], a[76:79], v[192:195]\n"
"v_add_u32 v223, 0x2000, v223\n"
"v_mfma_f32_16x16x32_bf16 v[196:199], a[112:115], a[64:67], v[216:219]\n"
"v_mfma_f32_16x16x32_bf16 v[196:199], a[116:119], a[68:71], v[196:199]\n"
"v_mfma_f32_16x16x32_bf16 v[196:199], a[120:123], a[72:75], v[196:199]\n"
"v_mfma_f32_16x16x32_bf16 v[196:199], a[124:127], a[76:79], v[196:199]\n"
"v_smfmac_f32_16x16x64_bf16 v[228:231], v[240:243], v[208:215], v244\n"
"v_mfma_f32_16x16x32_bf16 v[96:99], a[160:163], v[212:215], v[96:99]\n"
"v_exp_f32 v192, v192\n"
"v_mfma_f32_16x16x32_bf16 v[100:103], a[164:167], v[212:215], v[100:103]\n"
"v_exp_f32 v193, v193\n"
"v_mfma_f32_16x16x32_bf16 v[104:107], a[168:171], v[212:215], v[104:107]\n"
"v_exp_f32 v194, v194\n"
"v_mfma_f32_16x16x32_bf16 v[108:111], a[172:175], v[212:215], v[108:111]\n"
"v_exp_f32 v195, v195\n"
"v_mfma_f32_16x16x32_bf16 v[112:115], a[176:179], v[212:215], v[112:115]\n"
"v_exp_f32 v196, v196\n"
"v_mfma_f32_16x16x32_bf16 v[116:119], a[180:183], v[212:215], v[116:119]\n"
"v_exp_f32 v197, v197\n"
"v_mfma_f32_16x16x32_bf16 v[120:123], a[184:187], v[212:215], v[120:123]\n"
"v_exp_f32 v198, v198\n"
"v_mfma_f32_16x16x32_bf16 v[124:127], a[188:191], v[212:215], v[124:127]\n"
"v_exp_f32 v199, v199\n"
"v_mfma_f32_16x16x32_bf16 v[200:203], a[96:99], a[80:83], v[216:219]\n"
"v_cvt_pk_bf16_f32 v208, v192, v193\n"
"v_cvt_pk_bf16_f32 v209, v194, v195\n"
"v_mfma_f32_16x16x32_bf16 v[200:203], a[100:103], a[84:87], v[200:203]\n"
"v_cvt_pk_bf16_f32 v210, v196, v197\n"
"v_cvt_pk_bf16_f32 v211, v198, v199\n"
"v_mfma_f32_16x16x32_bf16 v[200:203], a[104:107], a[88:91], v[200:203]\n"
"v_mfma_f32_16x16x32_bf16 v[200:203], a[108:111], a[92:95], v[200:203]\n"
"v_mfma_f32_16x16x32_bf16 v[204:207], a[112:115], a[80:83], v[216:219]\n"
"v_mfma_f32_16x16x32_bf16 v[204:207], a[116:119], a[84:87], v[204:207]\n"
"v_mfma_f32_16x16x32_bf16 v[204:207], a[120:123], a[88:91], v[204:207]\n"
"s_waitcnt vmcnt(8) lgkmcnt(0)\n"
"s_barrier\n"
"s_cmp_lt_u32 s75, 4\n"
"s_cbranch_scc1 Lsl01%=\n"
"s_sleep 2\n"
"s_cmp_lt_u32 s75, 5\n"
"s_cbranch_scc1 Lsl01%=\n"
"s_sleep 2\n"
"s_cmp_lt_u32 s75, 6\n"
"s_cbranch_scc1 Lsl01%=\n"
"s_sleep 1\n"
"Lsl01%=:\n"
"v_mfma_f32_16x16x32_bf16 v[204:207], a[124:127], a[92:95], v[204:207]\n"
"s_add_u32 m0, s70, 32768\n"
"v_mfma_f32_16x16x32_bf16 v[128:131], a[160:163], v[208:211], v[128:131]\n"
"buffer_load_dwordx4 v223, s[40:43], s60 offen offset:0 nt lds\n"
"v_mfma_f32_16x16x32_bf16 v[132:135], a[164:167], v[208:211], v[132:135]\n"
"v_exp_f32 v200, v200\n"
"v_mfma_f32_16x16x32_bf16 v[136:139], a[168:171], v[208:211], v[136:139]\n"
"v_exp_f32 v201, v201\n"
"v_mfma_f32_16x16x32_bf16 v[140:143], a[172:175], v[208:211], v[140:143]\n"
"v_exp_f32 v202, v202\n"
"v_mfma_f32_16x16x32_bf16 v[144:147], a[176:179], v[208:211], v[144:147]\n"
"v_exp_f32 v203, v203\n"
"v_mfma_f32_16x16x32_bf16 v[148:151], a[180:183], v[208:211], v[148:151]\n"
"v_exp_f32 v204, v204\n"
"v_mfma_f32_16x16x32_bf16 v[152:155], a[184:187], v[208:211], v[152:155]\n"
"v_exp_f32 v205, v205\n"
"v_mfma_f32_16x16x32_bf16 v[156:159], a[188:191], v[208:211], v[156:159]\n"
"v_exp_f32 v206, v206\n"
"v_mfma_f32_16x16x32_bf16 v[192:195], a[128:131], a[0:3], v[216:219]\n"
"v_exp_f32 v207, v207\n"
"v_mfma_f32_16x16x32_bf16 v[192:195], a[132:135], a[4:7], v[192:195]\n"
"v_cvt_pk_bf16_f32 v212, v200, v201\n"
"v_cvt_pk_bf16_f32 v213, v202, v203\n"
"v_mfma_f32_16x16x32_bf16 v[192:195], a[136:139], a[8:11], v[192:195]\n"
"v_cvt_pk_bf16_f32 v214, v204, v205\n"
"v_cvt_pk_bf16_f32 v215, v206, v207\n"
"v_mfma_f32_16x16x32_bf16 v[192:195], a[140:143], a[12:15], v[192:195]\n"
"buffer_load_dwordx4 v223, s[40:43], s61 offen offset:1024 nt lds\n"
"v_mfma_f32_16x16x32_bf16 v[196:199], a[144:147], a[0:3], v[216:219]\n"
"ds_read_b128 a[112:115], v220 offset:4096\n"
"v_mfma_f32_16x16x32_bf16 v[196:199], a[148:151], a[4:7], v[196:199]\n"
"ds_read_b128 a[116:119], v220 offset:5120\n"
"v_mfma_f32_16x16x32_bf16 v[196:199], a[152:155], a[8:11], v[196:199]\n"
"ds_read_b128 a[120:123], v220 offset:6144\n"
"v_mfma_f32_16x16x32_bf16 v[196:199], a[156:159], a[12:15], v[196:199]\n"
"ds_read_b128 a[124:127], v220 offset:7168\n"
"v_smfmac_f32_16x16x64_bf16 v[232:235], v[240:243], v[208:215], v244\n"
"v_mfma_f32_16x16x32_bf16 v[160:163], a[160:163], v[212:215], v[160:163]\n"
"v_exp_f32 v192, v192\n"
"v_mfma_f32_16x16x32_bf16 v[164:167], a[164:167], v[212:215], v[164:167]\n"
"v_exp_f32 v193, v193\n"
"v_mfma_f32_16x16x32_bf16 v[168:171], a[168:171], v[212:215], v[168:171]\n"
"v_exp_f32 v194, v194\n"
"v_mfma_f32_16x16x32_bf16 v[172:175], a[172:175], v[212:215], v[172:175]\n"
"v_exp_f32 v195, v195\n"
"v_mfma_f32_16x16x32_bf16 v[176:179], a[176:179], v[212:215], v[176:179]\n"
"v_exp_f32 v196, v196\n"
"v_mfma_f32_16x16x32_bf16 v[180:183], a[180:183], v[212:215], v[180:183]\n"
"v_exp_f32 v197, v197\n"
"v_mfma_f32_16x16x32_bf16 v[184:187], a[184:187], v[212:215], v[184:187]\n"
"v_exp_f32 v198, v198\n"
"v_mfma_f32_16x16x32_bf16 v[188:191], a[188:191], v[212:215], v[188:191]\n"
"v_exp_f32 v199, v199\n"
"v_mfma_f32_16x16x32_bf16 v[200:203], a[128:131], a[16:19], v[216:219]\n"
"v_cvt_pk_bf16_f32 v208, v192, v193\n"
"v_cvt_pk_bf16_f32 v209, v194, v195\n"
"v_mfma_f32_16x16x32_bf16 v[200:203], a[132:135], a[20:23], v[200:203]\n"
"v_cvt_pk_bf16_f32 v210, v196, v197\n"
"v_cvt_pk_bf16_f32 v211, v198, v199\n"
"v_mfma_f32_16x16x32_bf16 v[200:203], a[136:139], a[24:27], v[200:203]\n"
"buffer_load_dwordx4 v223, s[40:43], s62 offen offset:2048 nt lds\n"
"v_mfma_f32_16x16x32_bf16 v[200:203], a[140:143], a[28:31], v[200:203]\n"
"ds_read_b128 a[96:99], v220 offset:0\n"
"v_mfma_f32_16x16x32_bf16 v[204:207], a[144:147], a[16:19], v[216:219]\n"
"ds_read_b128 a[100:103], v220 offset:1024\n"
"v_mfma_f32_16x16x32_bf16 v[204:207], a[148:151], a[20:23], v[204:207]\n"
"ds_read_b128 a[104:107], v220 offset:2048\n"
"v_mfma_f32_16x16x32_bf16 v[204:207], a[152:155], a[24:27], v[204:207]\n"
"ds_read_b128 a[108:111], v220 offset:3072\n"
"ds_read_b64_tr_b16 a[160:161], v221 offset:0\n"
"v_mfma_f32_16x16x32_bf16 v[204:207], a[156:159], a[28:31], v[204:207]\n"
"buffer_load_dwordx4 v223, s[40:43], 0 offen offset:3072 nt lds\n"
"v_mfma_f32_16x16x32_bf16 v[0:3], a[192:195], v[208:211], v[0:3]\n"
"ds_read_b64_tr_b16 a[162:163], v221 offset:4096\n"
"ds_read_b64_tr_b16 a[164:165], v222 offset:0\n"
"v_mfma_f32_16x16x32_bf16 v[4:7], a[196:199], v[208:211], v[4:7]\n"
"v_exp_f32 v200, v200\n"
"v_mfma_f32_16x16x32_bf16 v[8:11], a[200:203], v[208:211], v[8:11]\n"
"v_exp_f32 v201, v201\n"
"v_mfma_f32_16x16x32_bf16 v[12:15], a[204:207], v[208:211], v[12:15]\n"
"v_exp_f32 v202, v202\n"
"v_mfma_f32_16x16x32_bf16 v[16:19], a[208:211], v[208:211], v[16:19]\n"
"v_exp_f32 v203, v203\n"
"v_mfma_f32_16x16x32_bf16 v[20:23], a[212:215], v[208:211], v[20:23]\n"
"v_exp_f32 v204, v204\n"
"v_mfma_f32_16x16x32_bf16 v[24:27], a[216:219], v[208:211], v[24:27]\n"
"v_exp_f32 v205, v205\n"
"v_mfma_f32_16x16x32_bf16 v[28:31], a[220:223], v[208:211], v[28:31]\n"
"v_exp_f32 v206, v206\n"
"v_mfma_f32_16x16x32_bf16 v[192:195], a[128:131], a[32:35], v[216:219]\n"
"v_exp_f32 v207, v207\n"
"v_mfma_f32_16x16x32_bf16 v[192:195], a[132:135], a[36:39], v[192:195]\n"
"v_cvt_pk_bf16_f32 v212, v200, v201\n"
"v_cvt_pk_bf16_f32 v213, v202, v203\n"
"v_mfma_f32_16x16x32_bf16 v[192:195], a[136:139], a[40:43], v[192:195]\n"
"v_cvt_pk_bf16_f32 v214, v204, v205\n"
"v_cvt_pk_bf16_f32 v215, v206, v207\n"
"v_mfma_f32_16x16x32_bf16 v[192:195], a[140:143], a[44:47], v[192:195]\n"
"s_add_u32 m0, s70, 40960\n"
"ds_read_b64_tr_b16 a[166:167], v222 offset:4096\n"
"v_mfma_f32_16x16x32_bf16 v[196:199], a[144:147], a[32:35], v[216:219]\n"
"buffer_load_dwordx4 v223, s[44:47], s60 offen offset:0 nt lds\n"
"v_mfma_f32_16x16x32_bf16 v[196:199], a[148:151], a[36:39], v[196:199]\n"
"ds_read_b64_tr_b16 a[168:169], v221 offset:1024\n"
"ds_read_b64_tr_b16 a[170:171], v221 offset:5120\n"
"v_mfma_f32_16x16x32_bf16 v[196:199], a[152:155], a[40:43], v[196:199]\n"
"ds_read_b64_tr_b16 a[172:173], v222 offset:1024\n"
"ds_read_b64_tr_b16 a[174:175], v222 offset:5120\n"
"v_mfma_f32_16x16x32_bf16 v[196:199], a[156:159], a[44:47], v[196:199]\n"
"ds_read_b64_tr_b16 a[176:177], v221 offset:2048\n"
"ds_read_b64_tr_b16 a[178:179], v221 offset:6144\n"
"v_smfmac_f32_16x16x64_bf16 v[224:227], v[240:243], v[208:215], v244\n"
"ds_read_b64_tr_b16 a[180:181], v222 offset:2048\n"
"v_mfma_f32_16x16x32_bf16 v[32:35], a[192:195], v[212:215], v[32:35]\n"
"v_exp_f32 v192, v192\n"
"v_mfma_f32_16x16x32_bf16 v[36:39], a[196:199], v[212:215], v[36:39]\n"
"v_exp_f32 v193, v193\n"
"v_mfma_f32_16x16x32_bf16 v[40:43], a[200:203], v[212:215], v[40:43]\n"
"v_exp_f32 v194, v194\n"
"v_mfma_f32_16x16x32_bf16 v[44:47], a[204:207], v[212:215], v[44:47]\n"
"v_exp_f32 v195, v195\n"
"v_mfma_f32_16x16x32_bf16 v[48:51], a[208:211], v[212:215], v[48:51]\n"
"v_exp_f32 v196, v196\n"
"v_mfma_f32_16x16x32_bf16 v[52:55], a[212:215], v[212:215], v[52:55]\n"
"v_exp_f32 v197, v197\n"
"v_mfma_f32_16x16x32_bf16 v[56:59], a[216:219], v[212:215], v[56:59]\n"
"v_exp_f32 v198, v198\n"
"v_mfma_f32_16x16x32_bf16 v[60:63], a[220:223], v[212:215], v[60:63]\n"
"v_exp_f32 v199, v199\n"
"v_mfma_f32_16x16x32_bf16 v[200:203], a[128:131], a[48:51], v[216:219]\n"
"v_cvt_pk_bf16_f32 v208, v192, v193\n"
"v_cvt_pk_bf16_f32 v209, v194, v195\n"
"v_mfma_f32_16x16x32_bf16 v[200:203], a[132:135], a[52:55], v[200:203]\n"
"v_cvt_pk_bf16_f32 v210, v196, v197\n"
"v_cvt_pk_bf16_f32 v211, v198, v199\n"
"v_mfma_f32_16x16x32_bf16 v[200:203], a[136:139], a[56:59], v[200:203]\n"
"buffer_load_dwordx4 v223, s[44:47], s61 offen offset:1024 nt lds\n"
"v_mfma_f32_16x16x32_bf16 v[200:203], a[140:143], a[60:63], v[200:203]\n"
"ds_read_b64_tr_b16 a[182:183], v222 offset:6144\n"
"ds_read_b64_tr_b16 a[184:185], v221 offset:3072\n"
"v_mfma_f32_16x16x32_bf16 v[204:207], a[144:147], a[48:51], v[216:219]\n"
"ds_read_b64_tr_b16 a[186:187], v221 offset:7168\n"
"ds_read_b64_tr_b16 a[188:189], v222 offset:3072\n"
"v_mfma_f32_16x16x32_bf16 v[204:207], a[148:151], a[52:55], v[204:207]\n"
"ds_read_b64_tr_b16 a[190:191], v222 offset:7168\n"
"v_mfma_f32_16x16x32_bf16 v[204:207], a[152:155], a[56:59], v[204:207]\n"
"v_mfma_f32_16x16x32_bf16 v[204:207], a[156:159], a[60:63], v[204:207]\n"
"buffer_load_dwordx4 v223, s[44:47], s62 offen offset:2048 nt lds\n"
"v_mfma_f32_16x16x32_bf16 v[64:67], a[192:195], v[208:211], v[64:67]\n"
"v_mfma_f32_16x16x32_bf16 v[68:71], a[196:199], v[208:211], v[68:71]\n"
"v_exp_f32 v200, v200\n"
"v_mfma_f32_16x16x32_bf16 v[72:75], a[200:203], v[208:211], v[72:75]\n"
"v_exp_f32 v201, v201\n"
"v_mfma_f32_16x16x32_bf16 v[76:79], a[204:207], v[208:211], v[76:79]\n"
"v_exp_f32 v202, v202\n"
"v_mfma_f32_16x16x32_bf16 v[80:83], a[208:211], v[208:211], v[80:83]\n"
"v_exp_f32 v203, v203\n"
"v_mfma_f32_16x16x32_bf16 v[84:87], a[212:215], v[208:211], v[84:87]\n"
"v_exp_f32 v204, v204\n"
"v_mfma_f32_16x16x32_bf16 v[88:91], a[216:219], v[208:211], v[88:91]\n"
"v_exp_f32 v205, v205\n"
"v_mfma_f32_16x16x32_bf16 v[92:95], a[220:223], v[208:211], v[92:95]\n"
"v_exp_f32 v206, v206\n"
"v_mfma_f32_16x16x32_bf16 v[192:195], a[128:131], a[64:67], v[216:219]\n"
"v_exp_f32 v207, v207\n"
"v_mfma_f32_16x16x32_bf16 v[192:195], a[132:135], a[68:71], v[192:195]\n"
"v_cvt_pk_bf16_f32 v212, v200, v201\n"
"v_cvt_pk_bf16_f32 v213, v202, v203\n"
"v_mfma_f32_16x16x32_bf16 v[192:195], a[136:139], a[72:75], v[192:195]\n"
"v_cvt_pk_bf16_f32 v214, v204, v205\n"
"v_cvt_pk_bf16_f32 v215, v206, v207\n"
"v_mfma_f32_16x16x32_bf16 v[192:195], a[140:143], a[76:79], v[192:195]\n"
"buffer_load_dwordx4 v223, s[44:47], 0 offen offset:3072 nt lds\n"
"v_mfma_f32_16x16x32_bf16 v[196:199], a[144:147], a[64:67], v[216:219]\n"
"v_mfma_f32_16x16x32_bf16 v[196:199], a[148:151], a[68:71], v[196:199]\n"
"v_mfma_f32_16x16x32_bf16 v[196:199], a[152:155], a[72:75], v[196:199]\n"
"v_mfma_f32_16x16x32_bf16 v[196:199], a[156:159], a[76:79], v[196:199]\n"
"v_smfmac_f32_16x16x64_bf16 v[228:231], v[240:243], v[208:215], v244\n"
"v_add_u32 v223, 0x2000, v223\n"
"v_mfma_f32_16x16x32_bf16 v[96:99], a[192:195], v[212:215], v[96:99]\n"
"v_exp_f32 v192, v192\n"
"v_mfma_f32_16x16x32_bf16 v[100:103], a[196:199], v[212:215], v[100:103]\n"
"v_exp_f32 v193, v193\n"
"v_mfma_f32_16x16x32_bf16 v[104:107], a[200:203], v[212:215], v[104:107]\n"
"v_exp_f32 v194, v194\n"
"v_mfma_f32_16x16x32_bf16 v[108:111], a[204:207], v[212:215], v[108:111]\n"
"v_exp_f32 v195, v195\n"
"v_mfma_f32_16x16x32_bf16 v[112:115], a[208:211], v[212:215], v[112:115]\n"
"v_exp_f32 v196, v196\n"
"v_mfma_f32_16x16x32_bf16 v[116:119], a[212:215], v[212:215], v[116:119]\n"
"v_exp_f32 v197, v197\n"
"v_mfma_f32_16x16x32_bf16 v[120:123], a[216:219], v[212:215], v[120:123]\n"
"v_exp_f32 v198, v198\n"
"v_mfma_f32_16x16x32_bf16 v[124:127], a[220:223], v[212:215], v[124:127]\n"
"v_exp_f32 v199, v199\n"
"v_mfma_f32_16x16x32_bf16 v[200:203], a[128:131], a[80:83], v[216:219]\n"
"v_cvt_pk_bf16_f32 v208, v192, v193\n"
"v_cvt_pk_bf16_f32 v209, v194, v195\n"
"v_mfma_f32_16x16x32_bf16 v[200:203], a[132:135], a[84:87], v[200:203]\n"
"v_cvt_pk_bf16_f32 v210, v196, v197\n"
"v_cvt_pk_bf16_f32 v211, v198, v199\n"
"v_mfma_f32_16x16x32_bf16 v[200:203], a[136:139], a[88:91], v[200:203]\n"
"v_mfma_f32_16x16x32_bf16 v[200:203], a[140:143], a[92:95], v[200:203]\n"
"v_mfma_f32_16x16x32_bf16 v[204:207], a[144:147], a[80:83], v[216:219]\n"
"v_mfma_f32_16x16x32_bf16 v[204:207], a[148:151], a[84:87], v[204:207]\n"
"v_mfma_f32_16x16x32_bf16 v[204:207], a[152:155], a[88:91], v[204:207]\n"
"v_mfma_f32_16x16x32_bf16 v[204:207], a[156:159], a[92:95], v[204:207]\n"
"ds_read_b128 a[128:131], v245 offset:0\n"
"v_mfma_f32_16x16x32_bf16 v[128:131], a[192:195], v[208:211], v[128:131]\n"
"ds_read_b128 a[132:135], v245 offset:1024\n"
"v_mfma_f32_16x16x32_bf16 v[132:135], a[196:199], v[208:211], v[132:135]\n"
"v_exp_f32 v200, v200\n"
"v_mfma_f32_16x16x32_bf16 v[136:139], a[200:203], v[208:211], v[136:139]\n"
"v_exp_f32 v201, v201\n"
"v_mfma_f32_16x16x32_bf16 v[140:143], a[204:207], v[208:211], v[140:143]\n"
"v_exp_f32 v202, v202\n"
"v_mfma_f32_16x16x32_bf16 v[144:147], a[208:211], v[208:211], v[144:147]\n"
"v_exp_f32 v203, v203\n"
"v_mfma_f32_16x16x32_bf16 v[148:151], a[212:215], v[208:211], v[148:151]\n"
"v_exp_f32 v204, v204\n"
"v_mfma_f32_16x16x32_bf16 v[152:155], a[216:219], v[208:211], v[152:155]\n"
"v_exp_f32 v205, v205\n"
"v_mfma_f32_16x16x32_bf16 v[156:159], a[220:223], v[208:211], v[156:159]\n"
"v_exp_f32 v206, v206\n"
"s_waitcnt vmcnt(8) lgkmcnt(0)\n"
"s_barrier\n"
"s_cmp_lt_u32 s75, 4\n"
"s_cbranch_scc1 Lsl02%=\n"
"s_sleep 2\n"
"s_cmp_lt_u32 s75, 5\n"
"s_cbranch_scc1 Lsl02%=\n"
"s_sleep 2\n"
"s_cmp_lt_u32 s75, 6\n"
"s_cbranch_scc1 Lsl02%=\n"
"s_sleep 1\n"
"Lsl02%=:\n"
"v_mfma_f32_16x16x32_bf16 v[192:195], a[96:99], a[0:3], v[216:219]\n"
"v_exp_f32 v207, v207\n"
"v_mfma_f32_16x16x32_bf16 v[192:195], a[100:103], a[4:7], v[192:195]\n"
"v_cvt_pk_bf16_f32 v212, v200, v201\n"
"v_cvt_pk_bf16_f32 v213, v202, v203\n"
"v_mfma_f32_16x16x32_bf16 v[192:195], a[104:107], a[8:11], v[192:195]\n"
"v_cvt_pk_bf16_f32 v214, v204, v205\n"
"v_cvt_pk_bf16_f32 v215, v206, v207\n"
"v_mfma_f32_16x16x32_bf16 v[192:195], a[108:111], a[12:15], v[192:195]\n"
"s_add_u32 m0, s70, 49152\n"
"v_mfma_f32_16x16x32_bf16 v[196:199], a[112:115], a[0:3], v[216:219]\n"
"buffer_load_dwordx4 v223, s[40:43], s60 offen offset:0 nt lds\n"
"v_mfma_f32_16x16x32_bf16 v[196:199], a[116:119], a[4:7], v[196:199]\n"
"ds_read_b128 a[136:139], v245 offset:2048\n"
"v_mfma_f32_16x16x32_bf16 v[196:199], a[120:123], a[8:11], v[196:199]\n"
"ds_read_b128 a[140:143], v245 offset:3072\n"
"v_mfma_f32_16x16x32_bf16 v[196:199], a[124:127], a[12:15], v[196:199]\n"
"ds_read_b128 a[144:147], v220 offset:20480\n"
"v_smfmac_f32_16x16x64_bf16 v[232:235], v[240:243], v[208:215], v244\n"
"v_mfma_f32_16x16x32_bf16 v[160:163], a[192:195], v[212:215], v[160:163]\n"
"v_exp_f32 v192, v192\n"
"v_mfma_f32_16x16x32_bf16 v[164:167], a[196:199], v[212:215], v[164:167]\n"
"v_exp_f32 v193, v193\n"
"v_mfma_f32_16x16x32_bf16 v[168:171], a[200:203], v[212:215], v[168:171]\n"
"v_exp_f32 v194, v194\n"
"v_mfma_f32_16x16x32_bf16 v[172:175], a[204:207], v[212:215], v[172:175]\n"
"v_exp_f32 v195, v195\n"
"v_mfma_f32_16x16x32_bf16 v[176:179], a[208:211], v[212:215], v[176:179]\n"
"v_exp_f32 v196, v196\n"
"v_mfma_f32_16x16x32_bf16 v[180:183], a[212:215], v[212:215], v[180:183]\n"
"v_exp_f32 v197, v197\n"
"v_mfma_f32_16x16x32_bf16 v[184:187], a[216:219], v[212:215], v[184:187]\n"
"v_exp_f32 v198, v198\n"
"v_mfma_f32_16x16x32_bf16 v[188:191], a[220:223], v[212:215], v[188:191]\n"
"v_exp_f32 v199, v199\n"
"v_mfma_f32_16x16x32_bf16 v[200:203], a[96:99], a[16:19], v[216:219]\n"
"v_cvt_pk_bf16_f32 v208, v192, v193\n"
"v_cvt_pk_bf16_f32 v209, v194, v195\n"
"v_mfma_f32_16x16x32_bf16 v[200:203], a[100:103], a[20:23], v[200:203]\n"
"v_cvt_pk_bf16_f32 v210, v196, v197\n"
"v_cvt_pk_bf16_f32 v211, v198, v199\n"
"v_mfma_f32_16x16x32_bf16 v[200:203], a[104:107], a[24:27], v[200:203]\n"
"buffer_load_dwordx4 v223, s[40:43], s61 offen offset:1024 nt lds\n"
"v_mfma_f32_16x16x32_bf16 v[200:203], a[108:111], a[28:31], v[200:203]\n"
"ds_read_b128 a[148:151], v220 offset:21504\n"
"v_mfma_f32_16x16x32_bf16 v[204:207], a[112:115], a[16:19], v[216:219]\n"
"ds_read_b128 a[152:155], v220 offset:22528\n"
"v_mfma_f32_16x16x32_bf16 v[204:207], a[116:119], a[20:23], v[204:207]\n"
"ds_read_b128 a[156:159], v220 offset:23552\n"
"ds_read_b64_tr_b16 a[192:193], v221 offset:16384\n"
"v_mfma_f32_16x16x32_bf16 v[204:207], a[120:123], a[24:27], v[204:207]\n"
"ds_read_b64_tr_b16 a[194:195], v221 offset:20480\n"
"ds_read_b64_tr_b16 a[196:197], v222 offset:16384\n"
"v_mfma_f32_16x16x32_bf16 v[204:207], a[124:127], a[28:31], v[204:207]\n"
"buffer_load_dwordx4 v223, s[40:43], s62 offen offset:2048 nt lds\n"
"v_mfma_f32_16x16x32_bf16 v[0:3], a[160:163], v[208:211], v[0:3]\n"
"ds_read_b64_tr_b16 a[198:199], v222 offset:20480\n"
"ds_read_b64_tr_b16 a[200:201], v221 offset:17408\n"
"v_mfma_f32_16x16x32_bf16 v[4:7], a[164:167], v[208:211], v[4:7]\n"
"v_exp_f32 v200, v200\n"
"v_mfma_f32_16x16x32_bf16 v[8:11], a[168:171], v[208:211], v[8:11]\n"
"v_exp_f32 v201, v201\n"
"v_mfma_f32_16x16x32_bf16 v[12:15], a[172:175], v[208:211], v[12:15]\n"
"v_exp_f32 v202, v202\n"
"v_mfma_f32_16x16x32_bf16 v[16:19], a[176:179], v[208:211], v[16:19]\n"
"v_exp_f32 v203, v203\n"
"v_mfma_f32_16x16x32_bf16 v[20:23], a[180:183], v[208:211], v[20:23]\n"
"v_exp_f32 v204, v204\n"
"v_mfma_f32_16x16x32_bf16 v[24:27], a[184:187], v[208:211], v[24:27]\n"
"v_exp_f32 v205, v205\n"
"v_mfma_f32_16x16x32_bf16 v[28:31], a[188:191], v[208:211], v[28:31]\n"
"v_exp_f32 v206, v206\n"
"s_waitcnt lgkmcnt(9)\n"
"v_mfma_f32_16x16x32_bf16 v[192:195], a[96:99], a[128:131], v[216:219]\n"
"v_exp_f32 v207, v207\n"
"v_mfma_f32_16x16x32_bf16 v[192:195], a[100:103], a[132:135], v[192:195]\n"
"v_cvt_pk_bf16_f32 v212, v200, v201\n"
"v_cvt_pk_bf16_f32 v213, v202, v203\n"
"v_mfma_f32_16x16x32_bf16 v[192:195], a[104:107], a[136:139], v[192:195]\n"
"v_cvt_pk_bf16_f32 v214, v204, v205\n"
"v_cvt_pk_bf16_f32 v215, v206, v207\n"
"v_mfma_f32_16x16x32_bf16 v[192:195], a[108:111], a[140:143], v[192:195]\n"
"buffer_load_dwordx4 v223, s[40:43], 0 offen offset:3072 nt lds\n"
"v_mfma_f32_16x16x32_bf16 v[196:199], a[112:115], a[128:131], v[216:219]\n"
"ds_read_b64_tr_b16 a[202:203], v221 offset:21504\n"
"ds_read_b64_tr_b16 a[204:205], v222 offset:17408\n"
"v_mfma_f32_16x16x32_bf16 v[196:199], a[116:119], a[132:135], v[196:199]\n"
"ds_read_b64_tr_b16 a[206:207], v222 offset:21504\n"
"v_mfma_f32_16x16x32_bf16 v[196:199], a[120:123], a[136:139], v[196:199]\n"
"v_mfma_f32_16x16x32_bf16 v[196:199], a[124:127], a[140:143], v[196:199]\n"
"ds_read_b128 a[128:131], v220 offset:16384\n"
"v_smfmac_f32_16x16x64_bf16 v[224:227], v[240:243], v[208:215], v244\n"
"s_add_u32 m0, s70, 57344\n"
"v_mfma_f32_16x16x32_bf16 v[32:35], a[160:163], v[212:215], v[32:35]\n"
"v_exp_f32 v192, v192\n"
"v_mfma_f32_16x16x32_bf16 v[36:39], a[164:167], v[212:215], v[36:39]\n"
"v_exp_f32 v193, v193\n"
"v_mfma_f32_16x16x32_bf16 v[40:43], a[168:171], v[212:215], v[40:43]\n"
"v_exp_f32 v194, v194\n"
"v_mfma_f32_16x16x32_bf16 v[44:47], a[172:175], v[212:215], v[44:47]\n"
"v_exp_f32 v195, v195\n"
"v_mfma_f32_16x16x32_bf16 v[48:51], a[176:179], v[212:215], v[48:51]\n"
"v_exp_f32 v196, v196\n"
"v_mfma_f32_16x16x32_bf16 v[52:55], a[180:183], v[212:215], v[52:55]\n"
"v_exp_f32 v197, v197\n"
"v_mfma_f32_16x16x32_bf16 v[56:59], a[184:187], v[212:215], v[56:59]\n"
"v_exp_f32 v198, v198\n"
"v_mfma_f32_16x16x32_bf16 v[60:63], a[188:191], v[212:215], v[60:63]\n"
"v_exp_f32 v199, v199\n"
"v_mfma_f32_16x16x32_bf16 v[200:203], a[96:99], a[32:35], v[216:219]\n"
"v_cvt_pk_bf16_f32 v212, v192, v193\n"
"v_cvt_pk_bf16_f32 v213, v194, v195\n"
"v_mfma_f32_16x16x32_bf16 v[200:203], a[100:103], a[36:39], v[200:203]\n"
"v_cvt_pk_bf16_f32 v214, v196, v197\n"
"v_cvt_pk_bf16_f32 v215, v198, v199\n"
"v_mfma_f32_16x16x32_bf16 v[200:203], a[104:107], a[40:43], v[200:203]\n"
"buffer_load_dwordx4 v223, s[44:47], s60 offen offset:0 nt lds\n"
"v_mfma_f32_16x16x32_bf16 v[200:203], a[108:111], a[44:47], v[200:203]\n"
"ds_read_b128 a[132:135], v220 offset:17408\n"
"v_mfma_f32_16x16x32_bf16 v[204:207], a[112:115], a[32:35], v[216:219]\n"
"ds_read_b128 a[136:139], v220 offset:18432\n"
"v_mfma_f32_16x16x32_bf16 v[204:207], a[116:119], a[36:39], v[204:207]\n"
"ds_read_b128 a[140:143], v220 offset:19456\n"
"ds_read_b64_tr_b16 a[208:209], v221 offset:18432\n"
"v_mfma_f32_16x16x32_bf16 v[204:207], a[120:123], a[40:43], v[204:207]\n"
"ds_read_b64_tr_b16 a[210:211], v221 offset:22528\n"
"ds_read_b64_tr_b16 a[212:213], v222 offset:18432\n"
"v_mfma_f32_16x16x32_bf16 v[204:207], a[124:127], a[44:47], v[204:207]\n"
"buffer_load_dwordx4 v223, s[44:47], s61 offen offset:1024 nt lds\n"
"v_smfmac_f32_16x16x64_bf16 v[236:239], v[240:243], v[208:215], v244\n"
"ds_read_b64_tr_b16 a[214:215], v222 offset:22528\n"
"v_mfma_f32_16x16x32_bf16 a[224:227], a[160:163], v[212:215], a[224:227]\n"
"v_exp_f32 v200, v200\n"
"v_mfma_f32_16x16x32_bf16 a[228:231], a[164:167], v[212:215], a[228:231]\n"
"v_exp_f32 v201, v201\n"
"v_mfma_f32_16x16x32_bf16 a[232:235], a[168:171], v[212:215], a[232:235]\n"
"v_exp_f32 v202, v202\n"
"v_mfma_f32_16x16x32_bf16 a[236:239], a[172:175], v[212:215], a[236:239]\n"
"v_exp_f32 v203, v203\n"
"v_mfma_f32_16x16x32_bf16 a[240:243], a[176:179], v[212:215], a[240:243]\n"
"v_exp_f32 v204, v204\n"
"v_mfma_f32_16x16x32_bf16 a[244:247], a[180:183], v[212:215], a[244:247]\n"
"v_exp_f32 v205, v205\n"
"v_mfma_f32_16x16x32_bf16 a[248:251], a[184:187], v[212:215], a[248:251]\n"
"v_exp_f32 v206, v206\n"
"v_mfma_f32_16x16x32_bf16 a[252:255], a[188:191], v[212:215], a[252:255]\n"
"v_exp_f32 v207, v207\n"
"v_mfma_f32_16x16x32_bf16 v[192:195], a[96:99], a[48:51], v[216:219]\n"
"v_cvt_pk_bf16_f32 v208, v200, v201\n"
"v_cvt_pk_bf16_f32 v209, v202, v203\n"
"v_mfma_f32_16x16x32_bf16 v[192:195], a[100:103], a[52:55], v[192:195]\n"
"v_cvt_pk_bf16_f32 v210, v204, v205\n"
"v_cvt_pk_bf16_f32 v211, v206, v207\n"
"v_mfma_f32_16x16x32_bf16 v[192:195], a[104:107], a[56:59], v[192:195]\n"
"buffer_load_dwordx4 v223, s[44:47], s62 offen offset:2048 nt lds\n"
"v_mfma_f32_16x16x32_bf16 v[192:195], a[108:111], a[60:63], v[192:195]\n"
"ds_read_b64_tr_b16 a[216:217], v221 offset:19456\n"
"ds_read_b64_tr_b16 a[218:219], v221 offset:23552\n"
"v_mfma_f32_16x16x32_bf16 v[196:199], a[112:115], a[48:51], v[216:219]\n"
"ds_read_b64_tr_b16 a[220:221], v222 offset:19456\n"
"ds_read_b64_tr_b16 a[222:223], v222 offset:23552\n"
"v_mfma_f32_16x16x32_bf16 v[196:199], a[116:119], a[52:55], v[196:199]\n"
"v_mfma_f32_16x16x32_bf16 v[196:199], a[120:123], a[56:59], v[196:199]\n"
"v_mfma_f32_16x16x32_bf16 v[196:199], a[124:127], a[60:63], v[196:199]\n"
"buffer_load_dwordx4 v223, s[44:47], 0 offen offset:3072 nt lds\n"
"v_mfma_f32_16x16x32_bf16 v[64:67], a[160:163], v[208:211], v[64:67]\n"
"v_mfma_f32_16x16x32_bf16 v[68:71], a[164:167], v[208:211], v[68:71]\n"
"v_exp_f32 v192, v192\n"
"v_mfma_f32_16x16x32_bf16 v[72:75], a[168:171], v[208:211], v[72:75]\n"
"v_exp_f32 v193, v193\n"
"v_mfma_f32_16x16x32_bf16 v[76:79], a[172:175], v[208:211], v[76:79]\n"
"v_exp_f32 v194, v194\n"
"v_mfma_f32_16x16x32_bf16 v[80:83], a[176:179], v[208:211], v[80:83]\n"
"v_exp_f32 v195, v195\n"
"v_mfma_f32_16x16x32_bf16 v[84:87], a[180:183], v[208:211], v[84:87]\n"
"v_exp_f32 v196, v196\n"
"v_mfma_f32_16x16x32_bf16 v[88:91], a[184:187], v[208:211], v[88:91]\n"
"v_exp_f32 v197, v197\n"
"v_mfma_f32_16x16x32_bf16 v[92:95], a[188:191], v[208:211], v[92:95]\n"
"v_exp_f32 v198, v198\n"
"v_mfma_f32_16x16x32_bf16 v[200:203], a[96:99], a[64:67], v[216:219]\n"
"v_exp_f32 v199, v199\n"
"v_mfma_f32_16x16x32_bf16 v[200:203], a[100:103], a[68:71], v[200:203]\n"
"v_cvt_pk_bf16_f32 v212, v192, v193\n"
"v_cvt_pk_bf16_f32 v213, v194, v195\n"
"v_mfma_f32_16x16x32_bf16 v[200:203], a[104:107], a[72:75], v[200:203]\n"
"v_cvt_pk_bf16_f32 v214, v196, v197\n"
"v_cvt_pk_bf16_f32 v215, v198, v199\n"
"v_mfma_f32_16x16x32_bf16 v[200:203], a[108:111], a[76:79], v[200:203]\n"
"v_add_u32 v223, 0x2000, v223\n"
"v_mfma_f32_16x16x32_bf16 v[204:207], a[112:115], a[64:67], v[216:219]\n"
"v_mfma_f32_16x16x32_bf16 v[204:207], a[116:119], a[68:71], v[204:207]\n"
"v_mfma_f32_16x16x32_bf16 v[204:207], a[120:123], a[72:75], v[204:207]\n"
"v_mfma_f32_16x16x32_bf16 v[204:207], a[124:127], a[76:79], v[204:207]\n"
"v_smfmac_f32_16x16x64_bf16 v[228:231], v[240:243], v[208:215], v244\n"
"v_mfma_f32_16x16x32_bf16 v[96:99], a[160:163], v[212:215], v[96:99]\n"
"v_exp_f32 v200, v200\n"
"v_mfma_f32_16x16x32_bf16 v[100:103], a[164:167], v[212:215], v[100:103]\n"
"v_exp_f32 v201, v201\n"
"v_mfma_f32_16x16x32_bf16 v[104:107], a[168:171], v[212:215], v[104:107]\n"
"v_exp_f32 v202, v202\n"
"v_mfma_f32_16x16x32_bf16 v[108:111], a[172:175], v[212:215], v[108:111]\n"
"v_exp_f32 v203, v203\n"
"v_mfma_f32_16x16x32_bf16 v[112:115], a[176:179], v[212:215], v[112:115]\n"
"v_exp_f32 v204, v204\n"
"v_mfma_f32_16x16x32_bf16 v[116:119], a[180:183], v[212:215], v[116:119]\n"
"v_exp_f32 v205, v205\n"
"v_mfma_f32_16x16x32_bf16 v[120:123], a[184:187], v[212:215], v[120:123]\n"
"v_exp_f32 v206, v206\n"
"v_mfma_f32_16x16x32_bf16 v[124:127], a[188:191], v[212:215], v[124:127]\n"
"v_exp_f32 v207, v207\n"
"v_mfma_f32_16x16x32_bf16 v[192:195], a[96:99], a[80:83], v[216:219]\n"
"v_cvt_pk_bf16_f32 v208, v200, v201\n"
"v_cvt_pk_bf16_f32 v209, v202, v203\n"
"v_mfma_f32_16x16x32_bf16 v[192:195], a[100:103], a[84:87], v[192:195]\n"
"v_cvt_pk_bf16_f32 v210, v204, v205\n"
"v_cvt_pk_bf16_f32 v211, v206, v207\n"
"v_mfma_f32_16x16x32_bf16 v[192:195], a[104:107], a[88:91], v[192:195]\n"
"v_mfma_f32_16x16x32_bf16 v[192:195], a[108:111], a[92:95], v[192:195]\n"
"v_mfma_f32_16x16x32_bf16 v[196:199], a[112:115], a[80:83], v[216:219]\n"
"v_mfma_f32_16x16x32_bf16 v[196:199], a[116:119], a[84:87], v[196:199]\n"
"v_mfma_f32_16x16x32_bf16 v[196:199], a[120:123], a[88:91], v[196:199]\n"
"s_waitcnt vmcnt(8) lgkmcnt(0)\n"
"s_barrier\n"
"s_cmp_lt_u32 s75, 4\n"
"s_cbranch_scc1 Lsl03%=\n"
"s_sleep 2\n"
"s_cmp_lt_u32 s75, 5\n"
"s_cbranch_scc1 Lsl03%=\n"
"s_sleep 2\n"
"s_cmp_lt_u32 s75, 6\n"
"s_cbranch_scc1 Lsl03%=\n"
"s_sleep 1\n"
"Lsl03%=:\n"
"v_mfma_f32_16x16x32_bf16 v[196:199], a[124:127], a[92:95], v[196:199]\n"
"s_add_u32 m0, s70, 0\n"
"v_mfma_f32_16x16x32_bf16 v[128:131], a[160:163], v[208:211], v[128:131]\n"
"buffer_load_dwordx4 v223, s[40:43], s60 offen offset:0 nt lds\n"
"v_mfma_f32_16x16x32_bf16 v[132:135], a[164:167], v[208:211], v[132:135]\n"
"v_exp_f32 v192, v192\n"
"v_mfma_f32_16x16x32_bf16 v[136:139], a[168:171], v[208:211], v[136:139]\n"
"v_exp_f32 v193, v193\n"
"v_mfma_f32_16x16x32_bf16 v[140:143], a[172:175], v[208:211], v[140:143]\n"
"v_exp_f32 v194, v194\n"
"v_mfma_f32_16x16x32_bf16 v[144:147], a[176:179], v[208:211], v[144:147]\n"
"v_exp_f32 v195, v195\n"
"v_mfma_f32_16x16x32_bf16 v[148:151], a[180:183], v[208:211], v[148:151]\n"
"v_exp_f32 v196, v196\n"
"v_mfma_f32_16x16x32_bf16 v[152:155], a[184:187], v[208:211], v[152:155]\n"
"v_exp_f32 v197, v197\n"
"v_mfma_f32_16x16x32_bf16 v[156:159], a[188:191], v[208:211], v[156:159]\n"
"v_exp_f32 v198, v198\n"
"v_mfma_f32_16x16x32_bf16 v[200:203], a[128:131], a[0:3], v[216:219]\n"
"v_exp_f32 v199, v199\n"
"v_mfma_f32_16x16x32_bf16 v[200:203], a[132:135], a[4:7], v[200:203]\n"
"v_cvt_pk_bf16_f32 v212, v192, v193\n"
"v_cvt_pk_bf16_f32 v213, v194, v195\n"
"v_mfma_f32_16x16x32_bf16 v[200:203], a[136:139], a[8:11], v[200:203]\n"
"v_cvt_pk_bf16_f32 v214, v196, v197\n"
"v_cvt_pk_bf16_f32 v215, v198, v199\n"
"v_mfma_f32_16x16x32_bf16 v[200:203], a[140:143], a[12:15], v[200:203]\n"
"buffer_load_dwordx4 v223, s[40:43], s61 offen offset:1024 nt lds\n"
"v_mfma_f32_16x16x32_bf16 v[204:207], a[144:147], a[0:3], v[216:219]\n"
"ds_read_b128 a[112:115], v220 offset:36864\n"
"v_mfma_f32_16x16x32_bf16 v[204:207], a[148:151], a[4:7], v[204:207]\n"
"ds_read_b128 a[116:119], v220 offset:37888\n"
"v_mfma_f32_16x16x32_bf16 v[204:207], a[152:155], a[8:11], v[204:207]\n"
"ds_read_b128 a[120:123], v220 offset:38912\n"
"v_mfma_f32_16x16x32_bf16 v[204:207], a[156:159], a[12:15], v[204:207]\n"
"ds_read_b128 a[124:127], v220 offset:39936\n"
"v_smfmac_f32_16x16x64_bf16 v[232:235], v[240:243], v[208:215], v244\n"
"v_mfma_f32_16x16x32_bf16 v[160:163], a[160:163], v[212:215], v[160:163]\n"
"v_exp_f32 v200, v200\n"
"v_mfma_f32_16x16x32_bf16 v[164:167], a[164:167], v[212:215], v[164:167]\n"
"v_exp_f32 v201, v201\n"
"v_mfma_f32_16x16x32_bf16 v[168:171], a[168:171], v[212:215], v[168:171]\n"
"v_exp_f32 v202, v202\n"
"v_mfma_f32_16x16x32_bf16 v[172:175], a[172:175], v[212:215], v[172:175]\n"
"v_exp_f32 v203, v203\n"
"v_mfma_f32_16x16x32_bf16 v[176:179], a[176:179], v[212:215], v[176:179]\n"
"v_exp_f32 v204, v204\n"
"v_mfma_f32_16x16x32_bf16 v[180:183], a[180:183], v[212:215], v[180:183]\n"
"v_exp_f32 v205, v205\n"
"v_mfma_f32_16x16x32_bf16 v[184:187], a[184:187], v[212:215], v[184:187]\n"
"v_exp_f32 v206, v206\n"
"v_mfma_f32_16x16x32_bf16 v[188:191], a[188:191], v[212:215], v[188:191]\n"
"v_exp_f32 v207, v207\n"
"v_mfma_f32_16x16x32_bf16 v[192:195], a[128:131], a[16:19], v[216:219]\n"
"v_cvt_pk_bf16_f32 v208, v200, v201\n"
"v_cvt_pk_bf16_f32 v209, v202, v203\n"
"v_mfma_f32_16x16x32_bf16 v[192:195], a[132:135], a[20:23], v[192:195]\n"
"v_cvt_pk_bf16_f32 v210, v204, v205\n"
"v_cvt_pk_bf16_f32 v211, v206, v207\n"
"v_mfma_f32_16x16x32_bf16 v[192:195], a[136:139], a[24:27], v[192:195]\n"
"buffer_load_dwordx4 v223, s[40:43], s62 offen offset:2048 nt lds\n"
"v_mfma_f32_16x16x32_bf16 v[192:195], a[140:143], a[28:31], v[192:195]\n"
"ds_read_b128 a[96:99], v220 offset:32768\n"
"v_mfma_f32_16x16x32_bf16 v[196:199], a[144:147], a[16:19], v[216:219]\n"
"ds_read_b128 a[100:103], v220 offset:33792\n"
"v_mfma_f32_16x16x32_bf16 v[196:199], a[148:151], a[20:23], v[196:199]\n"
"ds_read_b128 a[104:107], v220 offset:34816\n"
"v_mfma_f32_16x16x32_bf16 v[196:199], a[152:155], a[24:27], v[196:199]\n"
"ds_read_b128 a[108:111], v220 offset:35840\n"
"ds_read_b64_tr_b16 a[160:161], v221 offset:32768\n"
"v_mfma_f32_16x16x32_bf16 v[196:199], a[156:159], a[28:31], v[196:199]\n"
"buffer_load_dwordx4 v223, s[40:43], 0 offen offset:3072 nt lds\n"
"v_mfma_f32_16x16x32_bf16 v[0:3], a[192:195], v[208:211], v[0:3]\n"
"ds_read_b64_tr_b16 a[162:163], v221 offset:36864\n"
"ds_read_b64_tr_b16 a[164:165], v222 offset:32768\n"
"v_mfma_f32_16x16x32_bf16 v[4:7], a[196:199], v[208:211], v[4:7]\n"
"v_exp_f32 v192, v192\n"
"v_mfma_f32_16x16x32_bf16 v[8:11], a[200:203], v[208:211], v[8:11]\n"
"v_exp_f32 v193, v193\n"
"v_mfma_f32_16x16x32_bf16 v[12:15], a[204:207], v[208:211], v[12:15]\n"
"v_exp_f32 v194, v194\n"
"v_mfma_f32_16x16x32_bf16 v[16:19], a[208:211], v[208:211], v[16:19]\n"
"v_exp_f32 v195, v195\n"
"v_mfma_f32_16x16x32_bf16 v[20:23], a[212:215], v[208:211], v[20:23]\n"
"v_exp_f32 v196, v196\n"
"v_mfma_f32_16x16x32_bf16 v[24:27], a[216:219], v[208:211], v[24:27]\n"
"v_exp_f32 v197, v197\n"
"v_mfma_f32_16x16x32_bf16 v[28:31], a[220:223], v[208:211], v[28:31]\n"
"v_exp_f32 v198, v198\n"
"v_mfma_f32_16x16x32_bf16 v[200:203], a[128:131], a[32:35], v[216:219]\n"
"v_exp_f32 v199, v199\n"
"v_mfma_f32_16x16x32_bf16 v[200:203], a[132:135], a[36:39], v[200:203]\n"
"v_cvt_pk_bf16_f32 v212, v192, v193\n"
"v_cvt_pk_bf16_f32 v213, v194, v195\n"
"v_mfma_f32_16x16x32_bf16 v[200:203], a[136:139], a[40:43], v[200:203]\n"
"v_cvt_pk_bf16_f32 v214, v196, v197\n"
"v_cvt_pk_bf16_f32 v215, v198, v199\n"
"v_mfma_f32_16x16x32_bf16 v[200:203], a[140:143], a[44:47], v[200:203]\n"
"s_add_u32 m0, s70, 8192\n"
"ds_read_b64_tr_b16 a[166:167], v222 offset:36864\n"
"v_mfma_f32_16x16x32_bf16 v[204:207], a[144:147], a[32:35], v[216:219]\n"
"buffer_load_dwordx4 v223, s[44:47], s60 offen offset:0 nt lds\n"
"v_mfma_f32_16x16x32_bf16 v[204:207], a[148:151], a[36:39], v[204:207]\n"
"ds_read_b64_tr_b16 a[168:169], v221 offset:33792\n"
"ds_read_b64_tr_b16 a[170:171], v221 offset:37888\n"
"v_mfma_f32_16x16x32_bf16 v[204:207], a[152:155], a[40:43], v[204:207]\n"
"ds_read_b64_tr_b16 a[172:173], v222 offset:33792\n"
"ds_read_b64_tr_b16 a[174:175], v222 offset:37888\n"
"v_mfma_f32_16x16x32_bf16 v[204:207], a[156:159], a[44:47], v[204:207]\n"
"ds_read_b64_tr_b16 a[176:177], v221 offset:34816\n"
"ds_read_b64_tr_b16 a[178:179], v221 offset:38912\n"
"v_smfmac_f32_16x16x64_bf16 v[224:227], v[240:243], v[208:215], v244\n"
"ds_read_b64_tr_b16 a[180:181], v222 offset:34816\n"
"v_mfma_f32_16x16x32_bf16 v[32:35], a[192:195], v[212:215], v[32:35]\n"
"v_exp_f32 v200, v200\n"
"v_mfma_f32_16x16x32_bf16 v[36:39], a[196:199], v[212:215], v[36:39]\n"
"v_exp_f32 v201, v201\n"
"v_mfma_f32_16x16x32_bf16 v[40:43], a[200:203], v[212:215], v[40:43]\n"
"v_exp_f32 v202, v202\n"
"v_mfma_f32_16x16x32_bf16 v[44:47], a[204:207], v[212:215], v[44:47]\n"
"v_exp_f32 v203, v203\n"
"v_mfma_f32_16x16x32_bf16 v[48:51], a[208:211], v[212:215], v[48:51]\n"
"v_exp_f32 v204, v204\n"
"v_mfma_f32_16x16x32_bf16 v[52:55], a[212:215], v[212:215], v[52:55]\n"
"v_exp_f32 v205, v205\n"
"v_mfma_f32_16x16x32_bf16 v[56:59], a[216:219], v[212:215], v[56:59]\n"
"v_exp_f32 v206, v206\n"
"v_mfma_f32_16x16x32_bf16 v[60:63], a[220:223], v[212:215], v[60:63]\n"
"v_exp_f32 v207, v207\n"
"v_mfma_f32_16x16x32_bf16 v[192:195], a[128:131], a[48:51], v[216:219]\n"
"v_cvt_pk_bf16_f32 v208, v200, v201\n"
"v_cvt_pk_bf16_f32 v209, v202, v203\n"
"v_mfma_f32_16x16x32_bf16 v[192:195], a[132:135], a[52:55], v[192:195]\n"
"v_cvt_pk_bf16_f32 v210, v204, v205\n"
"v_cvt_pk_bf16_f32 v211, v206, v207\n"
"v_mfma_f32_16x16x32_bf16 v[192:195], a[136:139], a[56:59], v[192:195]\n"
"buffer_load_dwordx4 v223, s[44:47], s61 offen offset:1024 nt lds\n"
"v_mfma_f32_16x16x32_bf16 v[192:195], a[140:143], a[60:63], v[192:195]\n"
"ds_read_b64_tr_b16 a[182:183], v222 offset:38912\n"
"ds_read_b64_tr_b16 a[184:185], v221 offset:35840\n"
"v_mfma_f32_16x16x32_bf16 v[196:199], a[144:147], a[48:51], v[216:219]\n"
"ds_read_b64_tr_b16 a[186:187], v221 offset:39936\n"
"ds_read_b64_tr_b16 a[188:189], v222 offset:35840\n"
"v_mfma_f32_16x16x32_bf16 v[196:199], a[148:151], a[52:55], v[196:199]\n"
"ds_read_b64_tr_b16 a[190:191], v222 offset:39936\n"
"v_mfma_f32_16x16x32_bf16 v[196:199], a[152:155], a[56:59], v[196:199]\n"
"v_mfma_f32_16x16x32_bf16 v[196:199], a[156:159], a[60:63], v[196:199]\n"
"buffer_load_dwordx4 v223, s[44:47], s62 offen offset:2048 nt lds\n"
"v_mfma_f32_16x16x32_bf16 v[64:67], a[192:195], v[208:211], v[64:67]\n"
"v_mfma_f32_16x16x32_bf16 v[68:71], a[196:199], v[208:211], v[68:71]\n"
"v_exp_f32 v192, v192\n"
"v_mfma_f32_16x16x32_bf16 v[72:75], a[200:203], v[208:211], v[72:75]\n"
"v_exp_f32 v193, v193\n"
"v_mfma_f32_16x16x32_bf16 v[76:79], a[204:207], v[208:211], v[76:79]\n"
"v_exp_f32 v194, v194\n"
"v_mfma_f32_16x16x32_bf16 v[80:83], a[208:211], v[208:211], v[80:83]\n"
"v_exp_f32 v195, v195\n"
"v_mfma_f32_16x16x32_bf16 v[84:87], a[212:215], v[208:211], v[84:87]\n"
"v_exp_f32 v196, v196\n"
"v_mfma_f32_16x16x32_bf16 v[88:91], a[216:219], v[208:211], v[88:91]\n"
"v_exp_f32 v197, v197\n"
"v_mfma_f32_16x16x32_bf16 v[92:95], a[220:223], v[208:211], v[92:95]\n"
"v_exp_f32 v198, v198\n"
"v_mfma_f32_16x16x32_bf16 v[200:203], a[128:131], a[64:67], v[216:219]\n"
"v_exp_f32 v199, v199\n"
"v_mfma_f32_16x16x32_bf16 v[200:203], a[132:135], a[68:71], v[200:203]\n"
"v_cvt_pk_bf16_f32 v212, v192, v193\n"
"v_cvt_pk_bf16_f32 v213, v194, v195\n"
"v_mfma_f32_16x16x32_bf16 v[200:203], a[136:139], a[72:75], v[200:203]\n"
"v_cvt_pk_bf16_f32 v214, v196, v197\n"
"v_cvt_pk_bf16_f32 v215, v198, v199\n"
"v_mfma_f32_16x16x32_bf16 v[200:203], a[140:143], a[76:79], v[200:203]\n"
"buffer_load_dwordx4 v223, s[44:47], 0 offen offset:3072 nt lds\n"
"v_mfma_f32_16x16x32_bf16 v[204:207], a[144:147], a[64:67], v[216:219]\n"
"v_mfma_f32_16x16x32_bf16 v[204:207], a[148:151], a[68:71], v[204:207]\n"
"v_mfma_f32_16x16x32_bf16 v[204:207], a[152:155], a[72:75], v[204:207]\n"
"v_mfma_f32_16x16x32_bf16 v[204:207], a[156:159], a[76:79], v[204:207]\n"
"v_smfmac_f32_16x16x64_bf16 v[228:231], v[240:243], v[208:215], v244\n"
"v_add_u32 v223, 0x2000, v223\n"
"v_mfma_f32_16x16x32_bf16 v[96:99], a[192:195], v[212:215], v[96:99]\n"
"v_exp_f32 v200, v200\n"
"v_mfma_f32_16x16x32_bf16 v[100:103], a[196:199], v[212:215], v[100:103]\n"
"v_exp_f32 v201, v201\n"
"v_mfma_f32_16x16x32_bf16 v[104:107], a[200:203], v[212:215], v[104:107]\n"
"v_exp_f32 v202, v202\n"
"v_mfma_f32_16x16x32_bf16 v[108:111], a[204:207], v[212:215], v[108:111]\n"
"v_exp_f32 v203, v203\n"
"v_mfma_f32_16x16x32_bf16 v[112:115], a[208:211], v[212:215], v[112:115]\n"
"v_exp_f32 v204, v204\n"
"v_mfma_f32_16x16x32_bf16 v[116:119], a[212:215], v[212:215], v[116:119]\n"
"v_exp_f32 v205, v205\n"
"v_mfma_f32_16x16x32_bf16 v[120:123], a[216:219], v[212:215], v[120:123]\n"
"v_exp_f32 v206, v206\n"
"v_mfma_f32_16x16x32_bf16 v[124:127], a[220:223], v[212:215], v[124:127]\n"
"v_exp_f32 v207, v207\n"
"v_mfma_f32_16x16x32_bf16 v[192:195], a[128:131], a[80:83], v[216:219]\n"
"v_cvt_pk_bf16_f32 v208, v200, v201\n"
"v_cvt_pk_bf16_f32 v209, v202, v203\n"
"v_mfma_f32_16x16x32_bf16 v[192:195], a[132:135], a[84:87], v[192:195]\n"
"v_cvt_pk_bf16_f32 v210, v204, v205\n"
"v_cvt_pk_bf16_f32 v211, v206, v207\n"
"v_mfma_f32_16x16x32_bf16 v[192:195], a[136:139], a[88:91], v[192:195]\n"
"v_mfma_f32_16x16x32_bf16 v[192:195], a[140:143], a[92:95], v[192:195]\n"
"v_mfma_f32_16x16x32_bf16 v[196:199], a[144:147], a[80:83], v[216:219]\n"
"v_mfma_f32_16x16x32_bf16 v[196:199], a[148:151], a[84:87], v[196:199]\n"
"v_mfma_f32_16x16x32_bf16 v[196:199], a[152:155], a[88:91], v[196:199]\n"
"v_mfma_f32_16x16x32_bf16 v[196:199], a[156:159], a[92:95], v[196:199]\n"
"ds_read_b128 a[128:131], v245 offset:0\n"
"v_mfma_f32_16x16x32_bf16 v[128:131], a[192:195], v[208:211], v[128:131]\n"
"ds_read_b128 a[132:135], v245 offset:1024\n"
"v_mfma_f32_16x16x32_bf16 v[132:135], a[196:199], v[208:211], v[132:135]\n"
"v_exp_f32 v192, v192\n"
"v_mfma_f32_16x16x32_bf16 v[136:139], a[200:203], v[208:211], v[136:139]\n"
"v_exp_f32 v193, v193\n"
"v_mfma_f32_16x16x32_bf16 v[140:143], a[204:207], v[208:211], v[140:143]\n"
"v_exp_f32 v194, v194\n"
"v_mfma_f32_16x16x32_bf16 v[144:147], a[208:211], v[208:211], v[144:147]\n"
"v_exp_f32 v195, v195\n"
"v_mfma_f32_16x16x32_bf16 v[148:151], a[212:215], v[208:211], v[148:151]\n"
"v_exp_f32 v196, v196\n"
"v_mfma_f32_16x16x32_bf16 v[152:155], a[216:219], v[208:211], v[152:155]\n"
"v_exp_f32 v197, v197\n"
"v_mfma_f32_16x16x32_bf16 v[156:159], a[220:223], v[208:211], v[156:159]\n"
"v_exp_f32 v198, v198\n"
"s_waitcnt vmcnt(8) lgkmcnt(0)\n"
"s_barrier\n"
"s_cmp_lt_u32 s75, 4\n"
"s_cbranch_scc1 Lsl04%=\n"
"s_sleep 2\n"
"s_cmp_lt_u32 s75, 5\n"
"s_cbranch_scc1 Lsl04%=\n"
"s_sleep 2\n"
"s_cmp_lt_u32 s75, 6\n"
"s_cbranch_scc1 Lsl04%=\n"
"s_sleep 1\n"
"Lsl04%=:\n"
"s_sub_u32 s75, s75, 1\n"
"s_cbranch_scc0 La0%=\n"
"v_mfma_f32_16x16x32_bf16 v[200:203], a[96:99], a[0:3], v[216:219]\n"
"v_exp_f32 v199, v199\n"
"s_mov_b64 s[48:49], %8\n"
"s_mov_b32 s50, 8388608\n"
"v_mfma_f32_16x16x32_bf16 v[200:203], a[100:103], a[4:7], v[200:203]\n"
"v_cvt_pk_bf16_f32 v212, v192, v193\n"
"v_cvt_pk_bf16_f32 v213, v194, v195\n"
"s_and_b32 s72, s78, 7\n"
"s_and_b32 s80, s76, 1\n"
"v_mfma_f32_16x16x32_bf16 v[200:203], a[104:107], a[8:11], v[200:203]\n"
"v_cvt_pk_bf16_f32 v214, v196, v197\n"
"v_cvt_pk_bf16_f32 v215, v198, v199\n"
"s_lshr_b32 s81, s76, 1\n"
"s_lshl_b32 s66, s80, 15\n"
"v_mfma_f32_16x16x32_bf16 v[200:203], a[108:111], a[12:15], v[200:203]\n"
"ds_read_b128 a[128:131], v220 offset:49152\n"
"ds_read_b128 a[132:135], v220 offset:50176\n"
"s_lshl_b32 s82, s72, 8\n"
"s_add_u32 s66, s66, s82\n"
"v_mfma_f32_16x16x32_bf16 v[204:207], a[112:115], a[0:3], v[216:219]\n"
"ds_read_b128 a[136:139], v220 offset:51200\n"
"ds_read_b128 a[140:143], v220 offset:52224\n"
"s_add_u32 s67, s66, 8192\n"
"s_add_u32 s68, s67, 8192\n"
"v_mfma_f32_16x16x32_bf16 v[204:207], a[116:119], a[4:7], v[204:207]\n"
"ds_read_b128 a[144:147], v220 offset:53248\n"
"ds_read_b128 a[148:151], v220 offset:54272\n"
"s_add_u32 s69, s68, 8192\n"
"s_mul_i32 s71, s81, 67584\n"
"v_mfma_f32_16x16x32_bf16 v[204:207], a[120:123], a[8:11], v[204:207]\n"
"ds_read_b128 a[152:155], v220 offset:55296\n"
"ds_read_b128 a[156:159], v220 offset:56320\n"
"s_mul_i32 s80, s80, 4224\n"
"s_add_u32 s71, s71, s80\n"
"v_mfma_f32_16x16x32_bf16 v[204:207], a[124:127], a[12:15], v[204:207]\n"
"s_lshl_b32 s82, s79, 18\n"
"v_lshrrev_b32 v249, 8, v255\n"
"v_lshlrev_b32 v248, 11, v249\n"
"v_smfmac_f32_16x16x64_bf16 v[232:235], v[240:243], v[208:215], v244\n"
"v_lshlrev_b32 v249, 6, v249\n"
"v_mfma_f32_16x16x32_bf16 v[160:163], a[192:195], v[212:215], v[160:163]\n"
"v_exp_f32 v200, v200\n"
"v_mfma_f32_16x16x32_bf16 v[164:167], a[196:199], v[212:215], v[164:167]\n"
"v_exp_f32 v201, v201\n"
"v_mfma_f32_16x16x32_bf16 v[168:171], a[200:203], v[212:215], v[168:171]\n"
"v_exp_f32 v202, v202\n"
"v_mfma_f32_16x16x32_bf16 v[172:175], a[204:207], v[212:215], v[172:175]\n"
"v_exp_f32 v203, v203\n"
"v_mfma_f32_16x16x32_bf16 v[176:179], a[208:211], v[212:215], v[176:179]\n"
"v_exp_f32 v204, v204\n"
"v_mfma_f32_16x16x32_bf16 v[180:183], a[212:215], v[212:215], v[180:183]\n"
"v_exp_f32 v205, v205\n"
"v_mfma_f32_16x16x32_bf16 v[184:187], a[216:219], v[212:215], v[184:187]\n"
"v_exp_f32 v206, v206\n"
"v_mfma_f32_16x16x32_bf16 v[188:191], a[220:223], v[212:215], v[188:191]\n"
"v_exp_f32 v207, v207\n"
"v_mfma_f32_16x16x32_bf16 v[192:195], a[96:99], a[16:19], v[216:219]\n"
"v_cvt_pk_bf16_f32 v208, v200, v201\n"
"v_cvt_pk_bf16_f32 v209, v202, v203\n"
"v_mfma_f32_16x16x32_bf16 v[192:195], a[100:103], a[20:23], v[192:195]\n"
"v_cvt_pk_bf16_f32 v210, v204, v205\n"
"v_cvt_pk_bf16_f32 v211, v206, v207\n"
"v_mfma_f32_16x16x32_bf16 v[192:195], a[104:107], a[24:27], v[192:195]\n"
"ds_read_b64_tr_b16 a[192:193], v221 offset:49152\n"
"ds_read_b64_tr_b16 a[194:195], v221 offset:53248\n"
"v_mfma_f32_16x16x32_bf16 v[192:195], a[108:111], a[28:31], v[192:195]\n"
"ds_read_b64_tr_b16 a[196:197], v222 offset:49152\n"
"ds_read_b64_tr_b16 a[198:199], v222 offset:53248\n"
"v_mfma_f32_16x16x32_bf16 v[196:199], a[112:115], a[16:19], v[216:219]\n"
"ds_read_b64_tr_b16 a[200:201], v221 offset:50176\n"
"ds_read_b64_tr_b16 a[202:203], v221 offset:54272\n"
"v_mfma_f32_16x16x32_bf16 v[196:199], a[116:119], a[20:23], v[196:199]\n"
"ds_read_b64_tr_b16 a[204:205], v222 offset:50176\n"
"ds_read_b64_tr_b16 a[206:207], v222 offset:54272\n"
"v_mfma_f32_16x16x32_bf16 v[196:199], a[120:123], a[24:27], v[196:199]\n"
"ds_read_b64_tr_b16 a[208:209], v221 offset:51200\n"
"ds_read_b64_tr_b16 a[210:211], v221 offset:55296\n"
"v_mfma_f32_16x16x32_bf16 v[196:199], a[124:127], a[28:31], v[196:199]\n"
"ds_read_b64_tr_b16 a[212:213], v222 offset:51200\n"
"ds_read_b64_tr_b16 a[214:215], v222 offset:55296\n"
"v_mfma_f32_16x16x32_bf16 v[0:3], a[160:163], v[208:211], v[0:3]\n"
"ds_read_b64_tr_b16 a[216:217], v221 offset:52224\n"
"ds_read_b64_tr_b16 a[218:219], v221 offset:56320\n"
"v_mfma_f32_16x16x32_bf16 v[4:7], a[164:167], v[208:211], v[4:7]\n"
"v_exp_f32 v192, v192\n"
"v_mfma_f32_16x16x32_bf16 v[8:11], a[168:171], v[208:211], v[8:11]\n"
"v_exp_f32 v193, v193\n"
"v_mfma_f32_16x16x32_bf16 v[12:15], a[172:175], v[208:211], v[12:15]\n"
"v_exp_f32 v194, v194\n"
"v_mfma_f32_16x16x32_bf16 v[16:19], a[176:179], v[208:211], v[16:19]\n"
"v_exp_f32 v195, v195\n"
"v_mfma_f32_16x16x32_bf16 v[20:23], a[180:183], v[208:211], v[20:23]\n"
"v_exp_f32 v196, v196\n"
"v_mfma_f32_16x16x32_bf16 v[24:27], a[184:187], v[208:211], v[24:27]\n"
"v_exp_f32 v197, v197\n"
"v_mfma_f32_16x16x32_bf16 v[28:31], a[188:191], v[208:211], v[28:31]\n"
"v_exp_f32 v198, v198\n"
"s_waitcnt lgkmcnt(14)\n"
"v_mfma_f32_16x16x32_bf16 v[200:203], a[128:131], a[0:3], v[216:219]\n"
"v_exp_f32 v199, v199\n"
"v_mfma_f32_16x16x32_bf16 v[200:203], a[132:135], a[4:7], v[200:203]\n"
"v_cvt_pk_bf16_f32 v212, v192, v193\n"
"v_cvt_pk_bf16_f32 v213, v194, v195\n"
"v_mfma_f32_16x16x32_bf16 v[200:203], a[136:139], a[8:11], v[200:203]\n"
"v_cvt_pk_bf16_f32 v214, v196, v197\n"
"v_cvt_pk_bf16_f32 v215, v198, v199\n"
"v_mfma_f32_16x16x32_bf16 v[200:203], a[140:143], a[12:15], v[200:203]\n"
"ds_read_b64_tr_b16 a[220:221], v222 offset:52224\n"
"ds_read_b64_tr_b16 a[222:223], v222 offset:56320\n"
"v_mfma_f32_16x16x32_bf16 v[204:207], a[144:147], a[0:3], v[216:219]\n"
"v_add_u32 v248, s82, v248\n"
"v_xor_b32 v249, v249, v255\n"
"v_mfma_f32_16x16x32_bf16 v[204:207], a[148:151], a[4:7], v[204:207]\n"
"v_and_b32 v249, 0xf0, v249\n"
"v_add_u32 v248, v248, v249\n"
"s_lshl_b32 s82, s79, 12\n"
"v_mfma_f32_16x16x32_bf16 v[204:207], a[152:155], a[8:11], v[204:207]\n"
"v_add_u32 v249, s82, v255\n"
"s_and_b32 s80, s76, 1\n"
"s_mul_i32 s82, s77, 13\n"
"v_mfma_f32_16x16x32_bf16 v[204:207], a[156:159], a[12:15], v[204:207]\n"
"ds_read_b128 a[0:3], v245 offset:0\n"
"ds_read_b128 a[4:7], v245 offset:1024\n"
"s_mul_i32 s81, s80, 6\n"
"s_add_u32 s81, s81, s82\n"
"v_smfmac_f32_16x16x64_bf16 v[224:227], v[240:243], v[208:215], v244\n"
"ds_read_b128 a[8:11], v245 offset:2048\n"
"s_mul_i32 s60, s81, 132096\n"
"s_add_u32 s61, s60, 132096\n"
"v_mfma_f32_16x16x32_bf16 v[32:35], a[160:163], v[212:215], v[32:35]\n"
"v_exp_f32 v200, v200\n"
"s_add_u32 s62, s61, 132096\n"
"s_add_u32 s63, s62, 132096\n"
"v_mfma_f32_16x16x32_bf16 v[36:39], a[164:167], v[212:215], v[36:39]\n"
"v_exp_f32 v201, v201\n"
"s_add_u32 s64, s63, 132096\n"
"s_add_u32 s65, s64, 132096\n"
"v_mfma_f32_16x16x32_bf16 v[40:43], a[168:171], v[212:215], v[40:43]\n"
"v_exp_f32 v202, v202\n"
"s_add_u32 s82, s82, 12\n"
"s_mul_i32 s70, s82, 132096\n"
"s_waitcnt vmcnt(0)\n"
"s_barrier\n"
"v_mfma_f32_16x16x32_bf16 v[44:47], a[172:175], v[212:215], v[44:47]\n"
"v_exp_f32 v203, v203\n"
"s_add_u32 m0, s71, 0\n"
"v_mfma_f32_16x16x32_bf16 v[48:51], a[176:179], v[212:215], v[48:51]\n"
"v_exp_f32 v204, v204\n"
"buffer_load_dwordx4 v248, s[48:51], s66 offen  lds\n"
"v_mfma_f32_16x16x32_bf16 v[52:55], a[180:183], v[212:215], v[52:55]\n"
"v_exp_f32 v205, v205\n"
"v_mfma_f32_16x16x32_bf16 v[56:59], a[184:187], v[212:215], v[56:59]\n"
"v_exp_f32 v206, v206\n"
"s_add_u32 m0, s71, 1056\n"
"v_mfma_f32_16x16x32_bf16 v[60:63], a[188:191], v[212:215], v[60:63]\n"
"v_exp_f32 v207, v207\n"
"buffer_load_dwordx4 v248, s[48:51], s67 offen  lds\n"
"v_mfma_f32_16x16x32_bf16 v[192:195], a[128:131], a[16:19], v[216:219]\n"
"v_cvt_pk_bf16_f32 v208, v200, v201\n"
"v_cvt_pk_bf16_f32 v209, v202, v203\n"
"v_mfma_f32_16x16x32_bf16 v[192:195], a[132:135], a[20:23], v[192:195]\n"
"v_cvt_pk_bf16_f32 v210, v204, v205\n"
"v_cvt_pk_bf16_f32 v211, v206, v207\n"
"s_add_u32 m0, s71, 2112\n"
"v_mfma_f32_16x16x32_bf16 v[192:195], a[136:139], a[24:27], v[192:195]\n"
"ds_read_b128 a[12:15], v245 offset:3072\n"
"buffer_load_dwordx4 v248, s[48:51], s68 offen  lds\n"
"v_mfma_f32_16x16x32_bf16 v[192:195], a[140:143], a[28:31], v[192:195]\n"
"v_mfma_f32_16x16x32_bf16 v[196:199], a[144:147], a[16:19], v[216:219]\n"
"s_add_u32 m0, s71, 3168\n"
"v_mfma_f32_16x16x32_bf16 v[196:199], a[148:151], a[20:23], v[196:199]\n"
"buffer_load_dwordx4 v248, s[48:51], s69 offen  lds\n"
"v_mfma_f32_16x16x32_bf16 v[196:199], a[152:155], a[24:27], v[196:199]\n"
"v_mfma_f32_16x16x32_bf16 v[196:199], a[156:159], a[28:31], v[196:199]\n"
"v_xor_b32 v248, 0x10000, v248\n"
"s_add_u32 m0, s71, 8448\n"
"s_waitcnt lgkmcnt(4)\n"
"v_mfma_f32_16x16x32_bf16 v[0:3], a[192:195], v[208:211], v[0:3]\n"
"buffer_load_dwordx4 v248, s[48:51], s66 offen  lds\n"
"v_mfma_f32_16x16x32_bf16 v[4:7], a[196:199], v[208:211], v[4:7]\n"
"v_exp_f32 v192, v192\n"
"v_mfma_f32_16x16x32_bf16 v[8:11], a[200:203], v[208:211], v[8:11]\n"
"v_exp_f32 v193, v193\n"
"s_add_u32 m0, s71, 9504\n"
"v_mfma_f32_16x16x32_bf16 v[12:15], a[204:207], v[208:211], v[12:15]\n"
"v_exp_f32 v194, v194\n"
"buffer_load_dwordx4 v248, s[48:51], s67 offen  lds\n"
"v_mfma_f32_16x16x32_bf16 v[16:19], a[208:211], v[208:211], v[16:19]\n"
"v_exp_f32 v195, v195\n"
"v_mfma_f32_16x16x32_bf16 v[20:23], a[212:215], v[208:211], v[20:23]\n"
"v_exp_f32 v196, v196\n"
"s_add_u32 m0, s71, 10560\n"
"v_mfma_f32_16x16x32_bf16 v[24:27], a[216:219], v[208:211], v[24:27]\n"
"v_exp_f32 v197, v197\n"
"buffer_load_dwordx4 v248, s[48:51], s68 offen  lds\n"
"v_mfma_f32_16x16x32_bf16 v[28:31], a[220:223], v[208:211], v[28:31]\n"
"v_exp_f32 v198, v198\n"
"s_waitcnt lgkmcnt(0)\n"
"v_mfma_f32_16x16x32_bf16 v[200:203], a[96:99], a[0:3], v[216:219]\n"
"v_exp_f32 v199, v199\n"
"s_add_u32 m0, s71, 11616\n"
"v_mfma_f32_16x16x32_bf16 v[200:203], a[100:103], a[4:7], v[200:203]\n"
"v_cvt_pk_bf16_f32 v212, v192, v193\n"
"v_cvt_pk_bf16_f32 v213, v194, v195\n"
"buffer_load_dwordx4 v248, s[48:51], s69 offen  lds\n"
"v_mfma_f32_16x16x32_bf16 v[200:203], a[104:107], a[8:11], v[200:203]\n"
"v_cvt_pk_bf16_f32 v214, v196, v197\n"
"v_cvt_pk_bf16_f32 v215, v198, v199\n"
"v_mfma_f32_16x16x32_bf16 v[200:203], a[108:111], a[12:15], v[200:203]\n"
"v_xor_b32 v248, 0x30000, v248\n"
"s_add_u32 m0, s71, 16896\n"
"v_mfma_f32_16x16x32_bf16 v[204:207], a[112:115], a[0:3], v[216:219]\n"
"buffer_load_dwordx4 v248, s[48:51], s66 offen  lds\n"
"v_mfma_f32_16x16x32_bf16 v[204:207], a[116:119], a[4:7], v[204:207]\n"
"v_mfma_f32_16x16x32_bf16 v[204:207], a[120:123], a[8:11], v[204:207]\n"
"s_add_u32 m0, s71, 17952\n"
"v_mfma_f32_16x16x32_bf16 v[204:207], a[124:127], a[12:15], v[204:207]\n"
"buffer_load_dwordx4 v248, s[48:51], s67 offen  lds\n"
"v_smfmac_f32_16x16x64_bf16 v[224:227], v[240:243], v[208:215], v244\n"
"v_mfma_f32_16x16x32_bf16 v[32:35], a[192:195], v[212:215], v[32:35]\n"
"v_exp_f32 v200, v200\n"
"s_add_u32 m0, s71, 19008\n"
"v_mfma_f32_16x16x32_bf16 v[36:39], a[196:199], v[212:215], v[36:39]\n"
"v_exp_f32 v201, v201\n"
"buffer_load_dwordx4 v248, s[48:51], s68 offen  lds\n"
"v_mfma_f32_16x16x32_bf16 v[40:43], a[200:203], v[212:215], v[40:43]\n"
"v_exp_f32 v202, v202\n"
"v_mfma_f32_16x16x32_bf16 v[44:47], a[204:207], v[212:215], v[44:47]\n"
"v_exp_f32 v203, v203\n"
"s_add_u32 m0, s71, 20064\n"
"v_add_f32 v225, v224, v225\n"
"v_mfma_f32_16x16x32_bf16 v[48:51], a[208:211], v[212:215], v[48:51]\n"
"v_exp_f32 v204, v204\n"
"buffer_load_dwordx4 v248, s[48:51], s69 offen  lds\n"
"v_add_f32 v227, v226, v227\n"
"v_mfma_f32_16x16x32_bf16 v[52:55], a[212:215], v[212:215], v[52:55]\n"
"v_exp_f32 v205, v205\n"
"v_mfma_f32_16x16x32_bf16 v[56:59], a[216:219], v[212:215], v[56:59]\n"
"v_exp_f32 v206, v206\n"
"v_mfma_f32_16x16x32_bf16 v[60:63], a[220:223], v[212:215], v[60:63]\n"
"v_exp_f32 v207, v207\n"
"v_mfma_f32_16x16x32_bf16 v[192:195], a[96:99], a[32:35], v[216:219]\n"
"v_cvt_pk_bf16_f32 v212, v200, v201\n"
"v_cvt_pk_bf16_f32 v213, v202, v203\n"
"v_mfma_f32_16x16x32_bf16 v[192:195], a[100:103], a[36:39], v[192:195]\n"
"v_cvt_pk_bf16_f32 v214, v204, v205\n"
"v_cvt_pk_bf16_f32 v215, v206, v207\n"
"v_mfma_f32_16x16x32_bf16 v[192:195], a[104:107], a[40:43], v[192:195]\n"
"v_xor_b32 v248, 0x10000, v248\n"
"s_add_u32 m0, s71, 25344\n"
"v_rcp_f32 v224, v225\n"
"v_mfma_f32_16x16x32_bf16 v[192:195], a[108:111], a[44:47], v[192:195]\n"
"buffer_load_dwordx4 v248, s[48:51], s66 offen  lds\n"
"v_rcp_f32 v226, v227\n"
"v_mfma_f32_16x16x32_bf16 v[196:199], a[112:115], a[32:35], v[216:219]\n"
"v_mul_f32 v0, v224, v0\n"
"v_mul_f32 v1, v224, v1\n"
"v_mul_f32 v2, v224, v2\n"
"v_mfma_f32_16x16x32_bf16 v[196:199], a[116:119], a[36:39], v[196:199]\n"
"s_add_u32 m0, s71, 26400\n"
"v_mul_f32 v3, v224, v3\n"
"v_mul_f32 v4, v224, v4\n"
"v_mul_f32 v5, v224, v5\n"
"v_mfma_f32_16x16x32_bf16 v[196:199], a[120:123], a[40:43], v[196:199]\n"
"buffer_load_dwordx4 v248, s[48:51], s67 offen  lds\n"
"v_mul_f32 v6, v224, v6\n"
"v_mul_f32 v7, v224, v7\n"
"v_cvt_pk_bf16_f32 v0, v0, v1\n"
"v_mfma_f32_16x16x32_bf16 v[196:199], a[124:127], a[44:47], v[196:199]\n"
"v_cvt_pk_bf16_f32 v1, v2, v3\n"
"v_cvt_pk_bf16_f32 v2, v4, v5\n"
"v_cvt_pk_bf16_f32 v3, v6, v7\n"
"v_smfmac_f32_16x16x64_bf16 v[236:239], v[240:243], v[208:215], v244\n"
"s_add_u32 m0, s71, 27456\n"
"buffer_store_dwordx4 v[0:3], v249, s[56:59], s60 offen offset:0 sc1\n"
"v_mul_f32 v8, v224, v8\n"
"v_mfma_f32_16x16x32_bf16 a[224:227], a[160:163], v[212:215], a[224:227]\n"
"v_exp_f32 v192, v192\n"
"buffer_load_dwordx4 v248, s[48:51], s68 offen  lds\n"
"v_mul_f32 v9, v224, v9\n"
"v_mfma_f32_16x16x32_bf16 a[228:231], a[164:167], v[212:215], a[228:231]\n"
"v_exp_f32 v193, v193\n"
"v_mul_f32 v10, v224, v10\n"
"v_mfma_f32_16x16x32_bf16 a[232:235], a[168:171], v[212:215], a[232:235]\n"
"v_exp_f32 v194, v194\n"
"s_add_u32 m0, s71, 28512\n"
"v_mul_f32 v11, v224, v11\n"
"v_mfma_f32_16x16x32_bf16 a[236:239], a[172:175], v[212:215], a[236:239]\n"
"v_exp_f32 v195, v195\n"
"buffer_load_dwordx4 v248, s[48:51], s69 offen  lds\n"
"v_mul_f32 v12, v224, v12\n"
"v_mfma_f32_16x16x32_bf16 a[240:243], a[176:179], v[212:215], a[240:243]\n"
"v_exp_f32 v196, v196\n"
"v_mul_f32 v13, v224, v13\n"
"v_mfma_f32_16x16x32_bf16 a[244:247], a[180:183], v[212:215], a[244:247]\n"
"v_exp_f32 v197, v197\n"
"s_sub_u32 s73, 0, s72\n"
"s_cmp_eq_u32 s72, 0\n"
"v_mul_f32 v14, v224, v14\n"
"v_mfma_f32_16x16x32_bf16 a[248:251], a[184:187], v[212:215], a[248:251]\n"
"v_exp_f32 v198, v198\n"
"s_cselect_b32 s73, 1, s73\n"
"v_mul_f32 v15, v224, v15\n"
"v_mfma_f32_16x16x32_bf16 a[252:255], a[188:191], v[212:215], a[252:255]\n"
"v_exp_f32 v199, v199\n"
"v_cvt_pk_bf16_f32 v8, v8, v9\n"
"v_mfma_f32_16x16x32_bf16 v[200:203], a[96:99], a[48:51], v[216:219]\n"
"v_cvt_pk_bf16_f32 v208, v192, v193\n"
"v_cvt_pk_bf16_f32 v209, v194, v195\n"
"v_cvt_pk_bf16_f32 v9, v10, v11\n"
"v_mfma_f32_16x16x32_bf16 v[200:203], a[100:103], a[52:55], v[200:203]\n"
"v_cvt_pk_bf16_f32 v210, v196, v197\n"
"v_cvt_pk_bf16_f32 v211, v198, v199\n"
"v_cvt_pk_bf16_f32 v10, v12, v13\n"
"v_mfma_f32_16x16x32_bf16 v[200:203], a[104:107], a[56:59], v[200:203]\n"
"v_lshl_add_u32 v248, s73, 19, v248\n"
"s_add_u32 s73, s77, s78\n"
"s_and_b32 s73, s73, 3\n"
"v_cvt_pk_bf16_f32 v11, v14, v15\n"
"v_mul_f32 v16, v224, v16\n"
"v_mfma_f32_16x16x32_bf16 v[200:203], a[108:111], a[60:63], v[200:203]\n"
"s_xor_b32 s73, s73, 3\n"
"s_lshl_b32 s73, s73, 16\n"
"v_xor_b32 v248, s73, v248\n"
"buffer_store_dwordx4 v[8:11], v249, s[56:59], s60 offen offset:1024 sc1\n"
"v_mul_f32 v17, v224, v17\n"
"v_mfma_f32_16x16x32_bf16 v[204:207], a[112:115], a[48:51], v[216:219]\n"
"v_mul_f32 v18, v224, v18\n"
"v_mul_f32 v19, v224, v19\n"
"v_mul_f32 v20, v224, v20\n"
"v_mfma_f32_16x16x32_bf16 v[204:207], a[116:119], a[52:55], v[204:207]\n"
"v_mul_f32 v21, v224, v21\n"
"v_mul_f32 v22, v224, v22\n"
"v_mul_f32 v23, v224, v23\n"
"v_mfma_f32_16x16x32_bf16 v[204:207], a[120:123], a[56:59], v[204:207]\n"
"v_cvt_pk_bf16_f32 v16, v16, v17\n"
"v_cvt_pk_bf16_f32 v17, v18, v19\n"
"v_cvt_pk_bf16_f32 v18, v20, v21\n"
"v_mfma_f32_16x16x32_bf16 v[204:207], a[124:127], a[60:63], v[204:207]\n"
"v_cvt_pk_bf16_f32 v19, v22, v23\n"
"v_mul_f32 v24, v224, v24\n"
"v_mul_f32 v25, v224, v25\n"
"v_mfma_f32_16x16x32_bf16 v[64:67], a[160:163], v[208:211], v[64:67]\n"
"v_mul_f32 v26, v224, v26\n"
"v_mul_f32 v27, v224, v27\n"
"v_mul_f32 v28, v224, v28\n"
"v_mfma_f32_16x16x32_bf16 v[68:71], a[164:167], v[208:211], v[68:71]\n"
"v_exp_f32 v200, v200\n"
"v_mul_f32 v29, v224, v29\n"
"v_mfma_f32_16x16x32_bf16 v[72:75], a[168:171], v[208:211], v[72:75]\n"
"v_exp_f32 v201, v201\n"
"v_mul_f32 v30, v224, v30\n"
"v_mfma_f32_16x16x32_bf16 v[76:79], a[172:175], v[208:211], v[76:79]\n"
"v_exp_f32 v202, v202\n"
"v_mul_f32 v31, v224, v31\n"
"v_mfma_f32_16x16x32_bf16 v[80:83], a[176:179], v[208:211], v[80:83]\n"
"v_exp_f32 v203, v203\n"
"v_cvt_pk_bf16_f32 v24, v24, v25\n"
"v_mfma_f32_16x16x32_bf16 v[84:87], a[180:183], v[208:211], v[84:87]\n"
"v_exp_f32 v204, v204\n"
"v_cvt_pk_bf16_f32 v25, v26, v27\n"
"v_mfma_f32_16x16x32_bf16 v[88:91], a[184:187], v[208:211], v[88:91]\n"
"v_exp_f32 v205, v205\n"
"v_cvt_pk_bf16_f32 v26, v28, v29\n"
"v_mfma_f32_16x16x32_bf16 v[92:95], a[188:191], v[208:211], v[92:95]\n"
"v_exp_f32 v206, v206\n"
"buffer_store_dwordx4 v[16:19], v249, s[56:59], s60 offen offset:2048 sc1\n"
"v_mfma_f32_16x16x32_bf16 v[192:195], a[128:131], a[32:35], v[216:219]\n"
"v_exp_f32 v207, v207\n"
"v_cvt_pk_bf16_f32 v27, v30, v31\n"
"v_mfma_f32_16x16x32_bf16 v[192:195], a[132:135], a[36:39], v[192:195]\n"
"v_cvt_pk_bf16_f32 v212, v200, v201\n"
"v_cvt_pk_bf16_f32 v213, v202, v203\n"
"v_mul_f32 v32, v226, v32\n"
"v_mfma_f32_16x16x32_bf16 v[192:195], a[136:139], a[40:43], v[192:195]\n"
"v_cvt_pk_bf16_f32 v214, v204, v205\n"
"v_cvt_pk_bf16_f32 v215, v206, v207\n"
"v_mul_f32 v33, v226, v33\n"
"v_mfma_f32_16x16x32_bf16 v[192:195], a[140:143], a[44:47], v[192:195]\n"
"v_mul_f32 v34, v226, v34\n"
"v_mul_f32 v35, v226, v35\n"
"v_mul_f32 v36, v226, v36\n"
"v_mfma_f32_16x16x32_bf16 v[196:199], a[144:147], a[32:35], v[216:219]\n"
"v_mul_f32 v37, v226, v37\n"
"v_mul_f32 v38, v226, v38\n"
"v_mul_f32 v39, v226, v39\n"
"v_mfma_f32_16x16x32_bf16 v[196:199], a[148:151], a[36:39], v[196:199]\n"
"v_cvt_pk_bf16_f32 v32, v32, v33\n"
"v_cvt_pk_bf16_f32 v33, v34, v35\n"
"v_cvt_pk_bf16_f32 v34, v36, v37\n"
"v_mfma_f32_16x16x32_bf16 v[196:199], a[152:155], a[40:43], v[196:199]\n"
"v_cvt_pk_bf16_f32 v35, v38, v39\n"
"v_mul_f32 v40, v226, v40\n"
"v_mul_f32 v41, v226, v41\n"
"v_mfma_f32_16x16x32_bf16 v[196:199], a[156:159], a[44:47], v[196:199]\n"
"v_mul_f32 v42, v226, v42\n"
"v_mul_f32 v43, v226, v43\n"
"v_mul_f32 v44, v226, v44\n"
"v_smfmac_f32_16x16x64_bf16 v[228:231], v[240:243], v[208:215], v244\n"
"v_mul_f32 v45, v226, v45\n"
"v_mul_f32 v46, v226, v46\n"
"v_mfma_f32_16x16x32_bf16 v[96:99], a[160:163], v[212:215], v[96:99]\n"
"v_exp_f32 v192, v192\n"
"v_mul_f32 v47, v226, v47\n"
"v_mfma_f32_16x16x32_bf16 v[100:103], a[164:167], v[212:215], v[100:103]\n"
"v_exp_f32 v193, v193\n"
"v_cvt_pk_bf16_f32 v40, v40, v41\n"
"v_mfma_f32_16x16x32_bf16 v[104:107], a[168:171], v[212:215], v[104:107]\n"
"v_exp_f32 v194, v194\n"
"buffer_store_dwordx4 v[24:27], v249, s[56:59], s60 offen offset:3072 sc1\n"
"v_mfma_f32_16x16x32_bf16 v[108:111], a[172:175], v[212:215], v[108:111]\n"
"v_exp_f32 v195, v195\n"
"v_cvt_pk_bf16_f32 v41, v42, v43\n"
"v_mfma_f32_16x16x32_bf16 v[112:115], a[176:179], v[212:215], v[112:115]\n"
"v_exp_f32 v196, v196\n"
"v_cvt_pk_bf16_f32 v42, v44, v45\n"
"v_mfma_f32_16x16x32_bf16 v[116:119], a[180:183], v[212:215], v[116:119]\n"
"v_exp_f32 v197, v197\n"
"v_cvt_pk_bf16_f32 v43, v46, v47\n"
"v_mfma_f32_16x16x32_bf16 v[120:123], a[184:187], v[212:215], v[120:123]\n"
"v_exp_f32 v198, v198\n"
"v_mul_f32 v48, v226, v48\n"
"v_mfma_f32_16x16x32_bf16 v[124:127], a[188:191], v[212:215], v[124:127]\n"
"v_exp_f32 v199, v199\n"
"v_mul_f32 v49, v226, v49\n"
"v_mfma_f32_16x16x32_bf16 v[200:203], a[128:131], a[48:51], v[216:219]\n"
"v_cvt_pk_bf16_f32 v208, v192, v193\n"
"v_cvt_pk_bf16_f32 v209, v194, v195\n"
"v_mul_f32 v50, v226, v50\n"
"v_mfma_f32_16x16x32_bf16 v[200:203], a[132:135], a[52:55], v[200:203]\n"
"v_cvt_pk_bf16_f32 v210, v196, v197\n"
"v_cvt_pk_bf16_f32 v211, v198, v199\n"
"v_mul_f32 v51, v226, v51\n"
"v_mfma_f32_16x16x32_bf16 v[200:203], a[136:139], a[56:59], v[200:203]\n"
"v_mul_f32 v52, v226, v52\n"
"v_mul_f32 v53, v226, v53\n"
"v_mul_f32 v54, v226, v54\n"
"v_mfma_f32_16x16x32_bf16 v[200:203], a[140:143], a[60:63], v[200:203]\n"
"v_mul_f32 v55, v226, v55\n"
"v_cvt_pk_bf16_f32 v48, v48, v49\n"
"v_cvt_pk_bf16_f32 v49, v50, v51\n"
"v_mfma_f32_16x16x32_bf16 v[204:207], a[144:147], a[48:51], v[216:219]\n"
"v_cvt_pk_bf16_f32 v50, v52, v53\n"
"v_cvt_pk_bf16_f32 v51, v54, v55\n"
"v_mul_f32 v56, v226, v56\n"
"v_mfma_f32_16x16x32_bf16 v[204:207], a[148:151], a[52:55], v[204:207]\n"
"v_mul_f32 v57, v226, v57\n"
"v_mul_f32 v58, v226, v58\n"
"v_mul_f32 v59, v226, v59\n"
"v_mfma_f32_16x16x32_bf16 v[204:207], a[152:155], a[56:59], v[204:207]\n"
"buffer_store_dwordx4 v[32:35], v249, s[56:59], s61 offen offset:0 sc1\n"
"v_mul_f32 v60, v226, v60\n"
"v_mul_f32 v61, v226, v61\n"
"v_mfma_f32_16x16x32_bf16 v[204:207], a[156:159], a[60:63], v[204:207]\n"
"v_mul_f32 v62, v226, v62\n"
"v_mul_f32 v63, v226, v63\n"
"v_cvt_pk_bf16_f32 v56, v56, v57\n"
"v_mfma_f32_16x16x32_bf16 v[64:67], a[192:195], v[208:211], v[64:67]\n"
"v_cvt_pk_bf16_f32 v57, v58, v59\n"
"v_cvt_pk_bf16_f32 v58, v60, v61\n"
"v_cvt_pk_bf16_f32 v59, v62, v63\n"
"v_mfma_f32_16x16x32_bf16 v[68:71], a[196:199], v[208:211], v[68:71]\n"
"v_exp_f32 v200, v200\n"
"v_mfma_f32_16x16x32_bf16 v[72:75], a[200:203], v[208:211], v[72:75]\n"
"v_exp_f32 v201, v201\n"
"v_mfma_f32_16x16x32_bf16 v[76:79], a[204:207], v[208:211], v[76:79]\n"
"v_exp_f32 v202, v202\n"
"v_mfma_f32_16x16x32_bf16 v[80:83], a[208:211], v[208:211], v[80:83]\n"
"v_exp_f32 v203, v203\n"
"v_mfma_f32_16x16x32_bf16 v[84:87], a[212:215], v[208:211], v[84:87]\n"
"v_exp_f32 v204, v204\n"
"v_mfma_f32_16x16x32_bf16 v[88:91], a[216:219], v[208:211], v[88:91]\n"
"v_exp_f32 v205, v205\n"
"v_mfma_f32_16x16x32_bf16 v[92:95], a[220:223], v[208:211], v[92:95]\n"
"v_exp_f32 v206, v206\n"
"v_mfma_f32_16x16x32_bf16 v[192:195], a[96:99], a[64:67], v[216:219]\n"
"v_exp_f32 v207, v207\n"
"v_mfma_f32_16x16x32_bf16 v[192:195], a[100:103], a[68:71], v[192:195]\n"
"v_cvt_pk_bf16_f32 v212, v200, v201\n"
"v_cvt_pk_bf16_f32 v213, v202, v203\n"
"v_mfma_f32_16x16x32_bf16 v[192:195], a[104:107], a[72:75], v[192:195]\n"
"v_cvt_pk_bf16_f32 v214, v204, v205\n"
"v_cvt_pk_bf16_f32 v215, v206, v207\n"
"buffer_store_dwordx4 v[40:43], v249, s[56:59], s61 offen offset:1024 sc1\n"
"v_mfma_f32_16x16x32_bf16 v[192:195], a[108:111], a[76:79], v[192:195]\n"
"v_mfma_f32_16x16x32_bf16 v[196:199], a[112:115], a[64:67], v[216:219]\n"
"v_mfma_f32_16x16x32_bf16 v[196:199], a[116:119], a[68:71], v[196:199]\n"
"v_mfma_f32_16x16x32_bf16 v[196:199], a[120:123], a[72:75], v[196:199]\n"
"v_mfma_f32_16x16x32_bf16 v[196:199], a[124:127], a[76:79], v[196:199]\n"
"v_smfmac_f32_16x16x64_bf16 v[228:231], v[240:243], v[208:215], v244\n"
"v_mfma_f32_16x16x32_bf16 v[96:99], a[192:195], v[212:215], v[96:99]\n"
"v_exp_f32 v192, v192\n"
"v_mfma_f32_16x16x32_bf16 v[100:103], a[196:199], v[212:215], v[100:103]\n"
"v_exp_f32 v193, v193\n"
"v_mfma_f32_16x16x32_bf16 v[104:107], a[200:203], v[212:215], v[104:107]\n"
"v_exp_f32 v194, v194\n"
"v_mfma_f32_16x16x32_bf16 v[108:111], a[204:207], v[212:215], v[108:111]\n"
"v_exp_f32 v195, v195\n"
"v_add_f32 v229, v228, v229\n"
"v_mfma_f32_16x16x32_bf16 v[112:115], a[208:211], v[212:215], v[112:115]\n"
"v_exp_f32 v196, v196\n"
"v_add_f32 v231, v230, v231\n"
"v_mfma_f32_16x16x32_bf16 v[116:119], a[212:215], v[212:215], v[116:119]\n"
"v_exp_f32 v197, v197\n"
"buffer_store_dwordx4 v[48:51], v249, s[56:59], s61 offen offset:2048 sc1\n"
"v_mfma_f32_16x16x32_bf16 v[120:123], a[216:219], v[212:215], v[120:123]\n"
"v_exp_f32 v198, v198\n"
"v_mfma_f32_16x16x32_bf16 v[124:127], a[220:223], v[212:215], v[124:127]\n"
"v_exp_f32 v199, v199\n"
"v_mfma_f32_16x16x32_bf16 v[200:203], a[96:99], a[80:83], v[216:219]\n"
"v_cvt_pk_bf16_f32 v208, v192, v193\n"
"v_cvt_pk_bf16_f32 v209, v194, v195\n"
"v_mfma_f32_16x16x32_bf16 v[200:203], a[100:103], a[84:87], v[200:203]\n"
"v_cvt_pk_bf16_f32 v210, v196, v197\n"
"v_cvt_pk_bf16_f32 v211, v198, v199\n"
"v_mfma_f32_16x16x32_bf16 v[200:203], a[104:107], a[88:91], v[200:203]\n"
"v_rcp_f32 v228, v229\n"
"v_mfma_f32_16x16x32_bf16 v[200:203], a[108:111], a[92:95], v[200:203]\n"
"v_rcp_f32 v230, v231\n"
"v_mfma_f32_16x16x32_bf16 v[204:207], a[112:115], a[80:83], v[216:219]\n"
"v_mul_f32 v64, v228, v64\n"
"v_mul_f32 v65, v228, v65\n"
"v_mul_f32 v66, v228, v66\n"
"v_mfma_f32_16x16x32_bf16 v[204:207], a[116:119], a[84:87], v[204:207]\n"
"v_mul_f32 v67, v228, v67\n"
"v_mul_f32 v68, v228, v68\n"
"v_mul_f32 v69, v228, v69\n"
"v_mfma_f32_16x16x32_bf16 v[204:207], a[120:123], a[88:91], v[204:207]\n"
"v_mul_f32 v70, v228, v70\n"
"v_mul_f32 v71, v228, v71\n"
"v_cvt_pk_bf16_f32 v64, v64, v65\n"
"v_mfma_f32_16x16x32_bf16 v[204:207], a[124:127], a[92:95], v[204:207]\n"
"v_cvt_pk_bf16_f32 v65, v66, v67\n"
"v_cvt_pk_bf16_f32 v66, v68, v69\n"
"v_cvt_pk_bf16_f32 v67, v70, v71\n"
"v_mfma_f32_16x16x32_bf16 v[128:131], a[160:163], v[208:211], v[128:131]\n"
"v_mul_f32 v72, v228, v72\n"
"v_mul_f32 v73, v228, v73\n"
"v_mul_f32 v74, v228, v74\n"
"v_mfma_f32_16x16x32_bf16 v[132:135], a[164:167], v[208:211], v[132:135]\n"
"v_exp_f32 v200, v200\n"
"buffer_store_dwordx4 v[56:59], v249, s[56:59], s61 offen offset:3072 sc1\n"
"v_mfma_f32_16x16x32_bf16 v[136:139], a[168:171], v[208:211], v[136:139]\n"
"v_exp_f32 v201, v201\n"
"v_mul_f32 v75, v228, v75\n"
"v_mfma_f32_16x16x32_bf16 v[140:143], a[172:175], v[208:211], v[140:143]\n"
"v_exp_f32 v202, v202\n"
"v_mul_f32 v76, v228, v76\n"
"v_mfma_f32_16x16x32_bf16 v[144:147], a[176:179], v[208:211], v[144:147]\n"
"v_exp_f32 v203, v203\n"
"v_mul_f32 v77, v228, v77\n"
"v_mfma_f32_16x16x32_bf16 v[148:151], a[180:183], v[208:211], v[148:151]\n"
"v_exp_f32 v204, v204\n"
"v_mul_f32 v78, v228, v78\n"
"v_mfma_f32_16x16x32_bf16 v[152:155], a[184:187], v[208:211], v[152:155]\n"
"v_exp_f32 v205, v205\n"
"v_mul_f32 v79, v228, v79\n"
"v_mfma_f32_16x16x32_bf16 v[156:159], a[188:191], v[208:211], v[156:159]\n"
"v_exp_f32 v206, v206\n"
"v_cvt_pk_bf16_f32 v72, v72, v73\n"
"v_mfma_f32_16x16x32_bf16 v[192:195], a[128:131], a[64:67], v[216:219]\n"
"v_exp_f32 v207, v207\n"
"v_cvt_pk_bf16_f32 v73, v74, v75\n"
"v_mfma_f32_16x16x32_bf16 v[192:195], a[132:135], a[68:71], v[192:195]\n"
"v_cvt_pk_bf16_f32 v212, v200, v201\n"
"v_cvt_pk_bf16_f32 v213, v202, v203\n"
"v_cvt_pk_bf16_f32 v74, v76, v77\n"
"v_mfma_f32_16x16x32_bf16 v[192:195], a[136:139], a[72:75], v[192:195]\n"
"v_cvt_pk_bf16_f32 v214, v204, v205\n"
"v_cvt_pk_bf16_f32 v215, v206, v207\n"
"v_cvt_pk_bf16_f32 v75, v78, v79\n"
"v_mfma_f32_16x16x32_bf16 v[192:195], a[140:143], a[76:79], v[192:195]\n"
"v_mul_f32 v80, v228, v80\n"
"v_mul_f32 v81, v228, v81\n"
"v_mul_f32 v82, v228, v82\n"
"v_mfma_f32_16x16x32_bf16 v[196:199], a[144:147], a[64:67], v[216:219]\n"
"v_mul_f32 v83, v228, v83\n"
"v_mul_f32 v84, v228, v84\n"
"v_mul_f32 v85, v228, v85\n"
"v_mfma_f32_16x16x32_bf16 v[196:199], a[148:151], a[68:71], v[196:199]\n"
"buffer_store_dwordx4 v[64:67], v249, s[56:59], s62 offen offset:0 sc1\n"
"v_mul_f32 v86, v228, v86\n"
"v_mul_f32 v87, v228, v87\n"
"v_mfma_f32_16x16x32_bf16 v[196:199], a[152:155], a[72:75], v[196:199]\n"
"v_cvt_pk_bf16_f32 v80, v80, v81\n"
"v_cvt_pk_bf16_f32 v81, v82, v83\n"
"v_cvt_pk_bf16_f32 v82, v84, v85\n"
"v_mfma_f32_16x16x32_bf16 v[196:199], a[156:159], a[76:79], v[196:199]\n"
"v_cvt_pk_bf16_f32 v83, v86, v87\n"
"v_smfmac_f32_16x16x64_bf16 v[232:235], v[240:243], v[208:215], v244\n"
"v_mfma_f32_16x16x32_bf16 v[160:163], a[160:163], v[212:215], v[160:163]\n"
"v_exp_f32 v192, v192\n"
"v_mfma_f32_16x16x32_bf16 v[164:167], a[164:167], v[212:215], v[164:167]\n"
"v_exp_f32 v193, v193\n"
"v_mfma_f32_16x16x32_bf16 v[168:171], a[168:171], v[212:215], v[168:171]\n"
"v_exp_f32 v194, v194\n"
"v_mfma_f32_16x16x32_bf16 v[172:175], a[172:175], v[212:215], v[172:175]\n"
"v_exp_f32 v195, v195\n"
"v_mfma_f32_16x16x32_bf16 v[176:179], a[176:179], v[212:215], v[176:179]\n"
"v_exp_f32 v196, v196\n"
"v_mfma_f32_16x16x32_bf16 v[180:183], a[180:183], v[212:215], v[180:183]\n"
"v_exp_f32 v197, v197\n"
"v_mfma_f32_16x16x32_bf16 v[184:187], a[184:187], v[212:215], v[184:187]\n"
"v_exp_f32 v198, v198\n"
"v_mfma_f32_16x16x32_bf16 v[188:191], a[188:191], v[212:215], v[188:191]\n"
"v_exp_f32 v199, v199\n"
"v_mfma_f32_16x16x32_bf16 v[200:203], a[128:131], a[80:83], v[216:219]\n"
"v_cvt_pk_bf16_f32 v208, v192, v193\n"
"v_cvt_pk_bf16_f32 v209, v194, v195\n"
"buffer_store_dwordx4 v[72:75], v249, s[56:59], s62 offen offset:1024 sc1\n"
"v_mfma_f32_16x16x32_bf16 v[200:203], a[132:135], a[84:87], v[200:203]\n"
"v_cvt_pk_bf16_f32 v210, v196, v197\n"
"v_cvt_pk_bf16_f32 v211, v198, v199\n"
"v_mfma_f32_16x16x32_bf16 v[200:203], a[136:139], a[88:91], v[200:203]\n"
"v_mfma_f32_16x16x32_bf16 v[200:203], a[140:143], a[92:95], v[200:203]\n"
"v_mfma_f32_16x16x32_bf16 v[204:207], a[144:147], a[80:83], v[216:219]\n"
"v_mfma_f32_16x16x32_bf16 v[204:207], a[148:151], a[84:87], v[204:207]\n"
"v_mfma_f32_16x16x32_bf16 v[204:207], a[152:155], a[88:91], v[204:207]\n"
"v_mfma_f32_16x16x32_bf16 v[204:207], a[156:159], a[92:95], v[204:207]\n"
"v_mfma_f32_16x16x32_bf16 v[128:131], a[192:195], v[208:211], v[128:131]\n"
"v_mfma_f32_16x16x32_bf16 v[132:135], a[196:199], v[208:211], v[132:135]\n"
"v_exp_f32 v200, v200\n"
"v_mfma_f32_16x16x32_bf16 v[136:139], a[200:203], v[208:211], v[136:139]\n"
"v_exp_f32 v201, v201\n"
"v_mfma_f32_16x16x32_bf16 v[140:143], a[204:207], v[208:211], v[140:143]\n"
"v_exp_f32 v202, v202\n"
"v_mfma_f32_16x16x32_bf16 v[144:147], a[208:211], v[208:211], v[144:147]\n"
"v_exp_f32 v203, v203\n"
"buffer_store_dwordx4 v[80:83], v249, s[56:59], s62 offen offset:2048 sc1\n"
"v_mfma_f32_16x16x32_bf16 v[148:151], a[212:215], v[208:211], v[148:151]\n"
"v_exp_f32 v204, v204\n"
"v_mfma_f32_16x16x32_bf16 v[152:155], a[216:219], v[208:211], v[152:155]\n"
"v_exp_f32 v205, v205\n"
"v_mfma_f32_16x16x32_bf16 v[156:159], a[220:223], v[208:211], v[156:159]\n"
"v_exp_f32 v206, v206\n"
"v_exp_f32 v207, v207\n"
"v_cvt_pk_bf16_f32 v212, v200, v201\n"
"v_cvt_pk_bf16_f32 v213, v202, v203\n"
"v_cvt_pk_bf16_f32 v214, v204, v205\n"
"v_cvt_pk_bf16_f32 v215, v206, v207\n"
"s_nop 1\n"
"v_smfmac_f32_16x16x64_bf16 v[232:235], v[240:243], v[208:215], v244\n"
"v_mfma_f32_16x16x32_bf16 v[160:163], a[192:195], v[212:215], v[160:163]\n"
"v_mfma_f32_16x16x32_bf16 v[164:167], a[196:199], v[212:215], v[164:167]\n"
"v_mfma_f32_16x16x32_bf16 v[168:171], a[200:203], v[212:215], v[168:171]\n"
"v_mfma_f32_16x16x32_bf16 v[172:175], a[204:207], v[212:215], v[172:175]\n"
"v_mfma_f32_16x16x32_bf16 v[176:179], a[208:211], v[212:215], v[176:179]\n"
"v_mfma_f32_16x16x32_bf16 v[180:183], a[212:215], v[212:215], v[180:183]\n"
"v_mfma_f32_16x16x32_bf16 v[184:187], a[216:219], v[212:215], v[184:187]\n"
"v_mfma_f32_16x16x32_bf16 v[188:191], a[220:223], v[212:215], v[188:191]\n"
"s_mul_i32 s80, s76, 5120\n"
"s_add_u32 s80, s80, 139264\n"
"v_add_u32 v253, s80, v255\n"
"s_xor_b32 s81, s76, 1\n"
"s_mul_i32 s81, s81, 5120\n"
"s_add_u32 s81, s81, 139264\n"
"v_add_u32 v254, s81, v255\n"
"v_lshrrev_b32 v252, 1, v255\n"
"v_and_b32 v252, 0x78, v252\n"
"s_lshl_b32 s47, s77, 4\n"
"s_add_u32 s47, s47, s78\n"
"s_lshl_b32 s47, s47, 7\n"
"s_add_u32 s47, s47, 33685504\n"
"buffer_load_dwordx2 v[250:251], v252, s[56:59], s47 offen sc1\n"
"v_add_f32 v237, v238, v239\n"
"v_add_f32 v233, v232, v233\n"
"v_add_f32 v235, v234, v235\n"
"v_rcp_f32 v232, v233\n"
"v_rcp_f32 v234, v235\n"
"ds_write_b128 v253, a[240:243] offset:0\n"
"ds_write_b128 v253, a[244:247] offset:1024\n"
"ds_write_b128 v253, a[248:251] offset:2048\n"
"ds_write_b128 v253, a[252:255] offset:3072\n"
"ds_write_b32 v253, v237 offset:4096\n"
"v_pk_mul_f32 v[88:89], v[88:89], v[228:229] op_sel_hi:[1,0]\n"
"v_pk_mul_f32 v[90:91], v[90:91], v[228:229] op_sel_hi:[1,0]\n"
"v_pk_mul_f32 v[92:93], v[92:93], v[228:229] op_sel_hi:[1,0]\n"
"v_pk_mul_f32 v[94:95], v[94:95], v[228:229] op_sel_hi:[1,0]\n"
"v_cvt_pk_bf16_f32 v88, v88, v89\n"
"v_cvt_pk_bf16_f32 v89, v90, v91\n"
"v_cvt_pk_bf16_f32 v90, v92, v93\n"
"v_cvt_pk_bf16_f32 v91, v94, v95\n"
"buffer_store_dwordx4 v[88:91], v249, s[56:59], s62 offen offset:3072 sc1\n"
"s_waitcnt lgkmcnt(0)\n"
"s_barrier\n"
"ds_read_b128 v[208:211], v254 offset:0\n"
"ds_read_b128 v[212:215], v254 offset:1024\n"
"ds_read_b128 v[216:219], v254 offset:2048\n"
"ds_read_b128 v[220:223], v254 offset:3072\n"
"ds_read_b32 v252, v254 offset:4096\n"
"s_lshl_b32 s44, s78, 3\n"
"s_add_u32 s44, s44, 163328\n"
"v_mov_b32 v7, s44\n"
"ds_read_b64 v[238:239], v7\n"
"v_pk_mul_f32 v[96:97], v[96:97], v[230:231] op_sel_hi:[1,0]\n"
"v_pk_mul_f32 v[98:99], v[98:99], v[230:231] op_sel_hi:[1,0]\n"
"v_pk_mul_f32 v[100:101], v[100:101], v[230:231] op_sel_hi:[1,0]\n"
"v_pk_mul_f32 v[102:103], v[102:103], v[230:231] op_sel_hi:[1,0]\n"
"v_cvt_pk_bf16_f32 v96, v96, v97\n"
"v_cvt_pk_bf16_f32 v97, v98, v99\n"
"v_cvt_pk_bf16_f32 v98, v100, v101\n"
"v_cvt_pk_bf16_f32 v99, v102, v103\n"
"buffer_store_dwordx4 v[96:99], v249, s[56:59], s63 offen offset:0 sc1\n"
"v_pk_mul_f32 v[104:105], v[104:105], v[230:231] op_sel_hi:[1,0]\n"
"v_pk_mul_f32 v[106:107], v[106:107], v[230:231] op_sel_hi:[1,0]\n"
"v_pk_mul_f32 v[108:109], v[108:109], v[230:231] op_sel_hi:[1,0]\n"
"v_pk_mul_f32 v[110:111], v[110:111], v[230:231] op_sel_hi:[1,0]\n"
"v_cvt_pk_bf16_f32 v104, v104, v105\n"
"v_cvt_pk_bf16_f32 v105, v106, v107\n"
"v_cvt_pk_bf16_f32 v106, v108, v109\n"
"v_cvt_pk_bf16_f32 v107, v110, v111\n"
"buffer_store_dwordx4 v[104:107], v249, s[56:59], s63 offen offset:1024 sc1\n"
"v_pk_mul_f32 v[112:113], v[112:113], v[230:231] op_sel_hi:[1,0]\n"
"v_pk_mul_f32 v[114:115], v[114:115], v[230:231] op_sel_hi:[1,0]\n"
"v_pk_mul_f32 v[116:117], v[116:117], v[230:231] op_sel_hi:[1,0]\n"
"v_pk_mul_f32 v[118:119], v[118:119], v[230:231] op_sel_hi:[1,0]\n"
"v_cvt_pk_bf16_f32 v112, v112, v113\n"
"v_cvt_pk_bf16_f32 v113, v114, v115\n"
"v_cvt_pk_bf16_f32 v114, v116, v117\n"
"v_cvt_pk_bf16_f32 v115, v118, v119\n"
"buffer_store_dwordx4 v[112:115], v249, s[56:59], s63 offen offset:2048 sc1\n"
"v_pk_mul_f32 v[120:121], v[120:121], v[230:231] op_sel_hi:[1,0]\n"
"v_pk_mul_f32 v[122:123], v[122:123], v[230:231] op_sel_hi:[1,0]\n"
"v_pk_mul_f32 v[124:125], v[124:125], v[230:231] op_sel_hi:[1,0]\n"
"v_pk_mul_f32 v[126:127], v[126:127], v[230:231] op_sel_hi:[1,0]\n"
"v_cvt_pk_bf16_f32 v120, v120, v121\n"
"v_cvt_pk_bf16_f32 v121, v122, v123\n"
"v_cvt_pk_bf16_f32 v122, v124, v125\n"
"v_cvt_pk_bf16_f32 v123, v126, v127\n"
"buffer_store_dwordx4 v[120:123], v249, s[56:59], s63 offen offset:3072 sc1\n"
"v_pk_mul_f32 v[128:129], v[128:129], v[232:233] op_sel_hi:[1,0]\n"
"v_pk_mul_f32 v[130:131], v[130:131], v[232:233] op_sel_hi:[1,0]\n"
"v_pk_mul_f32 v[132:133], v[132:133], v[232:233] op_sel_hi:[1,0]\n"
"v_pk_mul_f32 v[134:135], v[134:135], v[232:233] op_sel_hi:[1,0]\n"
"v_cvt_pk_bf16_f32 v128, v128, v129\n"
"v_cvt_pk_bf16_f32 v129, v130, v131\n"
"v_cvt_pk_bf16_f32 v130, v132, v133\n"
"v_cvt_pk_bf16_f32 v131, v134, v135\n"
"buffer_store_dwordx4 v[128:131], v249, s[56:59], s64 offen offset:0 sc1\n"
"v_pk_mul_f32 v[136:137], v[136:137], v[232:233] op_sel_hi:[1,0]\n"
"v_pk_mul_f32 v[138:139], v[138:139], v[232:233] op_sel_hi:[1,0]\n"
"v_pk_mul_f32 v[140:141], v[140:141], v[232:233] op_sel_hi:[1,0]\n"
"v_pk_mul_f32 v[142:143], v[142:143], v[232:233] op_sel_hi:[1,0]\n"
"v_cvt_pk_bf16_f32 v136, v136, v137\n"
"v_cvt_pk_bf16_f32 v137, v138, v139\n"
"v_cvt_pk_bf16_f32 v138, v140, v141\n"
"v_cvt_pk_bf16_f32 v139, v142, v143\n"
"buffer_store_dwordx4 v[136:139], v249, s[56:59], s64 offen offset:1024 sc1\n"
"v_pk_mul_f32 v[144:145], v[144:145], v[232:233] op_sel_hi:[1,0]\n"
"v_pk_mul_f32 v[146:147], v[146:147], v[232:233] op_sel_hi:[1,0]\n"
"v_pk_mul_f32 v[148:149], v[148:149], v[232:233] op_sel_hi:[1,0]\n"
"v_pk_mul_f32 v[150:151], v[150:151], v[232:233] op_sel_hi:[1,0]\n"
"v_cvt_pk_bf16_f32 v144, v144, v145\n"
"v_cvt_pk_bf16_f32 v145, v146, v147\n"
"v_cvt_pk_bf16_f32 v146, v148, v149\n"
"v_cvt_pk_bf16_f32 v147, v150, v151\n"
"buffer_store_dwordx4 v[144:147], v249, s[56:59], s64 offen offset:2048 sc1\n"
"v_pk_mul_f32 v[152:153], v[152:153], v[232:233] op_sel_hi:[1,0]\n"
"v_pk_mul_f32 v[154:155], v[154:155], v[232:233] op_sel_hi:[1,0]\n"
"v_pk_mul_f32 v[156:157], v[156:157], v[232:233] op_sel_hi:[1,0]\n"
"v_pk_mul_f32 v[158:159], v[158:159], v[232:233] op_sel_hi:[1,0]\n"
"v_cvt_pk_bf16_f32 v152, v152, v153\n"
"v_cvt_pk_bf16_f32 v153, v154, v155\n"
"v_cvt_pk_bf16_f32 v154, v156, v157\n"
"v_cvt_pk_bf16_f32 v155, v158, v159\n"
"buffer_store_dwordx4 v[152:155], v249, s[56:59], s64 offen offset:3072 sc1\n"
"v_accvgpr_read_b32 v192, a224\n"
"v_accvgpr_read_b32 v193, a225\n"
"v_accvgpr_read_b32 v194, a226\n"
"v_accvgpr_read_b32 v195, a227\n"
"v_accvgpr_read_b32 v196, a228\n"
"v_accvgpr_read_b32 v197, a229\n"
"v_accvgpr_read_b32 v198, a230\n"
"v_accvgpr_read_b32 v199, a231\n"
"v_accvgpr_read_b32 v200, a232\n"
"v_accvgpr_read_b32 v201, a233\n"
"v_accvgpr_read_b32 v202, a234\n"
"v_accvgpr_read_b32 v203, a235\n"
"v_accvgpr_read_b32 v204, a236\n"
"v_accvgpr_read_b32 v205, a237\n"
"v_accvgpr_read_b32 v206, a238\n"
"v_accvgpr_read_b32 v207, a239\n"
"s_waitcnt lgkmcnt(1)\n"
"v_add_f32 v237, v237, v252\n"
"v_rcp_f32 v236, v237\n"
"v_pk_add_f32 v[208:209], v[208:209], v[192:193]\n"
"v_pk_add_f32 v[210:211], v[210:211], v[194:195]\n"
"v_pk_add_f32 v[212:213], v[212:213], v[196:197]\n"
"v_pk_add_f32 v[214:215], v[214:215], v[198:199]\n"
"v_pk_add_f32 v[216:217], v[216:217], v[200:201]\n"
"v_pk_add_f32 v[218:219], v[218:219], v[202:203]\n"
"v_pk_add_f32 v[220:221], v[220:221], v[204:205]\n"
"v_pk_add_f32 v[222:223], v[222:223], v[206:207]\n"
"v_pk_mul_f32 v[208:209], v[208:209], v[236:237] op_sel_hi:[1,0]\n"
"v_pk_mul_f32 v[210:211], v[210:211], v[236:237] op_sel_hi:[1,0]\n"
"v_pk_mul_f32 v[212:213], v[212:213], v[236:237] op_sel_hi:[1,0]\n"
"v_pk_mul_f32 v[214:215], v[214:215], v[236:237] op_sel_hi:[1,0]\n"
"v_cvt_pk_bf16_f32 v192, v208, v209\n"
"v_cvt_pk_bf16_f32 v193, v210, v211\n"
"v_cvt_pk_bf16_f32 v194, v212, v213\n"
"v_cvt_pk_bf16_f32 v195, v214, v215\n"
"buffer_store_dwordx4 v[192:195], v249, s[56:59], s70 offen offset:0 sc1\n"
"ds_write_b128 v254, v[192:195] offset:0\n"
"v_pk_mul_f32 v[216:217], v[216:217], v[236:237] op_sel_hi:[1,0]\n"
"v_pk_mul_f32 v[218:219], v[218:219], v[236:237] op_sel_hi:[1,0]\n"
"v_pk_mul_f32 v[220:221], v[220:221], v[236:237] op_sel_hi:[1,0]\n"
"v_pk_mul_f32 v[222:223], v[222:223], v[236:237] op_sel_hi:[1,0]\n"
"v_cvt_pk_bf16_f32 v196, v216, v217\n"
"v_cvt_pk_bf16_f32 v197, v218, v219\n"
"v_cvt_pk_bf16_f32 v198, v220, v221\n"
"v_cvt_pk_bf16_f32 v199, v222, v223\n"
"buffer_store_dwordx4 v[196:199], v249, s[56:59], s70 offen offset:1024 sc1\n"
"ds_write_b128 v254, v[196:199] offset:1024\n"
"v_pk_mul_f32 v[160:161], v[160:161], v[234:235] op_sel_hi:[1,0]\n"
"v_pk_mul_f32 v[162:163], v[162:163], v[234:235] op_sel_hi:[1,0]\n"
"v_pk_mul_f32 v[164:165], v[164:165], v[234:235] op_sel_hi:[1,0]\n"
"v_pk_mul_f32 v[166:167], v[166:167], v[234:235] op_sel_hi:[1,0]\n"
"v_cvt_pk_bf16_f32 v160, v160, v161\n"
"v_cvt_pk_bf16_f32 v161, v162, v163\n"
"v_cvt_pk_bf16_f32 v162, v164, v165\n"
"v_cvt_pk_bf16_f32 v163, v166, v167\n"
"buffer_store_dwordx4 v[160:163], v249, s[56:59], s65 offen offset:0 sc1\n"
"v_pk_mul_f32 v[168:169], v[168:169], v[234:235] op_sel_hi:[1,0]\n"
"v_pk_mul_f32 v[170:171], v[170:171], v[234:235] op_sel_hi:[1,0]\n"
"v_pk_mul_f32 v[172:173], v[172:173], v[234:235] op_sel_hi:[1,0]\n"
"v_pk_mul_f32 v[174:175], v[174:175], v[234:235] op_sel_hi:[1,0]\n"
"v_cvt_pk_bf16_f32 v168, v168, v169\n"
"v_cvt_pk_bf16_f32 v169, v170, v171\n"
"v_cvt_pk_bf16_f32 v170, v172, v173\n"
"v_cvt_pk_bf16_f32 v171, v174, v175\n"
"buffer_store_dwordx4 v[168:171], v249, s[56:59], s65 offen offset:1024 sc1\n"
"v_pk_mul_f32 v[176:177], v[176:177], v[234:235] op_sel_hi:[1,0]\n"
"v_pk_mul_f32 v[178:179], v[178:179], v[234:235] op_sel_hi:[1,0]\n"
"v_pk_mul_f32 v[180:181], v[180:181], v[234:235] op_sel_hi:[1,0]\n"
"v_pk_mul_f32 v[182:183], v[182:183], v[234:235] op_sel_hi:[1,0]\n"
"v_cvt_pk_bf16_f32 v176, v176, v177\n"
"v_cvt_pk_bf16_f32 v177, v178, v179\n"
"v_cvt_pk_bf16_f32 v178, v180, v181\n"
"v_cvt_pk_bf16_f32 v179, v182, v183\n"
"buffer_store_dwordx4 v[176:179], v249, s[56:59], s65 offen offset:2048 sc1\n"
"v_pk_mul_f32 v[184:185], v[184:185], v[234:235] op_sel_hi:[1,0]\n"
"v_pk_mul_f32 v[186:187], v[186:187], v[234:235] op_sel_hi:[1,0]\n"
"v_pk_mul_f32 v[188:189], v[188:189], v[234:235] op_sel_hi:[1,0]\n"
"v_pk_mul_f32 v[190:191], v[190:191], v[234:235] op_sel_hi:[1,0]\n"
"v_cvt_pk_bf16_f32 v184, v184, v185\n"
"v_cvt_pk_bf16_f32 v185, v186, v187\n"
"v_cvt_pk_bf16_f32 v186, v188, v189\n"
"v_cvt_pk_bf16_f32 v187, v190, v191\n"
"buffer_store_dwordx4 v[184:187], v249, s[56:59], s65 offen offset:3072 sc1\n"
"v_subrev_u32 v225, 796917760, v225\n"
"v_subrev_u32 v227, 796917760, v227\n"
"v_subrev_u32 v229, 796917760, v229\n"
"v_subrev_u32 v231, 796917760, v231\n"
"v_subrev_u32 v233, 796917760, v233\n"
"v_subrev_u32 v235, 796917760, v235\n"
"v_subrev_u32 v237, 796917760, v237\n"
"v_max3_u32 v225, v225, v227, v229\n"
"v_max3_u32 v231, v231, v233, v235\n"
"v_max3_u32 v225, v225, v231, v237\n"
"v_cmp_lt_u32 vcc, 536870912, v225\n"
"s_nop 1\n"
"s_or_b32 s85, vcc_lo, vcc_hi\n"
"s_waitcnt vmcnt(15) lgkmcnt(2)\n"
"v_cmp_ne_u64 vcc, v[250:251], v[238:239]\n"
"s_nop 1\n"
"s_or_b32 s44, vcc_lo, vcc_hi\n"
"s_cmp_eq_u32 s76, 0\n"
"s_cselect_b32 s44, s44, 0\n"
"s_and_b32 s80, s76, 1\n"
"s_lshr_b32 s81, s76, 1\n"
"s_lshr_b32 s82, s78, 3\n"
"v_lshrrev_b32 v12, 4, v255\n"
"v_lshrrev_b32 v13, 2, v12\n"
"v_lshlrev_b32 v14, 8, v13\n"
"v_and_b32 v13, 3, v13\n"
"v_lshlrev_b32 v13, 6, v13\n"
"v_lshrrev_b32 v15, 4, v12\n"
"v_lshl_add_u32 v14, v15, 5, v14\n"
"v_and_b32 v15, 3, v12\n"
"v_lshl_add_u32 v14, v15, 3, v14\n"
"s_mul_i32 s83, s81, 67584\n"
"v_add_u32 v14, s83, v14\n"
"s_xor_b32 s83, s82, 1\n"
"s_lshl_b32 s83, s83, 1\n"
"s_add_u32 s83, s83, s81\n"
"s_lshl_b32 s83, s83, 1\n"
"s_add_u32 s83, s83, s80\n"
"s_lshl_b32 s83, s83, 5\n"
"v_xor_b32 v13, s83, v13\n"
"v_xor_b32 v240, 0, v13\n"
"v_add_u32 v240, v240, v14\n"
"v_xor_b32 v241, 32, v13\n"
"v_add_u32 v241, v241, v14\n"
"v_xor_b32 v242, 64, v13\n"
"v_add_u32 v242, v242, v14\n"
"v_xor_b32 v243, 96, v13\n"
"v_add_u32 v243, v243, v14\n"
"v_xor_b32 v244, 128, v13\n"
"v_add_u32 v244, v244, v14\n"
"v_xor_b32 v245, 160, v13\n"
"v_add_u32 v245, v245, v14\n"
"v_xor_b32 v246, 192, v13\n"
"v_add_u32 v246, v246, v14\n"
"v_xor_b32 v247, 224, v13\n"
"v_add_u32 v247, v247, v14\n"
"v_and_b32 v13, 7, v12\n"
"s_lshl_b32 s83, s82, 3\n"
"v_add_u32 v13, s83, v13\n"
"v_lshlrev_b32 v254, 7, v13\n"
"v_lshlrev_b32 v13, 3, v13\n"
"v_add_u32 v13, 163328, v13\n"
"ds_read_b64 v[250:251], v13\n"
"s_lshl_b32 s46, s78, 7\n"
"s_lshl_b32 s83, s78, 3\n"
"s_add_u32 s83, s83, 163328\n"
"v_mov_b32 v15, s83\n"
"ds_read_b64 v[4:5], v15\n"
"s_lshl_b32 s45, s77, 11\n"
"s_add_u32 s45, s45, 33554432\n"
"s_add_u32 s46, s46, s45\n"
"v_or_b32 v6, s76, v255\n"
"v_cmp_eq_u32 vcc, 0, v6\n"
"v_mov_b32 v6, 0x40000000\n"
"s_nop 0\n"
"v_cndmask_b32 v6, v6, v255, vcc\n"
"s_mov_b64 s[40:41], %9\n"
"s_mov_b32 s42, 6815744\n"
"s_mov_b32 s43, 0x00020000\n"
"s_cmp_lg_u32 s85, 0\n"
"s_cbranch_scc1 Lx%=\n"
".Lmha_j0:\n"
"s_waitcnt lgkmcnt(0)\n"
"s_barrier\n"
"ds_read_b128 v[200:203], v253 offset:0\n"
"ds_read_b128 v[204:207], v253 offset:1024\n"
"s_branch Lk2%=\n"
"Lp1%=:\n"
"s_add_u32 m0, s71, 16384\n"
"s_nop 0\n"
"buffer_load_dwordx4 v253, s[48:51], s63 offen offset:0  lds\n"
"buffer_load_dwordx4 v253, s[48:51], s64 offen offset:1024  lds\n"
"buffer_load_dwordx4 v253, s[48:51], s65 offen offset:2048  lds\n"
"buffer_load_dwordx4 v253, s[48:51], 0 offen offset:3072  lds\n"
"v_add_u32 v253, 0x40000, v253\n"
"s_add_u32 m0, s73, 16384\n"
"s_nop 0\n"
"buffer_load_dwordx4 v254, s[52:55], s66 offen offset:0  lds\n"
"buffer_load_dwordx4 v254, s[52:55], s67 offen offset:1024  lds\n"
"buffer_load_dwordx4 v254, s[52:55], s68 offen offset:2048  lds\n"
"buffer_load_dwordx4 v254, s[52:55], s69 offen offset:3072  lds\n"
"v_add_u32 v254, 64, v254\n"
"s_add_u32 m0, s71, 32768\n"
"s_nop 0\n"
"buffer_load_dwordx4 v253, s[48:51], s63 offen offset:0  lds\n"
"buffer_load_dwordx4 v253, s[48:51], s64 offen offset:1024  lds\n"
"buffer_load_dwordx4 v253, s[48:51], s65 offen offset:2048  lds\n"
"buffer_load_dwordx4 v253, s[48:51], 0 offen offset:3072  lds\n"
"v_add_u32 v253, 0x40000, v253\n"
"s_add_u32 m0, s73, 32768\n"
"s_nop 0\n"
"buffer_load_dwordx4 v254, s[52:55], s66 offen offset:0  lds\n"
"buffer_load_dwordx4 v254, s[52:55], s67 offen offset:1024  lds\n"
"buffer_load_dwordx4 v254, s[52:55], s68 offen offset:2048  lds\n"
"buffer_load_dwordx4 v254, s[52:55], s69 offen offset:3072  lds\n"
"v_add_u32 v254, 64, v254\n"
"v_and_b32 v8, 15, v0\n"
"v_lshrrev_b32 v3, 2, v8\n"
"v_lshlrev_b32 v3, 1, v3\n"
"v_lshrrev_b32 v3, v3, s85\n"
"v_and_b32 v3, 3, v3\n"
"v_xor_b32 v3, v3, v5\n"
"v_lshlrev_b32 v3, 4, v3\n"
"v_lshlrev_b32 v4, 6, v8\n"
"v_add_u32 v246, v3, v4\n"
"v_add_u32 v247, 0x10000, v246\n"
"v_add_u32 v248, 0x20000, v246\n"
"v_and_b32 v3, 3, v2\n"
"v_lshlrev_b32 v4, 7, v3\n"
"v_lshrrev_b32 v3, 1, v3\n"
"v_lshlrev_b32 v3, 6, v3\n"
"v_add_u32 v4, v4, v3\n"
"v_lshlrev_b32 v3, 4, v7\n"
"v_add_u32 v4, v4, v3\n"
"v_lshrrev_b32 v3, 1, v5\n"
"v_lshlrev_b32 v3, 12, v3\n"
"v_add_u32 v4, v4, v3\n"
"s_lshl_b32 s80, s83, 11\n"
"v_add_u32 v4, s80, v4\n"
"v_and_b32 v3, 1, v5\n"
"v_mul_u32_u24 v9, 65544, v3\n"
"v_add_u32 v249, v4, v9\n"
"v_xor_b32 v250, 64, v249\n"
"v_mul_u32_u24 v9, 16392, v3\n"
"v_add_u32 v9, v4, v9\n"
"v_add_u32 v251, 0x20000, v9\n"
"v_xor_b32 v252, 64, v251\n"
"s_add_u32 m0, s71, 49152\n"
"s_nop 0\n"
"buffer_load_dwordx4 v253, s[48:51], s63 offen offset:0  lds\n"
"buffer_load_dwordx4 v253, s[48:51], s65 offen offset:2048  lds\n"
"v_add_u32 v253, 0x40000, v253\n"
"s_mov_b32 m0, s72\n"
"s_nop 0\n"
"buffer_load_dwordx4 v253, s[48:51], s63 offen offset:0  lds\n"
"buffer_load_dwordx4 v253, s[48:51], s65 offen offset:2048  lds\n"
"v_subrev_u32 v253, 0x40000, v253\n"
"s_add_u32 m0, s73, 49152\n"
"s_nop 0\n"
"buffer_load_dwordx4 v254, s[52:55], s66 offen offset:0  lds\n"
"buffer_load_dwordx4 v254, s[52:55], s67 offen offset:1024  lds\n"
"buffer_load_dwordx4 v254, s[52:55], s68 offen offset:2048  lds\n"
"buffer_load_dwordx4 v254, s[52:55], s69 offen offset:3072  lds\n"
"v_add_u32 v254, 64, v254\n"
"s_and_b32 s80, s78, 3\n"
"s_lshl_b32 s80, s80, 2\n"
"s_add_u32 s80, s80, s76\n"
"s_mul_i32 s80, s80, 26624\n"
"s_add_u32 s80, s80, 256\n"
"v_lshrrev_b32 v1, 1, v0\n"
"v_lshlrev_b32 v1, 11, v1\n"
"v_and_b32 v3, 1, v0\n"
"v_lshlrev_b32 v3, 7, v3\n"
"v_add3_u32 v1, v1, v3, s80\n"
"v_cmp_gt_u32 vcc, 26, v0\n"
"v_mov_b32 v3, 0x40000000\n"
"s_nop 0\n"
"v_cndmask_b32 v209, v3, v1, vcc\n"
"s_and_b32 s80, s77, 7\n"
"s_lshl_b32 s80, s80, 2\n"
"s_add_u32 s80, s80, s76\n"
"s_lshl_b32 s80, s80, 15\n"
"s_add_u32 s80, s80, 1051264\n"
"v_lshrrev_b32 v1, 2, v0\n"
"v_lshlrev_b32 v1, 13, v1\n"
"v_and_b32 v4, 3, v0\n"
"v_lshlrev_b32 v4, 7, v4\n"
"v_add3_u32 v1, v1, v4, s80\n"
"v_cmp_gt_u32 vcc, 16, v0\n"
"s_nop 1\n"
"v_cndmask_b32 v210, v3, v1, vcc\n"
"buffer_load_dword v208, v209, s[52:55], 0 offen \n"
"v_add_u32 v209, 0x100, v209\n"
"buffer_load_dword v208, v210, s[48:51], 0 offen \n"
"v_add_u32 v210, 0x100000, v210\n"
"s_lshl_b32 s80, s77, 5\n"
"s_add_u32 s80, s80, s79\n"
"s_lshl_b32 s80, s80, 18\n"
"s_mov_b64 s[40:41], %2\n"
"s_add_u32 s40, s40, s80\n"
"s_addc_u32 s41, s41, 0\n"
"s_sub_u32 s40, s40, 2880\n"
"s_subb_u32 s41, s41, 0\n"
"s_mov_b32 s42, 265024\n"
"s_mov_b32 s43, 0x00020000\n"
"s_mov_b64 s[44:45], %3\n"
"s_add_u32 s44, s44, s80\n"
"s_addc_u32 s45, s45, 0\n"
"s_sub_u32 s44, s44, 2880\n"
"s_subb_u32 s45, s45, 0\n"
"s_mov_b32 s46, 265024\n"
"s_mov_b32 s47, 0x00020000\n"
"s_movk_i32 s60, 2880\n"
"s_movk_i32 s61, 1920\n"
"s_movk_i32 s62, 960\n"
"s_mov_b64 s[56:57], %5\n"
"s_mov_b32 s58, 50331648\n"
"s_mov_b32 s59, 0x00020000\n"
"v_and_b32 v214, 3, v0\n"
"v_lshlrev_b32 v215, 3, v214\n"
"v_lshlrev_b32 v214, 7, v214\n"
"s_lshl_b32 s80, s77, 2\n"
"s_add_u32 s80, s80, s76\n"
"s_lshl_b32 s80, s80, 9\n"
"s_add_u32 s80, s80, 33554432\n"
"buffer_load_dwordx2 v[212:213], v214, s[56:59], s80 offen sc1\n"
"s_lshl_b32 s80, s76, 5\n"
"s_add_u32 s80, s80, 163328\n"
"v_add_u32 v215, s80, v215\n"
"v_cmp_gt_u32 vcc, 4, v0\n"
"v_mov_b32 v217, 0x40000000\n"
"s_nop 0\n"
"v_cndmask_b32 v216, v217, v214, vcc\n"
"s_lshl_b32 s81, s83, 16\n"
"s_mov_b32 s98, 1040356334\n"
"v_add_u32 v220, s81, v246\n"
"v_lshrrev_b32 v3, 1, v7\n"
"v_xor_b32 v3, v3, v6\n"
"v_lshlrev_b32 v3, 4, v3\n"
"v_and_b32 v4, 1, v7\n"
"v_lshlrev_b32 v4, 3, v4\n"
"v_add_u32 v3, v3, v4\n"
"v_lshlrev_b32 v4, 6, v2\n"
"v_add_u32 v3, v3, v4\n"
"s_add_u32 s80, s81, 8192\n"
"v_add_u32 v221, s80, v3\n"
"v_xor_b32 v222, 32, v221\n"
"s_lshl_b32 s80, s83, 12\n"
"s_add_u32 s80, s80, 131072\n"
"v_lshlrev_b32 v255, 4, v0\n"
"v_add_u32 v245, s80, v255\n"
"v_lshrrev_b32 v3, 1, v8\n"
"v_and_b32 v3, 1, v3\n"
"v_lshrrev_b32 v4, 1, v5\n"
"v_cmp_eq_u32 vcc, v3, v4\n"
"v_mov_b32 v9, 0x3f803f80\n"
"s_nop 0\n"
"v_cndmask_b32 v240, 0, v9, vcc\n"
"v_cndmask_b32 v241, 0, v9, vcc\n"
"v_cndmask_b32 v242, 0, v9, vcc\n"
"v_cndmask_b32 v243, 0, v9, vcc\n"
"v_and_b32 v3, 1, v8\n"
"v_mul_u32_u24 v3, 0xAAAA, v3\n"
"v_add_u32 v244, 0x4444, v3\n"
"s_lshl_b32 s80, s99, 2\n"
"s_add_u32 s80, s80, s76\n"
"s_lshl_b32 s80, s80, 11\n"
"v_lshrrev_b32 v211, 2, v0\n"
"v_lshlrev_b32 v211, 7, v211\n"
"v_add_u32 v211, s80, v211\n"
"s_getpc_b64 s[80:81]\n"
"Lcpa1%=:\n"
"s_sub_u32 s80, s80, Lcpa1%=-Lbeg%=\n"
"s_subb_u32 s81, s81, 0\n"
"s_and_b32 s80, s80, 0xffffff80\n"
"s_mov_b32 s82, Lx%=-Lbeg%=\n"
"s_mov_b32 s83, 0x00020000\n"
"s_lshr_b32 s84, s99, 3\n"
"s_lshl_b32 s84, s84, 2\n"
"s_add_u32 s84, s84, s76\n"
"s_lshl_b32 s84, s84, 10\n"
"v_cmp_gt_u32 vcc, 128, v255\n"
"v_lshl_add_u32 v1, v255, 3, s84\n"
"v_mov_b32 v3, 0x40000000\n"
"v_cndmask_b32 v1, v3, v1, vcc\n"
"buffer_load_dword v208, v1, s[80:83], 0 offen \n"
"s_waitcnt vmcnt(20)\n"
"s_barrier\n"
"ds_read_b128 a[160:163], v247 offset:24592\n"
"ds_read_b64_tr_b16 a[96:97], v249 offset:16384\n"
"ds_read_b64_tr_b16 a[98:99], v249 offset:16896\n"
"ds_read_b64_tr_b16 a[100:101], v249 offset:16392\n"
"ds_read_b64_tr_b16 a[102:103], v249 offset:16904\n"
"ds_read_b64_tr_b16 a[104:105], v250 offset:16384\n"
"ds_read_b64_tr_b16 a[106:107], v250 offset:16896\n"
"ds_read_b64_tr_b16 a[108:109], v250 offset:16392\n"
"ds_read_b64_tr_b16 a[110:111], v250 offset:16904\n"
"ds_read_b64_tr_b16 a[112:113], v249 offset:17408\n"
"ds_read_b64_tr_b16 a[114:115], v249 offset:17920\n"
"ds_read_b64_tr_b16 a[116:117], v249 offset:17416\n"
"ds_read_b64_tr_b16 a[118:119], v249 offset:17928\n"
"ds_read_b64_tr_b16 a[120:121], v250 offset:17408\n"
"ds_read_b64_tr_b16 a[122:123], v250 offset:17920\n"
"ds_read_b64_tr_b16 a[124:125], v250 offset:17416\n"
"ds_read_b64_tr_b16 a[126:127], v250 offset:17928\n"
"ds_read_b128 a[164:167], v247 offset:25616\n"
"ds_read_b128 a[168:171], v247 offset:26640\n"
"ds_read_b128 a[172:175], v247 offset:27664\n"
"ds_read_b128 a[176:179], v247 offset:28688\n"
"ds_read_b128 a[180:183], v247 offset:29712\n"
"ds_read_b128 a[184:187], v247 offset:30736\n"
"s_mov_b32 s75, 3\n"
"s_waitcnt vmcnt(12)\n"
"s_barrier\n"
"ds_read_b128 a[192:195], v247 offset:40976\n"
"ds_read_b64_tr_b16 a[128:129], v249 offset:32768\n"
"ds_read_b64_tr_b16 a[130:131], v249 offset:33280\n"
"ds_read_b128 a[196:199], v247 offset:42000\n"
"ds_read_b128 a[200:203], v247 offset:43024\n"
"ds_read_b128 a[204:207], v247 offset:44048\n"
"ds_read_b128 a[208:211], v247 offset:45072\n"
"ds_read_b128 a[212:215], v247 offset:46096\n"
"ds_read_b128 a[216:219], v247 offset:47120\n"
"s_waitcnt lgkmcnt(0)\n"
"v_mfma_f32_16x16x32_bf16 v[0:3], a[96:99], a[160:163], 0\n"
"s_add_u32 m0, s71, 49152\n"
"ds_read_b64_tr_b16 a[132:133], v249 offset:32776\n"
"ds_read_b64_tr_b16 a[134:135], v249 offset:33288\n"
"v_mfma_f32_16x16x32_bf16 v[0:3], a[128:131], a[192:195], v[0:3]\n"
"buffer_load_dwordx4 v253, s[48:51], s64 offen offset:1024  lds\n"
"ds_read_b64_tr_b16 a[136:137], v250 offset:32768\n"
"ds_read_b64_tr_b16 a[138:139], v250 offset:33280\n"
"v_mfma_f32_16x16x32_bf16 v[32:35], a[96:99], a[164:167], 0\n"
"buffer_load_dwordx4 v253, s[48:51], 0 offen offset:3072  lds\n"
"ds_read_b64_tr_b16 a[140:141], v250 offset:32776\n"
"ds_read_b64_tr_b16 a[142:143], v250 offset:33288\n"
"v_mfma_f32_16x16x32_bf16 v[32:35], a[128:131], a[196:199], v[32:35]\n"
"v_add_u32 v253, 0x40000, v253\n"
"s_mov_b32 m0, s72\n"
"ds_read_b64_tr_b16 a[144:145], v249 offset:33792\n"
"v_mfma_f32_16x16x32_bf16 v[64:67], a[96:99], a[168:171], 0\n"
"buffer_load_dwordx4 v253, s[48:51], s64 offen offset:1024  lds\n"
"ds_read_b64_tr_b16 a[146:147], v249 offset:34304\n"
"ds_read_b64_tr_b16 a[148:149], v249 offset:33800\n"
"v_mfma_f32_16x16x32_bf16 v[64:67], a[128:131], a[200:203], v[64:67]\n"
"buffer_load_dwordx4 v253, s[48:51], 0 offen offset:3072  lds\n"
"ds_read_b64_tr_b16 a[150:151], v249 offset:34312\n"
"ds_read_b64_tr_b16 a[152:153], v250 offset:33792\n"
"v_mfma_f32_16x16x32_bf16 v[96:99], a[96:99], a[172:175], 0\n"
"v_add_u32 v253, 0x40000, v253\n"
"s_mov_b32 m0, s74\n"
"ds_read_b64_tr_b16 a[154:155], v250 offset:34304\n"
"v_mfma_f32_16x16x32_bf16 v[96:99], a[128:131], a[204:207], v[96:99]\n"
"buffer_load_dwordx4 v254, s[52:55], s66 offen offset:0  lds\n"
"ds_read_b64_tr_b16 a[156:157], v250 offset:33800\n"
"ds_read_b64_tr_b16 a[158:159], v250 offset:34312\n"
"v_mfma_f32_16x16x32_bf16 v[128:131], a[96:99], a[176:179], 0\n"
"buffer_load_dwordx4 v254, s[52:55], s67 offen offset:1024  lds\n"
"v_mfma_f32_16x16x32_bf16 v[128:131], a[128:131], a[208:211], v[128:131]\n"
"buffer_load_dwordx4 v254, s[52:55], s68 offen offset:2048  lds\n"
"v_mfma_f32_16x16x32_bf16 v[160:163], a[96:99], a[180:183], 0\n"
"buffer_load_dwordx4 v254, s[52:55], s69 offen offset:3072  lds\n"
"v_mfma_f32_16x16x32_bf16 v[160:163], a[128:131], a[212:215], v[160:163]\n"
"v_add_u32 v254, 64, v254\n"
"s_waitcnt lgkmcnt(12)\n"
"v_mfma_f32_16x16x32_bf16 v[4:7], a[100:103], a[160:163], 0\n"
"v_mfma_f32_16x16x32_bf16 v[4:7], a[132:135], a[192:195], v[4:7]\n"
"s_waitcnt vmcnt(12) lgkmcnt(0)\n"
"s_barrier\n"
"v_mfma_f32_16x16x32_bf16 v[36:39], a[100:103], a[164:167], 0\n"
"s_add_u32 m0, s71, 16384\n"
"ds_read_b64_tr_b16 a[96:97], v249 offset:49152\n"
"v_mfma_f32_16x16x32_bf16 v[36:39], a[132:135], a[196:199], v[36:39]\n"
"buffer_load_dwordx4 v253, s[48:51], s63 offen offset:0  lds\n"
"v_mfma_f32_16x16x32_bf16 v[68:71], a[100:103], a[168:171], 0\n"
"ds_read_b64_tr_b16 a[98:99], v249 offset:49664\n"
"ds_read_b64_tr_b16 a[128:129], v251 offset:0\n"
"v_mfma_f32_16x16x32_bf16 v[68:71], a[132:135], a[200:203], v[68:71]\n"
"buffer_load_dwordx4 v253, s[48:51], s65 offen offset:2048  lds\n"
"v_mfma_f32_16x16x32_bf16 v[100:103], a[100:103], a[172:175], 0\n"
"ds_read_b64_tr_b16 a[130:131], v251 offset:512\n"
"ds_read_b128 a[0:3], v247 offset:57360\n"
"v_mfma_f32_16x16x32_bf16 v[100:103], a[132:135], a[204:207], v[100:103]\n"
"v_add_u32 v253, 0x40000, v253\n"
"s_add_u32 m0, s71, 32768\n"
"v_mfma_f32_16x16x32_bf16 v[132:135], a[100:103], a[176:179], 0\n"
"buffer_load_dwordx4 v253, s[48:51], s63 offen offset:0  lds\n"
"v_mfma_f32_16x16x32_bf16 v[132:135], a[132:135], a[208:211], v[132:135]\n"
"ds_read_b128 a[4:7], v247 offset:58384\n"
"ds_read_b128 a[8:11], v247 offset:59408\n"
"v_mfma_f32_16x16x32_bf16 v[164:167], a[100:103], a[180:183], 0\n"
"buffer_load_dwordx4 v253, s[48:51], s65 offen offset:2048  lds\n"
"v_mfma_f32_16x16x32_bf16 v[164:167], a[132:135], a[212:215], v[164:167]\n"
"ds_read_b64_tr_b16 a[100:101], v249 offset:49160\n"
"ds_read_b64_tr_b16 a[102:103], v249 offset:49672\n"
"v_mfma_f32_16x16x32_bf16 v[8:11], a[104:107], a[160:163], 0\n"
"v_subrev_u32 v253, 0x40000, v253\n"
"s_add_u32 m0, s73, 16384\n"
"v_mfma_f32_16x16x32_bf16 v[8:11], a[136:139], a[192:195], v[8:11]\n"
"buffer_load_dwordx4 v254, s[52:55], s66 offen offset:0  lds\n"
"v_mfma_f32_16x16x32_bf16 v[40:43], a[104:107], a[164:167], 0\n"
"ds_read_b64_tr_b16 a[132:133], v251 offset:8\n"
"ds_read_b64_tr_b16 a[134:135], v251 offset:520\n"
"v_mfma_f32_16x16x32_bf16 v[40:43], a[136:139], a[196:199], v[40:43]\n"
"buffer_load_dwordx4 v254, s[52:55], s67 offen offset:1024  lds\n"
"v_mfma_f32_16x16x32_bf16 v[72:75], a[104:107], a[168:171], 0\n"
"ds_read_b128 a[12:15], v247 offset:60432\n"
"ds_read_b128 a[16:19], v247 offset:61456\n"
"v_mfma_f32_16x16x32_bf16 v[72:75], a[136:139], a[200:203], v[72:75]\n"
"buffer_load_dwordx4 v254, s[52:55], s68 offen offset:2048  lds\n"
"v_mfma_f32_16x16x32_bf16 v[104:107], a[104:107], a[172:175], 0\n"
"ds_read_b128 a[20:23], v247 offset:62480\n"
"ds_read_b128 a[24:27], v247 offset:63504\n"
"v_mfma_f32_16x16x32_bf16 v[104:107], a[136:139], a[204:207], v[104:107]\n"
"buffer_load_dwordx4 v254, s[52:55], s69 offen offset:3072  lds\n"
"v_mfma_f32_16x16x32_bf16 v[136:139], a[104:107], a[176:179], 0\n"
"v_mfma_f32_16x16x32_bf16 v[136:139], a[136:139], a[208:211], v[136:139]\n"
"v_add_u32 v254, 64, v254\n"
"s_add_u32 m0, s71, 16384\n"
"v_mfma_f32_16x16x32_bf16 v[168:171], a[104:107], a[180:183], 0\n"
"buffer_load_dwordx4 v253, s[48:51], s64 offen offset:1024  lds\n"
"v_mfma_f32_16x16x32_bf16 v[168:171], a[136:139], a[212:215], v[168:171]\n"
"ds_read_b64_tr_b16 a[104:105], v250 offset:49152\n"
"ds_read_b64_tr_b16 a[106:107], v250 offset:49664\n"
"v_mfma_f32_16x16x32_bf16 v[12:15], a[108:111], a[160:163], 0\n"
"buffer_load_dwordx4 v253, s[48:51], 0 offen offset:3072  lds\n"
"v_mfma_f32_16x16x32_bf16 v[12:15], a[140:143], a[192:195], v[12:15]\n"
"ds_read_b64_tr_b16 a[136:137], v252 offset:0\n"
"ds_read_b64_tr_b16 a[138:139], v252 offset:512\n"
"v_mfma_f32_16x16x32_bf16 v[44:47], a[108:111], a[164:167], 0\n"
"v_add_u32 v253, 0x40000, v253\n"
"s_add_u32 m0, s71, 32768\n"
"v_mfma_f32_16x16x32_bf16 v[44:47], a[140:143], a[196:199], v[44:47]\n"
"buffer_load_dwordx4 v253, s[48:51], s64 offen offset:1024  lds\n"
"v_mfma_f32_16x16x32_bf16 v[76:79], a[108:111], a[168:171], 0\n"
"v_mfma_f32_16x16x32_bf16 v[76:79], a[140:143], a[200:203], v[76:79]\n"
"buffer_load_dwordx4 v253, s[48:51], 0 offen offset:3072  lds\n"
"v_mfma_f32_16x16x32_bf16 v[108:111], a[108:111], a[172:175], 0\n"
"v_mfma_f32_16x16x32_bf16 v[108:111], a[140:143], a[204:207], v[108:111]\n"
"v_add_u32 v253, 0x40000, v253\n"
"s_add_u32 m0, s73, 32768\n"
"v_mfma_f32_16x16x32_bf16 v[140:143], a[108:111], a[176:179], 0\n"
"buffer_load_dwordx4 v254, s[52:55], s66 offen offset:0  lds\n"
"v_mfma_f32_16x16x32_bf16 v[140:143], a[140:143], a[208:211], v[140:143]\n"
"v_mfma_f32_16x16x32_bf16 v[172:175], a[108:111], a[180:183], 0\n"
"buffer_load_dwordx4 v254, s[52:55], s67 offen offset:1024  lds\n"
"v_mfma_f32_16x16x32_bf16 v[172:175], a[140:143], a[212:215], v[172:175]\n"
"ds_read_b64_tr_b16 a[108:109], v250 offset:49160\n"
"ds_read_b64_tr_b16 a[110:111], v250 offset:49672\n"
"v_mfma_f32_16x16x32_bf16 v[16:19], a[112:115], a[160:163], 0\n"
"buffer_load_dwordx4 v254, s[52:55], s68 offen offset:2048  lds\n"
"v_mfma_f32_16x16x32_bf16 v[16:19], a[144:147], a[192:195], v[16:19]\n"
"ds_read_b64_tr_b16 a[140:141], v252 offset:8\n"
"ds_read_b64_tr_b16 a[142:143], v252 offset:520\n"
"v_mfma_f32_16x16x32_bf16 v[48:51], a[112:115], a[164:167], 0\n"
"buffer_load_dwordx4 v254, s[52:55], s69 offen offset:3072  lds\n"
"v_mfma_f32_16x16x32_bf16 v[48:51], a[144:147], a[196:199], v[48:51]\n"
"v_mfma_f32_16x16x32_bf16 v[80:83], a[112:115], a[168:171], 0\n"
"v_add_u32 v254, 64, v254\n"
"v_mfma_f32_16x16x32_bf16 v[80:83], a[144:147], a[200:203], v[80:83]\n"
"buffer_load_dword v208, v209, s[52:55], 0 offen \n"
"v_mfma_f32_16x16x32_bf16 v[112:115], a[112:115], a[172:175], 0\n"
"v_mfma_f32_16x16x32_bf16 v[112:115], a[144:147], a[204:207], v[112:115]\n"
"v_add_u32 v209, 0x100, v209\n"
"v_mfma_f32_16x16x32_bf16 v[144:147], a[112:115], a[176:179], 0\n"
"buffer_load_dword v208, v210, s[48:51], 0 offen \n"
"v_mfma_f32_16x16x32_bf16 v[144:147], a[144:147], a[208:211], v[144:147]\n"
"v_mfma_f32_16x16x32_bf16 v[176:179], a[112:115], a[180:183], 0\n"
"v_add_u32 v210, 0x100000, v210\n"
"v_mfma_f32_16x16x32_bf16 v[176:179], a[144:147], a[212:215], v[176:179]\n"
"v_mfma_f32_16x16x32_bf16 v[192:195], a[112:115], a[184:187], 0\n"
"v_mfma_f32_16x16x32_bf16 v[192:195], a[144:147], a[216:219], v[192:195]\n"
"v_mfma_f32_16x16x32_bf16 v[20:23], a[116:119], a[160:163], 0\n"
"v_mfma_f32_16x16x32_bf16 v[20:23], a[148:151], a[192:195], v[20:23]\n"
"v_mfma_f32_16x16x32_bf16 v[52:55], a[116:119], a[164:167], 0\n"
"v_mfma_f32_16x16x32_bf16 v[52:55], a[148:151], a[196:199], v[52:55]\n"
"v_mfma_f32_16x16x32_bf16 v[84:87], a[116:119], a[168:171], 0\n"
"v_mfma_f32_16x16x32_bf16 v[84:87], a[148:151], a[200:203], v[84:87]\n"
"s_waitcnt vmcnt(18) lgkmcnt(0)\n"
"s_barrier\n"
"v_mfma_f32_16x16x32_bf16 v[116:119], a[116:119], a[172:175], 0\n"
"s_add_u32 m0, s71, 49152\n"
"ds_read_b128 a[28:31], v248 offset:24592\n"
"v_mfma_f32_16x16x32_bf16 v[116:119], a[148:151], a[204:207], v[116:119]\n"
"buffer_load_dwordx4 v253, s[48:51], s63 offen offset:0  lds\n"
"v_mfma_f32_16x16x32_bf16 v[148:151], a[116:119], a[176:179], 0\n"
"ds_read_b128 a[32:35], v248 offset:25616\n"
"ds_read_b128 a[36:39], v248 offset:26640\n"
"v_mfma_f32_16x16x32_bf16 v[148:151], a[148:151], a[208:211], v[148:151]\n"
"ds_read_b128 a[40:43], v248 offset:27664\n"
"ds_read_b128 a[44:47], v248 offset:28688\n"
"v_mfma_f32_16x16x32_bf16 v[180:183], a[116:119], a[180:183], 0\n"
"buffer_load_dwordx4 v253, s[48:51], s65 offen offset:2048  lds\n"
"v_mfma_f32_16x16x32_bf16 v[180:183], a[148:151], a[212:215], v[180:183]\n"
"ds_read_b128 a[48:51], v248 offset:29712\n"
"ds_read_b128 a[52:55], v248 offset:30736\n"
"v_mfma_f32_16x16x32_bf16 v[196:199], a[116:119], a[184:187], 0\n"
"ds_read_b64_tr_b16 a[112:113], v249 offset:50176\n"
"ds_read_b64_tr_b16 a[114:115], v249 offset:50688\n"
"v_mfma_f32_16x16x32_bf16 v[196:199], a[148:151], a[216:219], v[196:199]\n"
"v_add_u32 v253, 0x40000, v253\n"
"s_mov_b32 m0, s72\n"
"v_mfma_f32_16x16x32_bf16 v[24:27], a[120:123], a[160:163], 0\n"
"buffer_load_dwordx4 v253, s[48:51], s63 offen offset:0  lds\n"
"v_mfma_f32_16x16x32_bf16 v[24:27], a[152:155], a[192:195], v[24:27]\n"
"ds_read_b64_tr_b16 a[144:145], v251 offset:1024\n"
"ds_read_b64_tr_b16 a[146:147], v251 offset:1536\n"
"v_mfma_f32_16x16x32_bf16 v[56:59], a[120:123], a[164:167], 0\n"
"ds_read_b64_tr_b16 a[116:117], v249 offset:50184\n"
"ds_read_b64_tr_b16 a[118:119], v249 offset:50696\n"
"v_mfma_f32_16x16x32_bf16 v[56:59], a[152:155], a[196:199], v[56:59]\n"
"buffer_load_dwordx4 v253, s[48:51], s65 offen offset:2048  lds\n"
"v_mfma_f32_16x16x32_bf16 v[88:91], a[120:123], a[168:171], 0\n"
"ds_read_b64_tr_b16 a[148:149], v251 offset:1032\n"
"ds_read_b64_tr_b16 a[150:151], v251 offset:1544\n"
"v_mfma_f32_16x16x32_bf16 v[88:91], a[152:155], a[200:203], v[88:91]\n"
"v_mfma_f32_16x16x32_bf16 v[120:123], a[120:123], a[172:175], 0\n"
"v_subrev_u32 v253, 0x40000, v253\n"
"s_add_u32 m0, s73, 49152\n"
"v_mfma_f32_16x16x32_bf16 v[120:123], a[152:155], a[204:207], v[120:123]\n"
"buffer_load_dwordx4 v254, s[52:55], s66 offen offset:0  lds\n"
"v_mfma_f32_16x16x32_bf16 v[152:155], a[120:123], a[176:179], 0\n"
"v_mfma_f32_16x16x32_bf16 v[152:155], a[152:155], a[208:211], v[152:155]\n"
"v_mfma_f32_16x16x32_bf16 v[184:187], a[120:123], a[180:183], 0\n"
"buffer_load_dwordx4 v254, s[52:55], s67 offen offset:1024  lds\n"
"v_mfma_f32_16x16x32_bf16 v[184:187], a[152:155], a[212:215], v[184:187]\n"
"v_mfma_f32_16x16x32_bf16 v[200:203], a[120:123], a[184:187], 0\n"
"v_mfma_f32_16x16x32_bf16 v[200:203], a[152:155], a[216:219], v[200:203]\n"
"buffer_load_dwordx4 v254, s[52:55], s68 offen offset:2048  lds\n"
"v_mfma_f32_16x16x32_bf16 v[28:31], a[124:127], a[160:163], 0\n"
"ds_read_b64_tr_b16 a[120:121], v250 offset:50176\n"
"ds_read_b64_tr_b16 a[122:123], v250 offset:50688\n"
"v_mfma_f32_16x16x32_bf16 v[28:31], a[156:159], a[192:195], v[28:31]\n"
"ds_read_b64_tr_b16 a[152:153], v252 offset:1024\n"
"ds_read_b64_tr_b16 a[154:155], v252 offset:1536\n"
"v_mfma_f32_16x16x32_bf16 v[60:63], a[124:127], a[164:167], 0\n"
"buffer_load_dwordx4 v254, s[52:55], s69 offen offset:3072  lds\n"
"v_mfma_f32_16x16x32_bf16 v[60:63], a[156:159], a[196:199], v[60:63]\n"
"v_mfma_f32_16x16x32_bf16 v[92:95], a[124:127], a[168:171], 0\n"
"v_mfma_f32_16x16x32_bf16 v[92:95], a[156:159], a[200:203], v[92:95]\n"
"v_add_u32 v254, 64, v254\n"
"v_mfma_f32_16x16x32_bf16 v[124:127], a[124:127], a[172:175], 0\n"
"v_mfma_f32_16x16x32_bf16 v[124:127], a[156:159], a[204:207], v[124:127]\n"
"v_mfma_f32_16x16x32_bf16 v[156:159], a[124:127], a[176:179], 0\n"
"v_mfma_f32_16x16x32_bf16 v[156:159], a[156:159], a[208:211], v[156:159]\n"
"v_mfma_f32_16x16x32_bf16 v[188:191], a[124:127], a[180:183], 0\n"
"v_mfma_f32_16x16x32_bf16 v[188:191], a[156:159], a[212:215], v[188:191]\n"
"v_mfma_f32_16x16x32_bf16 v[204:207], a[124:127], a[184:187], 0\n"
"v_mfma_f32_16x16x32_bf16 v[204:207], a[156:159], a[216:219], v[204:207]\n"
"s_waitcnt lgkmcnt(12)\n"
"v_mfma_f32_16x16x32_bf16 v[0:3], a[96:99], a[0:3], v[0:3]\n"
"ds_read_b64_tr_b16 a[124:125], v250 offset:50184\n"
"v_mfma_f32_16x16x32_bf16 v[0:3], a[128:131], a[28:31], v[0:3]\n"
"ds_read_b64_tr_b16 a[126:127], v250 offset:50696\n"
"v_mfma_f32_16x16x32_bf16 v[32:35], a[96:99], a[4:7], v[32:35]\n"
"ds_read_b64_tr_b16 a[156:157], v252 offset:1032\n"
"v_mfma_f32_16x16x32_bf16 v[32:35], a[128:131], a[32:35], v[32:35]\n"
"ds_read_b64_tr_b16 a[158:159], v252 offset:1544\n"
"v_mfma_f32_16x16x32_bf16 v[64:67], a[96:99], a[8:11], v[64:67]\n"
"v_mfma_f32_16x16x32_bf16 v[64:67], a[128:131], a[36:39], v[64:67]\n"
"v_mfma_f32_16x16x32_bf16 v[96:99], a[96:99], a[12:15], v[96:99]\n"
"v_mfma_f32_16x16x32_bf16 v[96:99], a[128:131], a[40:43], v[96:99]\n"
"v_mfma_f32_16x16x32_bf16 v[128:131], a[96:99], a[16:19], v[128:131]\n"
"v_mfma_f32_16x16x32_bf16 v[128:131], a[128:131], a[44:47], v[128:131]\n"
"v_mfma_f32_16x16x32_bf16 v[160:163], a[96:99], a[20:23], v[160:163]\n"
"v_mfma_f32_16x16x32_bf16 v[160:163], a[128:131], a[48:51], v[160:163]\n"
"v_mfma_f32_16x16x32_bf16 v[4:7], a[100:103], a[0:3], v[4:7]\n"
"v_mfma_f32_16x16x32_bf16 v[4:7], a[132:135], a[28:31], v[4:7]\n"
"s_waitcnt vmcnt(18) lgkmcnt(0)\n"
"s_barrier\n"
"v_mfma_f32_16x16x32_bf16 v[36:39], a[100:103], a[4:7], v[36:39]\n"
"s_add_u32 m0, s71, 49152\n"
"v_mfma_f32_16x16x32_bf16 v[36:39], a[132:135], a[32:35], v[36:39]\n"
"buffer_load_dwordx4 v253, s[48:51], s64 offen offset:1024  lds\n"
"v_mfma_f32_16x16x32_bf16 v[68:71], a[100:103], a[8:11], v[68:71]\n"
"ds_read_b64_tr_b16 a[96:97], v249 offset:16384\n"
"v_mfma_f32_16x16x32_bf16 v[68:71], a[132:135], a[36:39], v[68:71]\n"
"ds_read_b64_tr_b16 a[98:99], v249 offset:16896\n"
"v_mfma_f32_16x16x32_bf16 v[100:103], a[100:103], a[12:15], v[100:103]\n"
"ds_read_b64_tr_b16 a[128:129], v249 offset:32768\n"
"v_mfma_f32_16x16x32_bf16 v[100:103], a[132:135], a[40:43], v[100:103]\n"
"buffer_load_dwordx4 v253, s[48:51], 0 offen offset:3072  lds\n"
"v_mfma_f32_16x16x32_bf16 v[132:135], a[100:103], a[16:19], v[132:135]\n"
"ds_read_b64_tr_b16 a[130:131], v249 offset:33280\n"
"v_mfma_f32_16x16x32_bf16 v[132:135], a[132:135], a[44:47], v[132:135]\n"
"ds_read_b128 a[160:163], v247 offset:24592\n"
"v_mfma_f32_16x16x32_bf16 v[164:167], a[100:103], a[20:23], v[164:167]\n"
"ds_read_b128 a[164:167], v247 offset:25616\n"
"v_mfma_f32_16x16x32_bf16 v[164:167], a[132:135], a[48:51], v[164:167]\n"
"v_add_u32 v253, 0x40000, v253\n"
"s_mov_b32 m0, s72\n"
"v_mfma_f32_16x16x32_bf16 v[8:11], a[104:107], a[0:3], v[8:11]\n"
"buffer_load_dwordx4 v253, s[48:51], s64 offen offset:1024  lds\n"
"v_mfma_f32_16x16x32_bf16 v[8:11], a[136:139], a[28:31], v[8:11]\n"
"ds_read_b64_tr_b16 a[100:101], v249 offset:16392\n"
"v_mfma_f32_16x16x32_bf16 v[40:43], a[104:107], a[4:7], v[40:43]\n"
"ds_read_b64_tr_b16 a[102:103], v249 offset:16904\n"
"v_mfma_f32_16x16x32_bf16 v[40:43], a[136:139], a[32:35], v[40:43]\n"
"ds_read_b64_tr_b16 a[132:133], v249 offset:32776\n"
"v_mfma_f32_16x16x32_bf16 v[72:75], a[104:107], a[8:11], v[72:75]\n"
"buffer_load_dwordx4 v253, s[48:51], 0 offen offset:3072  lds\n"
"v_mfma_f32_16x16x32_bf16 v[72:75], a[136:139], a[36:39], v[72:75]\n"
"ds_read_b64_tr_b16 a[134:135], v249 offset:33288\n"
"v_mfma_f32_16x16x32_bf16 v[104:107], a[104:107], a[12:15], v[104:107]\n"
"ds_read_b128 a[168:171], v247 offset:26640\n"
"v_add_u32 v212, 1, v212\n"
"v_mfma_f32_16x16x32_bf16 v[104:107], a[136:139], a[40:43], v[104:107]\n"
"ds_read_b128 a[172:175], v247 offset:27664\n"
"s_lshl_b32 s80, s77, 2\n"
"v_mfma_f32_16x16x32_bf16 v[136:139], a[104:107], a[16:19], v[136:139]\n"
"v_add_u32 v253, 0x40000, v253\n"
"s_mov_b32 m0, s74\n"
"v_mfma_f32_16x16x32_bf16 v[136:139], a[136:139], a[44:47], v[136:139]\n"
"buffer_load_dwordx4 v254, s[52:55], s66 offen offset:0  lds\n"
"v_mfma_f32_16x16x32_bf16 v[168:171], a[104:107], a[20:23], v[168:171]\n"
"ds_read_b128 a[176:179], v247 offset:28688\n"
"s_add_u32 s80, s80, s76\n"
"v_mfma_f32_16x16x32_bf16 v[168:171], a[136:139], a[48:51], v[168:171]\n"
"ds_read_b64_tr_b16 a[104:105], v250 offset:16384\n"
"s_lshl_b32 s80, s80, 9\n"
"v_mfma_f32_16x16x32_bf16 v[12:15], a[108:111], a[0:3], v[12:15]\n"
"ds_read_b64_tr_b16 a[106:107], v250 offset:16896\n"
"s_lshl_b32 s81, s78, 3\n"
"v_mfma_f32_16x16x32_bf16 v[12:15], a[140:143], a[28:31], v[12:15]\n"
"buffer_load_dwordx4 v254, s[52:55], s67 offen offset:1024  lds\n"
"v_mfma_f32_16x16x32_bf16 v[44:47], a[108:111], a[4:7], v[44:47]\n"
"ds_read_b64_tr_b16 a[136:137], v250 offset:32768\n"
"s_add_u32 s80, s80, s81\n"
"v_mfma_f32_16x16x32_bf16 v[44:47], a[140:143], a[32:35], v[44:47]\n"
"ds_read_b64_tr_b16 a[138:139], v250 offset:33280\n"
"s_add_u32 s80, s80, 33685504\n"
"v_mfma_f32_16x16x32_bf16 v[76:79], a[108:111], a[8:11], v[76:79]\n"
"ds_read_b128 a[180:183], v247 offset:29712\n"
"ds_write_b64 v215, v[212:213]\n"
"v_mfma_f32_16x16x32_bf16 v[76:79], a[140:143], a[36:39], v[76:79]\n"
"buffer_load_dwordx4 v254, s[52:55], s68 offen offset:2048  lds\n"
"v_mfma_f32_16x16x32_bf16 v[108:111], a[108:111], a[12:15], v[108:111]\n"
"ds_read_b128 a[184:187], v247 offset:30736\n"
"v_mfma_f32_16x16x32_bf16 v[108:111], a[140:143], a[40:43], v[108:111]\n"
"buffer_store_dwordx2 v[212:213], v216, s[56:59], s80 offen sc1\n"
"v_mfma_f32_16x16x32_bf16 v[140:143], a[108:111], a[16:19], v[140:143]\n"
"v_mfma_f32_16x16x32_bf16 v[140:143], a[140:143], a[44:47], v[140:143]\n"
"buffer_load_dwordx4 v254, s[52:55], s69 offen offset:3072  lds\n"
"v_mfma_f32_16x16x32_bf16 v[172:175], a[108:111], a[20:23], v[172:175]\n"
"v_mfma_f32_16x16x32_bf16 v[172:175], a[140:143], a[48:51], v[172:175]\n"
"ds_read_b64_tr_b16 a[108:109], v250 offset:16392\n"
"v_mfma_f32_16x16x32_bf16 v[16:19], a[112:115], a[0:3], v[16:19]\n"
"ds_read_b64_tr_b16 a[110:111], v250 offset:16904\n"
"v_mfma_f32_16x16x32_bf16 v[16:19], a[144:147], a[28:31], v[16:19]\n"
"v_add_u32 v254, 64, v254\n"
"v_mfma_f32_16x16x32_bf16 v[48:51], a[112:115], a[4:7], v[48:51]\n"
"ds_read_b64_tr_b16 a[140:141], v250 offset:32776\n"
"v_mfma_f32_16x16x32_bf16 v[48:51], a[144:147], a[32:35], v[48:51]\n"
"ds_read_b64_tr_b16 a[142:143], v250 offset:33288\n"
"v_mfma_f32_16x16x32_bf16 v[80:83], a[112:115], a[8:11], v[80:83]\n"
"v_mov_b32 v224, 0\n"
"v_mov_b32 v225, 0\n"
"v_mfma_f32_16x16x32_bf16 v[80:83], a[144:147], a[36:39], v[80:83]\n"
"v_mov_b32 v226, 0\n"
"v_mov_b32 v227, 0\n"
"v_mfma_f32_16x16x32_bf16 v[112:115], a[112:115], a[12:15], v[112:115]\n"
"v_mov_b32 v228, 0\n"
"v_mov_b32 v229, 0\n"
"v_mfma_f32_16x16x32_bf16 v[112:115], a[144:147], a[40:43], v[112:115]\n"
"v_mov_b32 v230, 0\n"
"v_mov_b32 v231, 0\n"
"v_mfma_f32_16x16x32_bf16 v[144:147], a[112:115], a[16:19], v[144:147]\n"
"v_mov_b32 v232, 0\n"
"v_mov_b32 v233, 0\n"
"v_mfma_f32_16x16x32_bf16 v[144:147], a[144:147], a[44:47], v[144:147]\n"
"v_mov_b32 v234, 0\n"
"v_mov_b32 v235, 0\n"
"v_mfma_f32_16x16x32_bf16 v[176:179], a[112:115], a[20:23], v[176:179]\n"
"v_mov_b32 v236, 0\n"
"v_mov_b32 v237, 0\n"
"v_mfma_f32_16x16x32_bf16 v[176:179], a[144:147], a[48:51], v[176:179]\n"
"v_mov_b32 v238, 0\n"
"v_mov_b32 v239, 0\n"
"v_mfma_f32_16x16x32_bf16 v[192:195], a[112:115], a[24:27], v[192:195]\n"
"v_accvgpr_write_b32 a224, 0\n"
"v_accvgpr_write_b32 a225, 0\n"
"v_mfma_f32_16x16x32_bf16 v[192:195], a[144:147], a[52:55], v[192:195]\n"
"v_accvgpr_write_b32 a226, 0\n"
"v_accvgpr_write_b32 a227, 0\n"
"v_mfma_f32_16x16x32_bf16 v[20:23], a[116:119], a[0:3], v[20:23]\n"
"v_accvgpr_write_b32 a228, 0\n"
"v_accvgpr_write_b32 a229, 0\n"
"v_mfma_f32_16x16x32_bf16 v[20:23], a[148:151], a[28:31], v[20:23]\n"
"v_accvgpr_write_b32 a230, 0\n"
"v_accvgpr_write_b32 a231, 0\n"
"v_mfma_f32_16x16x32_bf16 v[52:55], a[116:119], a[4:7], v[52:55]\n"
"v_accvgpr_write_b32 a232, 0\n"
"v_accvgpr_write_b32 a233, 0\n"
"v_mfma_f32_16x16x32_bf16 v[52:55], a[148:151], a[32:35], v[52:55]\n"
"v_accvgpr_write_b32 a234, 0\n"
"v_accvgpr_write_b32 a235, 0\n"
"v_mfma_f32_16x16x32_bf16 v[84:87], a[116:119], a[8:11], v[84:87]\n"
"v_accvgpr_write_b32 a236, 0\n"
"v_accvgpr_write_b32 a237, 0\n"
"v_mfma_f32_16x16x32_bf16 v[84:87], a[148:151], a[36:39], v[84:87]\n"
"s_waitcnt vmcnt(19) lgkmcnt(0)\n"
"s_barrier\n"
"v_accvgpr_write_b32 a238, 0\n"
"v_mfma_f32_16x16x32_bf16 v[116:119], a[116:119], a[12:15], v[116:119]\n"
"s_add_u32 m0, s71, 16384\n"
"v_accvgpr_write_b32 a239, 0\n"
"v_mfma_f32_16x16x32_bf16 v[116:119], a[148:151], a[40:43], v[116:119]\n"
"buffer_load_dwordx4 v253, s[48:51], s63 offen offset:0  lds\n"
"v_mfma_f32_16x16x32_bf16 v[148:151], a[116:119], a[16:19], v[148:151]\n"
"ds_read_b128 a[192:195], v247 offset:40976\n"
"v_accvgpr_write_b32 a240, 0\n"
"v_mfma_f32_16x16x32_bf16 v[148:151], a[148:151], a[44:47], v[148:151]\n"
"ds_read_b128 a[196:199], v247 offset:42000\n"
"v_accvgpr_write_b32 a241, 0\n"
"v_mfma_f32_16x16x32_bf16 v[180:183], a[116:119], a[20:23], v[180:183]\n"
"buffer_load_dwordx4 v253, s[48:51], s65 offen offset:2048  lds\n"
"v_mfma_f32_16x16x32_bf16 v[180:183], a[148:151], a[48:51], v[180:183]\n"
"ds_read_b128 a[200:203], v247 offset:43024\n"
"v_accvgpr_write_b32 a242, 0\n"
"v_mfma_f32_16x16x32_bf16 v[196:199], a[116:119], a[24:27], v[196:199]\n"
"ds_read_b128 a[204:207], v247 offset:44048\n"
"v_accvgpr_write_b32 a243, 0\n"
"v_mfma_f32_16x16x32_bf16 v[196:199], a[148:151], a[52:55], v[196:199]\n"
"v_add_u32 v253, 0x40000, v253\n"
"s_add_u32 m0, s71, 32768\n"
"v_mfma_f32_16x16x32_bf16 v[24:27], a[120:123], a[0:3], v[24:27]\n"
"buffer_load_dwordx4 v253, s[48:51], s63 offen offset:0  lds\n"
"v_mfma_f32_16x16x32_bf16 v[24:27], a[152:155], a[28:31], v[24:27]\n"
"ds_read_b128 a[208:211], v247 offset:45072\n"
"v_accvgpr_write_b32 a244, 0\n"
"v_mfma_f32_16x16x32_bf16 v[56:59], a[120:123], a[4:7], v[56:59]\n"
"ds_read_b128 a[212:215], v247 offset:46096\n"
"v_accvgpr_write_b32 a245, 0\n"
"v_mfma_f32_16x16x32_bf16 v[56:59], a[152:155], a[32:35], v[56:59]\n"
"buffer_load_dwordx4 v253, s[48:51], s65 offen offset:2048  lds\n"
"v_mfma_f32_16x16x32_bf16 v[88:91], a[120:123], a[8:11], v[88:91]\n"
"ds_read_b128 a[216:219], v247 offset:47120\n"
"v_accvgpr_write_b32 a246, 0\n"
"v_mfma_f32_16x16x32_bf16 v[88:91], a[152:155], a[36:39], v[88:91]\n"
"ds_read_b64_tr_b16 a[112:113], v249 offset:17408\n"
"v_accvgpr_write_b32 a247, 0\n"
"v_mfma_f32_16x16x32_bf16 v[120:123], a[120:123], a[12:15], v[120:123]\n"
"v_subrev_u32 v253, 0x40000, v253\n"
"s_add_u32 m0, s73, 16384\n"
"v_mfma_f32_16x16x32_bf16 v[120:123], a[152:155], a[40:43], v[120:123]\n"
"buffer_load_dwordx4 v254, s[52:55], s66 offen offset:0  lds\n"
"v_mfma_f32_16x16x32_bf16 v[152:155], a[120:123], a[16:19], v[152:155]\n"
"ds_read_b64_tr_b16 a[114:115], v249 offset:17920\n"
"v_accvgpr_write_b32 a248, 0\n"
"v_mfma_f32_16x16x32_bf16 v[152:155], a[152:155], a[44:47], v[152:155]\n"
"ds_read_b64_tr_b16 a[144:145], v249 offset:33792\n"
"v_accvgpr_write_b32 a249, 0\n"
"v_mfma_f32_16x16x32_bf16 v[184:187], a[120:123], a[20:23], v[184:187]\n"
"buffer_load_dwordx4 v254, s[52:55], s67 offen offset:1024  lds\n"
"v_mfma_f32_16x16x32_bf16 v[184:187], a[152:155], a[48:51], v[184:187]\n"
"ds_read_b64_tr_b16 a[146:147], v249 offset:34304\n"
"v_accvgpr_write_b32 a250, 0\n"
"v_mfma_f32_16x16x32_bf16 v[200:203], a[120:123], a[24:27], v[200:203]\n"
"ds_read_b64_tr_b16 a[116:117], v249 offset:17416\n"
"v_accvgpr_write_b32 a251, 0\n"
"v_mfma_f32_16x16x32_bf16 v[200:203], a[152:155], a[52:55], v[200:203]\n"
"buffer_load_dwordx4 v254, s[52:55], s68 offen offset:2048  lds\n"
"v_mfma_f32_16x16x32_bf16 v[28:31], a[124:127], a[0:3], v[28:31]\n"
"ds_read_b64_tr_b16 a[118:119], v249 offset:17928\n"
"v_accvgpr_write_b32 a252, 0\n"
"v_mfma_f32_16x16x32_bf16 v[28:31], a[156:159], a[28:31], v[28:31]\n"
"ds_read_b64_tr_b16 a[148:149], v249 offset:33800\n"
"v_accvgpr_write_b32 a253, 0\n"
"v_mfma_f32_16x16x32_bf16 v[60:63], a[124:127], a[4:7], v[60:63]\n"
"buffer_load_dwordx4 v254, s[52:55], s69 offen offset:3072  lds\n"
"v_mfma_f32_16x16x32_bf16 v[60:63], a[156:159], a[32:35], v[60:63]\n"
"ds_read_b64_tr_b16 a[150:151], v249 offset:34312\n"
"v_accvgpr_write_b32 a254, 0\n"
"v_mfma_f32_16x16x32_bf16 v[92:95], a[124:127], a[8:11], v[92:95]\n"
"ds_read_b64_tr_b16 a[120:121], v250 offset:17408\n"
"v_accvgpr_write_b32 a255, 0\n"
"v_mfma_f32_16x16x32_bf16 v[92:95], a[156:159], a[36:39], v[92:95]\n"
"v_add_u32 v254, 64, v254\n"
"v_mfma_f32_16x16x32_bf16 v[124:127], a[124:127], a[12:15], v[124:127]\n"
"ds_read_b64_tr_b16 a[122:123], v250 offset:17920\n"
"v_mfma_f32_16x16x32_bf16 v[124:127], a[156:159], a[40:43], v[124:127]\n"
"ds_read_b64_tr_b16 a[152:153], v250 offset:33792\n"
"v_mfma_f32_16x16x32_bf16 v[156:159], a[124:127], a[16:19], v[156:159]\n"
"ds_read_b64_tr_b16 a[154:155], v250 offset:34304\n"
"v_mfma_f32_16x16x32_bf16 v[156:159], a[156:159], a[44:47], v[156:159]\n"
"v_mfma_f32_16x16x32_bf16 v[188:191], a[124:127], a[20:23], v[188:191]\n"
"v_mfma_f32_16x16x32_bf16 v[188:191], a[156:159], a[48:51], v[188:191]\n"
"v_mfma_f32_16x16x32_bf16 v[204:207], a[124:127], a[24:27], v[204:207]\n"
"v_mfma_f32_16x16x32_bf16 v[204:207], a[156:159], a[52:55], v[204:207]\n"
"s_waitcnt lgkmcnt(12)\n"
".p2align 6\n"
"Lq1%=:\n"
"v_mfma_f32_16x16x32_bf16 v[0:3], a[96:99], a[160:163], v[0:3]\n"
"ds_read_b64_tr_b16 a[124:125], v250 offset:17416\n"
"v_mfma_f32_16x16x32_bf16 v[0:3], a[128:131], a[192:195], v[0:3]\n"
"ds_read_b64_tr_b16 a[126:127], v250 offset:17928\n"
"v_mfma_f32_16x16x32_bf16 v[32:35], a[96:99], a[164:167], v[32:35]\n"
"ds_read_b64_tr_b16 a[156:157], v250 offset:33800\n"
"v_mfma_f32_16x16x32_bf16 v[32:35], a[128:131], a[196:199], v[32:35]\n"
"ds_read_b64_tr_b16 a[158:159], v250 offset:34312\n"
"v_mfma_f32_16x16x32_bf16 v[64:67], a[96:99], a[168:171], v[64:67]\n"
"v_mfma_f32_16x16x32_bf16 v[64:67], a[128:131], a[200:203], v[64:67]\n"
"v_mfma_f32_16x16x32_bf16 v[96:99], a[96:99], a[172:175], v[96:99]\n"
"v_mfma_f32_16x16x32_bf16 v[96:99], a[128:131], a[204:207], v[96:99]\n"
"v_mfma_f32_16x16x32_bf16 v[128:131], a[96:99], a[176:179], v[128:131]\n"
"v_mfma_f32_16x16x32_bf16 v[128:131], a[128:131], a[208:211], v[128:131]\n"
"v_mfma_f32_16x16x32_bf16 v[160:163], a[96:99], a[180:183], v[160:163]\n"
"v_mfma_f32_16x16x32_bf16 v[160:163], a[128:131], a[212:215], v[160:163]\n"
"v_mfma_f32_16x16x32_bf16 v[4:7], a[100:103], a[160:163], v[4:7]\n"
"v_mfma_f32_16x16x32_bf16 v[4:7], a[132:135], a[192:195], v[4:7]\n"
"s_waitcnt vmcnt(16) lgkmcnt(0)\n"
"s_barrier\n"
"v_mfma_f32_16x16x32_bf16 v[36:39], a[100:103], a[164:167], v[36:39]\n"
"s_add_u32 m0, s71, 16384\n"
"v_mfma_f32_16x16x32_bf16 v[36:39], a[132:135], a[196:199], v[36:39]\n"
"buffer_load_dwordx4 v253, s[48:51], s64 offen offset:1024  lds\n"
"v_mfma_f32_16x16x32_bf16 v[68:71], a[100:103], a[168:171], v[68:71]\n"
"ds_read_b64_tr_b16 a[96:97], v249 offset:49152\n"
"v_mfma_f32_16x16x32_bf16 v[68:71], a[132:135], a[200:203], v[68:71]\n"
"ds_read_b64_tr_b16 a[98:99], v249 offset:49664\n"
"v_mfma_f32_16x16x32_bf16 v[100:103], a[100:103], a[172:175], v[100:103]\n"
"ds_read_b64_tr_b16 a[128:129], v251 offset:0\n"
"v_mfma_f32_16x16x32_bf16 v[100:103], a[132:135], a[204:207], v[100:103]\n"
"buffer_load_dwordx4 v253, s[48:51], 0 offen offset:3072  lds\n"
"v_mfma_f32_16x16x32_bf16 v[132:135], a[100:103], a[176:179], v[132:135]\n"
"ds_read_b64_tr_b16 a[130:131], v251 offset:512\n"
"v_mfma_f32_16x16x32_bf16 v[132:135], a[132:135], a[208:211], v[132:135]\n"
"ds_read_b128 a[0:3], v247 offset:57360\n"
"v_mfma_f32_16x16x32_bf16 v[164:167], a[100:103], a[180:183], v[164:167]\n"
"ds_read_b128 a[4:7], v247 offset:58384\n"
"v_mfma_f32_16x16x32_bf16 v[164:167], a[132:135], a[212:215], v[164:167]\n"
"v_add_u32 v253, 0x40000, v253\n"
"s_add_u32 m0, s71, 32768\n"
"v_mfma_f32_16x16x32_bf16 v[8:11], a[104:107], a[160:163], v[8:11]\n"
"buffer_load_dwordx4 v253, s[48:51], s64 offen offset:1024  lds\n"
"v_mfma_f32_16x16x32_bf16 v[8:11], a[136:139], a[192:195], v[8:11]\n"
"ds_read_b64_tr_b16 a[100:101], v249 offset:49160\n"
"v_mfma_f32_16x16x32_bf16 v[40:43], a[104:107], a[164:167], v[40:43]\n"
"ds_read_b64_tr_b16 a[102:103], v249 offset:49672\n"
"v_mfma_f32_16x16x32_bf16 v[40:43], a[136:139], a[196:199], v[40:43]\n"
"ds_read_b64_tr_b16 a[132:133], v251 offset:8\n"
"v_mfma_f32_16x16x32_bf16 v[72:75], a[104:107], a[168:171], v[72:75]\n"
"buffer_load_dwordx4 v253, s[48:51], 0 offen offset:3072  lds\n"
"v_mfma_f32_16x16x32_bf16 v[72:75], a[136:139], a[200:203], v[72:75]\n"
"ds_read_b64_tr_b16 a[134:135], v251 offset:520\n"
"v_mfma_f32_16x16x32_bf16 v[104:107], a[104:107], a[172:175], v[104:107]\n"
"ds_read_b128 a[8:11], v247 offset:59408\n"
"v_mfma_f32_16x16x32_bf16 v[104:107], a[136:139], a[204:207], v[104:107]\n"
"ds_read_b128 a[12:15], v247 offset:60432\n"
"v_mfma_f32_16x16x32_bf16 v[136:139], a[104:107], a[176:179], v[136:139]\n"
"v_add_u32 v253, 0x40000, v253\n"
"s_add_u32 m0, s73, 32768\n"
"v_mfma_f32_16x16x32_bf16 v[136:139], a[136:139], a[208:211], v[136:139]\n"
"buffer_load_dwordx4 v254, s[52:55], s66 offen offset:0  lds\n"
"v_mfma_f32_16x16x32_bf16 v[168:171], a[104:107], a[180:183], v[168:171]\n"
"ds_read_b128 a[16:19], v247 offset:61456\n"
"v_mfma_f32_16x16x32_bf16 v[168:171], a[136:139], a[212:215], v[168:171]\n"
"ds_read_b64_tr_b16 a[104:105], v250 offset:49152\n"
"v_mfma_f32_16x16x32_bf16 v[12:15], a[108:111], a[160:163], v[12:15]\n"
"ds_read_b64_tr_b16 a[106:107], v250 offset:49664\n"
"v_mfma_f32_16x16x32_bf16 v[12:15], a[140:143], a[192:195], v[12:15]\n"
"buffer_load_dwordx4 v254, s[52:55], s67 offen offset:1024  lds\n"
"v_mfma_f32_16x16x32_bf16 v[44:47], a[108:111], a[164:167], v[44:47]\n"
"ds_read_b64_tr_b16 a[136:137], v252 offset:0\n"
"v_mfma_f32_16x16x32_bf16 v[44:47], a[140:143], a[196:199], v[44:47]\n"
"ds_read_b64_tr_b16 a[138:139], v252 offset:512\n"
"v_mfma_f32_16x16x32_bf16 v[76:79], a[108:111], a[168:171], v[76:79]\n"
"ds_read_b128 a[20:23], v247 offset:62480\n"
"v_mfma_f32_16x16x32_bf16 v[76:79], a[140:143], a[200:203], v[76:79]\n"
"buffer_load_dwordx4 v254, s[52:55], s68 offen offset:2048  lds\n"
"v_mfma_f32_16x16x32_bf16 v[108:111], a[108:111], a[172:175], v[108:111]\n"
"ds_read_b128 a[24:27], v247 offset:63504\n"
"v_mfma_f32_16x16x32_bf16 v[108:111], a[140:143], a[204:207], v[108:111]\n"
"v_mfma_f32_16x16x32_bf16 v[140:143], a[108:111], a[176:179], v[140:143]\n"
"v_mfma_f32_16x16x32_bf16 v[140:143], a[140:143], a[208:211], v[140:143]\n"
"buffer_load_dwordx4 v254, s[52:55], s69 offen offset:3072  lds\n"
"v_mfma_f32_16x16x32_bf16 v[172:175], a[108:111], a[180:183], v[172:175]\n"
"v_mfma_f32_16x16x32_bf16 v[172:175], a[140:143], a[212:215], v[172:175]\n"
"ds_read_b64_tr_b16 a[108:109], v250 offset:49160\n"
"v_mfma_f32_16x16x32_bf16 v[16:19], a[112:115], a[160:163], v[16:19]\n"
"ds_read_b64_tr_b16 a[110:111], v250 offset:49672\n"
"v_mfma_f32_16x16x32_bf16 v[16:19], a[144:147], a[192:195], v[16:19]\n"
"v_add_u32 v254, 64, v254\n"
"v_mfma_f32_16x16x32_bf16 v[48:51], a[112:115], a[164:167], v[48:51]\n"
"buffer_load_dword v208, v209, s[52:55], 0 offen \n"
"v_mfma_f32_16x16x32_bf16 v[48:51], a[144:147], a[196:199], v[48:51]\n"
"ds_read_b64_tr_b16 a[140:141], v252 offset:8\n"
"v_mfma_f32_16x16x32_bf16 v[80:83], a[112:115], a[168:171], v[80:83]\n"
"ds_read_b64_tr_b16 a[142:143], v252 offset:520\n"
"v_mfma_f32_16x16x32_bf16 v[80:83], a[144:147], a[200:203], v[80:83]\n"
"v_mfma_f32_16x16x32_bf16 v[112:115], a[112:115], a[172:175], v[112:115]\n"
"v_add_u32 v209, 0x100, v209\n"
"v_mfma_f32_16x16x32_bf16 v[112:115], a[144:147], a[204:207], v[112:115]\n"
"buffer_load_dword v208, v210, s[48:51], 0 offen \n"
"v_mfma_f32_16x16x32_bf16 v[144:147], a[112:115], a[176:179], v[144:147]\n"
"v_mfma_f32_16x16x32_bf16 v[144:147], a[144:147], a[208:211], v[144:147]\n"
"v_mfma_f32_16x16x32_bf16 v[176:179], a[112:115], a[180:183], v[176:179]\n"
"v_mfma_f32_16x16x32_bf16 v[176:179], a[144:147], a[212:215], v[176:179]\n"
"v_add_u32 v210, 0x100000, v210\n"
"v_mfma_f32_16x16x32_bf16 v[192:195], a[112:115], a[184:187], v[192:195]\n"
"global_load_dword v208, v211, %8\n"
"v_mfma_f32_16x16x32_bf16 v[192:195], a[144:147], a[216:219], v[192:195]\n"
"v_mfma_f32_16x16x32_bf16 v[20:23], a[116:119], a[160:163], v[20:23]\n"
"v_mfma_f32_16x16x32_bf16 v[20:23], a[148:151], a[192:195], v[20:23]\n"
"v_mfma_f32_16x16x32_bf16 v[52:55], a[116:119], a[164:167], v[52:55]\n"
"v_add_u32 v211, 2097152, v211\n"
"v_mfma_f32_16x16x32_bf16 v[52:55], a[148:151], a[196:199], v[52:55]\n"
"v_mfma_f32_16x16x32_bf16 v[84:87], a[116:119], a[168:171], v[84:87]\n"
"v_mfma_f32_16x16x32_bf16 v[84:87], a[148:151], a[200:203], v[84:87]\n"
"s_waitcnt vmcnt(19) lgkmcnt(0)\n"
"s_barrier\n"
"v_mfma_f32_16x16x32_bf16 v[116:119], a[116:119], a[172:175], v[116:119]\n"
"s_add_u32 m0, s71, 49152\n"
"v_mfma_f32_16x16x32_bf16 v[116:119], a[148:151], a[204:207], v[116:119]\n"
"buffer_load_dwordx4 v253, s[48:51], s63 offen offset:0  lds\n"
"v_mfma_f32_16x16x32_bf16 v[148:151], a[116:119], a[176:179], v[148:151]\n"
"ds_read_b128 a[28:31], v248 offset:24592\n"
"v_mfma_f32_16x16x32_bf16 v[148:151], a[148:151], a[208:211], v[148:151]\n"
"ds_read_b128 a[32:35], v248 offset:25616\n"
"v_mfma_f32_16x16x32_bf16 v[180:183], a[116:119], a[180:183], v[180:183]\n"
"buffer_load_dwordx4 v253, s[48:51], s65 offen offset:2048  lds\n"
"v_mfma_f32_16x16x32_bf16 v[180:183], a[148:151], a[212:215], v[180:183]\n"
"ds_read_b128 a[36:39], v248 offset:26640\n"
"v_mfma_f32_16x16x32_bf16 v[196:199], a[116:119], a[184:187], v[196:199]\n"
"ds_read_b128 a[40:43], v248 offset:27664\n"
"v_mfma_f32_16x16x32_bf16 v[196:199], a[148:151], a[216:219], v[196:199]\n"
"v_add_u32 v253, 0x40000, v253\n"
"s_mov_b32 m0, s72\n"
"v_mfma_f32_16x16x32_bf16 v[24:27], a[120:123], a[160:163], v[24:27]\n"
"buffer_load_dwordx4 v253, s[48:51], s63 offen offset:0  lds\n"
"v_mfma_f32_16x16x32_bf16 v[24:27], a[152:155], a[192:195], v[24:27]\n"
"ds_read_b128 a[44:47], v248 offset:28688\n"
"v_mfma_f32_16x16x32_bf16 v[56:59], a[120:123], a[164:167], v[56:59]\n"
"ds_read_b128 a[48:51], v248 offset:29712\n"
"v_mfma_f32_16x16x32_bf16 v[56:59], a[152:155], a[196:199], v[56:59]\n"
"buffer_load_dwordx4 v253, s[48:51], s65 offen offset:2048  lds\n"
"v_mfma_f32_16x16x32_bf16 v[88:91], a[120:123], a[168:171], v[88:91]\n"
"ds_read_b128 a[52:55], v248 offset:30736\n"
"v_mfma_f32_16x16x32_bf16 v[88:91], a[152:155], a[200:203], v[88:91]\n"
"ds_read_b64_tr_b16 a[112:113], v249 offset:50176\n"
"v_mfma_f32_16x16x32_bf16 v[120:123], a[120:123], a[172:175], v[120:123]\n"
"v_subrev_u32 v253, 0x40000, v253\n"
"s_add_u32 m0, s73, 49152\n"
"v_mfma_f32_16x16x32_bf16 v[120:123], a[152:155], a[204:207], v[120:123]\n"
"buffer_load_dwordx4 v254, s[52:55], s66 offen offset:0  lds\n"
"v_mfma_f32_16x16x32_bf16 v[152:155], a[120:123], a[176:179], v[152:155]\n"
"ds_read_b64_tr_b16 a[114:115], v249 offset:50688\n"
"v_mfma_f32_16x16x32_bf16 v[152:155], a[152:155], a[208:211], v[152:155]\n"
"ds_read_b64_tr_b16 a[144:145], v251 offset:1024\n"
"v_mfma_f32_16x16x32_bf16 v[184:187], a[120:123], a[180:183], v[184:187]\n"
"buffer_load_dwordx4 v254, s[52:55], s67 offen offset:1024  lds\n"
"v_mfma_f32_16x16x32_bf16 v[184:187], a[152:155], a[212:215], v[184:187]\n"
"ds_read_b64_tr_b16 a[146:147], v251 offset:1536\n"
"v_mfma_f32_16x16x32_bf16 v[200:203], a[120:123], a[184:187], v[200:203]\n"
"ds_read_b64_tr_b16 a[116:117], v249 offset:50184\n"
"v_mfma_f32_16x16x32_bf16 v[200:203], a[152:155], a[216:219], v[200:203]\n"
"buffer_load_dwordx4 v254, s[52:55], s68 offen offset:2048  lds\n"
"v_mfma_f32_16x16x32_bf16 v[28:31], a[124:127], a[160:163], v[28:31]\n"
"ds_read_b64_tr_b16 a[118:119], v249 offset:50696\n"
"v_mfma_f32_16x16x32_bf16 v[28:31], a[156:159], a[192:195], v[28:31]\n"
"ds_read_b64_tr_b16 a[148:149], v251 offset:1032\n"
"v_mfma_f32_16x16x32_bf16 v[60:63], a[124:127], a[164:167], v[60:63]\n"
"buffer_load_dwordx4 v254, s[52:55], s69 offen offset:3072  lds\n"
"v_mfma_f32_16x16x32_bf16 v[60:63], a[156:159], a[196:199], v[60:63]\n"
"ds_read_b64_tr_b16 a[150:151], v251 offset:1544\n"
"v_mfma_f32_16x16x32_bf16 v[92:95], a[124:127], a[168:171], v[92:95]\n"
"ds_read_b64_tr_b16 a[120:121], v250 offset:50176\n"
"v_mfma_f32_16x16x32_bf16 v[92:95], a[156:159], a[200:203], v[92:95]\n"
"v_add_u32 v254, 64, v254\n"
"v_mfma_f32_16x16x32_bf16 v[124:127], a[124:127], a[172:175], v[124:127]\n"
"ds_read_b64_tr_b16 a[122:123], v250 offset:50688\n"
"v_mfma_f32_16x16x32_bf16 v[124:127], a[156:159], a[204:207], v[124:127]\n"
"ds_read_b64_tr_b16 a[152:153], v252 offset:1024\n"
"v_mfma_f32_16x16x32_bf16 v[156:159], a[124:127], a[176:179], v[156:159]\n"
"ds_read_b64_tr_b16 a[154:155], v252 offset:1536\n"
"v_mfma_f32_16x16x32_bf16 v[156:159], a[156:159], a[208:211], v[156:159]\n"
"v_mfma_f32_16x16x32_bf16 v[188:191], a[124:127], a[180:183], v[188:191]\n"
"v_mfma_f32_16x16x32_bf16 v[188:191], a[156:159], a[212:215], v[188:191]\n"
"v_mfma_f32_16x16x32_bf16 v[204:207], a[124:127], a[184:187], v[204:207]\n"
"v_mfma_f32_16x16x32_bf16 v[204:207], a[156:159], a[216:219], v[204:207]\n"
"s_waitcnt lgkmcnt(12)\n"
"v_mfma_f32_16x16x32_bf16 v[0:3], a[96:99], a[0:3], v[0:3]\n"
"ds_read_b64_tr_b16 a[124:125], v250 offset:50184\n"
"v_mfma_f32_16x16x32_bf16 v[0:3], a[128:131], a[28:31], v[0:3]\n"
"ds_read_b64_tr_b16 a[126:127], v250 offset:50696\n"
"v_mfma_f32_16x16x32_bf16 v[32:35], a[96:99], a[4:7], v[32:35]\n"
"ds_read_b64_tr_b16 a[156:157], v252 offset:1032\n"
"v_mfma_f32_16x16x32_bf16 v[32:35], a[128:131], a[32:35], v[32:35]\n"
"ds_read_b64_tr_b16 a[158:159], v252 offset:1544\n"
"v_mfma_f32_16x16x32_bf16 v[64:67], a[96:99], a[8:11], v[64:67]\n"
"v_mfma_f32_16x16x32_bf16 v[64:67], a[128:131], a[36:39], v[64:67]\n"
"v_mfma_f32_16x16x32_bf16 v[96:99], a[96:99], a[12:15], v[96:99]\n"
"v_mfma_f32_16x16x32_bf16 v[96:99], a[128:131], a[40:43], v[96:99]\n"
"v_mfma_f32_16x16x32_bf16 v[128:131], a[96:99], a[16:19], v[128:131]\n"
"v_mfma_f32_16x16x32_bf16 v[128:131], a[128:131], a[44:47], v[128:131]\n"
"v_mfma_f32_16x16x32_bf16 v[160:163], a[96:99], a[20:23], v[160:163]\n"
"v_mfma_f32_16x16x32_bf16 v[160:163], a[128:131], a[48:51], v[160:163]\n"
"v_mfma_f32_16x16x32_bf16 v[4:7], a[100:103], a[0:3], v[4:7]\n"
"v_mfma_f32_16x16x32_bf16 v[4:7], a[132:135], a[28:31], v[4:7]\n"
"s_waitcnt vmcnt(19) lgkmcnt(0)\n"
"s_barrier\n"
"v_mfma_f32_16x16x32_bf16 v[36:39], a[100:103], a[4:7], v[36:39]\n"
"s_add_u32 m0, s71, 49152\n"
"v_mfma_f32_16x16x32_bf16 v[36:39], a[132:135], a[32:35], v[36:39]\n"
"buffer_load_dwordx4 v253, s[48:51], s64 offen offset:1024  lds\n"
"v_mfma_f32_16x16x32_bf16 v[68:71], a[100:103], a[8:11], v[68:71]\n"
"ds_read_b64_tr_b16 a[96:97], v249 offset:16384\n"
"v_mfma_f32_16x16x32_bf16 v[68:71], a[132:135], a[36:39], v[68:71]\n"
"ds_read_b64_tr_b16 a[98:99], v249 offset:16896\n"
"v_mfma_f32_16x16x32_bf16 v[100:103], a[100:103], a[12:15], v[100:103]\n"
"ds_read_b64_tr_b16 a[128:129], v249 offset:32768\n"
"v_mfma_f32_16x16x32_bf16 v[100:103], a[132:135], a[40:43], v[100:103]\n"
"buffer_load_dwordx4 v253, s[48:51], 0 offen offset:3072  lds\n"
"v_mfma_f32_16x16x32_bf16 v[132:135], a[100:103], a[16:19], v[132:135]\n"
"ds_read_b64_tr_b16 a[130:131], v249 offset:33280\n"
"v_mfma_f32_16x16x32_bf16 v[132:135], a[132:135], a[44:47], v[132:135]\n"
"ds_read_b128 a[160:163], v247 offset:24592\n"
"v_mfma_f32_16x16x32_bf16 v[164:167], a[100:103], a[20:23], v[164:167]\n"
"ds_read_b128 a[164:167], v247 offset:25616\n"
"v_mfma_f32_16x16x32_bf16 v[164:167], a[132:135], a[48:51], v[164:167]\n"
"v_add_u32 v253, 0x40000, v253\n"
"s_mov_b32 m0, s72\n"
"v_mfma_f32_16x16x32_bf16 v[8:11], a[104:107], a[0:3], v[8:11]\n"
"buffer_load_dwordx4 v253, s[48:51], s64 offen offset:1024  lds\n"
"v_mfma_f32_16x16x32_bf16 v[8:11], a[136:139], a[28:31], v[8:11]\n"
"ds_read_b64_tr_b16 a[100:101], v249 offset:16392\n"
"v_mfma_f32_16x16x32_bf16 v[40:43], a[104:107], a[4:7], v[40:43]\n"
"ds_read_b64_tr_b16 a[102:103], v249 offset:16904\n"
"v_mfma_f32_16x16x32_bf16 v[40:43], a[136:139], a[32:35], v[40:43]\n"
"ds_read_b64_tr_b16 a[132:133], v249 offset:32776\n"
"v_mfma_f32_16x16x32_bf16 v[72:75], a[104:107], a[8:11], v[72:75]\n"
"buffer_load_dwordx4 v253, s[48:51], 0 offen offset:3072  lds\n"
"v_mfma_f32_16x16x32_bf16 v[72:75], a[136:139], a[36:39], v[72:75]\n"
"ds_read_b64_tr_b16 a[134:135], v249 offset:33288\n"
"v_mfma_f32_16x16x32_bf16 v[104:107], a[104:107], a[12:15], v[104:107]\n"
"ds_read_b128 a[168:171], v247 offset:26640\n"
"v_mfma_f32_16x16x32_bf16 v[104:107], a[136:139], a[40:43], v[104:107]\n"
"ds_read_b128 a[172:175], v247 offset:27664\n"
"v_mfma_f32_16x16x32_bf16 v[136:139], a[104:107], a[16:19], v[136:139]\n"
"v_add_u32 v253, 0x40000, v253\n"
"s_mov_b32 m0, s74\n"
"v_mfma_f32_16x16x32_bf16 v[136:139], a[136:139], a[44:47], v[136:139]\n"
"buffer_load_dwordx4 v254, s[52:55], s66 offen offset:0  lds\n"
"v_mfma_f32_16x16x32_bf16 v[168:171], a[104:107], a[20:23], v[168:171]\n"
"ds_read_b128 a[176:179], v247 offset:28688\n"
"v_mfma_f32_16x16x32_bf16 v[168:171], a[136:139], a[48:51], v[168:171]\n"
"ds_read_b64_tr_b16 a[104:105], v250 offset:16384\n"
"v_mfma_f32_16x16x32_bf16 v[12:15], a[108:111], a[0:3], v[12:15]\n"
"ds_read_b64_tr_b16 a[106:107], v250 offset:16896\n"
"v_mfma_f32_16x16x32_bf16 v[12:15], a[140:143], a[28:31], v[12:15]\n"
"buffer_load_dwordx4 v254, s[52:55], s67 offen offset:1024  lds\n"
"v_mfma_f32_16x16x32_bf16 v[44:47], a[108:111], a[4:7], v[44:47]\n"
"ds_read_b64_tr_b16 a[136:137], v250 offset:32768\n"
"v_mfma_f32_16x16x32_bf16 v[44:47], a[140:143], a[32:35], v[44:47]\n"
"ds_read_b64_tr_b16 a[138:139], v250 offset:33280\n"
"v_mfma_f32_16x16x32_bf16 v[76:79], a[108:111], a[8:11], v[76:79]\n"
"ds_read_b128 a[180:183], v247 offset:29712\n"
"v_mfma_f32_16x16x32_bf16 v[76:79], a[140:143], a[36:39], v[76:79]\n"
"buffer_load_dwordx4 v254, s[52:55], s68 offen offset:2048  lds\n"
"v_mfma_f32_16x16x32_bf16 v[108:111], a[108:111], a[12:15], v[108:111]\n"
"ds_read_b128 a[184:187], v247 offset:30736\n"
"v_mfma_f32_16x16x32_bf16 v[108:111], a[140:143], a[40:43], v[108:111]\n"
"v_mfma_f32_16x16x32_bf16 v[140:143], a[108:111], a[16:19], v[140:143]\n"
"v_mfma_f32_16x16x32_bf16 v[140:143], a[140:143], a[44:47], v[140:143]\n"
"buffer_load_dwordx4 v254, s[52:55], s69 offen offset:3072  lds\n"
"v_mfma_f32_16x16x32_bf16 v[172:175], a[108:111], a[20:23], v[172:175]\n"
"v_mfma_f32_16x16x32_bf16 v[172:175], a[140:143], a[48:51], v[172:175]\n"
"ds_read_b64_tr_b16 a[108:109], v250 offset:16392\n"
"v_mfma_f32_16x16x32_bf16 v[16:19], a[112:115], a[0:3], v[16:19]\n"
"ds_read_b64_tr_b16 a[110:111], v250 offset:16904\n"
"v_mfma_f32_16x16x32_bf16 v[16:19], a[144:147], a[28:31], v[16:19]\n"
"v_add_u32 v254, 64, v254\n"
"v_mfma_f32_16x16x32_bf16 v[48:51], a[112:115], a[4:7], v[48:51]\n"
"ds_read_b64_tr_b16 a[140:141], v250 offset:32776\n"
"v_mfma_f32_16x16x32_bf16 v[48:51], a[144:147], a[32:35], v[48:51]\n"
"ds_read_b64_tr_b16 a[142:143], v250 offset:33288\n"
"v_mfma_f32_16x16x32_bf16 v[80:83], a[112:115], a[8:11], v[80:83]\n"
"v_mfma_f32_16x16x32_bf16 v[80:83], a[144:147], a[36:39], v[80:83]\n"
"v_mfma_f32_16x16x32_bf16 v[112:115], a[112:115], a[12:15], v[112:115]\n"
"v_mfma_f32_16x16x32_bf16 v[112:115], a[144:147], a[40:43], v[112:115]\n"
"v_mfma_f32_16x16x32_bf16 v[144:147], a[112:115], a[16:19], v[144:147]\n"
"v_mfma_f32_16x16x32_bf16 v[144:147], a[144:147], a[44:47], v[144:147]\n"
"v_mfma_f32_16x16x32_bf16 v[176:179], a[112:115], a[20:23], v[176:179]\n"
"v_mfma_f32_16x16x32_bf16 v[176:179], a[144:147], a[48:51], v[176:179]\n"
"v_mfma_f32_16x16x32_bf16 v[192:195], a[112:115], a[24:27], v[192:195]\n"
"v_mfma_f32_16x16x32_bf16 v[192:195], a[144:147], a[52:55], v[192:195]\n"
"v_mfma_f32_16x16x32_bf16 v[20:23], a[116:119], a[0:3], v[20:23]\n"
"v_mfma_f32_16x16x32_bf16 v[20:23], a[148:151], a[28:31], v[20:23]\n"
"v_mfma_f32_16x16x32_bf16 v[52:55], a[116:119], a[4:7], v[52:55]\n"
"v_mfma_f32_16x16x32_bf16 v[52:55], a[148:151], a[32:35], v[52:55]\n"
"v_mfma_f32_16x16x32_bf16 v[84:87], a[116:119], a[8:11], v[84:87]\n"
"v_mfma_f32_16x16x32_bf16 v[84:87], a[148:151], a[36:39], v[84:87]\n"
"s_waitcnt vmcnt(19) lgkmcnt(0)\n"
"s_barrier\n"
"v_mfma_f32_16x16x32_bf16 v[116:119], a[116:119], a[12:15], v[116:119]\n"
"s_add_u32 m0, s71, 16384\n"
"v_mfma_f32_16x16x32_bf16 v[116:119], a[148:151], a[40:43], v[116:119]\n"
"buffer_load_dwordx4 v253, s[48:51], s63 offen offset:0  lds\n"
"v_mfma_f32_16x16x32_bf16 v[148:151], a[116:119], a[16:19], v[148:151]\n"
"ds_read_b128 a[192:195], v247 offset:40976\n"
"v_mfma_f32_16x16x32_bf16 v[148:151], a[148:151], a[44:47], v[148:151]\n"
"ds_read_b128 a[196:199], v247 offset:42000\n"
"v_mfma_f32_16x16x32_bf16 v[180:183], a[116:119], a[20:23], v[180:183]\n"
"buffer_load_dwordx4 v253, s[48:51], s65 offen offset:2048  lds\n"
"v_mfma_f32_16x16x32_bf16 v[180:183], a[148:151], a[48:51], v[180:183]\n"
"ds_read_b128 a[200:203], v247 offset:43024\n"
"v_mfma_f32_16x16x32_bf16 v[196:199], a[116:119], a[24:27], v[196:199]\n"
"ds_read_b128 a[204:207], v247 offset:44048\n"
"v_mfma_f32_16x16x32_bf16 v[196:199], a[148:151], a[52:55], v[196:199]\n"
"v_add_u32 v253, 0x40000, v253\n"
"s_add_u32 m0, s71, 32768\n"
"v_mfma_f32_16x16x32_bf16 v[24:27], a[120:123], a[0:3], v[24:27]\n"
"buffer_load_dwordx4 v253, s[48:51], s63 offen offset:0  lds\n"
"v_mfma_f32_16x16x32_bf16 v[24:27], a[152:155], a[28:31], v[24:27]\n"
"ds_read_b128 a[208:211], v247 offset:45072\n"
"v_mfma_f32_16x16x32_bf16 v[56:59], a[120:123], a[4:7], v[56:59]\n"
"ds_read_b128 a[212:215], v247 offset:46096\n"
"v_mfma_f32_16x16x32_bf16 v[56:59], a[152:155], a[32:35], v[56:59]\n"
"buffer_load_dwordx4 v253, s[48:51], s65 offen offset:2048  lds\n"
"v_mfma_f32_16x16x32_bf16 v[88:91], a[120:123], a[8:11], v[88:91]\n"
"ds_read_b128 a[216:219], v247 offset:47120\n"
"v_mfma_f32_16x16x32_bf16 v[88:91], a[152:155], a[36:39], v[88:91]\n"
"ds_read_b64_tr_b16 a[112:113], v249 offset:17408\n"
"v_mfma_f32_16x16x32_bf16 v[120:123], a[120:123], a[12:15], v[120:123]\n"
"v_subrev_u32 v253, 0x40000, v253\n"
"s_add_u32 m0, s73, 16384\n"
"v_mfma_f32_16x16x32_bf16 v[120:123], a[152:155], a[40:43], v[120:123]\n"
"buffer_load_dwordx4 v254, s[52:55], s66 offen offset:0  lds\n"
"v_mfma_f32_16x16x32_bf16 v[152:155], a[120:123], a[16:19], v[152:155]\n"
"ds_read_b64_tr_b16 a[114:115], v249 offset:17920\n"
"v_mfma_f32_16x16x32_bf16 v[152:155], a[152:155], a[44:47], v[152:155]\n"
"ds_read_b64_tr_b16 a[144:145], v249 offset:33792\n"
"v_mfma_f32_16x16x32_bf16 v[184:187], a[120:123], a[20:23], v[184:187]\n"
"buffer_load_dwordx4 v254, s[52:55], s67 offen offset:1024  lds\n"
"v_mfma_f32_16x16x32_bf16 v[184:187], a[152:155], a[48:51], v[184:187]\n"
"ds_read_b64_tr_b16 a[146:147], v249 offset:34304\n"
"v_mfma_f32_16x16x32_bf16 v[200:203], a[120:123], a[24:27], v[200:203]\n"
"ds_read_b64_tr_b16 a[116:117], v249 offset:17416\n"
"v_mfma_f32_16x16x32_bf16 v[200:203], a[152:155], a[52:55], v[200:203]\n"
"buffer_load_dwordx4 v254, s[52:55], s68 offen offset:2048  lds\n"
"v_mfma_f32_16x16x32_bf16 v[28:31], a[124:127], a[0:3], v[28:31]\n"
"ds_read_b64_tr_b16 a[118:119], v249 offset:17928\n"
"v_mfma_f32_16x16x32_bf16 v[28:31], a[156:159], a[28:31], v[28:31]\n"
"ds_read_b64_tr_b16 a[148:149], v249 offset:33800\n"
"v_mfma_f32_16x16x32_bf16 v[60:63], a[124:127], a[4:7], v[60:63]\n"
"buffer_load_dwordx4 v254, s[52:55], s69 offen offset:3072  lds\n"
"v_mfma_f32_16x16x32_bf16 v[60:63], a[156:159], a[32:35], v[60:63]\n"
"ds_read_b64_tr_b16 a[150:151], v249 offset:34312\n"
"v_mfma_f32_16x16x32_bf16 v[92:95], a[124:127], a[8:11], v[92:95]\n"
"ds_read_b64_tr_b16 a[120:121], v250 offset:17408\n"
"v_mfma_f32_16x16x32_bf16 v[92:95], a[156:159], a[36:39], v[92:95]\n"
"v_add_u32 v254, 64, v254\n"
"v_mfma_f32_16x16x32_bf16 v[124:127], a[124:127], a[12:15], v[124:127]\n"
"ds_read_b64_tr_b16 a[122:123], v250 offset:17920\n"
"v_mfma_f32_16x16x32_bf16 v[124:127], a[156:159], a[40:43], v[124:127]\n"
"ds_read_b64_tr_b16 a[152:153], v250 offset:33792\n"
"v_mfma_f32_16x16x32_bf16 v[156:159], a[124:127], a[16:19], v[156:159]\n"
"ds_read_b64_tr_b16 a[154:155], v250 offset:34304\n"
"v_mfma_f32_16x16x32_bf16 v[156:159], a[156:159], a[44:47], v[156:159]\n"
"v_mfma_f32_16x16x32_bf16 v[188:191], a[124:127], a[20:23], v[188:191]\n"
"v_mfma_f32_16x16x32_bf16 v[188:191], a[156:159], a[48:51], v[188:191]\n"
"v_mfma_f32_16x16x32_bf16 v[204:207], a[124:127], a[24:27], v[204:207]\n"
"v_mfma_f32_16x16x32_bf16 v[204:207], a[156:159], a[52:55], v[204:207]\n"
"s_waitcnt lgkmcnt(12)\n"
"s_sub_u32 s75, s75, 1\n"
"s_cbranch_scc0 Lq1%=\n"
"v_mfma_f32_16x16x32_bf16 v[0:3], a[96:99], a[160:163], v[0:3]\n"
"ds_read_b64_tr_b16 a[124:125], v250 offset:17416\n"
"v_mfma_f32_16x16x32_bf16 v[0:3], a[128:131], a[192:195], v[0:3]\n"
"ds_read_b64_tr_b16 a[126:127], v250 offset:17928\n"
"v_mfma_f32_16x16x32_bf16 v[32:35], a[96:99], a[164:167], v[32:35]\n"
"ds_read_b64_tr_b16 a[156:157], v250 offset:33800\n"
"v_mfma_f32_16x16x32_bf16 v[32:35], a[128:131], a[196:199], v[32:35]\n"
"ds_read_b64_tr_b16 a[158:159], v250 offset:34312\n"
"v_mfma_f32_16x16x32_bf16 v[64:67], a[96:99], a[168:171], v[64:67]\n"
"s_add_u32 m0, s70, 0\n"
"v_mfma_f32_16x16x32_bf16 v[64:67], a[128:131], a[200:203], v[64:67]\n"
"buffer_load_dwordx4 v223, s[40:43], s60 offen offset:0 nt lds\n"
"v_mfma_f32_16x16x32_bf16 v[96:99], a[96:99], a[172:175], v[96:99]\n"
"buffer_load_dwordx4 v223, s[40:43], s61 offen offset:1024 nt lds\n"
"v_mfma_f32_16x16x32_bf16 v[96:99], a[128:131], a[204:207], v[96:99]\n"
"buffer_load_dwordx4 v223, s[40:43], s62 offen offset:2048 nt lds\n"
"v_mfma_f32_16x16x32_bf16 v[128:131], a[96:99], a[176:179], v[128:131]\n"
"buffer_load_dwordx4 v223, s[40:43], 0 offen offset:3072 nt lds\n"
"v_mfma_f32_16x16x32_bf16 v[128:131], a[128:131], a[208:211], v[128:131]\n"
"v_mfma_f32_16x16x32_bf16 v[160:163], a[96:99], a[180:183], v[160:163]\n"
"v_mfma_f32_16x16x32_bf16 v[160:163], a[128:131], a[212:215], v[160:163]\n"
"v_mfma_f32_16x16x32_bf16 v[4:7], a[100:103], a[160:163], v[4:7]\n"
"v_mfma_f32_16x16x32_bf16 v[4:7], a[132:135], a[192:195], v[4:7]\n"
"s_waitcnt vmcnt(20) lgkmcnt(0)\n"
"s_barrier\n"
"v_mfma_f32_16x16x32_bf16 v[36:39], a[100:103], a[164:167], v[36:39]\n"
"s_add_u32 m0, s71, 16384\n"
"v_mfma_f32_16x16x32_bf16 v[36:39], a[132:135], a[196:199], v[36:39]\n"
"buffer_load_dwordx4 v253, s[48:51], s64 offen offset:1024  lds\n"
"v_mfma_f32_16x16x32_bf16 v[68:71], a[100:103], a[168:171], v[68:71]\n"
"ds_read_b64_tr_b16 a[96:97], v249 offset:49152\n"
"v_mfma_f32_16x16x32_bf16 v[68:71], a[132:135], a[200:203], v[68:71]\n"
"ds_read_b64_tr_b16 a[98:99], v249 offset:49664\n"
"v_mfma_f32_16x16x32_bf16 v[100:103], a[100:103], a[172:175], v[100:103]\n"
"ds_read_b64_tr_b16 a[128:129], v251 offset:0\n"
"v_mfma_f32_16x16x32_bf16 v[100:103], a[132:135], a[204:207], v[100:103]\n"
"buffer_load_dwordx4 v253, s[48:51], 0 offen offset:3072  lds\n"
"v_mfma_f32_16x16x32_bf16 v[132:135], a[100:103], a[176:179], v[132:135]\n"
"ds_read_b64_tr_b16 a[130:131], v251 offset:512\n"
"v_mfma_f32_16x16x32_bf16 v[132:135], a[132:135], a[208:211], v[132:135]\n"
"ds_read_b128 a[0:3], v247 offset:57360\n"
"v_mfma_f32_16x16x32_bf16 v[164:167], a[100:103], a[180:183], v[164:167]\n"
"ds_read_b128 a[4:7], v247 offset:58384\n"
"v_mfma_f32_16x16x32_bf16 v[164:167], a[132:135], a[212:215], v[164:167]\n"
"v_add_u32 v253, 0x40000, v253\n"
"s_add_u32 m0, s71, 32768\n"
"v_mfma_f32_16x16x32_bf16 v[8:11], a[104:107], a[160:163], v[8:11]\n"
"buffer_load_dwordx4 v253, s[48:51], s64 offen offset:1024  lds\n"
"v_mfma_f32_16x16x32_bf16 v[8:11], a[136:139], a[192:195], v[8:11]\n"
"ds_read_b64_tr_b16 a[100:101], v249 offset:49160\n"
"v_mfma_f32_16x16x32_bf16 v[40:43], a[104:107], a[164:167], v[40:43]\n"
"ds_read_b64_tr_b16 a[102:103], v249 offset:49672\n"
"v_mfma_f32_16x16x32_bf16 v[40:43], a[136:139], a[196:199], v[40:43]\n"
"ds_read_b64_tr_b16 a[132:133], v251 offset:8\n"
"v_mfma_f32_16x16x32_bf16 v[72:75], a[104:107], a[168:171], v[72:75]\n"
"buffer_load_dwordx4 v253, s[48:51], 0 offen offset:3072  lds\n"
"v_mfma_f32_16x16x32_bf16 v[72:75], a[136:139], a[200:203], v[72:75]\n"
"ds_read_b64_tr_b16 a[134:135], v251 offset:520\n"
"v_mfma_f32_16x16x32_bf16 v[104:107], a[104:107], a[172:175], v[104:107]\n"
"ds_read_b128 a[8:11], v247 offset:59408\n"
"v_mfma_f32_16x16x32_bf16 v[104:107], a[136:139], a[204:207], v[104:107]\n"
"ds_read_b128 a[12:15], v247 offset:60432\n"
"v_mfma_f32_16x16x32_bf16 v[136:139], a[104:107], a[176:179], v[136:139]\n"
"v_add_u32 v253, 0x40000, v253\n"
"s_add_u32 m0, s73, 32768\n"
"v_mfma_f32_16x16x32_bf16 v[136:139], a[136:139], a[208:211], v[136:139]\n"
"buffer_load_dwordx4 v254, s[52:55], s66 offen offset:0  lds\n"
"v_mfma_f32_16x16x32_bf16 v[168:171], a[104:107], a[180:183], v[168:171]\n"
"ds_read_b128 a[16:19], v247 offset:61456\n"
"v_mfma_f32_16x16x32_bf16 v[168:171], a[136:139], a[212:215], v[168:171]\n"
"ds_read_b64_tr_b16 a[104:105], v250 offset:49152\n"
"v_mfma_f32_16x16x32_bf16 v[12:15], a[108:111], a[160:163], v[12:15]\n"
"ds_read_b64_tr_b16 a[106:107], v250 offset:49664\n"
"v_mfma_f32_16x16x32_bf16 v[12:15], a[140:143], a[192:195], v[12:15]\n"
"buffer_load_dwordx4 v254, s[52:55], s67 offen offset:1024  lds\n"
"v_mfma_f32_16x16x32_bf16 v[44:47], a[108:111], a[164:167], v[44:47]\n"
"ds_read_b64_tr_b16 a[136:137], v252 offset:0\n"
"v_mfma_f32_16x16x32_bf16 v[44:47], a[140:143], a[196:199], v[44:47]\n"
"ds_read_b64_tr_b16 a[138:139], v252 offset:512\n"
"v_mfma_f32_16x16x32_bf16 v[76:79], a[108:111], a[168:171], v[76:79]\n"
"ds_read_b128 a[20:23], v247 offset:62480\n"
"v_mfma_f32_16x16x32_bf16 v[76:79], a[140:143], a[200:203], v[76:79]\n"
"buffer_load_dwordx4 v254, s[52:55], s68 offen offset:2048  lds\n"
"v_mfma_f32_16x16x32_bf16 v[108:111], a[108:111], a[172:175], v[108:111]\n"
"ds_read_b128 a[24:27], v247 offset:63504\n"
"v_mfma_f32_16x16x32_bf16 v[108:111], a[140:143], a[204:207], v[108:111]\n"
"v_mfma_f32_16x16x32_bf16 v[140:143], a[108:111], a[176:179], v[140:143]\n"
"v_mfma_f32_16x16x32_bf16 v[140:143], a[140:143], a[208:211], v[140:143]\n"
"buffer_load_dwordx4 v254, s[52:55], s69 offen offset:3072  lds\n"
"v_mfma_f32_16x16x32_bf16 v[172:175], a[108:111], a[180:183], v[172:175]\n"
"v_mfma_f32_16x16x32_bf16 v[172:175], a[140:143], a[212:215], v[172:175]\n"
"ds_read_b64_tr_b16 a[108:109], v250 offset:49160\n"
"v_mfma_f32_16x16x32_bf16 v[16:19], a[112:115], a[160:163], v[16:19]\n"
"ds_read_b64_tr_b16 a[110:111], v250 offset:49672\n"
"v_mfma_f32_16x16x32_bf16 v[16:19], a[144:147], a[192:195], v[16:19]\n"
"v_add_u32 v254, 64, v254\n"
"v_mfma_f32_16x16x32_bf16 v[48:51], a[112:115], a[164:167], v[48:51]\n"
"buffer_load_dword v208, v209, s[52:55], 0 offen \n"
"v_mfma_f32_16x16x32_bf16 v[48:51], a[144:147], a[196:199], v[48:51]\n"
"ds_read_b64_tr_b16 a[140:141], v252 offset:8\n"
"v_mfma_f32_16x16x32_bf16 v[80:83], a[112:115], a[168:171], v[80:83]\n"
"ds_read_b64_tr_b16 a[142:143], v252 offset:520\n"
"v_mfma_f32_16x16x32_bf16 v[80:83], a[144:147], a[200:203], v[80:83]\n"
"v_mfma_f32_16x16x32_bf16 v[112:115], a[112:115], a[172:175], v[112:115]\n"
"v_add_u32 v209, 0x100, v209\n"
"v_mfma_f32_16x16x32_bf16 v[112:115], a[144:147], a[204:207], v[112:115]\n"
"buffer_load_dword v208, v210, s[48:51], 0 offen \n"
"v_mfma_f32_16x16x32_bf16 v[144:147], a[112:115], a[176:179], v[144:147]\n"
"v_mfma_f32_16x16x32_bf16 v[144:147], a[144:147], a[208:211], v[144:147]\n"
"v_mfma_f32_16x16x32_bf16 v[176:179], a[112:115], a[180:183], v[176:179]\n"
"v_mfma_f32_16x16x32_bf16 v[176:179], a[144:147], a[212:215], v[176:179]\n"
"v_add_u32 v210, 0x100000, v210\n"
"v_mfma_f32_16x16x32_bf16 v[192:195], a[112:115], a[184:187], v[192:195]\n"
"v_mfma_f32_16x16x32_bf16 v[192:195], a[144:147], a[216:219], v[192:195]\n"
"v_mfma_f32_16x16x32_bf16 v[20:23], a[116:119], a[160:163], v[20:23]\n"
"v_mfma_f32_16x16x32_bf16 v[20:23], a[148:151], a[192:195], v[20:23]\n"
"v_mfma_f32_16x16x32_bf16 v[52:55], a[116:119], a[164:167], v[52:55]\n"
"v_mfma_f32_16x16x32_bf16 v[52:55], a[148:151], a[196:199], v[52:55]\n"
"v_mfma_f32_16x16x32_bf16 v[84:87], a[116:119], a[168:171], v[84:87]\n"
"v_mfma_f32_16x16x32_bf16 v[84:87], a[148:151], a[200:203], v[84:87]\n"
"s_waitcnt vmcnt(22) lgkmcnt(0)\n"
"s_barrier\n"
"v_mfma_f32_16x16x32_bf16 v[116:119], a[116:119], a[172:175], v[116:119]\n"
"s_add_u32 m0, s71, 49152\n"
"v_mfma_f32_16x16x32_bf16 v[116:119], a[148:151], a[204:207], v[116:119]\n"
"buffer_load_dwordx4 v253, s[48:51], s63 offen offset:0  lds\n"
"v_mfma_f32_16x16x32_bf16 v[148:151], a[116:119], a[176:179], v[148:151]\n"
"ds_read_b128 a[28:31], v248 offset:24592\n"
"v_mfma_f32_16x16x32_bf16 v[148:151], a[148:151], a[208:211], v[148:151]\n"
"ds_read_b128 a[32:35], v248 offset:25616\n"
"v_mfma_f32_16x16x32_bf16 v[180:183], a[116:119], a[180:183], v[180:183]\n"
"buffer_load_dwordx4 v253, s[48:51], s65 offen offset:2048  lds\n"
"v_mfma_f32_16x16x32_bf16 v[180:183], a[148:151], a[212:215], v[180:183]\n"
"ds_read_b128 a[36:39], v248 offset:26640\n"
"v_mfma_f32_16x16x32_bf16 v[196:199], a[116:119], a[184:187], v[196:199]\n"
"ds_read_b128 a[40:43], v248 offset:27664\n"
"v_mfma_f32_16x16x32_bf16 v[196:199], a[148:151], a[216:219], v[196:199]\n"
"v_add_u32 v253, 0x40000, v253\n"
"s_mov_b32 m0, s72\n"
"v_mfma_f32_16x16x32_bf16 v[24:27], a[120:123], a[160:163], v[24:27]\n"
"buffer_load_dwordx4 v253, s[48:51], s63 offen offset:0  lds\n"
"v_mfma_f32_16x16x32_bf16 v[24:27], a[152:155], a[192:195], v[24:27]\n"
"ds_read_b128 a[44:47], v248 offset:28688\n"
"v_mfma_f32_16x16x32_bf16 v[56:59], a[120:123], a[164:167], v[56:59]\n"
"ds_read_b128 a[48:51], v248 offset:29712\n"
"v_mfma_f32_16x16x32_bf16 v[56:59], a[152:155], a[196:199], v[56:59]\n"
"buffer_load_dwordx4 v253, s[48:51], s65 offen offset:2048  lds\n"
"v_mfma_f32_16x16x32_bf16 v[88:91], a[120:123], a[168:171], v[88:91]\n"
"ds_read_b128 a[52:55], v248 offset:30736\n"
"v_mfma_f32_16x16x32_bf16 v[88:91], a[152:155], a[200:203], v[88:91]\n"
"ds_read_b64_tr_b16 a[112:113], v249 offset:50176\n"
"v_mfma_f32_16x16x32_bf16 v[120:123], a[120:123], a[172:175], v[120:123]\n"
"v_subrev_u32 v253, 0x40000, v253\n"
"s_add_u32 m0, s73, 49152\n"
"v_mfma_f32_16x16x32_bf16 v[120:123], a[152:155], a[204:207], v[120:123]\n"
"buffer_load_dwordx4 v254, s[52:55], s66 offen offset:0  lds\n"
"v_mfma_f32_16x16x32_bf16 v[152:155], a[120:123], a[176:179], v[152:155]\n"
"ds_read_b64_tr_b16 a[114:115], v249 offset:50688\n"
"v_mfma_f32_16x16x32_bf16 v[152:155], a[152:155], a[208:211], v[152:155]\n"
"ds_read_b64_tr_b16 a[144:145], v251 offset:1024\n"
"v_mfma_f32_16x16x32_bf16 v[184:187], a[120:123], a[180:183], v[184:187]\n"
"buffer_load_dwordx4 v254, s[52:55], s67 offen offset:1024  lds\n"
"v_mfma_f32_16x16x32_bf16 v[184:187], a[152:155], a[212:215], v[184:187]\n"
"ds_read_b64_tr_b16 a[146:147], v251 offset:1536\n"
"v_mfma_f32_16x16x32_bf16 v[200:203], a[120:123], a[184:187], v[200:203]\n"
"ds_read_b64_tr_b16 a[116:117], v249 offset:50184\n"
"v_mfma_f32_16x16x32_bf16 v[200:203], a[152:155], a[216:219], v[200:203]\n"
"buffer_load_dwordx4 v254, s[52:55], s68 offen offset:2048  lds\n"
"v_mfma_f32_16x16x32_bf16 v[28:31], a[124:127], a[160:163], v[28:31]\n"
"ds_read_b64_tr_b16 a[118:119], v249 offset:50696\n"
"v_mfma_f32_16x16x32_bf16 v[28:31], a[156:159], a[192:195], v[28:31]\n"
"ds_read_b64_tr_b16 a[148:149], v251 offset:1032\n"
"v_mfma_f32_16x16x32_bf16 v[60:63], a[124:127], a[164:167], v[60:63]\n"
"buffer_load_dwordx4 v254, s[52:55], s69 offen offset:3072  lds\n"
"v_mfma_f32_16x16x32_bf16 v[60:63], a[156:159], a[196:199], v[60:63]\n"
"ds_read_b64_tr_b16 a[150:151], v251 offset:1544\n"
"v_mfma_f32_16x16x32_bf16 v[92:95], a[124:127], a[168:171], v[92:95]\n"
"ds_read_b64_tr_b16 a[120:121], v250 offset:50176\n"
"v_mfma_f32_16x16x32_bf16 v[92:95], a[156:159], a[200:203], v[92:95]\n"
"v_add_u32 v254, 64, v254\n"
"v_mfma_f32_16x16x32_bf16 v[124:127], a[124:127], a[172:175], v[124:127]\n"
"ds_read_b64_tr_b16 a[122:123], v250 offset:50688\n"
"v_mfma_f32_16x16x32_bf16 v[124:127], a[156:159], a[204:207], v[124:127]\n"
"ds_read_b64_tr_b16 a[152:153], v252 offset:1024\n"
"v_mfma_f32_16x16x32_bf16 v[156:159], a[124:127], a[176:179], v[156:159]\n"
"ds_read_b64_tr_b16 a[154:155], v252 offset:1536\n"
"v_mfma_f32_16x16x32_bf16 v[156:159], a[156:159], a[208:211], v[156:159]\n"
"v_mfma_f32_16x16x32_bf16 v[188:191], a[124:127], a[180:183], v[188:191]\n"
"v_mfma_f32_16x16x32_bf16 v[188:191], a[156:159], a[212:215], v[188:191]\n"
"v_mfma_f32_16x16x32_bf16 v[204:207], a[124:127], a[184:187], v[204:207]\n"
"v_mfma_f32_16x16x32_bf16 v[204:207], a[156:159], a[216:219], v[204:207]\n"
"s_waitcnt lgkmcnt(12)\n"
"v_mfma_f32_16x16x32_bf16 v[0:3], a[96:99], a[0:3], v[0:3]\n"
"ds_read_b64_tr_b16 a[124:125], v250 offset:50184\n"
"v_mfma_f32_16x16x32_bf16 v[0:3], a[128:131], a[28:31], v[0:3]\n"
"ds_read_b64_tr_b16 a[126:127], v250 offset:50696\n"
"v_mfma_f32_16x16x32_bf16 v[32:35], a[96:99], a[4:7], v[32:35]\n"
"ds_read_b64_tr_b16 a[156:157], v252 offset:1032\n"
"v_mfma_f32_16x16x32_bf16 v[32:35], a[128:131], a[32:35], v[32:35]\n"
"ds_read_b64_tr_b16 a[158:159], v252 offset:1544\n"
"v_mfma_f32_16x16x32_bf16 v[64:67], a[96:99], a[8:11], v[64:67]\n"
"v_mfma_f32_16x16x32_bf16 v[64:67], a[128:131], a[36:39], v[64:67]\n"
"v_mfma_f32_16x16x32_bf16 v[96:99], a[96:99], a[12:15], v[96:99]\n"
"v_mfma_f32_16x16x32_bf16 v[96:99], a[128:131], a[40:43], v[96:99]\n"
"v_mfma_f32_16x16x32_bf16 v[128:131], a[96:99], a[16:19], v[128:131]\n"
"v_mfma_f32_16x16x32_bf16 v[128:131], a[128:131], a[44:47], v[128:131]\n"
"v_mfma_f32_16x16x32_bf16 v[160:163], a[96:99], a[20:23], v[160:163]\n"
"v_mfma_f32_16x16x32_bf16 v[160:163], a[128:131], a[48:51], v[160:163]\n"
"v_mfma_f32_16x16x32_bf16 v[4:7], a[100:103], a[0:3], v[4:7]\n"
"v_mfma_f32_16x16x32_bf16 v[4:7], a[132:135], a[28:31], v[4:7]\n"
"s_waitcnt vmcnt(22) lgkmcnt(0)\n"
"s_barrier\n"
"v_mfma_f32_16x16x32_bf16 v[36:39], a[100:103], a[4:7], v[36:39]\n"
"s_add_u32 m0, s71, 49152\n"
"v_mfma_f32_16x16x32_bf16 v[36:39], a[132:135], a[32:35], v[36:39]\n"
"buffer_load_dwordx4 v253, s[48:51], s64 offen offset:1024  lds\n"
"v_mfma_f32_16x16x32_bf16 v[68:71], a[100:103], a[8:11], v[68:71]\n"
"ds_read_b64_tr_b16 a[96:97], v249 offset:16384\n"
"v_mfma_f32_16x16x32_bf16 v[68:71], a[132:135], a[36:39], v[68:71]\n"
"ds_read_b64_tr_b16 a[98:99], v249 offset:16896\n"
"v_mfma_f32_16x16x32_bf16 v[100:103], a[100:103], a[12:15], v[100:103]\n"
"ds_read_b64_tr_b16 a[128:129], v249 offset:32768\n"
"v_mfma_f32_16x16x32_bf16 v[100:103], a[132:135], a[40:43], v[100:103]\n"
"buffer_load_dwordx4 v253, s[48:51], 0 offen offset:3072  lds\n"
"v_mfma_f32_16x16x32_bf16 v[132:135], a[100:103], a[16:19], v[132:135]\n"
"ds_read_b64_tr_b16 a[130:131], v249 offset:33280\n"
"v_mfma_f32_16x16x32_bf16 v[132:135], a[132:135], a[44:47], v[132:135]\n"
"ds_read_b128 a[160:163], v247 offset:24592\n"
"v_mfma_f32_16x16x32_bf16 v[164:167], a[100:103], a[20:23], v[164:167]\n"
"ds_read_b128 a[164:167], v247 offset:25616\n"
"v_mfma_f32_16x16x32_bf16 v[164:167], a[132:135], a[48:51], v[164:167]\n"
"v_add_u32 v253, 0x40000, v253\n"
"s_mov_b32 m0, s72\n"
"v_mfma_f32_16x16x32_bf16 v[8:11], a[104:107], a[0:3], v[8:11]\n"
"buffer_load_dwordx4 v253, s[48:51], s64 offen offset:1024  lds\n"
"v_mfma_f32_16x16x32_bf16 v[8:11], a[136:139], a[28:31], v[8:11]\n"
"ds_read_b64_tr_b16 a[100:101], v249 offset:16392\n"
"v_mfma_f32_16x16x32_bf16 v[40:43], a[104:107], a[4:7], v[40:43]\n"
"ds_read_b64_tr_b16 a[102:103], v249 offset:16904\n"
"v_mfma_f32_16x16x32_bf16 v[40:43], a[136:139], a[32:35], v[40:43]\n"
"ds_read_b64_tr_b16 a[132:133], v249 offset:32776\n"
"v_mfma_f32_16x16x32_bf16 v[72:75], a[104:107], a[8:11], v[72:75]\n"
"buffer_load_dwordx4 v253, s[48:51], 0 offen offset:3072  lds\n"
"v_mfma_f32_16x16x32_bf16 v[72:75], a[136:139], a[36:39], v[72:75]\n"
"ds_read_b64_tr_b16 a[134:135], v249 offset:33288\n"
"v_mfma_f32_16x16x32_bf16 v[104:107], a[104:107], a[12:15], v[104:107]\n"
"ds_read_b128 a[168:171], v247 offset:26640\n"
"v_mfma_f32_16x16x32_bf16 v[104:107], a[136:139], a[40:43], v[104:107]\n"
"ds_read_b128 a[172:175], v247 offset:27664\n"
"v_mfma_f32_16x16x32_bf16 v[136:139], a[104:107], a[16:19], v[136:139]\n"
"v_add_u32 v253, 0x40000, v253\n"
"s_mov_b32 m0, s74\n"
"v_mfma_f32_16x16x32_bf16 v[136:139], a[136:139], a[44:47], v[136:139]\n"
"buffer_load_dwordx4 v254, s[52:55], s66 offen offset:0  lds\n"
"v_mfma_f32_16x16x32_bf16 v[168:171], a[104:107], a[20:23], v[168:171]\n"
"ds_read_b128 a[176:179], v247 offset:28688\n"
"v_mfma_f32_16x16x32_bf16 v[168:171], a[136:139], a[48:51], v[168:171]\n"
"ds_read_b64_tr_b16 a[104:105], v250 offset:16384\n"
"v_mfma_f32_16x16x32_bf16 v[12:15], a[108:111], a[0:3], v[12:15]\n"
"ds_read_b64_tr_b16 a[106:107], v250 offset:16896\n"
"v_mfma_f32_16x16x32_bf16 v[12:15], a[140:143], a[28:31], v[12:15]\n"
"buffer_load_dwordx4 v254, s[52:55], s67 offen offset:1024  lds\n"
"v_mfma_f32_16x16x32_bf16 v[44:47], a[108:111], a[4:7], v[44:47]\n"
"ds_read_b64_tr_b16 a[136:137], v250 offset:32768\n"
"v_mfma_f32_16x16x32_bf16 v[44:47], a[140:143], a[32:35], v[44:47]\n"
"ds_read_b64_tr_b16 a[138:139], v250 offset:33280\n"
"v_mfma_f32_16x16x32_bf16 v[76:79], a[108:111], a[8:11], v[76:79]\n"
"ds_read_b128 a[180:183], v247 offset:29712\n"
"v_mfma_f32_16x16x32_bf16 v[76:79], a[140:143], a[36:39], v[76:79]\n"
"buffer_load_dwordx4 v254, s[52:55], s68 offen offset:2048  lds\n"
"v_mfma_f32_16x16x32_bf16 v[108:111], a[108:111], a[12:15], v[108:111]\n"
"ds_read_b128 a[184:187], v247 offset:30736\n"
"v_mfma_f32_16x16x32_bf16 v[108:111], a[140:143], a[40:43], v[108:111]\n"
"v_mfma_f32_16x16x32_bf16 v[140:143], a[108:111], a[16:19], v[140:143]\n"
"v_mfma_f32_16x16x32_bf16 v[140:143], a[140:143], a[44:47], v[140:143]\n"
"buffer_load_dwordx4 v254, s[52:55], s69 offen offset:3072  lds\n"
"v_mfma_f32_16x16x32_bf16 v[172:175], a[108:111], a[20:23], v[172:175]\n"
"v_mfma_f32_16x16x32_bf16 v[172:175], a[140:143], a[48:51], v[172:175]\n"
"ds_read_b64_tr_b16 a[108:109], v250 offset:16392\n"
"v_mfma_f32_16x16x32_bf16 v[16:19], a[112:115], a[0:3], v[16:19]\n"
"ds_read_b64_tr_b16 a[110:111], v250 offset:16904\n"
"v_mfma_f32_16x16x32_bf16 v[16:19], a[144:147], a[28:31], v[16:19]\n"
"v_add_u32 v254, 64, v254\n"
"v_mfma_f32_16x16x32_bf16 v[48:51], a[112:115], a[4:7], v[48:51]\n"
"ds_read_b64_tr_b16 a[140:141], v250 offset:32776\n"
"v_mfma_f32_16x16x32_bf16 v[48:51], a[144:147], a[32:35], v[48:51]\n"
"ds_read_b64_tr_b16 a[142:143], v250 offset:33288\n"
"v_mfma_f32_16x16x32_bf16 v[80:83], a[112:115], a[8:11], v[80:83]\n"
"v_mfma_f32_16x16x32_bf16 v[80:83], a[144:147], a[36:39], v[80:83]\n"
"v_mfma_f32_16x16x32_bf16 v[112:115], a[112:115], a[12:15], v[112:115]\n"
"v_mfma_f32_16x16x32_bf16 v[112:115], a[144:147], a[40:43], v[112:115]\n"
"v_mfma_f32_16x16x32_bf16 v[144:147], a[112:115], a[16:19], v[144:147]\n"
"v_mfma_f32_16x16x32_bf16 v[144:147], a[144:147], a[44:47], v[144:147]\n"
"v_mfma_f32_16x16x32_bf16 v[176:179], a[112:115], a[20:23], v[176:179]\n"
"v_mfma_f32_16x16x32_bf16 v[176:179], a[144:147], a[48:51], v[176:179]\n"
"v_mfma_f32_16x16x32_bf16 v[192:195], a[112:115], a[24:27], v[192:195]\n"
"v_mfma_f32_16x16x32_bf16 v[192:195], a[144:147], a[52:55], v[192:195]\n"
"v_mfma_f32_16x16x32_bf16 v[20:23], a[116:119], a[0:3], v[20:23]\n"
"v_mfma_f32_16x16x32_bf16 v[20:23], a[148:151], a[28:31], v[20:23]\n"
"v_mfma_f32_16x16x32_bf16 v[52:55], a[116:119], a[4:7], v[52:55]\n"
"v_mfma_f32_16x16x32_bf16 v[52:55], a[148:151], a[32:35], v[52:55]\n"
"v_mfma_f32_16x16x32_bf16 v[84:87], a[116:119], a[8:11], v[84:87]\n"
"v_mfma_f32_16x16x32_bf16 v[84:87], a[148:151], a[36:39], v[84:87]\n"
"s_waitcnt vmcnt(18) lgkmcnt(0)\n"
"s_barrier\n"
"v_mfma_f32_16x16x32_bf16 v[116:119], a[116:119], a[12:15], v[116:119]\n"
"s_add_u32 m0, s71, 16384\n"
"v_mfma_f32_16x16x32_bf16 v[116:119], a[148:151], a[40:43], v[116:119]\n"
"buffer_load_dwordx4 v253, s[48:51], s63 offen offset:0  lds\n"
"v_mfma_f32_16x16x32_bf16 v[148:151], a[116:119], a[16:19], v[148:151]\n"
"ds_read_b128 a[192:195], v247 offset:40976\n"
"v_mfma_f32_16x16x32_bf16 v[148:151], a[148:151], a[44:47], v[148:151]\n"
"ds_read_b128 a[196:199], v247 offset:42000\n"
"v_mfma_f32_16x16x32_bf16 v[180:183], a[116:119], a[20:23], v[180:183]\n"
"buffer_load_dwordx4 v253, s[48:51], s65 offen offset:2048  lds\n"
"v_mfma_f32_16x16x32_bf16 v[180:183], a[148:151], a[48:51], v[180:183]\n"
"ds_read_b128 a[200:203], v247 offset:43024\n"
"v_mfma_f32_16x16x32_bf16 v[196:199], a[116:119], a[24:27], v[196:199]\n"
"ds_read_b128 a[204:207], v247 offset:44048\n"
"v_mfma_f32_16x16x32_bf16 v[196:199], a[148:151], a[52:55], v[196:199]\n"
"v_add_u32 v253, 0x40000, v253\n"
"s_add_u32 m0, s71, 32768\n"
"v_mfma_f32_16x16x32_bf16 v[24:27], a[120:123], a[0:3], v[24:27]\n"
"buffer_load_dwordx4 v253, s[48:51], s63 offen offset:0  lds\n"
"v_mfma_f32_16x16x32_bf16 v[24:27], a[152:155], a[28:31], v[24:27]\n"
"ds_read_b128 a[208:211], v247 offset:45072\n"
"v_mfma_f32_16x16x32_bf16 v[56:59], a[120:123], a[4:7], v[56:59]\n"
"ds_read_b128 a[212:215], v247 offset:46096\n"
"v_mfma_f32_16x16x32_bf16 v[56:59], a[152:155], a[32:35], v[56:59]\n"
"buffer_load_dwordx4 v253, s[48:51], s65 offen offset:2048  lds\n"
"v_mfma_f32_16x16x32_bf16 v[88:91], a[120:123], a[8:11], v[88:91]\n"
"ds_read_b128 a[216:219], v247 offset:47120\n"
"v_mfma_f32_16x16x32_bf16 v[88:91], a[152:155], a[36:39], v[88:91]\n"
"ds_read_b64_tr_b16 a[112:113], v249 offset:17408\n"
"v_mfma_f32_16x16x32_bf16 v[120:123], a[120:123], a[12:15], v[120:123]\n"
"v_subrev_u32 v253, 0x40000, v253\n"
"s_add_u32 m0, s73, 16384\n"
"v_mfma_f32_16x16x32_bf16 v[120:123], a[152:155], a[40:43], v[120:123]\n"
"buffer_load_dwordx4 v254, s[52:55], s66 offen offset:0  lds\n"
"v_mfma_f32_16x16x32_bf16 v[152:155], a[120:123], a[16:19], v[152:155]\n"
"ds_read_b64_tr_b16 a[114:115], v249 offset:17920\n"
"v_mfma_f32_16x16x32_bf16 v[152:155], a[152:155], a[44:47], v[152:155]\n"
"ds_read_b64_tr_b16 a[144:145], v249 offset:33792\n"
"v_mfma_f32_16x16x32_bf16 v[184:187], a[120:123], a[20:23], v[184:187]\n"
"buffer_load_dwordx4 v254, s[52:55], s67 offen offset:1024  lds\n"
"v_mfma_f32_16x16x32_bf16 v[184:187], a[152:155], a[48:51], v[184:187]\n"
"ds_read_b64_tr_b16 a[146:147], v249 offset:34304\n"
"v_mfma_f32_16x16x32_bf16 v[200:203], a[120:123], a[24:27], v[200:203]\n"
"ds_read_b64_tr_b16 a[116:117], v249 offset:17416\n"
"v_mfma_f32_16x16x32_bf16 v[200:203], a[152:155], a[52:55], v[200:203]\n"
"buffer_load_dwordx4 v254, s[52:55], s68 offen offset:2048  lds\n"
"v_mfma_f32_16x16x32_bf16 v[28:31], a[124:127], a[0:3], v[28:31]\n"
"ds_read_b64_tr_b16 a[118:119], v249 offset:17928\n"
"v_mfma_f32_16x16x32_bf16 v[28:31], a[156:159], a[28:31], v[28:31]\n"
"ds_read_b64_tr_b16 a[148:149], v249 offset:33800\n"
"v_mfma_f32_16x16x32_bf16 v[60:63], a[124:127], a[4:7], v[60:63]\n"
"buffer_load_dwordx4 v254, s[52:55], s69 offen offset:3072  lds\n"
"v_mfma_f32_16x16x32_bf16 v[60:63], a[156:159], a[32:35], v[60:63]\n"
"ds_read_b64_tr_b16 a[150:151], v249 offset:34312\n"
"v_mfma_f32_16x16x32_bf16 v[92:95], a[124:127], a[8:11], v[92:95]\n"
"ds_read_b64_tr_b16 a[120:121], v250 offset:17408\n"
"v_mfma_f32_16x16x32_bf16 v[92:95], a[156:159], a[36:39], v[92:95]\n"
"v_add_u32 v254, 64, v254\n"
"v_mfma_f32_16x16x32_bf16 v[124:127], a[124:127], a[12:15], v[124:127]\n"
"ds_read_b64_tr_b16 a[122:123], v250 offset:17920\n"
"v_mfma_f32_16x16x32_bf16 v[124:127], a[156:159], a[40:43], v[124:127]\n"
"ds_read_b64_tr_b16 a[152:153], v250 offset:33792\n"
"v_mfma_f32_16x16x32_bf16 v[156:159], a[124:127], a[16:19], v[156:159]\n"
"ds_read_b64_tr_b16 a[154:155], v250 offset:34304\n"
"v_mfma_f32_16x16x32_bf16 v[156:159], a[156:159], a[44:47], v[156:159]\n"
"v_mfma_f32_16x16x32_bf16 v[188:191], a[124:127], a[20:23], v[188:191]\n"
"v_mfma_f32_16x16x32_bf16 v[188:191], a[156:159], a[48:51], v[188:191]\n"
"v_mfma_f32_16x16x32_bf16 v[204:207], a[124:127], a[24:27], v[204:207]\n"
"v_mfma_f32_16x16x32_bf16 v[204:207], a[156:159], a[52:55], v[204:207]\n"
"s_waitcnt lgkmcnt(12)\n"
"v_mfma_f32_16x16x32_bf16 v[0:3], a[96:99], a[160:163], v[0:3]\n"
"ds_read_b64_tr_b16 a[124:125], v250 offset:17416\n"
"v_mfma_f32_16x16x32_bf16 v[0:3], a[128:131], a[192:195], v[0:3]\n"
"ds_read_b64_tr_b16 a[126:127], v250 offset:17928\n"
"v_mfma_f32_16x16x32_bf16 v[32:35], a[96:99], a[164:167], v[32:35]\n"
"ds_read_b64_tr_b16 a[156:157], v250 offset:33800\n"
"v_mfma_f32_16x16x32_bf16 v[32:35], a[128:131], a[196:199], v[32:35]\n"
"ds_read_b64_tr_b16 a[158:159], v250 offset:34312\n"
"v_mfma_f32_16x16x32_bf16 v[64:67], a[96:99], a[168:171], v[64:67]\n"
"s_add_u32 m0, s70, 8192\n"
"v_mfma_f32_16x16x32_bf16 v[64:67], a[128:131], a[200:203], v[64:67]\n"
"buffer_load_dwordx4 v223, s[44:47], s60 offen offset:0 nt lds\n"
"v_mfma_f32_16x16x32_bf16 v[96:99], a[96:99], a[172:175], v[96:99]\n"
"buffer_load_dwordx4 v223, s[44:47], s61 offen offset:1024 nt lds\n"
"v_mfma_f32_16x16x32_bf16 v[96:99], a[128:131], a[204:207], v[96:99]\n"
"buffer_load_dwordx4 v223, s[44:47], s62 offen offset:2048 nt lds\n"
"v_mfma_f32_16x16x32_bf16 v[128:131], a[96:99], a[176:179], v[128:131]\n"
"buffer_load_dwordx4 v223, s[44:47], 0 offen offset:3072 nt lds\n"
"v_mfma_f32_16x16x32_bf16 v[128:131], a[128:131], a[208:211], v[128:131]\n"
"v_add_u32 v223, 0x2000, v223\n"
"v_mfma_f32_16x16x32_bf16 v[160:163], a[96:99], a[180:183], v[160:163]\n"
"v_mfma_f32_16x16x32_bf16 v[160:163], a[128:131], a[212:215], v[160:163]\n"
"v_mfma_f32_16x16x32_bf16 v[4:7], a[100:103], a[160:163], v[4:7]\n"
"v_mfma_f32_16x16x32_bf16 v[4:7], a[132:135], a[192:195], v[4:7]\n"
"s_waitcnt vmcnt(20) lgkmcnt(0)\n"
"s_barrier\n"
"v_mfma_f32_16x16x32_bf16 v[36:39], a[100:103], a[164:167], v[36:39]\n"
"s_add_u32 m0, s71, 16384\n"
"v_mfma_f32_16x16x32_bf16 v[36:39], a[132:135], a[196:199], v[36:39]\n"
"buffer_load_dwordx4 v253, s[48:51], s64 offen offset:1024  lds\n"
"v_mfma_f32_16x16x32_bf16 v[68:71], a[100:103], a[168:171], v[68:71]\n"
"ds_read_b64_tr_b16 a[96:97], v249 offset:49152\n"
"v_mfma_f32_16x16x32_bf16 v[68:71], a[132:135], a[200:203], v[68:71]\n"
"ds_read_b64_tr_b16 a[98:99], v249 offset:49664\n"
"v_mfma_f32_16x16x32_bf16 v[100:103], a[100:103], a[172:175], v[100:103]\n"
"ds_read_b64_tr_b16 a[128:129], v251 offset:0\n"
"v_mfma_f32_16x16x32_bf16 v[100:103], a[132:135], a[204:207], v[100:103]\n"
"buffer_load_dwordx4 v253, s[48:51], 0 offen offset:3072  lds\n"
"v_mfma_f32_16x16x32_bf16 v[132:135], a[100:103], a[176:179], v[132:135]\n"
"ds_read_b64_tr_b16 a[130:131], v251 offset:512\n"
"v_mfma_f32_16x16x32_bf16 v[132:135], a[132:135], a[208:211], v[132:135]\n"
"ds_read_b128 a[0:3], v247 offset:57360\n"
"v_mfma_f32_16x16x32_bf16 v[164:167], a[100:103], a[180:183], v[164:167]\n"
"ds_read_b128 a[4:7], v247 offset:58384\n"
"v_mfma_f32_16x16x32_bf16 v[164:167], a[132:135], a[212:215], v[164:167]\n"
"v_add_u32 v253, 0x40000, v253\n"
"s_add_u32 m0, s71, 32768\n"
"v_mfma_f32_16x16x32_bf16 v[8:11], a[104:107], a[160:163], v[8:11]\n"
"buffer_load_dwordx4 v253, s[48:51], s64 offen offset:1024  lds\n"
"v_mfma_f32_16x16x32_bf16 v[8:11], a[136:139], a[192:195], v[8:11]\n"
"ds_read_b64_tr_b16 a[100:101], v249 offset:49160\n"
"v_mfma_f32_16x16x32_bf16 v[40:43], a[104:107], a[164:167], v[40:43]\n"
"ds_read_b64_tr_b16 a[102:103], v249 offset:49672\n"
"v_mfma_f32_16x16x32_bf16 v[40:43], a[136:139], a[196:199], v[40:43]\n"
"ds_read_b64_tr_b16 a[132:133], v251 offset:8\n"
"v_mfma_f32_16x16x32_bf16 v[72:75], a[104:107], a[168:171], v[72:75]\n"
"buffer_load_dwordx4 v253, s[48:51], 0 offen offset:3072  lds\n"
"v_mfma_f32_16x16x32_bf16 v[72:75], a[136:139], a[200:203], v[72:75]\n"
"ds_read_b64_tr_b16 a[134:135], v251 offset:520\n"
"v_mfma_f32_16x16x32_bf16 v[104:107], a[104:107], a[172:175], v[104:107]\n"
"ds_read_b128 a[8:11], v247 offset:59408\n"
"v_mfma_f32_16x16x32_bf16 v[104:107], a[136:139], a[204:207], v[104:107]\n"
"ds_read_b128 a[12:15], v247 offset:60432\n"
"v_mfma_f32_16x16x32_bf16 v[136:139], a[104:107], a[176:179], v[136:139]\n"
"v_add_u32 v253, 0x40000, v253\n"
"s_add_u32 m0, s73, 32768\n"
"v_mfma_f32_16x16x32_bf16 v[136:139], a[136:139], a[208:211], v[136:139]\n"
"buffer_load_dwordx4 v254, s[52:55], s66 offen offset:0  lds\n"
"v_mfma_f32_16x16x32_bf16 v[168:171], a[104:107], a[180:183], v[168:171]\n"
"ds_read_b128 a[16:19], v247 offset:61456\n"
"v_mfma_f32_16x16x32_bf16 v[168:171], a[136:139], a[212:215], v[168:171]\n"
"ds_read_b64_tr_b16 a[104:105], v250 offset:49152\n"
"v_mfma_f32_16x16x32_bf16 v[12:15], a[108:111], a[160:163], v[12:15]\n"
"ds_read_b64_tr_b16 a[106:107], v250 offset:49664\n"
"v_mfma_f32_16x16x32_bf16 v[12:15], a[140:143], a[192:195], v[12:15]\n"
"buffer_load_dwordx4 v254, s[52:55], s67 offen offset:1024  lds\n"
"v_mfma_f32_16x16x32_bf16 v[44:47], a[108:111], a[164:167], v[44:47]\n"
"ds_read_b64_tr_b16 a[136:137], v252 offset:0\n"
"v_mfma_f32_16x16x32_bf16 v[44:47], a[140:143], a[196:199], v[44:47]\n"
"ds_read_b64_tr_b16 a[138:139], v252 offset:512\n"
"v_mfma_f32_16x16x32_bf16 v[76:79], a[108:111], a[168:171], v[76:79]\n"
"ds_read_b128 a[20:23], v247 offset:62480\n"
"v_mfma_f32_16x16x32_bf16 v[76:79], a[140:143], a[200:203], v[76:79]\n"
"buffer_load_dwordx4 v254, s[52:55], s68 offen offset:2048  lds\n"
"v_mfma_f32_16x16x32_bf16 v[108:111], a[108:111], a[172:175], v[108:111]\n"
"ds_read_b128 a[24:27], v247 offset:63504\n"
"v_mfma_f32_16x16x32_bf16 v[108:111], a[140:143], a[204:207], v[108:111]\n"
"v_mfma_f32_16x16x32_bf16 v[140:143], a[108:111], a[176:179], v[140:143]\n"
"v_mfma_f32_16x16x32_bf16 v[140:143], a[140:143], a[208:211], v[140:143]\n"
"buffer_load_dwordx4 v254, s[52:55], s69 offen offset:3072  lds\n"
"v_mfma_f32_16x16x32_bf16 v[172:175], a[108:111], a[180:183], v[172:175]\n"
"v_mfma_f32_16x16x32_bf16 v[172:175], a[140:143], a[212:215], v[172:175]\n"
"ds_read_b64_tr_b16 a[108:109], v250 offset:49160\n"
"v_mfma_f32_16x16x32_bf16 v[16:19], a[112:115], a[160:163], v[16:19]\n"
"ds_read_b64_tr_b16 a[110:111], v250 offset:49672\n"
"v_mfma_f32_16x16x32_bf16 v[16:19], a[144:147], a[192:195], v[16:19]\n"
"v_add_u32 v254, 64, v254\n"
"v_mfma_f32_16x16x32_bf16 v[48:51], a[112:115], a[164:167], v[48:51]\n"
"ds_read_b64_tr_b16 a[140:141], v252 offset:8\n"
"v_mfma_f32_16x16x32_bf16 v[48:51], a[144:147], a[196:199], v[48:51]\n"
"ds_read_b64_tr_b16 a[142:143], v252 offset:520\n"
"v_mfma_f32_16x16x32_bf16 v[80:83], a[112:115], a[168:171], v[80:83]\n"
"v_mfma_f32_16x16x32_bf16 v[80:83], a[144:147], a[200:203], v[80:83]\n"
"v_mfma_f32_16x16x32_bf16 v[112:115], a[112:115], a[172:175], v[112:115]\n"
"v_mfma_f32_16x16x32_bf16 v[112:115], a[144:147], a[204:207], v[112:115]\n"
"v_mfma_f32_16x16x32_bf16 v[144:147], a[112:115], a[176:179], v[144:147]\n"
"v_mfma_f32_16x16x32_bf16 v[144:147], a[144:147], a[208:211], v[144:147]\n"
"v_mfma_f32_16x16x32_bf16 v[176:179], a[112:115], a[180:183], v[176:179]\n"
"v_mfma_f32_16x16x32_bf16 v[176:179], a[144:147], a[212:215], v[176:179]\n"
"v_mfma_f32_16x16x32_bf16 v[192:195], a[112:115], a[184:187], v[192:195]\n"
"v_mfma_f32_16x16x32_bf16 v[192:195], a[144:147], a[216:219], v[192:195]\n"
"v_mfma_f32_16x16x32_bf16 v[20:23], a[116:119], a[160:163], v[20:23]\n"
"v_mfma_f32_16x16x32_bf16 v[20:23], a[148:151], a[192:195], v[20:23]\n"
"v_mfma_f32_16x16x32_bf16 v[52:55], a[116:119], a[164:167], v[52:55]\n"
"v_mfma_f32_16x16x32_bf16 v[52:55], a[148:151], a[196:199], v[52:55]\n"
"v_mfma_f32_16x16x32_bf16 v[84:87], a[116:119], a[168:171], v[84:87]\n"
"v_mfma_f32_16x16x32_bf16 v[84:87], a[148:151], a[200:203], v[84:87]\n"
"s_waitcnt vmcnt(20) lgkmcnt(0)\n"
"s_barrier\n"
"v_mfma_f32_16x16x32_bf16 v[116:119], a[116:119], a[172:175], v[116:119]\n"
"s_add_u32 m0, s71, 49152\n"
"v_mfma_f32_16x16x32_bf16 v[116:119], a[148:151], a[204:207], v[116:119]\n"
"buffer_load_dwordx4 v253, s[48:51], s63 offen offset:0  lds\n"
"v_mfma_f32_16x16x32_bf16 v[148:151], a[116:119], a[176:179], v[148:151]\n"
"ds_read_b128 a[28:31], v248 offset:24592\n"
"v_mfma_f32_16x16x32_bf16 v[148:151], a[148:151], a[208:211], v[148:151]\n"
"ds_read_b128 a[32:35], v248 offset:25616\n"
"v_mfma_f32_16x16x32_bf16 v[180:183], a[116:119], a[180:183], v[180:183]\n"
"buffer_load_dwordx4 v253, s[48:51], s65 offen offset:2048  lds\n"
"v_mfma_f32_16x16x32_bf16 v[180:183], a[148:151], a[212:215], v[180:183]\n"
"ds_read_b128 a[36:39], v248 offset:26640\n"
"v_mfma_f32_16x16x32_bf16 v[196:199], a[116:119], a[184:187], v[196:199]\n"
"ds_read_b128 a[40:43], v248 offset:27664\n"
"v_mfma_f32_16x16x32_bf16 v[196:199], a[148:151], a[216:219], v[196:199]\n"
"v_add_u32 v253, 0x40000, v253\n"
"s_mov_b32 m0, s72\n"
"v_mfma_f32_16x16x32_bf16 v[24:27], a[120:123], a[160:163], v[24:27]\n"
"buffer_load_dwordx4 v253, s[48:51], s63 offen offset:0  lds\n"
"v_mfma_f32_16x16x32_bf16 v[24:27], a[152:155], a[192:195], v[24:27]\n"
"ds_read_b128 a[44:47], v248 offset:28688\n"
"v_mfma_f32_16x16x32_bf16 v[56:59], a[120:123], a[164:167], v[56:59]\n"
"ds_read_b128 a[48:51], v248 offset:29712\n"
"v_mfma_f32_16x16x32_bf16 v[56:59], a[152:155], a[196:199], v[56:59]\n"
"buffer_load_dwordx4 v253, s[48:51], s65 offen offset:2048  lds\n"
"v_mfma_f32_16x16x32_bf16 v[88:91], a[120:123], a[168:171], v[88:91]\n"
"ds_read_b128 a[52:55], v248 offset:30736\n"
"v_mfma_f32_16x16x32_bf16 v[88:91], a[152:155], a[200:203], v[88:91]\n"
"ds_read_b64_tr_b16 a[112:113], v249 offset:50176\n"
"v_mfma_f32_16x16x32_bf16 v[120:123], a[120:123], a[172:175], v[120:123]\n"
"v_subrev_u32 v253, 0x40000, v253\n"
"s_add_u32 m0, s73, 49152\n"
"v_mfma_f32_16x16x32_bf16 v[120:123], a[152:155], a[204:207], v[120:123]\n"
"buffer_load_dwordx4 v254, s[52:55], s66 offen offset:0  lds\n"
"v_mfma_f32_16x16x32_bf16 v[152:155], a[120:123], a[176:179], v[152:155]\n"
"ds_read_b64_tr_b16 a[114:115], v249 offset:50688\n"
"v_mfma_f32_16x16x32_bf16 v[152:155], a[152:155], a[208:211], v[152:155]\n"
"ds_read_b64_tr_b16 a[144:145], v251 offset:1024\n"
"v_mfma_f32_16x16x32_bf16 v[184:187], a[120:123], a[180:183], v[184:187]\n"
"buffer_load_dwordx4 v254, s[52:55], s67 offen offset:1024  lds\n"
"v_mfma_f32_16x16x32_bf16 v[184:187], a[152:155], a[212:215], v[184:187]\n"
"ds_read_b64_tr_b16 a[146:147], v251 offset:1536\n"
"v_mfma_f32_16x16x32_bf16 v[200:203], a[120:123], a[184:187], v[200:203]\n"
"ds_read_b64_tr_b16 a[116:117], v249 offset:50184\n"
"v_mfma_f32_16x16x32_bf16 v[200:203], a[152:155], a[216:219], v[200:203]\n"
"buffer_load_dwordx4 v254, s[52:55], s68 offen offset:2048  lds\n"
"v_mfma_f32_16x16x32_bf16 v[28:31], a[124:127], a[160:163], v[28:31]\n"
"ds_read_b64_tr_b16 a[118:119], v249 offset:50696\n"
"v_mfma_f32_16x16x32_bf16 v[28:31], a[156:159], a[192:195], v[28:31]\n"
"ds_read_b64_tr_b16 a[148:149], v251 offset:1032\n"
"v_mfma_f32_16x16x32_bf16 v[60:63], a[124:127], a[164:167], v[60:63]\n"
"buffer_load_dwordx4 v254, s[52:55], s69 offen offset:3072  lds\n"
"v_mfma_f32_16x16x32_bf16 v[60:63], a[156:159], a[196:199], v[60:63]\n"
"ds_read_b64_tr_b16 a[150:151], v251 offset:1544\n"
"v_mfma_f32_16x16x32_bf16 v[92:95], a[124:127], a[168:171], v[92:95]\n"
"ds_read_b64_tr_b16 a[120:121], v250 offset:50176\n"
"v_mfma_f32_16x16x32_bf16 v[92:95], a[156:159], a[200:203], v[92:95]\n"
"v_add_u32 v254, 64, v254\n"
"v_mfma_f32_16x16x32_bf16 v[124:127], a[124:127], a[172:175], v[124:127]\n"
"ds_read_b64_tr_b16 a[122:123], v250 offset:50688\n"
"v_mfma_f32_16x16x32_bf16 v[124:127], a[156:159], a[204:207], v[124:127]\n"
"ds_read_b64_tr_b16 a[152:153], v252 offset:1024\n"
"v_mfma_f32_16x16x32_bf16 v[156:159], a[124:127], a[176:179], v[156:159]\n"
"ds_read_b64_tr_b16 a[154:155], v252 offset:1536\n"
"v_mfma_f32_16x16x32_bf16 v[156:159], a[156:159], a[208:211], v[156:159]\n"
"v_mfma_f32_16x16x32_bf16 v[188:191], a[124:127], a[180:183], v[188:191]\n"
"v_mfma_f32_16x16x32_bf16 v[188:191], a[156:159], a[212:215], v[188:191]\n"
"v_mfma_f32_16x16x32_bf16 v[204:207], a[124:127], a[184:187], v[204:207]\n"
"v_mfma_f32_16x16x32_bf16 v[204:207], a[156:159], a[216:219], v[204:207]\n"
"s_waitcnt lgkmcnt(12)\n"
"v_mfma_f32_16x16x32_bf16 v[0:3], a[96:99], a[0:3], v[0:3]\n"
"ds_read_b64_tr_b16 a[124:125], v250 offset:50184\n"
"v_mfma_f32_16x16x32_bf16 v[0:3], a[128:131], a[28:31], v[0:3]\n"
"ds_read_b64_tr_b16 a[126:127], v250 offset:50696\n"
"v_mfma_f32_16x16x32_bf16 v[32:35], a[96:99], a[4:7], v[32:35]\n"
"ds_read_b64_tr_b16 a[156:157], v252 offset:1032\n"
"v_mfma_f32_16x16x32_bf16 v[32:35], a[128:131], a[32:35], v[32:35]\n"
"ds_read_b64_tr_b16 a[158:159], v252 offset:1544\n"
"v_mfma_f32_16x16x32_bf16 v[64:67], a[96:99], a[8:11], v[64:67]\n"
"v_mfma_f32_16x16x32_bf16 v[64:67], a[128:131], a[36:39], v[64:67]\n"
"v_mfma_f32_16x16x32_bf16 v[96:99], a[96:99], a[12:15], v[96:99]\n"
"v_mfma_f32_16x16x32_bf16 v[96:99], a[128:131], a[40:43], v[96:99]\n"
"v_mfma_f32_16x16x32_bf16 v[128:131], a[96:99], a[16:19], v[128:131]\n"
"v_mfma_f32_16x16x32_bf16 v[128:131], a[128:131], a[44:47], v[128:131]\n"
"v_mfma_f32_16x16x32_bf16 v[160:163], a[96:99], a[20:23], v[160:163]\n"
"v_mfma_f32_16x16x32_bf16 v[160:163], a[128:131], a[48:51], v[160:163]\n"
"v_mfma_f32_16x16x32_bf16 v[4:7], a[100:103], a[0:3], v[4:7]\n"
"v_mfma_f32_16x16x32_bf16 v[4:7], a[132:135], a[28:31], v[4:7]\n"
"s_waitcnt vmcnt(20) lgkmcnt(0)\n"
"s_barrier\n"
"v_mfma_f32_16x16x32_bf16 v[36:39], a[100:103], a[4:7], v[36:39]\n"
"s_add_u32 m0, s71, 49152\n"
"v_mfma_f32_16x16x32_bf16 v[36:39], a[132:135], a[32:35], v[36:39]\n"
"buffer_load_dwordx4 v253, s[48:51], s64 offen offset:1024  lds\n"
"v_mfma_f32_16x16x32_bf16 v[68:71], a[100:103], a[8:11], v[68:71]\n"
"ds_read_b128 a[160:163], v247 offset:24592\n"
"v_mfma_f32_16x16x32_bf16 v[68:71], a[132:135], a[36:39], v[68:71]\n"
"ds_read_b128 a[164:167], v247 offset:25616\n"
"v_mfma_f32_16x16x32_bf16 v[100:103], a[100:103], a[12:15], v[100:103]\n"
"ds_read_b128 a[168:171], v247 offset:26640\n"
"v_mfma_f32_16x16x32_bf16 v[100:103], a[132:135], a[40:43], v[100:103]\n"
"buffer_load_dwordx4 v253, s[48:51], 0 offen offset:3072  lds\n"
"v_mfma_f32_16x16x32_bf16 v[132:135], a[100:103], a[16:19], v[132:135]\n"
"ds_read_b128 a[172:175], v247 offset:27664\n"
"v_mfma_f32_16x16x32_bf16 v[132:135], a[132:135], a[44:47], v[132:135]\n"
"ds_read_b128 a[176:179], v247 offset:28688\n"
"v_mfma_f32_16x16x32_bf16 v[164:167], a[100:103], a[20:23], v[164:167]\n"
"ds_read_b128 a[180:183], v247 offset:29712\n"
"v_mfma_f32_16x16x32_bf16 v[164:167], a[132:135], a[48:51], v[164:167]\n"
"v_add_u32 v253, 0x40000, v253\n"
"s_mov_b32 m0, s72\n"
"v_mfma_f32_16x16x32_bf16 v[8:11], a[104:107], a[0:3], v[8:11]\n"
"buffer_load_dwordx4 v253, s[48:51], s64 offen offset:1024  lds\n"
"v_mfma_f32_16x16x32_bf16 v[8:11], a[136:139], a[28:31], v[8:11]\n"
"ds_read_b128 a[184:187], v247 offset:30736\n"
"v_mfma_f32_16x16x32_bf16 v[40:43], a[104:107], a[4:7], v[40:43]\n"
"ds_read_b64_tr_b16 a[56:57], v249 offset:16384\n"
"v_mfma_f32_16x16x32_bf16 v[40:43], a[136:139], a[32:35], v[40:43]\n"
"ds_read_b64_tr_b16 a[58:59], v249 offset:16896\n"
"v_mfma_f32_16x16x32_bf16 v[72:75], a[104:107], a[8:11], v[72:75]\n"
"buffer_load_dwordx4 v253, s[48:51], 0 offen offset:3072  lds\n"
"v_mfma_f32_16x16x32_bf16 v[72:75], a[136:139], a[36:39], v[72:75]\n"
"ds_read_b64_tr_b16 a[60:61], v249 offset:16392\n"
"v_mfma_f32_16x16x32_bf16 v[104:107], a[104:107], a[12:15], v[104:107]\n"
"ds_read_b64_tr_b16 a[62:63], v249 offset:16904\n"
"v_mfma_f32_16x16x32_bf16 v[104:107], a[136:139], a[40:43], v[104:107]\n"
"ds_read_b64_tr_b16 a[64:65], v250 offset:16384\n"
"v_mfma_f32_16x16x32_bf16 v[136:139], a[104:107], a[16:19], v[136:139]\n"
"v_add_u32 v253, 0x40000, v253\n"
"s_mov_b32 m0, s74\n"
"v_mfma_f32_16x16x32_bf16 v[136:139], a[136:139], a[44:47], v[136:139]\n"
"buffer_load_dwordx4 v254, s[52:55], s66 offen offset:0  lds\n"
"v_mfma_f32_16x16x32_bf16 v[168:171], a[104:107], a[20:23], v[168:171]\n"
"ds_read_b64_tr_b16 a[66:67], v250 offset:16896\n"
"v_mfma_f32_16x16x32_bf16 v[168:171], a[136:139], a[48:51], v[168:171]\n"
"ds_read_b64_tr_b16 a[68:69], v250 offset:16392\n"
"v_mfma_f32_16x16x32_bf16 v[12:15], a[108:111], a[0:3], v[12:15]\n"
"ds_read_b64_tr_b16 a[70:71], v250 offset:16904\n"
"v_mfma_f32_16x16x32_bf16 v[12:15], a[140:143], a[28:31], v[12:15]\n"
"buffer_load_dwordx4 v254, s[52:55], s67 offen offset:1024  lds\n"
"v_mfma_f32_16x16x32_bf16 v[44:47], a[108:111], a[4:7], v[44:47]\n"
"v_mfma_f32_16x16x32_bf16 v[44:47], a[140:143], a[32:35], v[44:47]\n"
"v_mfma_f32_16x16x32_bf16 v[76:79], a[108:111], a[8:11], v[76:79]\n"
"v_mfma_f32_16x16x32_bf16 v[76:79], a[140:143], a[36:39], v[76:79]\n"
"buffer_load_dwordx4 v254, s[52:55], s68 offen offset:2048  lds\n"
"v_mfma_f32_16x16x32_bf16 v[108:111], a[108:111], a[12:15], v[108:111]\n"
"v_mfma_f32_16x16x32_bf16 v[108:111], a[140:143], a[40:43], v[108:111]\n"
"v_mfma_f32_16x16x32_bf16 v[140:143], a[108:111], a[16:19], v[140:143]\n"
"v_mfma_f32_16x16x32_bf16 v[140:143], a[140:143], a[44:47], v[140:143]\n"
"buffer_load_dwordx4 v254, s[52:55], s69 offen offset:3072  lds\n"
"v_mfma_f32_16x16x32_bf16 v[172:175], a[108:111], a[20:23], v[172:175]\n"
"v_mfma_f32_16x16x32_bf16 v[172:175], a[140:143], a[48:51], v[172:175]\n"
"v_mfma_f32_16x16x32_bf16 v[16:19], a[112:115], a[0:3], v[16:19]\n"
"v_mfma_f32_16x16x32_bf16 v[16:19], a[144:147], a[28:31], v[16:19]\n"
"v_add_u32 v254, 64, v254\n"
"v_mfma_f32_16x16x32_bf16 v[48:51], a[112:115], a[4:7], v[48:51]\n"
"v_mfma_f32_16x16x32_bf16 v[48:51], a[144:147], a[32:35], v[48:51]\n"
"v_mfma_f32_16x16x32_bf16 v[80:83], a[112:115], a[8:11], v[80:83]\n"
"v_mfma_f32_16x16x32_bf16 v[80:83], a[144:147], a[36:39], v[80:83]\n"
"v_mfma_f32_16x16x32_bf16 v[112:115], a[112:115], a[12:15], v[112:115]\n"
"v_mfma_f32_16x16x32_bf16 v[112:115], a[144:147], a[40:43], v[112:115]\n"
"v_mfma_f32_16x16x32_bf16 v[144:147], a[112:115], a[16:19], v[144:147]\n"
"v_mfma_f32_16x16x32_bf16 v[144:147], a[144:147], a[44:47], v[144:147]\n"
"v_mfma_f32_16x16x32_bf16 v[176:179], a[112:115], a[20:23], v[176:179]\n"
"v_mfma_f32_16x16x32_bf16 v[176:179], a[144:147], a[48:51], v[176:179]\n"
"v_mfma_f32_16x16x32_bf16 v[192:195], a[112:115], a[24:27], v[192:195]\n"
"v_mfma_f32_16x16x32_bf16 v[192:195], a[144:147], a[52:55], v[192:195]\n"
"v_mfma_f32_16x16x32_bf16 v[20:23], a[116:119], a[0:3], v[20:23]\n"
"v_mfma_f32_16x16x32_bf16 v[20:23], a[148:151], a[28:31], v[20:23]\n"
"v_mfma_f32_16x16x32_bf16 v[52:55], a[116:119], a[4:7], v[52:55]\n"
"v_mfma_f32_16x16x32_bf16 v[52:55], a[148:151], a[32:35], v[52:55]\n"
"v_mfma_f32_16x16x32_bf16 v[84:87], a[116:119], a[8:11], v[84:87]\n"
"v_mfma_f32_16x16x32_bf16 v[84:87], a[148:151], a[36:39], v[84:87]\n"
"s_waitcnt vmcnt(16) lgkmcnt(0)\n"
"s_barrier\n"
"v_mfma_f32_16x16x32_bf16 v[116:119], a[116:119], a[12:15], v[116:119]\n"
"ds_read_b64_tr_b16 a[72:73], v249 offset:17408\n"
"v_mfma_f32_16x16x32_bf16 v[116:119], a[148:151], a[40:43], v[116:119]\n"
"ds_read_b64_tr_b16 a[74:75], v249 offset:17920\n"
"v_mfma_f32_16x16x32_bf16 v[148:151], a[116:119], a[16:19], v[148:151]\n"
"ds_read_b64_tr_b16 a[76:77], v249 offset:17416\n"
"v_mfma_f32_16x16x32_bf16 v[148:151], a[148:151], a[44:47], v[148:151]\n"
"ds_read_b64_tr_b16 a[78:79], v249 offset:17928\n"
"v_mfma_f32_16x16x32_bf16 v[180:183], a[116:119], a[20:23], v[180:183]\n"
"ds_read_b64_tr_b16 a[80:81], v250 offset:17408\n"
"v_mfma_f32_16x16x32_bf16 v[180:183], a[148:151], a[48:51], v[180:183]\n"
"ds_read_b64_tr_b16 a[82:83], v250 offset:17920\n"
"v_mfma_f32_16x16x32_bf16 v[196:199], a[116:119], a[24:27], v[196:199]\n"
"ds_read_b64_tr_b16 a[84:85], v250 offset:17416\n"
"v_mfma_f32_16x16x32_bf16 v[196:199], a[148:151], a[52:55], v[196:199]\n"
"ds_read_b64_tr_b16 a[86:87], v250 offset:17928\n"
"v_mfma_f32_16x16x32_bf16 v[24:27], a[120:123], a[0:3], v[24:27]\n"
"v_mfma_f32_16x16x32_bf16 v[24:27], a[152:155], a[28:31], v[24:27]\n"
"v_mfma_f32_16x16x32_bf16 v[56:59], a[120:123], a[4:7], v[56:59]\n"
"v_mfma_f32_16x16x32_bf16 v[56:59], a[152:155], a[32:35], v[56:59]\n"
"v_mfma_f32_16x16x32_bf16 v[88:91], a[120:123], a[8:11], v[88:91]\n"
"v_mfma_f32_16x16x32_bf16 v[88:91], a[152:155], a[36:39], v[88:91]\n"
"v_mfma_f32_16x16x32_bf16 v[120:123], a[120:123], a[12:15], v[120:123]\n"
"v_mfma_f32_16x16x32_bf16 v[120:123], a[152:155], a[40:43], v[120:123]\n"
"v_mfma_f32_16x16x32_bf16 v[152:155], a[120:123], a[16:19], v[152:155]\n"
"v_mfma_f32_16x16x32_bf16 v[152:155], a[152:155], a[44:47], v[152:155]\n"
"v_mfma_f32_16x16x32_bf16 v[184:187], a[120:123], a[20:23], v[184:187]\n"
"v_mfma_f32_16x16x32_bf16 v[184:187], a[152:155], a[48:51], v[184:187]\n"
"v_mfma_f32_16x16x32_bf16 v[200:203], a[120:123], a[24:27], v[200:203]\n"
"v_mfma_f32_16x16x32_bf16 v[200:203], a[152:155], a[52:55], v[200:203]\n"
"v_mfma_f32_16x16x32_bf16 v[28:31], a[124:127], a[0:3], v[28:31]\n"
"v_mfma_f32_16x16x32_bf16 v[28:31], a[156:159], a[28:31], v[28:31]\n"
"v_mfma_f32_16x16x32_bf16 v[60:63], a[124:127], a[4:7], v[60:63]\n"
"v_mfma_f32_16x16x32_bf16 v[60:63], a[156:159], a[32:35], v[60:63]\n"
"v_mfma_f32_16x16x32_bf16 v[92:95], a[124:127], a[8:11], v[92:95]\n"
"v_mfma_f32_16x16x32_bf16 v[92:95], a[156:159], a[36:39], v[92:95]\n"
"v_mfma_f32_16x16x32_bf16 v[124:127], a[124:127], a[12:15], v[124:127]\n"
"v_mfma_f32_16x16x32_bf16 v[124:127], a[156:159], a[40:43], v[124:127]\n"
"v_mfma_f32_16x16x32_bf16 v[156:159], a[124:127], a[16:19], v[156:159]\n"
"v_mfma_f32_16x16x32_bf16 v[156:159], a[156:159], a[44:47], v[156:159]\n"
"v_mfma_f32_16x16x32_bf16 v[188:191], a[124:127], a[20:23], v[188:191]\n"
"v_mfma_f32_16x16x32_bf16 v[188:191], a[156:159], a[48:51], v[188:191]\n"
"v_mfma_f32_16x16x32_bf16 v[204:207], a[124:127], a[24:27], v[204:207]\n"
"v_mfma_f32_16x16x32_bf16 v[204:207], a[156:159], a[52:55], v[204:207]\n"
"s_waitcnt lgkmcnt(0)\n"
"s_barrier\n"
"v_mfma_f32_16x16x32_bf16 v[0:3], a[56:59], a[160:163], v[0:3]\n"
"s_add_u32 m0, s70, 16384\n"
"v_mfma_f32_16x16x32_bf16 v[4:7], a[60:63], a[160:163], v[4:7]\n"
"buffer_load_dwordx4 v223, s[40:43], s60 offen offset:0 nt lds\n"
"v_mfma_f32_16x16x32_bf16 v[8:11], a[64:67], a[160:163], v[8:11]\n"
"ds_read_b128 a[192:195], v247 offset:40976\n"
"v_mfma_f32_16x16x32_bf16 v[12:15], a[68:71], a[160:163], v[12:15]\n"
"ds_read_b64_tr_b16 a[128:129], v249 offset:32768\n"
"v_mfma_f32_16x16x32_bf16 v[16:19], a[72:75], a[160:163], v[16:19]\n"
"ds_read_b64_tr_b16 a[130:131], v249 offset:33280\n"
"v_mfma_f32_16x16x32_bf16 v[20:23], a[76:79], a[160:163], v[20:23]\n"
"buffer_load_dwordx4 v223, s[40:43], s61 offen offset:1024 nt lds\n"
"v_mfma_f32_16x16x32_bf16 v[24:27], a[80:83], a[160:163], v[24:27]\n"
"ds_read_b64_tr_b16 a[132:133], v249 offset:32776\n"
"v_mfma_f32_16x16x32_bf16 v[28:31], a[84:87], a[160:163], v[28:31]\n"
"ds_read_b64_tr_b16 a[134:135], v249 offset:33288\n"
"v_mfma_f32_16x16x32_bf16 v[32:35], a[56:59], a[164:167], v[32:35]\n"
"ds_read_b64_tr_b16 a[136:137], v250 offset:32768\n"
"v_mfma_f32_16x16x32_bf16 v[36:39], a[60:63], a[164:167], v[36:39]\n"
"buffer_load_dwordx4 v223, s[40:43], s62 offen offset:2048 nt lds\n"
"v_mfma_f32_16x16x32_bf16 v[40:43], a[64:67], a[164:167], v[40:43]\n"
"ds_read_b64_tr_b16 a[138:139], v250 offset:33280\n"
"v_mfma_f32_16x16x32_bf16 v[44:47], a[68:71], a[164:167], v[44:47]\n"
"ds_read_b64_tr_b16 a[140:141], v250 offset:32776\n"
"v_mfma_f32_16x16x32_bf16 v[48:51], a[72:75], a[164:167], v[48:51]\n"
"ds_read_b64_tr_b16 a[142:143], v250 offset:33288\n"
"v_mfma_f32_16x16x32_bf16 v[52:55], a[76:79], a[164:167], v[52:55]\n"
"buffer_load_dwordx4 v223, s[40:43], 0 offen offset:3072 nt lds\n"
"v_mfma_f32_16x16x32_bf16 v[56:59], a[80:83], a[164:167], v[56:59]\n"
"ds_read_b64_tr_b16 a[144:145], v249 offset:33792\n"
"v_mfma_f32_16x16x32_bf16 v[60:63], a[84:87], a[164:167], v[60:63]\n"
"ds_read_b64_tr_b16 a[146:147], v249 offset:34304\n"
"v_mfma_f32_16x16x32_bf16 v[64:67], a[56:59], a[168:171], v[64:67]\n"
"ds_read_b64_tr_b16 a[148:149], v249 offset:33800\n"
"v_mfma_f32_16x16x32_bf16 v[68:71], a[60:63], a[168:171], v[68:71]\n"
"s_add_u32 m0, s70, 24576\n"
"v_mfma_f32_16x16x32_bf16 v[72:75], a[64:67], a[168:171], v[72:75]\n"
"buffer_load_dwordx4 v223, s[44:47], s60 offen offset:0 nt lds\n"
"v_mfma_f32_16x16x32_bf16 v[76:79], a[68:71], a[168:171], v[76:79]\n"
"ds_read_b64_tr_b16 a[150:151], v249 offset:34312\n"
"v_mfma_f32_16x16x32_bf16 v[80:83], a[72:75], a[168:171], v[80:83]\n"
"ds_read_b64_tr_b16 a[152:153], v250 offset:33792\n"
"v_mfma_f32_16x16x32_bf16 v[84:87], a[76:79], a[168:171], v[84:87]\n"
"ds_read_b64_tr_b16 a[154:155], v250 offset:34304\n"
"v_mfma_f32_16x16x32_bf16 v[88:91], a[80:83], a[168:171], v[88:91]\n"
"buffer_load_dwordx4 v223, s[44:47], s61 offen offset:1024 nt lds\n"
"v_mfma_f32_16x16x32_bf16 v[92:95], a[84:87], a[168:171], v[92:95]\n"
"ds_read_b64_tr_b16 a[156:157], v250 offset:33800\n"
"v_mfma_f32_16x16x32_bf16 v[96:99], a[56:59], a[172:175], v[96:99]\n"
"ds_read_b64_tr_b16 a[158:159], v250 offset:34312\n"
"v_mfma_f32_16x16x32_bf16 v[100:103], a[60:63], a[172:175], v[100:103]\n"
"ds_read_b128 a[196:199], v247 offset:42000\n"
"v_mfma_f32_16x16x32_bf16 v[104:107], a[64:67], a[172:175], v[104:107]\n"
"buffer_load_dwordx4 v223, s[44:47], s62 offen offset:2048 nt lds\n"
"v_mfma_f32_16x16x32_bf16 v[108:111], a[68:71], a[172:175], v[108:111]\n"
"ds_read_b128 a[200:203], v247 offset:43024\n"
"v_mfma_f32_16x16x32_bf16 v[112:115], a[72:75], a[172:175], v[112:115]\n"
"ds_read_b128 a[204:207], v247 offset:44048\n"
"v_mfma_f32_16x16x32_bf16 v[116:119], a[76:79], a[172:175], v[116:119]\n"
"ds_read_b128 a[208:211], v247 offset:45072\n"
"v_mfma_f32_16x16x32_bf16 v[120:123], a[80:83], a[172:175], v[120:123]\n"
"buffer_load_dwordx4 v223, s[44:47], 0 offen offset:3072 nt lds\n"
"v_mfma_f32_16x16x32_bf16 v[124:127], a[84:87], a[172:175], v[124:127]\n"
"ds_read_b128 a[212:215], v247 offset:46096\n"
"v_mfma_f32_16x16x32_bf16 v[128:131], a[56:59], a[176:179], v[128:131]\n"
"ds_read_b128 a[216:219], v247 offset:47120\n"
"v_mfma_f32_16x16x32_bf16 v[132:135], a[60:63], a[176:179], v[132:135]\n"
"v_mfma_f32_16x16x32_bf16 v[136:139], a[64:67], a[176:179], v[136:139]\n"
"v_add_u32 v223, 0x2000, v223\n"
"v_mfma_f32_16x16x32_bf16 v[140:143], a[68:71], a[176:179], v[140:143]\n"
"v_mfma_f32_16x16x32_bf16 v[144:147], a[72:75], a[176:179], v[144:147]\n"
"v_mfma_f32_16x16x32_bf16 v[148:151], a[76:79], a[176:179], v[148:151]\n"
"v_mfma_f32_16x16x32_bf16 v[152:155], a[80:83], a[176:179], v[152:155]\n"
"v_mfma_f32_16x16x32_bf16 v[156:159], a[84:87], a[176:179], v[156:159]\n"
"v_mfma_f32_16x16x32_bf16 v[160:163], a[56:59], a[180:183], v[160:163]\n"
"v_mfma_f32_16x16x32_bf16 v[164:167], a[60:63], a[180:183], v[164:167]\n"
"v_mfma_f32_16x16x32_bf16 v[168:171], a[64:67], a[180:183], v[168:171]\n"
"v_mfma_f32_16x16x32_bf16 v[172:175], a[68:71], a[180:183], v[172:175]\n"
"v_mfma_f32_16x16x32_bf16 v[176:179], a[72:75], a[180:183], v[176:179]\n"
"v_mfma_f32_16x16x32_bf16 v[180:183], a[76:79], a[180:183], v[180:183]\n"
"v_mfma_f32_16x16x32_bf16 v[184:187], a[80:83], a[180:183], v[184:187]\n"
"v_mfma_f32_16x16x32_bf16 v[188:191], a[84:87], a[180:183], v[188:191]\n"
"v_mfma_f32_16x16x32_bf16 v[192:195], a[72:75], a[184:187], v[192:195]\n"
"v_mfma_f32_16x16x32_bf16 v[196:199], a[76:79], a[184:187], v[196:199]\n"
"v_mfma_f32_16x16x32_bf16 v[200:203], a[80:83], a[184:187], v[200:203]\n"
"v_mfma_f32_16x16x32_bf16 v[204:207], a[84:87], a[184:187], v[204:207]\n"
"s_waitcnt vmcnt(14) lgkmcnt(0)\n"
"s_barrier\n"
"v_mfma_f32_16x16x32_bf16 v[0:3], a[128:131], a[192:195], v[0:3]\n"
"v_mfma_f32_16x16x32_bf16 v[4:7], a[132:135], a[192:195], v[4:7]\n"
"ds_read_b128 a[160:163], v247 offset:57360\n"
"v_mfma_f32_16x16x32_bf16 v[8:11], a[136:139], a[192:195], v[8:11]\n"
"ds_read_b64_tr_b16 a[96:97], v249 offset:49152\n"
"v_mfma_f32_16x16x32_bf16 v[12:15], a[140:143], a[192:195], v[12:15]\n"
"ds_read_b64_tr_b16 a[98:99], v249 offset:49664\n"
"v_mfma_f32_16x16x32_bf16 v[16:19], a[144:147], a[192:195], v[16:19]\n"
"ds_read_b64_tr_b16 a[100:101], v249 offset:49160\n"
"v_mfma_f32_16x16x32_bf16 v[20:23], a[148:151], a[192:195], v[20:23]\n"
"ds_read_b64_tr_b16 a[102:103], v249 offset:49672\n"
"v_mfma_f32_16x16x32_bf16 v[24:27], a[152:155], a[192:195], v[24:27]\n"
"ds_read_b64_tr_b16 a[104:105], v250 offset:49152\n"
"v_mfma_f32_16x16x32_bf16 v[28:31], a[156:159], a[192:195], v[28:31]\n"
"ds_read_b64_tr_b16 a[106:107], v250 offset:49664\n"
"v_mfma_f32_16x16x32_bf16 v[32:35], a[128:131], a[196:199], v[32:35]\n"
"ds_read_b64_tr_b16 a[108:109], v250 offset:49160\n"
"v_mfma_f32_16x16x32_bf16 v[36:39], a[132:135], a[196:199], v[36:39]\n"
"ds_read_b64_tr_b16 a[110:111], v250 offset:49672\n"
"v_mfma_f32_16x16x32_bf16 v[40:43], a[136:139], a[196:199], v[40:43]\n"
"ds_read_b64_tr_b16 a[112:113], v249 offset:50176\n"
"v_mfma_f32_16x16x32_bf16 v[44:47], a[140:143], a[196:199], v[44:47]\n"
"ds_read_b64_tr_b16 a[114:115], v249 offset:50688\n"
"v_mfma_f32_16x16x32_bf16 v[48:51], a[144:147], a[196:199], v[48:51]\n"
"ds_read_b64_tr_b16 a[116:117], v249 offset:50184\n"
"v_mfma_f32_16x16x32_bf16 v[52:55], a[148:151], a[196:199], v[52:55]\n"
"ds_read_b64_tr_b16 a[118:119], v249 offset:50696\n"
"v_mfma_f32_16x16x32_bf16 v[56:59], a[152:155], a[196:199], v[56:59]\n"
"ds_read_b64_tr_b16 a[120:121], v250 offset:50176\n"
"v_mfma_f32_16x16x32_bf16 v[60:63], a[156:159], a[196:199], v[60:63]\n"
"ds_read_b64_tr_b16 a[122:123], v250 offset:50688\n"
"v_mfma_f32_16x16x32_bf16 v[64:67], a[128:131], a[200:203], v[64:67]\n"
"ds_read_b64_tr_b16 a[124:125], v250 offset:50184\n"
"v_mfma_f32_16x16x32_bf16 v[68:71], a[132:135], a[200:203], v[68:71]\n"
"ds_read_b64_tr_b16 a[126:127], v250 offset:50696\n"
"v_mfma_f32_16x16x32_bf16 v[72:75], a[136:139], a[200:203], v[72:75]\n"
"ds_read_b128 a[164:167], v247 offset:58384\n"
"v_mfma_f32_16x16x32_bf16 v[76:79], a[140:143], a[200:203], v[76:79]\n"
"ds_read_b128 a[168:171], v247 offset:59408\n"
"v_mfma_f32_16x16x32_bf16 v[80:83], a[144:147], a[200:203], v[80:83]\n"
"s_add_u32 m0, s70, 32768\n"
"v_mfma_f32_16x16x32_bf16 v[84:87], a[148:151], a[200:203], v[84:87]\n"
"buffer_load_dwordx4 v223, s[40:43], s60 offen offset:0 nt lds\n"
"v_mfma_f32_16x16x32_bf16 v[88:91], a[152:155], a[200:203], v[88:91]\n"
"ds_read_b128 a[172:175], v247 offset:60432\n"
"v_mfma_f32_16x16x32_bf16 v[92:95], a[156:159], a[200:203], v[92:95]\n"
"ds_read_b128 a[176:179], v247 offset:61456\n"
"v_mfma_f32_16x16x32_bf16 v[96:99], a[128:131], a[204:207], v[96:99]\n"
"ds_read_b128 a[180:183], v247 offset:62480\n"
"v_mfma_f32_16x16x32_bf16 v[100:103], a[132:135], a[204:207], v[100:103]\n"
"ds_read_b128 a[184:187], v247 offset:63504\n"
"v_mfma_f32_16x16x32_bf16 v[104:107], a[136:139], a[204:207], v[104:107]\n"
"v_mfma_f32_16x16x32_bf16 v[108:111], a[140:143], a[204:207], v[108:111]\n"
"v_mfma_f32_16x16x32_bf16 v[112:115], a[144:147], a[204:207], v[112:115]\n"
"v_mfma_f32_16x16x32_bf16 v[116:119], a[148:151], a[204:207], v[116:119]\n"
"v_mfma_f32_16x16x32_bf16 v[120:123], a[152:155], a[204:207], v[120:123]\n"
"v_mfma_f32_16x16x32_bf16 v[124:127], a[156:159], a[204:207], v[124:127]\n"
"v_mfma_f32_16x16x32_bf16 v[128:131], a[128:131], a[208:211], v[128:131]\n"
"v_mfma_f32_16x16x32_bf16 v[132:135], a[132:135], a[208:211], v[132:135]\n"
"v_mfma_f32_16x16x32_bf16 v[136:139], a[136:139], a[208:211], v[136:139]\n"
"v_mfma_f32_16x16x32_bf16 v[140:143], a[140:143], a[208:211], v[140:143]\n"
"buffer_load_dwordx4 v223, s[40:43], s61 offen offset:1024 nt lds\n"
"v_mfma_f32_16x16x32_bf16 v[144:147], a[144:147], a[208:211], v[144:147]\n"
"v_mfma_f32_16x16x32_bf16 v[148:151], a[148:151], a[208:211], v[148:151]\n"
"v_mfma_f32_16x16x32_bf16 v[152:155], a[152:155], a[208:211], v[152:155]\n"
"v_mfma_f32_16x16x32_bf16 v[156:159], a[156:159], a[208:211], v[156:159]\n"
"v_mfma_f32_16x16x32_bf16 v[160:163], a[128:131], a[212:215], v[160:163]\n"
"v_mfma_f32_16x16x32_bf16 v[164:167], a[132:135], a[212:215], v[164:167]\n"
"v_mfma_f32_16x16x32_bf16 v[168:171], a[136:139], a[212:215], v[168:171]\n"
"v_mfma_f32_16x16x32_bf16 v[172:175], a[140:143], a[212:215], v[172:175]\n"
"v_mfma_f32_16x16x32_bf16 v[176:179], a[144:147], a[212:215], v[176:179]\n"
"v_mfma_f32_16x16x32_bf16 v[180:183], a[148:151], a[212:215], v[180:183]\n"
"v_mfma_f32_16x16x32_bf16 v[184:187], a[152:155], a[212:215], v[184:187]\n"
"v_mfma_f32_16x16x32_bf16 v[188:191], a[156:159], a[212:215], v[188:191]\n"
"v_mfma_f32_16x16x32_bf16 v[192:195], a[144:147], a[216:219], v[192:195]\n"
"v_mfma_f32_16x16x32_bf16 v[196:199], a[148:151], a[216:219], v[196:199]\n"
"v_mfma_f32_16x16x32_bf16 v[200:203], a[152:155], a[216:219], v[200:203]\n"
"v_mfma_f32_16x16x32_bf16 v[204:207], a[156:159], a[216:219], v[204:207]\n"
"s_waitcnt vmcnt(10) lgkmcnt(0)\n"
"s_barrier\n"
"v_mfma_f32_16x16x32_bf16 v[0:3], a[96:99], a[160:163], v[0:3]\n"
"v_mfma_f32_16x16x32_bf16 v[4:7], a[100:103], a[160:163], v[4:7]\n"
"ds_read_b128 a[192:195], v248 offset:24592\n"
"v_mfma_f32_16x16x32_bf16 v[8:11], a[104:107], a[160:163], v[8:11]\n"
"s_add_u32 m0, s70, 32768\n"
"v_mfma_f32_16x16x32_bf16 v[12:15], a[108:111], a[160:163], v[12:15]\n"
"buffer_load_dwordx4 v223, s[40:43], s62 offen offset:2048 nt lds\n"
"v_mfma_f32_16x16x32_bf16 v[16:19], a[112:115], a[160:163], v[16:19]\n"
"ds_read_b64_tr_b16 a[128:129], v251 offset:0\n"
"v_mfma_f32_16x16x32_bf16 v[20:23], a[116:119], a[160:163], v[20:23]\n"
"ds_read_b64_tr_b16 a[130:131], v251 offset:512\n"
"v_mfma_f32_16x16x32_bf16 v[24:27], a[120:123], a[160:163], v[24:27]\n"
"ds_read_b64_tr_b16 a[132:133], v251 offset:8\n"
"v_mfma_f32_16x16x32_bf16 v[28:31], a[124:127], a[160:163], v[28:31]\n"
"ds_read_b64_tr_b16 a[134:135], v251 offset:520\n"
"v_mfma_f32_16x16x32_bf16 v[32:35], a[96:99], a[164:167], v[32:35]\n"
"ds_read_b64_tr_b16 a[136:137], v252 offset:0\n"
"v_mfma_f32_16x16x32_bf16 v[36:39], a[100:103], a[164:167], v[36:39]\n"
"ds_read_b64_tr_b16 a[138:139], v252 offset:512\n"
"v_mfma_f32_16x16x32_bf16 v[40:43], a[104:107], a[164:167], v[40:43]\n"
"ds_read_b64_tr_b16 a[140:141], v252 offset:8\n"
"v_mfma_f32_16x16x32_bf16 v[44:47], a[108:111], a[164:167], v[44:47]\n"
"ds_read_b64_tr_b16 a[142:143], v252 offset:520\n"
"v_mfma_f32_16x16x32_bf16 v[48:51], a[112:115], a[164:167], v[48:51]\n"
"ds_read_b64_tr_b16 a[144:145], v251 offset:1024\n"
"v_mfma_f32_16x16x32_bf16 v[52:55], a[116:119], a[164:167], v[52:55]\n"
"ds_read_b64_tr_b16 a[146:147], v251 offset:1536\n"
"v_mfma_f32_16x16x32_bf16 v[56:59], a[120:123], a[164:167], v[56:59]\n"
"ds_read_b64_tr_b16 a[148:149], v251 offset:1032\n"
"v_mfma_f32_16x16x32_bf16 v[60:63], a[124:127], a[164:167], v[60:63]\n"
"ds_read_b64_tr_b16 a[150:151], v251 offset:1544\n"
"v_mfma_f32_16x16x32_bf16 v[64:67], a[96:99], a[168:171], v[64:67]\n"
"ds_read_b64_tr_b16 a[152:153], v252 offset:1024\n"
"v_mfma_f32_16x16x32_bf16 v[68:71], a[100:103], a[168:171], v[68:71]\n"
"ds_read_b64_tr_b16 a[154:155], v252 offset:1536\n"
"v_mfma_f32_16x16x32_bf16 v[72:75], a[104:107], a[168:171], v[72:75]\n"
"buffer_load_dwordx4 v223, s[40:43], 0 offen offset:3072 nt lds\n"
"v_mfma_f32_16x16x32_bf16 v[76:79], a[108:111], a[168:171], v[76:79]\n"
"ds_read_b64_tr_b16 a[156:157], v252 offset:1032\n"
"v_mfma_f32_16x16x32_bf16 v[80:83], a[112:115], a[168:171], v[80:83]\n"
"ds_read_b64_tr_b16 a[158:159], v252 offset:1544\n"
"v_mfma_f32_16x16x32_bf16 v[84:87], a[116:119], a[168:171], v[84:87]\n"
"ds_read_b128 a[196:199], v248 offset:25616\n"
"v_mfma_f32_16x16x32_bf16 v[88:91], a[120:123], a[168:171], v[88:91]\n"
"ds_read_b128 a[200:203], v248 offset:26640\n"
"v_mfma_f32_16x16x32_bf16 v[92:95], a[124:127], a[168:171], v[92:95]\n"
"ds_read_b128 a[204:207], v248 offset:27664\n"
"v_mfma_f32_16x16x32_bf16 v[96:99], a[96:99], a[172:175], v[96:99]\n"
"ds_read_b128 a[208:211], v248 offset:28688\n"
"v_mfma_f32_16x16x32_bf16 v[100:103], a[100:103], a[172:175], v[100:103]\n"
"ds_read_b128 a[212:215], v248 offset:29712\n"
"v_mfma_f32_16x16x32_bf16 v[104:107], a[104:107], a[172:175], v[104:107]\n"
"ds_read_b128 a[216:219], v248 offset:30736\n"
"v_mfma_f32_16x16x32_bf16 v[108:111], a[108:111], a[172:175], v[108:111]\n"
"v_mfma_f32_16x16x32_bf16 v[112:115], a[112:115], a[172:175], v[112:115]\n"
"v_mfma_f32_16x16x32_bf16 v[116:119], a[116:119], a[172:175], v[116:119]\n"
"v_mfma_f32_16x16x32_bf16 v[120:123], a[120:123], a[172:175], v[120:123]\n"
"v_mfma_f32_16x16x32_bf16 v[124:127], a[124:127], a[172:175], v[124:127]\n"
"v_mfma_f32_16x16x32_bf16 v[128:131], a[96:99], a[176:179], v[128:131]\n"
"v_mfma_f32_16x16x32_bf16 v[132:135], a[100:103], a[176:179], v[132:135]\n"
"s_add_u32 m0, s70, 40960\n"
"v_mfma_f32_16x16x32_bf16 v[136:139], a[104:107], a[176:179], v[136:139]\n"
"buffer_load_dwordx4 v223, s[44:47], s60 offen offset:0 nt lds\n"
"v_mfma_f32_16x16x32_bf16 v[140:143], a[108:111], a[176:179], v[140:143]\n"
"v_mfma_f32_16x16x32_bf16 v[144:147], a[112:115], a[176:179], v[144:147]\n"
"v_mfma_f32_16x16x32_bf16 v[148:151], a[116:119], a[176:179], v[148:151]\n"
"v_mfma_f32_16x16x32_bf16 v[152:155], a[120:123], a[176:179], v[152:155]\n"
"v_mfma_f32_16x16x32_bf16 v[156:159], a[124:127], a[176:179], v[156:159]\n"
"v_mfma_f32_16x16x32_bf16 v[160:163], a[96:99], a[180:183], v[160:163]\n"
"v_mfma_f32_16x16x32_bf16 v[164:167], a[100:103], a[180:183], v[164:167]\n"
"v_mfma_f32_16x16x32_bf16 v[168:171], a[104:107], a[180:183], v[168:171]\n"
"v_mfma_f32_16x16x32_bf16 v[172:175], a[108:111], a[180:183], v[172:175]\n"
"v_mfma_f32_16x16x32_bf16 v[176:179], a[112:115], a[180:183], v[176:179]\n"
"v_mfma_f32_16x16x32_bf16 v[180:183], a[116:119], a[180:183], v[180:183]\n"
"v_mfma_f32_16x16x32_bf16 v[184:187], a[120:123], a[180:183], v[184:187]\n"
"v_mfma_f32_16x16x32_bf16 v[188:191], a[124:127], a[180:183], v[188:191]\n"
"v_mfma_f32_16x16x32_bf16 v[192:195], a[112:115], a[184:187], v[192:195]\n"
"v_mfma_f32_16x16x32_bf16 v[196:199], a[116:119], a[184:187], v[196:199]\n"
"v_mfma_f32_16x16x32_bf16 v[200:203], a[120:123], a[184:187], v[200:203]\n"
"v_mfma_f32_16x16x32_bf16 v[204:207], a[124:127], a[184:187], v[204:207]\n"
"s_waitcnt lgkmcnt(0)\n"
"s_barrier\n"
"v_mfma_f32_16x16x32_bf16 v[192:195], a[144:147], a[216:219], v[192:195]\n"
"ds_read_b128 a[96:99], v220 offset:0\n"
"v_mfma_f32_16x16x32_bf16 v[196:199], a[148:151], a[216:219], v[196:199]\n"
"ds_read_b128 a[100:103], v220 offset:1024\n"
"v_mfma_f32_16x16x32_bf16 v[200:203], a[152:155], a[216:219], v[200:203]\n"
"s_add_u32 m0, s70, 40960\n"
"v_mfma_f32_16x16x32_bf16 v[204:207], a[156:159], a[216:219], v[204:207]\n"
"buffer_load_dwordx4 v223, s[44:47], s61 offen offset:1024 nt lds\n"
"v_mfma_f32_16x16x32_bf16 v[0:3], a[128:131], a[192:195], v[0:3]\n"
"ds_read_b128 a[104:107], v220 offset:2048\n"
"v_mfma_f32_16x16x32_bf16 v[4:7], a[132:135], a[192:195], v[4:7]\n"
"ds_read_b128 a[108:111], v220 offset:3072\n"
"v_mfma_f32_16x16x32_bf16 v[8:11], a[136:139], a[192:195], v[8:11]\n"
"ds_read_b128 a[112:115], v220 offset:4096\n"
"v_mul_f32 v192, s98, v192\n"
"v_mfma_f32_16x16x32_bf16 v[12:15], a[140:143], a[192:195], v[12:15]\n"
"ds_read_b128 a[116:119], v220 offset:5120\n"
"v_mul_f32 v193, s98, v193\n"
"v_mfma_f32_16x16x32_bf16 v[16:19], a[144:147], a[192:195], v[16:19]\n"
"ds_read_b128 a[120:123], v220 offset:6144\n"
"v_mul_f32 v194, s98, v194\n"
"v_mfma_f32_16x16x32_bf16 v[20:23], a[148:151], a[192:195], v[20:23]\n"
"ds_read_b128 a[124:127], v220 offset:7168\n"
"v_mul_f32 v195, s98, v195\n"
"v_mfma_f32_16x16x32_bf16 v[24:27], a[152:155], a[192:195], v[24:27]\n"
"ds_read_b64_tr_b16 a[160:161], v221 offset:0\n"
"v_mul_f32 v196, s98, v196\n"
"v_mfma_f32_16x16x32_bf16 v[28:31], a[156:159], a[192:195], v[28:31]\n"
"ds_read_b64_tr_b16 a[162:163], v221 offset:4096\n"
"v_mul_f32 v197, s98, v197\n"
"v_mfma_f32_16x16x32_bf16 v[32:35], a[128:131], a[196:199], v[32:35]\n"
"ds_read_b64_tr_b16 a[164:165], v222 offset:0\n"
"v_mul_f32 v198, s98, v198\n"
"v_mfma_f32_16x16x32_bf16 v[36:39], a[132:135], a[196:199], v[36:39]\n"
"ds_read_b64_tr_b16 a[166:167], v222 offset:4096\n"
"v_mul_f32 v199, s98, v199\n"
"v_mfma_f32_16x16x32_bf16 v[40:43], a[136:139], a[196:199], v[40:43]\n"
"ds_read_b64_tr_b16 a[168:169], v221 offset:1024\n"
"v_cvt_pk_bf16_f32 v192, v192, v193\n"
"v_mfma_f32_16x16x32_bf16 v[44:47], a[140:143], a[196:199], v[44:47]\n"
"ds_read_b64_tr_b16 a[170:171], v221 offset:5120\n"
"v_cvt_pk_bf16_f32 v193, v194, v195\n"
"v_mfma_f32_16x16x32_bf16 v[48:51], a[144:147], a[196:199], v[48:51]\n"
"ds_read_b64_tr_b16 a[172:173], v222 offset:1024\n"
"v_cvt_pk_bf16_f32 v194, v196, v197\n"
"v_mfma_f32_16x16x32_bf16 v[52:55], a[148:151], a[196:199], v[52:55]\n"
"ds_read_b64_tr_b16 a[174:175], v222 offset:5120\n"
"v_cvt_pk_bf16_f32 v195, v198, v199\n"
"v_mfma_f32_16x16x32_bf16 v[56:59], a[152:155], a[196:199], v[56:59]\n"
"buffer_load_dwordx4 v223, s[44:47], s62 offen offset:2048 nt lds\n"
"v_mfma_f32_16x16x32_bf16 v[60:63], a[156:159], a[196:199], v[60:63]\n"
"ds_read_b64_tr_b16 a[176:177], v221 offset:2048\n"
"ds_write_b128 v245, v[192:195] offset:2048\n"
"v_mfma_f32_16x16x32_bf16 v[64:67], a[128:131], a[200:203], v[64:67]\n"
"ds_read_b64_tr_b16 a[178:179], v221 offset:6144\n"
"v_mul_f32 v200, s98, v200\n"
"v_mfma_f32_16x16x32_bf16 v[68:71], a[132:135], a[200:203], v[68:71]\n"
"ds_read_b64_tr_b16 a[180:181], v222 offset:2048\n"
"v_mul_f32 v201, s98, v201\n"
"v_mfma_f32_16x16x32_bf16 v[72:75], a[136:139], a[200:203], v[72:75]\n"
"ds_read_b64_tr_b16 a[182:183], v222 offset:6144\n"
"v_mul_f32 v202, s98, v202\n"
"v_mfma_f32_16x16x32_bf16 v[76:79], a[140:143], a[200:203], v[76:79]\n"
"ds_read_b64_tr_b16 a[184:185], v221 offset:3072\n"
"v_mul_f32 v203, s98, v203\n"
"v_mfma_f32_16x16x32_bf16 v[80:83], a[144:147], a[200:203], v[80:83]\n"
"ds_read_b64_tr_b16 a[186:187], v221 offset:7168\n"
"v_mul_f32 v204, s98, v204\n"
"v_mfma_f32_16x16x32_bf16 v[84:87], a[148:151], a[200:203], v[84:87]\n"
"ds_read_b64_tr_b16 a[188:189], v222 offset:3072\n"
"v_mul_f32 v205, s98, v205\n"
"v_mfma_f32_16x16x32_bf16 v[88:91], a[152:155], a[200:203], v[88:91]\n"
"ds_read_b64_tr_b16 a[190:191], v222 offset:7168\n"
"v_mul_f32 v206, s98, v206\n"
"v_mfma_f32_16x16x32_bf16 v[92:95], a[156:159], a[200:203], v[92:95]\n"
"v_mul_f32 v207, s98, v207\n"
"v_cvt_pk_bf16_f32 v200, v200, v201\n"
"v_mfma_f32_16x16x32_bf16 v[96:99], a[128:131], a[204:207], v[96:99]\n"
"v_cvt_pk_bf16_f32 v201, v202, v203\n"
"v_cvt_pk_bf16_f32 v202, v204, v205\n"
"v_mfma_f32_16x16x32_bf16 v[100:103], a[132:135], a[204:207], v[100:103]\n"
"v_cvt_pk_bf16_f32 v203, v206, v207\n"
"ds_write_b128 v245, v[200:203] offset:3072\n"
"v_mfma_f32_16x16x32_bf16 v[104:107], a[136:139], a[204:207], v[104:107]\n"
"v_mul_f32 v0, s98, v0\n"
"v_mul_f32 v1, s98, v1\n"
"v_mfma_f32_16x16x32_bf16 v[108:111], a[140:143], a[204:207], v[108:111]\n"
"v_mul_f32 v2, s98, v2\n"
"v_mul_f32 v3, s98, v3\n"
"v_mfma_f32_16x16x32_bf16 v[112:115], a[144:147], a[204:207], v[112:115]\n"
"v_mul_f32 v4, s98, v4\n"
"v_mul_f32 v5, s98, v5\n"
"v_mfma_f32_16x16x32_bf16 v[116:119], a[148:151], a[204:207], v[116:119]\n"
"buffer_load_dwordx4 v223, s[44:47], 0 offen offset:3072 nt lds\n"
"v_mfma_f32_16x16x32_bf16 v[120:123], a[152:155], a[204:207], v[120:123]\n"
"v_mul_f32 v6, s98, v6\n"
"v_mul_f32 v7, s98, v7\n"
"v_mfma_f32_16x16x32_bf16 v[124:127], a[156:159], a[204:207], v[124:127]\n"
"v_cvt_pk_bf16_f32 v0, v0, v1\n"
"v_cvt_pk_bf16_f32 v1, v2, v3\n"
"v_mfma_f32_16x16x32_bf16 v[128:131], a[128:131], a[208:211], v[128:131]\n"
"v_cvt_pk_bf16_f32 v2, v4, v5\n"
"v_cvt_pk_bf16_f32 v3, v6, v7\n"
"v_mfma_f32_16x16x32_bf16 v[132:135], a[132:135], a[208:211], v[132:135]\n"
"v_accvgpr_write_b32 a0, v0\n"
"v_accvgpr_write_b32 a1, v1\n"
"v_mfma_f32_16x16x32_bf16 v[136:139], a[136:139], a[208:211], v[136:139]\n"
"v_accvgpr_write_b32 a2, v2\n"
"v_accvgpr_write_b32 a3, v3\n"
"v_mfma_f32_16x16x32_bf16 v[140:143], a[140:143], a[208:211], v[140:143]\n"
"v_mul_f32 v8, s98, v8\n"
"v_mul_f32 v9, s98, v9\n"
"v_mfma_f32_16x16x32_bf16 v[144:147], a[144:147], a[208:211], v[144:147]\n"
"v_mul_f32 v10, s98, v10\n"
"v_mul_f32 v11, s98, v11\n"
"v_mfma_f32_16x16x32_bf16 v[148:151], a[148:151], a[208:211], v[148:151]\n"
"v_mul_f32 v12, s98, v12\n"
"v_mul_f32 v13, s98, v13\n"
"v_mfma_f32_16x16x32_bf16 v[152:155], a[152:155], a[208:211], v[152:155]\n"
"v_mul_f32 v14, s98, v14\n"
"v_mul_f32 v15, s98, v15\n"
"v_mfma_f32_16x16x32_bf16 v[156:159], a[156:159], a[208:211], v[156:159]\n"
"v_cvt_pk_bf16_f32 v8, v8, v9\n"
"v_cvt_pk_bf16_f32 v9, v10, v11\n"
"v_mfma_f32_16x16x32_bf16 v[160:163], a[128:131], a[212:215], v[160:163]\n"
"v_cvt_pk_bf16_f32 v10, v12, v13\n"
"v_cvt_pk_bf16_f32 v11, v14, v15\n"
"v_mfma_f32_16x16x32_bf16 v[164:167], a[132:135], a[212:215], v[164:167]\n"
"v_accvgpr_write_b32 a4, v8\n"
"v_accvgpr_write_b32 a5, v9\n"
"v_mfma_f32_16x16x32_bf16 v[168:171], a[136:139], a[212:215], v[168:171]\n"
"v_accvgpr_write_b32 a6, v10\n"
"v_accvgpr_write_b32 a7, v11\n"
"v_mfma_f32_16x16x32_bf16 v[172:175], a[140:143], a[212:215], v[172:175]\n"
"v_mfma_f32_16x16x32_bf16 v[176:179], a[144:147], a[212:215], v[176:179]\n"
"v_add_u32 v223, 0x2000, v223\n"
"v_mfma_f32_16x16x32_bf16 v[180:183], a[148:151], a[212:215], v[180:183]\n"
"v_mfma_f32_16x16x32_bf16 v[184:187], a[152:155], a[212:215], v[184:187]\n"
"v_mfma_f32_16x16x32_bf16 v[188:191], a[156:159], a[212:215], v[188:191]\n"
"s_waitcnt lgkmcnt(0)\n"
"s_barrier\n"
"ds_read_b128 a[128:131], v245 offset:0\n"
"ds_read_b128 a[132:135], v245 offset:1024\n"
"ds_read_b128 a[136:139], v245 offset:2048\n"
"ds_read_b128 a[140:143], v245 offset:3072\n"
"s_add_u32 m0, s70, 49152\n"
"v_pk_mul_f32 v[16:17], v[16:17], s[98:99] op_sel_hi:[1,0]\n"
"buffer_load_dwordx4 v223, s[40:43], s60 offen offset:0 nt lds\n"
"v_pk_mul_f32 v[18:19], v[18:19], s[98:99] op_sel_hi:[1,0]\n"
"v_pk_mul_f32 v[20:21], v[20:21], s[98:99] op_sel_hi:[1,0]\n"
"v_pk_mul_f32 v[22:23], v[22:23], s[98:99] op_sel_hi:[1,0]\n"
"v_cvt_pk_bf16_f32 v16, v16, v17\n"
"v_cvt_pk_bf16_f32 v17, v18, v19\n"
"v_cvt_pk_bf16_f32 v18, v20, v21\n"
"v_cvt_pk_bf16_f32 v19, v22, v23\n"
"v_accvgpr_write_b32 a8, v16\n"
"v_accvgpr_write_b32 a9, v17\n"
"v_accvgpr_write_b32 a10, v18\n"
"v_accvgpr_write_b32 a11, v19\n"
"v_pk_mul_f32 v[24:25], v[24:25], s[98:99] op_sel_hi:[1,0]\n"
"v_pk_mul_f32 v[26:27], v[26:27], s[98:99] op_sel_hi:[1,0]\n"
"v_pk_mul_f32 v[28:29], v[28:29], s[98:99] op_sel_hi:[1,0]\n"
"v_pk_mul_f32 v[30:31], v[30:31], s[98:99] op_sel_hi:[1,0]\n"
"v_cvt_pk_bf16_f32 v24, v24, v25\n"
"v_cvt_pk_bf16_f32 v25, v26, v27\n"
"v_cvt_pk_bf16_f32 v26, v28, v29\n"
"v_cvt_pk_bf16_f32 v27, v30, v31\n"
"v_accvgpr_write_b32 a12, v24\n"
"buffer_load_dwordx4 v223, s[40:43], s61 offen offset:1024 nt lds\n"
"v_accvgpr_write_b32 a13, v25\n"
"v_accvgpr_write_b32 a14, v26\n"
"v_accvgpr_write_b32 a15, v27\n"
"s_waitcnt lgkmcnt(0)\n"
"v_pk_mul_f32 v[32:33], v[32:33], s[98:99] op_sel_hi:[1,0]\n"
"v_pk_mul_f32 v[34:35], v[34:35], s[98:99] op_sel_hi:[1,0]\n"
"v_pk_mul_f32 v[36:37], v[36:37], s[98:99] op_sel_hi:[1,0]\n"
"v_pk_mul_f32 v[38:39], v[38:39], s[98:99] op_sel_hi:[1,0]\n"
"v_mfma_f32_16x16x32_bf16 v[192:195], a[96:99], a[128:131], 0\n"
"v_cvt_pk_bf16_f32 v32, v32, v33\n"
"v_cvt_pk_bf16_f32 v33, v34, v35\n"
"v_cvt_pk_bf16_f32 v34, v36, v37\n"
"v_cvt_pk_bf16_f32 v35, v38, v39\n"
"v_mfma_f32_16x16x32_bf16 v[192:195], a[100:103], a[132:135], v[192:195]\n"
"v_accvgpr_write_b32 a16, v32\n"
"v_accvgpr_write_b32 a17, v33\n"
"v_accvgpr_write_b32 a18, v34\n"
"v_accvgpr_write_b32 a19, v35\n"
"v_mfma_f32_16x16x32_bf16 v[192:195], a[104:107], a[136:139], v[192:195]\n"
"v_pk_mul_f32 v[40:41], v[40:41], s[98:99] op_sel_hi:[1,0]\n"
"v_pk_mul_f32 v[42:43], v[42:43], s[98:99] op_sel_hi:[1,0]\n"
"v_pk_mul_f32 v[44:45], v[44:45], s[98:99] op_sel_hi:[1,0]\n"
"v_pk_mul_f32 v[46:47], v[46:47], s[98:99] op_sel_hi:[1,0]\n"
"v_mfma_f32_16x16x32_bf16 v[192:195], a[108:111], a[140:143], v[192:195]\n"
"v_cvt_pk_bf16_f32 v40, v40, v41\n"
"buffer_load_dwordx4 v223, s[40:43], s62 offen offset:2048 nt lds\n"
"v_cvt_pk_bf16_f32 v41, v42, v43\n"
"v_cvt_pk_bf16_f32 v42, v44, v45\n"
"v_cvt_pk_bf16_f32 v43, v46, v47\n"
"v_accvgpr_write_b32 a20, v40\n"
"v_accvgpr_write_b32 a21, v41\n"
"v_accvgpr_write_b32 a22, v42\n"
"v_accvgpr_write_b32 a23, v43\n"
"v_pk_mul_f32 v[48:49], v[48:49], s[98:99] op_sel_hi:[1,0]\n"
"v_pk_mul_f32 v[50:51], v[50:51], s[98:99] op_sel_hi:[1,0]\n"
"v_pk_mul_f32 v[52:53], v[52:53], s[98:99] op_sel_hi:[1,0]\n"
"v_pk_mul_f32 v[54:55], v[54:55], s[98:99] op_sel_hi:[1,0]\n"
"v_cvt_pk_bf16_f32 v48, v48, v49\n"
"v_cvt_pk_bf16_f32 v49, v50, v51\n"
"v_max3_f32 v192, v192, v193, v194\n"
"v_cvt_pk_bf16_f32 v50, v52, v53\n"
"v_cvt_pk_bf16_f32 v51, v54, v55\n"
"v_max_f32 v192, v192, v195\n"
"v_accvgpr_write_b32 a24, v48\n"
"v_accvgpr_write_b32 a25, v49\n"
"v_mov_b32 v193, v192\n"
"v_accvgpr_write_b32 a26, v50\n"
"v_accvgpr_write_b32 a27, v51\n"
"v_permlane32_swap_b32 v193, v192\n"
"v_pk_mul_f32 v[56:57], v[56:57], s[98:99] op_sel_hi:[1,0]\n"
"buffer_load_dwordx4 v223, s[40:43], 0 offen offset:3072 nt lds\n"
"v_pk_mul_f32 v[58:59], v[58:59], s[98:99] op_sel_hi:[1,0]\n"
"v_max_f32 v192, v192, v193\n"
"v_pk_mul_f32 v[60:61], v[60:61], s[98:99] op_sel_hi:[1,0]\n"
"v_pk_mul_f32 v[62:63], v[62:63], s[98:99] op_sel_hi:[1,0]\n"
"v_mov_b32 v193, v192\n"
"v_cvt_pk_bf16_f32 v56, v56, v57\n"
"v_cvt_pk_bf16_f32 v57, v58, v59\n"
"v_permlane16_swap_b32 v193, v192\n"
"v_cvt_pk_bf16_f32 v58, v60, v61\n"
"v_cvt_pk_bf16_f32 v59, v62, v63\n"
"v_max_f32 v192, v192, v193\n"
"v_accvgpr_write_b32 a28, v56\n"
"v_accvgpr_write_b32 a29, v57\n"
"v_max_f32_dpp v192, v192, v192 row_ror:8 row_mask:0xf bank_mask:0xf\n"
"v_accvgpr_write_b32 a30, v58\n"
"v_accvgpr_write_b32 a31, v59\n"
"v_max_f32_dpp v192, v192, v192 row_ror:4 row_mask:0xf bank_mask:0xf\n"
"v_pk_mul_f32 v[64:65], v[64:65], s[98:99] op_sel_hi:[1,0]\n"
"v_pk_mul_f32 v[66:67], v[66:67], s[98:99] op_sel_hi:[1,0]\n"
"v_max_f32_dpp v192, v192, v192 row_ror:2 row_mask:0xf bank_mask:0xf\n"
"v_pk_mul_f32 v[68:69], v[68:69], s[98:99] op_sel_hi:[1,0]\n"
"v_pk_mul_f32 v[70:71], v[70:71], s[98:99] op_sel_hi:[1,0]\n"
"v_max_f32_dpp v192, v192, v192 row_ror:1 row_mask:0xf bank_mask:0xf\n"
"v_cvt_pk_bf16_f32 v64, v64, v65\n"
"v_cvt_pk_bf16_f32 v65, v66, v67\n"
"v_xor_b32 v216, 0x80000000, v192\n"
"v_cvt_pk_bf16_f32 v66, v68, v69\n"
"v_cvt_pk_bf16_f32 v67, v70, v71\n"
"v_xor_b32 v217, 0x80000000, v192\n"
"v_accvgpr_write_b32 a32, v64\n"
"s_add_u32 m0, s70, 57344\n"
"v_accvgpr_write_b32 a33, v65\n"
"v_xor_b32 v218, 0x80000000, v192\n"
"buffer_load_dwordx4 v223, s[44:47], s60 offen offset:0 nt lds\n"
"v_accvgpr_write_b32 a34, v66\n"
"v_accvgpr_write_b32 a35, v67\n"
"v_xor_b32 v219, 0x80000000, v192\n"
"v_pk_mul_f32 v[72:73], v[72:73], s[98:99] op_sel_hi:[1,0]\n"
"v_pk_mul_f32 v[74:75], v[74:75], s[98:99] op_sel_hi:[1,0]\n"
"v_pk_mul_f32 v[76:77], v[76:77], s[98:99] op_sel_hi:[1,0]\n"
"v_pk_mul_f32 v[78:79], v[78:79], s[98:99] op_sel_hi:[1,0]\n"
"v_cvt_pk_bf16_f32 v72, v72, v73\n"
"v_cvt_pk_bf16_f32 v73, v74, v75\n"
"v_cvt_pk_bf16_f32 v74, v76, v77\n"
"v_cvt_pk_bf16_f32 v75, v78, v79\n"
"v_accvgpr_write_b32 a36, v72\n"
"v_accvgpr_write_b32 a37, v73\n"
"v_accvgpr_write_b32 a38, v74\n"
"v_accvgpr_write_b32 a39, v75\n"
"v_pk_mul_f32 v[80:81], v[80:81], s[98:99] op_sel_hi:[1,0]\n"
"v_pk_mul_f32 v[82:83], v[82:83], s[98:99] op_sel_hi:[1,0]\n"
"v_pk_mul_f32 v[84:85], v[84:85], s[98:99] op_sel_hi:[1,0]\n"
"v_pk_mul_f32 v[86:87], v[86:87], s[98:99] op_sel_hi:[1,0]\n"
"v_cvt_pk_bf16_f32 v80, v80, v81\n"
"v_cvt_pk_bf16_f32 v81, v82, v83\n"
"buffer_load_dwordx4 v223, s[44:47], s61 offen offset:1024 nt lds\n"
"v_cvt_pk_bf16_f32 v82, v84, v85\n"
"v_cvt_pk_bf16_f32 v83, v86, v87\n"
"v_accvgpr_write_b32 a40, v80\n"
"v_accvgpr_write_b32 a41, v81\n"
"v_accvgpr_write_b32 a42, v82\n"
"v_accvgpr_write_b32 a43, v83\n"
"v_pk_mul_f32 v[88:89], v[88:89], s[98:99] op_sel_hi:[1,0]\n"
"v_pk_mul_f32 v[90:91], v[90:91], s[98:99] op_sel_hi:[1,0]\n"
"v_pk_mul_f32 v[92:93], v[92:93], s[98:99] op_sel_hi:[1,0]\n"
"v_pk_mul_f32 v[94:95], v[94:95], s[98:99] op_sel_hi:[1,0]\n"
"v_cvt_pk_bf16_f32 v88, v88, v89\n"
"v_cvt_pk_bf16_f32 v89, v90, v91\n"
"v_cvt_pk_bf16_f32 v90, v92, v93\n"
"v_cvt_pk_bf16_f32 v91, v94, v95\n"
"v_accvgpr_write_b32 a44, v88\n"
"v_accvgpr_write_b32 a45, v89\n"
"v_accvgpr_write_b32 a46, v90\n"
"v_accvgpr_write_b32 a47, v91\n"
"v_pk_mul_f32 v[96:97], v[96:97], s[98:99] op_sel_hi:[1,0]\n"
"v_pk_mul_f32 v[98:99], v[98:99], s[98:99] op_sel_hi:[1,0]\n"
"buffer_load_dwordx4 v223, s[44:47], s62 offen offset:2048 nt lds\n"
"v_pk_mul_f32 v[100:101], v[100:101], s[98:99] op_sel_hi:[1,0]\n"
"v_pk_mul_f32 v[102:103], v[102:103], s[98:99] op_sel_hi:[1,0]\n"
"v_cvt_pk_bf16_f32 v96, v96, v97\n"
"v_cvt_pk_bf16_f32 v97, v98, v99\n"
"v_cvt_pk_bf16_f32 v98, v100, v101\n"
"v_cvt_pk_bf16_f32 v99, v102, v103\n"
"v_accvgpr_write_b32 a48, v96\n"
"v_accvgpr_write_b32 a49, v97\n"
"v_accvgpr_write_b32 a50, v98\n"
"v_accvgpr_write_b32 a51, v99\n"
"v_pk_mul_f32 v[104:105], v[104:105], s[98:99] op_sel_hi:[1,0]\n"
"v_pk_mul_f32 v[106:107], v[106:107], s[98:99] op_sel_hi:[1,0]\n"
"v_pk_mul_f32 v[108:109], v[108:109], s[98:99] op_sel_hi:[1,0]\n"
"v_pk_mul_f32 v[110:111], v[110:111], s[98:99] op_sel_hi:[1,0]\n"
"v_cvt_pk_bf16_f32 v104, v104, v105\n"
"v_cvt_pk_bf16_f32 v105, v106, v107\n"
"v_cvt_pk_bf16_f32 v106, v108, v109\n"
"v_cvt_pk_bf16_f32 v107, v110, v111\n"
"v_accvgpr_write_b32 a52, v104\n"
"v_accvgpr_write_b32 a53, v105\n"
"buffer_load_dwordx4 v223, s[44:47], 0 offen offset:3072 nt lds\n"
"v_accvgpr_write_b32 a54, v106\n"
"v_accvgpr_write_b32 a55, v107\n"
"v_pk_mul_f32 v[112:113], v[112:113], s[98:99] op_sel_hi:[1,0]\n"
"v_pk_mul_f32 v[114:115], v[114:115], s[98:99] op_sel_hi:[1,0]\n"
"v_pk_mul_f32 v[116:117], v[116:117], s[98:99] op_sel_hi:[1,0]\n"
"v_pk_mul_f32 v[118:119], v[118:119], s[98:99] op_sel_hi:[1,0]\n"
"v_cvt_pk_bf16_f32 v112, v112, v113\n"
"v_cvt_pk_bf16_f32 v113, v114, v115\n"
"v_cvt_pk_bf16_f32 v114, v116, v117\n"
"v_cvt_pk_bf16_f32 v115, v118, v119\n"
"v_accvgpr_write_b32 a56, v112\n"
"v_accvgpr_write_b32 a57, v113\n"
"v_accvgpr_write_b32 a58, v114\n"
"v_accvgpr_write_b32 a59, v115\n"
"v_pk_mul_f32 v[120:121], v[120:121], s[98:99] op_sel_hi:[1,0]\n"
"v_pk_mul_f32 v[122:123], v[122:123], s[98:99] op_sel_hi:[1,0]\n"
"v_pk_mul_f32 v[124:125], v[124:125], s[98:99] op_sel_hi:[1,0]\n"
"v_pk_mul_f32 v[126:127], v[126:127], s[98:99] op_sel_hi:[1,0]\n"
"v_cvt_pk_bf16_f32 v120, v120, v121\n"
"v_cvt_pk_bf16_f32 v121, v122, v123\n"
"v_add_u32 v223, 0x2000, v223\n"
"v_cvt_pk_bf16_f32 v122, v124, v125\n"
"v_cvt_pk_bf16_f32 v123, v126, v127\n"
"v_accvgpr_write_b32 a60, v120\n"
"v_accvgpr_write_b32 a61, v121\n"
"v_accvgpr_write_b32 a62, v122\n"
"v_accvgpr_write_b32 a63, v123\n"
"v_pk_mul_f32 v[128:129], v[128:129], s[98:99] op_sel_hi:[1,0]\n"
"v_pk_mul_f32 v[130:131], v[130:131], s[98:99] op_sel_hi:[1,0]\n"
"v_pk_mul_f32 v[132:133], v[132:133], s[98:99] op_sel_hi:[1,0]\n"
"v_pk_mul_f32 v[134:135], v[134:135], s[98:99] op_sel_hi:[1,0]\n"
"v_cvt_pk_bf16_f32 v128, v128, v129\n"
"v_cvt_pk_bf16_f32 v129, v130, v131\n"
"v_cvt_pk_bf16_f32 v130, v132, v133\n"
"v_cvt_pk_bf16_f32 v131, v134, v135\n"
"v_accvgpr_write_b32 a64, v128\n"
"v_accvgpr_write_b32 a65, v129\n"
"v_accvgpr_write_b32 a66, v130\n"
"v_accvgpr_write_b32 a67, v131\n"
"v_pk_mul_f32 v[136:137], v[136:137], s[98:99] op_sel_hi:[1,0]\n"
"v_pk_mul_f32 v[138:139], v[138:139], s[98:99] op_sel_hi:[1,0]\n"
"v_pk_mul_f32 v[140:141], v[140:141], s[98:99] op_sel_hi:[1,0]\n"
"v_pk_mul_f32 v[142:143], v[142:143], s[98:99] op_sel_hi:[1,0]\n"
"v_cvt_pk_bf16_f32 v136, v136, v137\n"
"v_cvt_pk_bf16_f32 v137, v138, v139\n"
"v_cvt_pk_bf16_f32 v138, v140, v141\n"
"v_cvt_pk_bf16_f32 v139, v142, v143\n"
"v_accvgpr_write_b32 a68, v136\n"
"v_accvgpr_write_b32 a69, v137\n"
"v_accvgpr_write_b32 a70, v138\n"
"v_accvgpr_write_b32 a71, v139\n"
"v_pk_mul_f32 v[144:145], v[144:145], s[98:99] op_sel_hi:[1,0]\n"
"v_pk_mul_f32 v[146:147], v[146:147], s[98:99] op_sel_hi:[1,0]\n"
"v_pk_mul_f32 v[148:149], v[148:149], s[98:99] op_sel_hi:[1,0]\n"
"v_pk_mul_f32 v[150:151], v[150:151], s[98:99] op_sel_hi:[1,0]\n"
"v_cvt_pk_bf16_f32 v144, v144, v145\n"
"v_cvt_pk_bf16_f32 v145, v146, v147\n"
"v_cvt_pk_bf16_f32 v146, v148, v149\n"
"v_cvt_pk_bf16_f32 v147, v150, v151\n"
"v_accvgpr_write_b32 a72, v144\n"
"v_accvgpr_write_b32 a73, v145\n"
"v_accvgpr_write_b32 a74, v146\n"
"v_accvgpr_write_b32 a75, v147\n"
"v_pk_mul_f32 v[152:153], v[152:153], s[98:99] op_sel_hi:[1,0]\n"
"v_pk_mul_f32 v[154:155], v[154:155], s[98:99] op_sel_hi:[1,0]\n"
"v_pk_mul_f32 v[156:157], v[156:157], s[98:99] op_sel_hi:[1,0]\n"
"v_pk_mul_f32 v[158:159], v[158:159], s[98:99] op_sel_hi:[1,0]\n"
"v_cvt_pk_bf16_f32 v152, v152, v153\n"
"v_cvt_pk_bf16_f32 v153, v154, v155\n"
"v_cvt_pk_bf16_f32 v154, v156, v157\n"
"v_cvt_pk_bf16_f32 v155, v158, v159\n"
"v_accvgpr_write_b32 a76, v152\n"
"v_accvgpr_write_b32 a77, v153\n"
"v_accvgpr_write_b32 a78, v154\n"
"v_accvgpr_write_b32 a79, v155\n"
"v_pk_mul_f32 v[160:161], v[160:161], s[98:99] op_sel_hi:[1,0]\n"
"v_pk_mul_f32 v[162:163], v[162:163], s[98:99] op_sel_hi:[1,0]\n"
"v_pk_mul_f32 v[164:165], v[164:165], s[98:99] op_sel_hi:[1,0]\n"
"v_pk_mul_f32 v[166:167], v[166:167], s[98:99] op_sel_hi:[1,0]\n"
"v_cvt_pk_bf16_f32 v160, v160, v161\n"
"v_cvt_pk_bf16_f32 v161, v162, v163\n"
"v_cvt_pk_bf16_f32 v162, v164, v165\n"
"v_cvt_pk_bf16_f32 v163, v166, v167\n"
"v_accvgpr_write_b32 a80, v160\n"
"v_accvgpr_write_b32 a81, v161\n"
"v_accvgpr_write_b32 a82, v162\n"
"v_accvgpr_write_b32 a83, v163\n"
"v_pk_mul_f32 v[168:169], v[168:169], s[98:99] op_sel_hi:[1,0]\n"
"v_pk_mul_f32 v[170:171], v[170:171], s[98:99] op_sel_hi:[1,0]\n"
"v_pk_mul_f32 v[172:173], v[172:173], s[98:99] op_sel_hi:[1,0]\n"
"v_pk_mul_f32 v[174:175], v[174:175], s[98:99] op_sel_hi:[1,0]\n"
"v_cvt_pk_bf16_f32 v168, v168, v169\n"
"v_cvt_pk_bf16_f32 v169, v170, v171\n"
"v_cvt_pk_bf16_f32 v170, v172, v173\n"
"v_cvt_pk_bf16_f32 v171, v174, v175\n"
"v_accvgpr_write_b32 a84, v168\n"
"v_accvgpr_write_b32 a85, v169\n"
"v_accvgpr_write_b32 a86, v170\n"
"v_accvgpr_write_b32 a87, v171\n"
"v_pk_mul_f32 v[176:177], v[176:177], s[98:99] op_sel_hi:[1,0]\n"
"v_pk_mul_f32 v[178:179], v[178:179], s[98:99] op_sel_hi:[1,0]\n"
"v_pk_mul_f32 v[180:181], v[180:181], s[98:99] op_sel_hi:[1,0]\n"
"v_pk_mul_f32 v[182:183], v[182:183], s[98:99] op_sel_hi:[1,0]\n"
"v_cvt_pk_bf16_f32 v176, v176, v177\n"
"v_cvt_pk_bf16_f32 v177, v178, v179\n"
"v_cvt_pk_bf16_f32 v178, v180, v181\n"
"v_cvt_pk_bf16_f32 v179, v182, v183\n"
"v_accvgpr_write_b32 a88, v176\n"
"v_accvgpr_write_b32 a89, v177\n"
"v_accvgpr_write_b32 a90, v178\n"
"v_accvgpr_write_b32 a91, v179\n"
"v_pk_mul_f32 v[184:185], v[184:185], s[98:99] op_sel_hi:[1,0]\n"
"v_pk_mul_f32 v[186:187], v[186:187], s[98:99] op_sel_hi:[1,0]\n"
"v_pk_mul_f32 v[188:189], v[188:189], s[98:99] op_sel_hi:[1,0]\n"
"v_pk_mul_f32 v[190:191], v[190:191], s[98:99] op_sel_hi:[1,0]\n"
"v_cvt_pk_bf16_f32 v184, v184, v185\n"
"v_cvt_pk_bf16_f32 v185, v186, v187\n"
"v_cvt_pk_bf16_f32 v186, v188, v189\n"
"v_cvt_pk_bf16_f32 v187, v190, v191\n"
"v_accvgpr_write_b32 a92, v184\n"
"v_accvgpr_write_b32 a93, v185\n"
"v_accvgpr_write_b32 a94, v186\n"
"v_accvgpr_write_b32 a95, v187\n"
"s_mov_b32 s75, 6\n"
"s_waitcnt vmcnt(16)\n"
"s_barrier\n"
"v_mfma_f32_16x16x32_bf16 v[192:195], a[96:99], a[0:3], v[216:219]\n"
"ds_read_b128 a[144:147], v220 offset:20480\n"
"v_mfma_f32_16x16x32_bf16 v[192:195], a[100:103], a[4:7], v[192:195]\n"
"ds_read_b128 a[148:151], v220 offset:21504\n"
"v_mfma_f32_16x16x32_bf16 v[192:195], a[104:107], a[8:11], v[192:195]\n"
"ds_read_b128 a[152:155], v220 offset:22528\n"
"v_mfma_f32_16x16x32_bf16 v[192:195], a[108:111], a[12:15], v[192:195]\n"
"ds_read_b128 a[156:159], v220 offset:23552\n"
"v_mfma_f32_16x16x32_bf16 v[196:199], a[112:115], a[0:3], v[216:219]\n"
"ds_read_b128 a[128:131], v220 offset:16384\n"
"v_mfma_f32_16x16x32_bf16 v[196:199], a[116:119], a[4:7], v[196:199]\n"
"ds_read_b128 a[132:135], v220 offset:17408\n"
"v_mfma_f32_16x16x32_bf16 v[196:199], a[120:123], a[8:11], v[196:199]\n"
"ds_read_b128 a[136:139], v220 offset:18432\n"
"v_mfma_f32_16x16x32_bf16 v[196:199], a[124:127], a[12:15], v[196:199]\n"
"ds_read_b128 a[140:143], v220 offset:19456\n"
"s_nop 4\n"
"v_exp_f32 v192, v192\n"
"v_exp_f32 v193, v193\n"
"v_exp_f32 v194, v194\n"
"v_exp_f32 v195, v195\n"
"v_exp_f32 v196, v196\n"
"v_exp_f32 v197, v197\n"
"v_exp_f32 v198, v198\n"
"v_mfma_f32_16x16x32_bf16 v[200:203], a[96:99], a[16:19], v[216:219]\n"
"v_exp_f32 v199, v199\n"
"v_mfma_f32_16x16x32_bf16 v[200:203], a[100:103], a[20:23], v[200:203]\n"
"v_cvt_pk_bf16_f32 v208, v192, v193\n"
"v_cvt_pk_bf16_f32 v209, v194, v195\n"
"v_mfma_f32_16x16x32_bf16 v[200:203], a[104:107], a[24:27], v[200:203]\n"
"v_cvt_pk_bf16_f32 v210, v196, v197\n"
"v_cvt_pk_bf16_f32 v211, v198, v199\n"
"v_mfma_f32_16x16x32_bf16 v[200:203], a[108:111], a[28:31], v[200:203]\n"
"ds_read_b64_tr_b16 a[192:193], v221 offset:16384\n"
"ds_read_b64_tr_b16 a[194:195], v221 offset:20480\n"
"v_mfma_f32_16x16x32_bf16 v[204:207], a[112:115], a[16:19], v[216:219]\n"
"ds_read_b64_tr_b16 a[196:197], v222 offset:16384\n"
"ds_read_b64_tr_b16 a[198:199], v222 offset:20480\n"
"v_mfma_f32_16x16x32_bf16 v[204:207], a[116:119], a[20:23], v[204:207]\n"
"ds_read_b64_tr_b16 a[200:201], v221 offset:17408\n"
"ds_read_b64_tr_b16 a[202:203], v221 offset:21504\n"
"v_mfma_f32_16x16x32_bf16 v[204:207], a[120:123], a[24:27], v[204:207]\n"
"ds_read_b64_tr_b16 a[204:205], v222 offset:17408\n"
"ds_read_b64_tr_b16 a[206:207], v222 offset:21504\n"
"v_mfma_f32_16x16x32_bf16 v[204:207], a[124:127], a[28:31], v[204:207]\n"
"ds_read_b64_tr_b16 a[208:209], v221 offset:18432\n"
"ds_read_b64_tr_b16 a[210:211], v221 offset:22528\n"
"v_mfma_f32_16x16x32_bf16 v[0:3], a[160:163], v[208:211], 0\n"
"ds_read_b64_tr_b16 a[212:213], v222 offset:18432\n"
"ds_read_b64_tr_b16 a[214:215], v222 offset:22528\n"
"v_mfma_f32_16x16x32_bf16 v[4:7], a[164:167], v[208:211], 0\n"
"v_exp_f32 v200, v200\n"
"v_mfma_f32_16x16x32_bf16 v[8:11], a[168:171], v[208:211], 0\n"
"v_exp_f32 v201, v201\n"
"v_mfma_f32_16x16x32_bf16 v[12:15], a[172:175], v[208:211], 0\n"
"v_exp_f32 v202, v202\n"
"v_mfma_f32_16x16x32_bf16 v[16:19], a[176:179], v[208:211], 0\n"
"v_exp_f32 v203, v203\n"
"v_mfma_f32_16x16x32_bf16 v[20:23], a[180:183], v[208:211], 0\n"
"v_exp_f32 v204, v204\n"
"v_mfma_f32_16x16x32_bf16 v[24:27], a[184:187], v[208:211], 0\n"
"v_exp_f32 v205, v205\n"
"v_mfma_f32_16x16x32_bf16 v[28:31], a[188:191], v[208:211], 0\n"
"v_exp_f32 v206, v206\n"
"v_mfma_f32_16x16x32_bf16 v[192:195], a[96:99], a[32:35], v[216:219]\n"
"v_exp_f32 v207, v207\n"
"v_mfma_f32_16x16x32_bf16 v[192:195], a[100:103], a[36:39], v[192:195]\n"
"v_cvt_pk_bf16_f32 v212, v200, v201\n"
"v_cvt_pk_bf16_f32 v213, v202, v203\n"
"v_mfma_f32_16x16x32_bf16 v[192:195], a[104:107], a[40:43], v[192:195]\n"
"v_cvt_pk_bf16_f32 v214, v204, v205\n"
"v_cvt_pk_bf16_f32 v215, v206, v207\n"
"v_mfma_f32_16x16x32_bf16 v[192:195], a[108:111], a[44:47], v[192:195]\n"
"ds_read_b64_tr_b16 a[216:217], v221 offset:19456\n"
"ds_read_b64_tr_b16 a[218:219], v221 offset:23552\n"
"v_mfma_f32_16x16x32_bf16 v[196:199], a[112:115], a[32:35], v[216:219]\n"
"ds_read_b64_tr_b16 a[220:221], v222 offset:19456\n"
"ds_read_b64_tr_b16 a[222:223], v222 offset:23552\n"
"v_mfma_f32_16x16x32_bf16 v[196:199], a[116:119], a[36:39], v[196:199]\n"
"v_mfma_f32_16x16x32_bf16 v[196:199], a[120:123], a[40:43], v[196:199]\n"
"v_mfma_f32_16x16x32_bf16 v[196:199], a[124:127], a[44:47], v[196:199]\n"
"v_smfmac_f32_16x16x64_bf16 v[224:227], v[240:243], v[208:215], v244\n"
"v_mfma_f32_16x16x32_bf16 v[32:35], a[160:163], v[212:215], 0\n"
"v_exp_f32 v192, v192\n"
"v_mfma_f32_16x16x32_bf16 v[36:39], a[164:167], v[212:215], 0\n"
"v_exp_f32 v193, v193\n"
"v_mfma_f32_16x16x32_bf16 v[40:43], a[168:171], v[212:215], 0\n"
"v_exp_f32 v194, v194\n"
"v_mfma_f32_16x16x32_bf16 v[44:47], a[172:175], v[212:215], 0\n"
"v_exp_f32 v195, v195\n"
"v_mfma_f32_16x16x32_bf16 v[48:51], a[176:179], v[212:215], 0\n"
"v_exp_f32 v196, v196\n"
"v_mfma_f32_16x16x32_bf16 v[52:55], a[180:183], v[212:215], 0\n"
"v_exp_f32 v197, v197\n"
"v_mfma_f32_16x16x32_bf16 v[56:59], a[184:187], v[212:215], 0\n"
"v_exp_f32 v198, v198\n"
"v_mfma_f32_16x16x32_bf16 v[60:63], a[188:191], v[212:215], 0\n"
"v_exp_f32 v199, v199\n"
"v_mfma_f32_16x16x32_bf16 v[200:203], a[96:99], a[48:51], v[216:219]\n"
"v_cvt_pk_bf16_f32 v208, v192, v193\n"
"v_cvt_pk_bf16_f32 v209, v194, v195\n"
"v_mfma_f32_16x16x32_bf16 v[200:203], a[100:103], a[52:55], v[200:203]\n"
"v_cvt_pk_bf16_f32 v210, v196, v197\n"
"v_cvt_pk_bf16_f32 v211, v198, v199\n"
"v_mfma_f32_16x16x32_bf16 v[200:203], a[104:107], a[56:59], v[200:203]\n"
"v_mfma_f32_16x16x32_bf16 v[200:203], a[108:111], a[60:63], v[200:203]\n"
"v_mfma_f32_16x16x32_bf16 v[204:207], a[112:115], a[48:51], v[216:219]\n"
"v_mfma_f32_16x16x32_bf16 v[204:207], a[116:119], a[52:55], v[204:207]\n"
"v_mfma_f32_16x16x32_bf16 v[204:207], a[120:123], a[56:59], v[204:207]\n"
"v_mfma_f32_16x16x32_bf16 v[204:207], a[124:127], a[60:63], v[204:207]\n"
"v_mfma_f32_16x16x32_bf16 v[64:67], a[160:163], v[208:211], 0\n"
"v_mfma_f32_16x16x32_bf16 v[68:71], a[164:167], v[208:211], 0\n"
"v_exp_f32 v200, v200\n"
"v_mfma_f32_16x16x32_bf16 v[72:75], a[168:171], v[208:211], 0\n"
"v_exp_f32 v201, v201\n"
"v_mfma_f32_16x16x32_bf16 v[76:79], a[172:175], v[208:211], 0\n"
"v_exp_f32 v202, v202\n"
"v_mfma_f32_16x16x32_bf16 v[80:83], a[176:179], v[208:211], 0\n"
"v_exp_f32 v203, v203\n"
"v_mfma_f32_16x16x32_bf16 v[84:87], a[180:183], v[208:211], 0\n"
"v_exp_f32 v204, v204\n"
"v_mfma_f32_16x16x32_bf16 v[88:91], a[184:187], v[208:211], 0\n"
"v_exp_f32 v205, v205\n"
"v_mfma_f32_16x16x32_bf16 v[92:95], a[188:191], v[208:211], 0\n"
"v_exp_f32 v206, v206\n"
"v_mfma_f32_16x16x32_bf16 v[192:195], a[96:99], a[64:67], v[216:219]\n"
"v_exp_f32 v207, v207\n"
"v_mfma_f32_16x16x32_bf16 v[192:195], a[100:103], a[68:71], v[192:195]\n"
"v_cvt_pk_bf16_f32 v212, v200, v201\n"
"v_cvt_pk_bf16_f32 v213, v202, v203\n"
"v_mfma_f32_16x16x32_bf16 v[192:195], a[104:107], a[72:75], v[192:195]\n"
"v_cvt_pk_bf16_f32 v214, v204, v205\n"
"v_cvt_pk_bf16_f32 v215, v206, v207\n"
"v_mfma_f32_16x16x32_bf16 v[192:195], a[108:111], a[76:79], v[192:195]\n"
"v_mfma_f32_16x16x32_bf16 v[196:199], a[112:115], a[64:67], v[216:219]\n"
"v_mfma_f32_16x16x32_bf16 v[196:199], a[116:119], a[68:71], v[196:199]\n"
"v_mfma_f32_16x16x32_bf16 v[196:199], a[120:123], a[72:75], v[196:199]\n"
"v_mfma_f32_16x16x32_bf16 v[196:199], a[124:127], a[76:79], v[196:199]\n"
"v_smfmac_f32_16x16x64_bf16 v[228:231], v[240:243], v[208:215], v244\n"
"v_mfma_f32_16x16x32_bf16 v[96:99], a[160:163], v[212:215], 0\n"
"v_exp_f32 v192, v192\n"
"v_mfma_f32_16x16x32_bf16 v[100:103], a[164:167], v[212:215], 0\n"
"v_exp_f32 v193, v193\n"
"v_mfma_f32_16x16x32_bf16 v[104:107], a[168:171], v[212:215], 0\n"
"v_exp_f32 v194, v194\n"
"v_mfma_f32_16x16x32_bf16 v[108:111], a[172:175], v[212:215], 0\n"
"v_exp_f32 v195, v195\n"
"v_mfma_f32_16x16x32_bf16 v[112:115], a[176:179], v[212:215], 0\n"
"v_exp_f32 v196, v196\n"
"v_mfma_f32_16x16x32_bf16 v[116:119], a[180:183], v[212:215], 0\n"
"v_exp_f32 v197, v197\n"
"v_mfma_f32_16x16x32_bf16 v[120:123], a[184:187], v[212:215], 0\n"
"v_exp_f32 v198, v198\n"
"v_mfma_f32_16x16x32_bf16 v[124:127], a[188:191], v[212:215], 0\n"
"v_exp_f32 v199, v199\n"
"v_mfma_f32_16x16x32_bf16 v[200:203], a[96:99], a[80:83], v[216:219]\n"
"v_cvt_pk_bf16_f32 v208, v192, v193\n"
"v_cvt_pk_bf16_f32 v209, v194, v195\n"
"v_mfma_f32_16x16x32_bf16 v[200:203], a[100:103], a[84:87], v[200:203]\n"
"v_cvt_pk_bf16_f32 v210, v196, v197\n"
"v_cvt_pk_bf16_f32 v211, v198, v199\n"
"v_mfma_f32_16x16x32_bf16 v[200:203], a[104:107], a[88:91], v[200:203]\n"
"v_mfma_f32_16x16x32_bf16 v[200:203], a[108:111], a[92:95], v[200:203]\n"
"v_mfma_f32_16x16x32_bf16 v[204:207], a[112:115], a[80:83], v[216:219]\n"
"v_mfma_f32_16x16x32_bf16 v[204:207], a[116:119], a[84:87], v[204:207]\n"
"v_mfma_f32_16x16x32_bf16 v[204:207], a[120:123], a[88:91], v[204:207]\n"
"v_mfma_f32_16x16x32_bf16 v[204:207], a[124:127], a[92:95], v[204:207]\n"
"ds_read_b128 a[96:99], v245 offset:0\n"
"v_mfma_f32_16x16x32_bf16 v[128:131], a[160:163], v[208:211], 0\n"
"ds_read_b128 a[100:103], v245 offset:1024\n"
"v_mfma_f32_16x16x32_bf16 v[132:135], a[164:167], v[208:211], 0\n"
"v_exp_f32 v200, v200\n"
"v_mfma_f32_16x16x32_bf16 v[136:139], a[168:171], v[208:211], 0\n"
"v_exp_f32 v201, v201\n"
"v_mfma_f32_16x16x32_bf16 v[140:143], a[172:175], v[208:211], 0\n"
"v_exp_f32 v202, v202\n"
"v_mfma_f32_16x16x32_bf16 v[144:147], a[176:179], v[208:211], 0\n"
"v_exp_f32 v203, v203\n"
"v_mfma_f32_16x16x32_bf16 v[148:151], a[180:183], v[208:211], 0\n"
"v_exp_f32 v204, v204\n"
"v_mfma_f32_16x16x32_bf16 v[152:155], a[184:187], v[208:211], 0\n"
"v_exp_f32 v205, v205\n"
"v_mfma_f32_16x16x32_bf16 v[156:159], a[188:191], v[208:211], 0\n"
"v_exp_f32 v206, v206\n"
"s_waitcnt lgkmcnt(2)\n"
"v_mfma_f32_16x16x32_bf16 v[192:195], a[128:131], a[0:3], v[216:219]\n"
"v_exp_f32 v207, v207\n"
"v_mfma_f32_16x16x32_bf16 v[192:195], a[132:135], a[4:7], v[192:195]\n"
"v_cvt_pk_bf16_f32 v212, v200, v201\n"
"v_cvt_pk_bf16_f32 v213, v202, v203\n"
"v_mfma_f32_16x16x32_bf16 v[192:195], a[136:139], a[8:11], v[192:195]\n"
"v_cvt_pk_bf16_f32 v214, v204, v205\n"
"v_cvt_pk_bf16_f32 v215, v206, v207\n"
"v_mfma_f32_16x16x32_bf16 v[192:195], a[140:143], a[12:15], v[192:195]\n"
"ds_read_b128 a[104:107], v245 offset:2048\n"
"v_mfma_f32_16x16x32_bf16 v[196:199], a[144:147], a[0:3], v[216:219]\n"
"ds_read_b128 a[108:111], v245 offset:3072\n"
"v_mfma_f32_16x16x32_bf16 v[196:199], a[148:151], a[4:7], v[196:199]\n"
"v_mfma_f32_16x16x32_bf16 v[196:199], a[152:155], a[8:11], v[196:199]\n"
"v_mfma_f32_16x16x32_bf16 v[196:199], a[156:159], a[12:15], v[196:199]\n"
"s_waitcnt vmcnt(8) lgkmcnt(0)\n"
"s_barrier\n"
"s_sleep 5\n"
"v_smfmac_f32_16x16x64_bf16 v[232:235], v[240:243], v[208:215], v244\n"
"s_add_u32 m0, s70, 0\n"
"v_mfma_f32_16x16x32_bf16 v[160:163], a[160:163], v[212:215], 0\n"
"v_exp_f32 v192, v192\n"
"v_mfma_f32_16x16x32_bf16 v[164:167], a[164:167], v[212:215], 0\n"
"v_exp_f32 v193, v193\n"
"v_mfma_f32_16x16x32_bf16 v[168:171], a[168:171], v[212:215], 0\n"
"v_exp_f32 v194, v194\n"
"v_mfma_f32_16x16x32_bf16 v[172:175], a[172:175], v[212:215], 0\n"
"v_exp_f32 v195, v195\n"
"v_mfma_f32_16x16x32_bf16 v[176:179], a[176:179], v[212:215], 0\n"
"v_exp_f32 v196, v196\n"
"v_mfma_f32_16x16x32_bf16 v[180:183], a[180:183], v[212:215], 0\n"
"v_exp_f32 v197, v197\n"
"v_mfma_f32_16x16x32_bf16 v[184:187], a[184:187], v[212:215], 0\n"
"v_exp_f32 v198, v198\n"
"v_mfma_f32_16x16x32_bf16 v[188:191], a[188:191], v[212:215], 0\n"
"v_exp_f32 v199, v199\n"
"v_mfma_f32_16x16x32_bf16 v[200:203], a[128:131], a[16:19], v[216:219]\n"
"v_cvt_pk_bf16_f32 v208, v192, v193\n"
"v_cvt_pk_bf16_f32 v209, v194, v195\n"
"v_mfma_f32_16x16x32_bf16 v[200:203], a[132:135], a[20:23], v[200:203]\n"
"v_cvt_pk_bf16_f32 v210, v196, v197\n"
"v_cvt_pk_bf16_f32 v211, v198, v199\n"
"v_mfma_f32_16x16x32_bf16 v[200:203], a[136:139], a[24:27], v[200:203]\n"
"buffer_load_dwordx4 v223, s[40:43], s60 offen offset:0 nt lds\n"
"v_mfma_f32_16x16x32_bf16 v[200:203], a[140:143], a[28:31], v[200:203]\n"
"ds_read_b128 a[112:115], v220 offset:36864\n"
"v_mfma_f32_16x16x32_bf16 v[204:207], a[144:147], a[16:19], v[216:219]\n"
"ds_read_b128 a[116:119], v220 offset:37888\n"
"v_mfma_f32_16x16x32_bf16 v[204:207], a[148:151], a[20:23], v[204:207]\n"
"ds_read_b128 a[120:123], v220 offset:38912\n"
"v_mfma_f32_16x16x32_bf16 v[204:207], a[152:155], a[24:27], v[204:207]\n"
"ds_read_b128 a[124:127], v220 offset:39936\n"
"ds_read_b64_tr_b16 a[160:161], v221 offset:32768\n"
"v_mfma_f32_16x16x32_bf16 v[204:207], a[156:159], a[28:31], v[204:207]\n"
"buffer_load_dwordx4 v223, s[40:43], s61 offen offset:1024 nt lds\n"
"v_mfma_f32_16x16x32_bf16 v[0:3], a[192:195], v[208:211], v[0:3]\n"
"ds_read_b64_tr_b16 a[162:163], v221 offset:36864\n"
"ds_read_b64_tr_b16 a[164:165], v222 offset:32768\n"
"v_mfma_f32_16x16x32_bf16 v[4:7], a[196:199], v[208:211], v[4:7]\n"
"v_exp_f32 v200, v200\n"
"v_mfma_f32_16x16x32_bf16 v[8:11], a[200:203], v[208:211], v[8:11]\n"
"v_exp_f32 v201, v201\n"
"v_mfma_f32_16x16x32_bf16 v[12:15], a[204:207], v[208:211], v[12:15]\n"
"v_exp_f32 v202, v202\n"
"v_mfma_f32_16x16x32_bf16 v[16:19], a[208:211], v[208:211], v[16:19]\n"
"v_exp_f32 v203, v203\n"
"v_mfma_f32_16x16x32_bf16 v[20:23], a[212:215], v[208:211], v[20:23]\n"
"v_exp_f32 v204, v204\n"
"v_mfma_f32_16x16x32_bf16 v[24:27], a[216:219], v[208:211], v[24:27]\n"
"v_exp_f32 v205, v205\n"
"v_mfma_f32_16x16x32_bf16 v[28:31], a[220:223], v[208:211], v[28:31]\n"
"v_exp_f32 v206, v206\n"
"v_mfma_f32_16x16x32_bf16 v[192:195], a[128:131], a[96:99], v[216:219]\n"
"v_exp_f32 v207, v207\n"
"v_mfma_f32_16x16x32_bf16 v[192:195], a[132:135], a[100:103], v[192:195]\n"
"v_cvt_pk_bf16_f32 v212, v200, v201\n"
"v_cvt_pk_bf16_f32 v213, v202, v203\n"
"v_mfma_f32_16x16x32_bf16 v[192:195], a[136:139], a[104:107], v[192:195]\n"
"v_cvt_pk_bf16_f32 v214, v204, v205\n"
"v_cvt_pk_bf16_f32 v215, v206, v207\n"
"v_mfma_f32_16x16x32_bf16 v[192:195], a[140:143], a[108:111], v[192:195]\n"
"buffer_load_dwordx4 v223, s[40:43], s62 offen offset:2048 nt lds\n"
"v_mfma_f32_16x16x32_bf16 v[196:199], a[144:147], a[96:99], v[216:219]\n"
"ds_read_b64_tr_b16 a[166:167], v222 offset:36864\n"
"ds_read_b64_tr_b16 a[168:169], v221 offset:33792\n"
"v_mfma_f32_16x16x32_bf16 v[196:199], a[148:151], a[100:103], v[196:199]\n"
"ds_read_b64_tr_b16 a[170:171], v221 offset:37888\n"
"ds_read_b64_tr_b16 a[172:173], v222 offset:33792\n"
"v_mfma_f32_16x16x32_bf16 v[196:199], a[152:155], a[104:107], v[196:199]\n"
"ds_read_b64_tr_b16 a[174:175], v222 offset:37888\n"
"v_mfma_f32_16x16x32_bf16 v[196:199], a[156:159], a[108:111], v[196:199]\n"
"ds_read_b128 a[96:99], v220 offset:32768\n"
"v_smfmac_f32_16x16x64_bf16 v[224:227], v[240:243], v[208:215], v244\n"
"v_mfma_f32_16x16x32_bf16 v[32:35], a[192:195], v[212:215], v[32:35]\n"
"v_exp_f32 v192, v192\n"
"v_mfma_f32_16x16x32_bf16 v[36:39], a[196:199], v[212:215], v[36:39]\n"
"v_exp_f32 v193, v193\n"
"v_mfma_f32_16x16x32_bf16 v[40:43], a[200:203], v[212:215], v[40:43]\n"
"v_exp_f32 v194, v194\n"
"v_mfma_f32_16x16x32_bf16 v[44:47], a[204:207], v[212:215], v[44:47]\n"
"v_exp_f32 v195, v195\n"
"v_mfma_f32_16x16x32_bf16 v[48:51], a[208:211], v[212:215], v[48:51]\n"
"v_exp_f32 v196, v196\n"
"v_mfma_f32_16x16x32_bf16 v[52:55], a[212:215], v[212:215], v[52:55]\n"
"v_exp_f32 v197, v197\n"
"v_mfma_f32_16x16x32_bf16 v[56:59], a[216:219], v[212:215], v[56:59]\n"
"v_exp_f32 v198, v198\n"
"v_mfma_f32_16x16x32_bf16 v[60:63], a[220:223], v[212:215], v[60:63]\n"
"v_exp_f32 v199, v199\n"
"v_mfma_f32_16x16x32_bf16 v[200:203], a[128:131], a[32:35], v[216:219]\n"
"v_cvt_pk_bf16_f32 v212, v192, v193\n"
"v_cvt_pk_bf16_f32 v213, v194, v195\n"
"v_mfma_f32_16x16x32_bf16 v[200:203], a[132:135], a[36:39], v[200:203]\n"
"v_cvt_pk_bf16_f32 v214, v196, v197\n"
"v_cvt_pk_bf16_f32 v215, v198, v199\n"
"v_mfma_f32_16x16x32_bf16 v[200:203], a[136:139], a[40:43], v[200:203]\n"
"buffer_load_dwordx4 v223, s[40:43], 0 offen offset:3072 nt lds\n"
"v_mfma_f32_16x16x32_bf16 v[200:203], a[140:143], a[44:47], v[200:203]\n"
"ds_read_b128 a[100:103], v220 offset:33792\n"
"v_mfma_f32_16x16x32_bf16 v[204:207], a[144:147], a[32:35], v[216:219]\n"
"ds_read_b128 a[104:107], v220 offset:34816\n"
"v_mfma_f32_16x16x32_bf16 v[204:207], a[148:151], a[36:39], v[204:207]\n"
"ds_read_b128 a[108:111], v220 offset:35840\n"
"ds_read_b64_tr_b16 a[176:177], v221 offset:34816\n"
"v_mfma_f32_16x16x32_bf16 v[204:207], a[152:155], a[40:43], v[204:207]\n"
"ds_read_b64_tr_b16 a[178:179], v221 offset:38912\n"
"ds_read_b64_tr_b16 a[180:181], v222 offset:34816\n"
"v_mfma_f32_16x16x32_bf16 v[204:207], a[156:159], a[44:47], v[204:207]\n"
"s_add_u32 m0, s70, 8192\n"
"ds_read_b64_tr_b16 a[182:183], v222 offset:38912\n"
"v_smfmac_f32_16x16x64_bf16 v[236:239], v[240:243], v[208:215], v244\n"
"ds_read_b64_tr_b16 a[184:185], v221 offset:35840\n"
"v_mfma_f32_16x16x32_bf16 a[224:227], a[192:195], v[212:215], a[224:227]\n"
"v_exp_f32 v200, v200\n"
"v_mfma_f32_16x16x32_bf16 a[228:231], a[196:199], v[212:215], a[228:231]\n"
"v_exp_f32 v201, v201\n"
"v_mfma_f32_16x16x32_bf16 a[232:235], a[200:203], v[212:215], a[232:235]\n"
"v_exp_f32 v202, v202\n"
"v_mfma_f32_16x16x32_bf16 a[236:239], a[204:207], v[212:215], a[236:239]\n"
"v_exp_f32 v203, v203\n"
"v_mfma_f32_16x16x32_bf16 a[240:243], a[208:211], v[212:215], a[240:243]\n"
"v_exp_f32 v204, v204\n"
"v_mfma_f32_16x16x32_bf16 a[244:247], a[212:215], v[212:215], a[244:247]\n"
"v_exp_f32 v205, v205\n"
"v_mfma_f32_16x16x32_bf16 a[248:251], a[216:219], v[212:215], a[248:251]\n"
"v_exp_f32 v206, v206\n"
"v_mfma_f32_16x16x32_bf16 a[252:255], a[220:223], v[212:215], a[252:255]\n"
"v_exp_f32 v207, v207\n"
"v_mfma_f32_16x16x32_bf16 v[192:195], a[128:131], a[48:51], v[216:219]\n"
"v_cvt_pk_bf16_f32 v208, v200, v201\n"
"v_cvt_pk_bf16_f32 v209, v202, v203\n"
"v_mfma_f32_16x16x32_bf16 v[192:195], a[132:135], a[52:55], v[192:195]\n"
"v_cvt_pk_bf16_f32 v210, v204, v205\n"
"v_cvt_pk_bf16_f32 v211, v206, v207\n"
"v_mfma_f32_16x16x32_bf16 v[192:195], a[136:139], a[56:59], v[192:195]\n"
"buffer_load_dwordx4 v223, s[44:47], s60 offen offset:0 nt lds\n"
"v_mfma_f32_16x16x32_bf16 v[192:195], a[140:143], a[60:63], v[192:195]\n"
"ds_read_b64_tr_b16 a[186:187], v221 offset:39936\n"
"ds_read_b64_tr_b16 a[188:189], v222 offset:35840\n"
"v_mfma_f32_16x16x32_bf16 v[196:199], a[144:147], a[48:51], v[216:219]\n"
"ds_read_b64_tr_b16 a[190:191], v222 offset:39936\n"
"v_mfma_f32_16x16x32_bf16 v[196:199], a[148:151], a[52:55], v[196:199]\n"
"v_mfma_f32_16x16x32_bf16 v[196:199], a[152:155], a[56:59], v[196:199]\n"
"v_mfma_f32_16x16x32_bf16 v[196:199], a[156:159], a[60:63], v[196:199]\n"
"buffer_load_dwordx4 v223, s[44:47], s61 offen offset:1024 nt lds\n"
"v_mfma_f32_16x16x32_bf16 v[64:67], a[192:195], v[208:211], v[64:67]\n"
"v_mfma_f32_16x16x32_bf16 v[68:71], a[196:199], v[208:211], v[68:71]\n"
"v_exp_f32 v192, v192\n"
"v_mfma_f32_16x16x32_bf16 v[72:75], a[200:203], v[208:211], v[72:75]\n"
"v_exp_f32 v193, v193\n"
"v_mfma_f32_16x16x32_bf16 v[76:79], a[204:207], v[208:211], v[76:79]\n"
"v_exp_f32 v194, v194\n"
"v_mfma_f32_16x16x32_bf16 v[80:83], a[208:211], v[208:211], v[80:83]\n"
"v_exp_f32 v195, v195\n"
"v_mfma_f32_16x16x32_bf16 v[84:87], a[212:215], v[208:211], v[84:87]\n"
"v_exp_f32 v196, v196\n"
"v_mfma_f32_16x16x32_bf16 v[88:91], a[216:219], v[208:211], v[88:91]\n"
"v_exp_f32 v197, v197\n"
"v_mfma_f32_16x16x32_bf16 v[92:95], a[220:223], v[208:211], v[92:95]\n"
"v_exp_f32 v198, v198\n"
"v_mfma_f32_16x16x32_bf16 v[200:203], a[128:131], a[64:67], v[216:219]\n"
"v_exp_f32 v199, v199\n"
"v_mfma_f32_16x16x32_bf16 v[200:203], a[132:135], a[68:71], v[200:203]\n"
"v_cvt_pk_bf16_f32 v212, v192, v193\n"
"v_cvt_pk_bf16_f32 v213, v194, v195\n"
"v_mfma_f32_16x16x32_bf16 v[200:203], a[136:139], a[72:75], v[200:203]\n"
"v_cvt_pk_bf16_f32 v214, v196, v197\n"
"v_cvt_pk_bf16_f32 v215, v198, v199\n"
"v_mfma_f32_16x16x32_bf16 v[200:203], a[140:143], a[76:79], v[200:203]\n"
"buffer_load_dwordx4 v223, s[44:47], s62 offen offset:2048 nt lds\n"
"v_mfma_f32_16x16x32_bf16 v[204:207], a[144:147], a[64:67], v[216:219]\n"
"v_mfma_f32_16x16x32_bf16 v[204:207], a[148:151], a[68:71], v[204:207]\n"
"v_mfma_f32_16x16x32_bf16 v[204:207], a[152:155], a[72:75], v[204:207]\n"
"v_mfma_f32_16x16x32_bf16 v[204:207], a[156:159], a[76:79], v[204:207]\n"
"v_smfmac_f32_16x16x64_bf16 v[228:231], v[240:243], v[208:215], v244\n"
"v_mfma_f32_16x16x32_bf16 v[96:99], a[192:195], v[212:215], v[96:99]\n"
"v_exp_f32 v200, v200\n"
"v_mfma_f32_16x16x32_bf16 v[100:103], a[196:199], v[212:215], v[100:103]\n"
"v_exp_f32 v201, v201\n"
"v_mfma_f32_16x16x32_bf16 v[104:107], a[200:203], v[212:215], v[104:107]\n"
"v_exp_f32 v202, v202\n"
"v_mfma_f32_16x16x32_bf16 v[108:111], a[204:207], v[212:215], v[108:111]\n"
"v_exp_f32 v203, v203\n"
"v_mfma_f32_16x16x32_bf16 v[112:115], a[208:211], v[212:215], v[112:115]\n"
"v_exp_f32 v204, v204\n"
"v_mfma_f32_16x16x32_bf16 v[116:119], a[212:215], v[212:215], v[116:119]\n"
"v_exp_f32 v205, v205\n"
"v_mfma_f32_16x16x32_bf16 v[120:123], a[216:219], v[212:215], v[120:123]\n"
"v_exp_f32 v206, v206\n"
"v_mfma_f32_16x16x32_bf16 v[124:127], a[220:223], v[212:215], v[124:127]\n"
"v_exp_f32 v207, v207\n"
"v_mfma_f32_16x16x32_bf16 v[192:195], a[128:131], a[80:83], v[216:219]\n"
"v_cvt_pk_bf16_f32 v208, v200, v201\n"
"v_cvt_pk_bf16_f32 v209, v202, v203\n"
"v_mfma_f32_16x16x32_bf16 v[192:195], a[132:135], a[84:87], v[192:195]\n"
"v_cvt_pk_bf16_f32 v210, v204, v205\n"
"v_cvt_pk_bf16_f32 v211, v206, v207\n"
"v_mfma_f32_16x16x32_bf16 v[192:195], a[136:139], a[88:91], v[192:195]\n"
"buffer_load_dwordx4 v223, s[44:47], 0 offen offset:3072 nt lds\n"
"v_mfma_f32_16x16x32_bf16 v[192:195], a[140:143], a[92:95], v[192:195]\n"
"v_mfma_f32_16x16x32_bf16 v[196:199], a[144:147], a[80:83], v[216:219]\n"
"v_mfma_f32_16x16x32_bf16 v[196:199], a[148:151], a[84:87], v[196:199]\n"
"v_mfma_f32_16x16x32_bf16 v[196:199], a[152:155], a[88:91], v[196:199]\n"
"v_mfma_f32_16x16x32_bf16 v[196:199], a[156:159], a[92:95], v[196:199]\n"
"v_add_u32 v223, 0x2000, v223\n"
"v_mfma_f32_16x16x32_bf16 v[128:131], a[192:195], v[208:211], v[128:131]\n"
"v_mfma_f32_16x16x32_bf16 v[132:135], a[196:199], v[208:211], v[132:135]\n"
"v_exp_f32 v192, v192\n"
"v_mfma_f32_16x16x32_bf16 v[136:139], a[200:203], v[208:211], v[136:139]\n"
"v_exp_f32 v193, v193\n"
"v_mfma_f32_16x16x32_bf16 v[140:143], a[204:207], v[208:211], v[140:143]\n"
"v_exp_f32 v194, v194\n"
"v_mfma_f32_16x16x32_bf16 v[144:147], a[208:211], v[208:211], v[144:147]\n"
"v_exp_f32 v195, v195\n"
"v_mfma_f32_16x16x32_bf16 v[148:151], a[212:215], v[208:211], v[148:151]\n"
"v_exp_f32 v196, v196\n"
"v_mfma_f32_16x16x32_bf16 v[152:155], a[216:219], v[208:211], v[152:155]\n"
"v_exp_f32 v197, v197\n"
"v_mfma_f32_16x16x32_bf16 v[156:159], a[220:223], v[208:211], v[156:159]\n"
"v_exp_f32 v198, v198\n"
"s_waitcnt vmcnt(8) lgkmcnt(0)\n"
"s_barrier\n"
"s_sleep 5\n"
".p2align 6\n"
"La1%=:\n"
"v_mfma_f32_16x16x32_bf16 v[200:203], a[96:99], a[0:3], v[216:219]\n"
"v_exp_f32 v199, v199\n"
"v_mfma_f32_16x16x32_bf16 v[200:203], a[100:103], a[4:7], v[200:203]\n"
"v_cvt_pk_bf16_f32 v212, v192, v193\n"
"v_cvt_pk_bf16_f32 v213, v194, v195\n"
"v_mfma_f32_16x16x32_bf16 v[200:203], a[104:107], a[8:11], v[200:203]\n"
"v_cvt_pk_bf16_f32 v214, v196, v197\n"
"v_cvt_pk_bf16_f32 v215, v198, v199\n"
"v_mfma_f32_16x16x32_bf16 v[200:203], a[108:111], a[12:15], v[200:203]\n"
"s_add_u32 m0, s70, 16384\n"
"v_mfma_f32_16x16x32_bf16 v[204:207], a[112:115], a[0:3], v[216:219]\n"
"buffer_load_dwordx4 v223, s[40:43], s60 offen offset:0 nt lds\n"
"v_mfma_f32_16x16x32_bf16 v[204:207], a[116:119], a[4:7], v[204:207]\n"
"ds_read_b128 a[144:147], v220 offset:53248\n"
"v_mfma_f32_16x16x32_bf16 v[204:207], a[120:123], a[8:11], v[204:207]\n"
"ds_read_b128 a[148:151], v220 offset:54272\n"
"v_mfma_f32_16x16x32_bf16 v[204:207], a[124:127], a[12:15], v[204:207]\n"
"ds_read_b128 a[152:155], v220 offset:55296\n"
"v_smfmac_f32_16x16x64_bf16 v[232:235], v[240:243], v[208:215], v244\n"
"v_mfma_f32_16x16x32_bf16 v[160:163], a[192:195], v[212:215], v[160:163]\n"
"v_exp_f32 v200, v200\n"
"v_mfma_f32_16x16x32_bf16 v[164:167], a[196:199], v[212:215], v[164:167]\n"
"v_exp_f32 v201, v201\n"
"v_mfma_f32_16x16x32_bf16 v[168:171], a[200:203], v[212:215], v[168:171]\n"
"v_exp_f32 v202, v202\n"
"v_mfma_f32_16x16x32_bf16 v[172:175], a[204:207], v[212:215], v[172:175]\n"
"v_exp_f32 v203, v203\n"
"v_mfma_f32_16x16x32_bf16 v[176:179], a[208:211], v[212:215], v[176:179]\n"
"v_exp_f32 v204, v204\n"
"v_mfma_f32_16x16x32_bf16 v[180:183], a[212:215], v[212:215], v[180:183]\n"
"v_exp_f32 v205, v205\n"
"v_mfma_f32_16x16x32_bf16 v[184:187], a[216:219], v[212:215], v[184:187]\n"
"v_exp_f32 v206, v206\n"
"v_mfma_f32_16x16x32_bf16 v[188:191], a[220:223], v[212:215], v[188:191]\n"
"v_exp_f32 v207, v207\n"
"v_mfma_f32_16x16x32_bf16 v[192:195], a[96:99], a[16:19], v[216:219]\n"
"v_cvt_pk_bf16_f32 v208, v200, v201\n"
"v_cvt_pk_bf16_f32 v209, v202, v203\n"
"v_mfma_f32_16x16x32_bf16 v[192:195], a[100:103], a[20:23], v[192:195]\n"
"v_cvt_pk_bf16_f32 v210, v204, v205\n"
"v_cvt_pk_bf16_f32 v211, v206, v207\n"
"v_mfma_f32_16x16x32_bf16 v[192:195], a[104:107], a[24:27], v[192:195]\n"
"buffer_load_dwordx4 v223, s[40:43], s61 offen offset:1024 nt lds\n"
"v_mfma_f32_16x16x32_bf16 v[192:195], a[108:111], a[28:31], v[192:195]\n"
"ds_read_b128 a[156:159], v220 offset:56320\n"
"v_mfma_f32_16x16x32_bf16 v[196:199], a[112:115], a[16:19], v[216:219]\n"
"ds_read_b128 a[128:131], v220 offset:49152\n"
"v_mfma_f32_16x16x32_bf16 v[196:199], a[116:119], a[20:23], v[196:199]\n"
"ds_read_b128 a[132:135], v220 offset:50176\n"
"v_mfma_f32_16x16x32_bf16 v[196:199], a[120:123], a[24:27], v[196:199]\n"
"ds_read_b128 a[136:139], v220 offset:51200\n"
"v_mfma_f32_16x16x32_bf16 v[196:199], a[124:127], a[28:31], v[196:199]\n"
"buffer_load_dwordx4 v223, s[40:43], s62 offen offset:2048 nt lds\n"
"v_mfma_f32_16x16x32_bf16 v[0:3], a[160:163], v[208:211], v[0:3]\n"
"ds_read_b128 a[140:143], v220 offset:52224\n"
"ds_read_b64_tr_b16 a[192:193], v221 offset:49152\n"
"v_mfma_f32_16x16x32_bf16 v[4:7], a[164:167], v[208:211], v[4:7]\n"
"v_exp_f32 v192, v192\n"
"v_mfma_f32_16x16x32_bf16 v[8:11], a[168:171], v[208:211], v[8:11]\n"
"v_exp_f32 v193, v193\n"
"v_mfma_f32_16x16x32_bf16 v[12:15], a[172:175], v[208:211], v[12:15]\n"
"v_exp_f32 v194, v194\n"
"v_mfma_f32_16x16x32_bf16 v[16:19], a[176:179], v[208:211], v[16:19]\n"
"v_exp_f32 v195, v195\n"
"v_mfma_f32_16x16x32_bf16 v[20:23], a[180:183], v[208:211], v[20:23]\n"
"v_exp_f32 v196, v196\n"
"v_mfma_f32_16x16x32_bf16 v[24:27], a[184:187], v[208:211], v[24:27]\n"
"v_exp_f32 v197, v197\n"
"v_mfma_f32_16x16x32_bf16 v[28:31], a[188:191], v[208:211], v[28:31]\n"
"v_exp_f32 v198, v198\n"
"v_mfma_f32_16x16x32_bf16 v[200:203], a[96:99], a[32:35], v[216:219]\n"
"v_exp_f32 v199, v199\n"
"v_mfma_f32_16x16x32_bf16 v[200:203], a[100:103], a[36:39], v[200:203]\n"
"v_cvt_pk_bf16_f32 v212, v192, v193\n"
"v_cvt_pk_bf16_f32 v213, v194, v195\n"
"v_mfma_f32_16x16x32_bf16 v[200:203], a[104:107], a[40:43], v[200:203]\n"
"v_cvt_pk_bf16_f32 v214, v196, v197\n"
"v_cvt_pk_bf16_f32 v215, v198, v199\n"
"v_mfma_f32_16x16x32_bf16 v[200:203], a[108:111], a[44:47], v[200:203]\n"
"buffer_load_dwordx4 v223, s[40:43], 0 offen offset:3072 nt lds\n"
"v_mfma_f32_16x16x32_bf16 v[204:207], a[112:115], a[32:35], v[216:219]\n"
"ds_read_b64_tr_b16 a[194:195], v221 offset:53248\n"
"ds_read_b64_tr_b16 a[196:197], v222 offset:49152\n"
"v_mfma_f32_16x16x32_bf16 v[204:207], a[116:119], a[36:39], v[204:207]\n"
"ds_read_b64_tr_b16 a[198:199], v222 offset:53248\n"
"ds_read_b64_tr_b16 a[200:201], v221 offset:50176\n"
"v_mfma_f32_16x16x32_bf16 v[204:207], a[120:123], a[40:43], v[204:207]\n"
"ds_read_b64_tr_b16 a[202:203], v221 offset:54272\n"
"ds_read_b64_tr_b16 a[204:205], v222 offset:50176\n"
"v_mfma_f32_16x16x32_bf16 v[204:207], a[124:127], a[44:47], v[204:207]\n"
"ds_read_b64_tr_b16 a[206:207], v222 offset:54272\n"
"ds_read_b64_tr_b16 a[208:209], v221 offset:51200\n"
"v_smfmac_f32_16x16x64_bf16 v[224:227], v[240:243], v[208:215], v244\n"
"s_add_u32 m0, s70, 24576\n"
"v_mfma_f32_16x16x32_bf16 v[32:35], a[160:163], v[212:215], v[32:35]\n"
"v_exp_f32 v200, v200\n"
"v_mfma_f32_16x16x32_bf16 v[36:39], a[164:167], v[212:215], v[36:39]\n"
"v_exp_f32 v201, v201\n"
"v_mfma_f32_16x16x32_bf16 v[40:43], a[168:171], v[212:215], v[40:43]\n"
"v_exp_f32 v202, v202\n"
"v_mfma_f32_16x16x32_bf16 v[44:47], a[172:175], v[212:215], v[44:47]\n"
"v_exp_f32 v203, v203\n"
"v_mfma_f32_16x16x32_bf16 v[48:51], a[176:179], v[212:215], v[48:51]\n"
"v_exp_f32 v204, v204\n"
"v_mfma_f32_16x16x32_bf16 v[52:55], a[180:183], v[212:215], v[52:55]\n"
"v_exp_f32 v205, v205\n"
"v_mfma_f32_16x16x32_bf16 v[56:59], a[184:187], v[212:215], v[56:59]\n"
"v_exp_f32 v206, v206\n"
"v_mfma_f32_16x16x32_bf16 v[60:63], a[188:191], v[212:215], v[60:63]\n"
"v_exp_f32 v207, v207\n"
"v_mfma_f32_16x16x32_bf16 v[192:195], a[96:99], a[48:51], v[216:219]\n"
"v_cvt_pk_bf16_f32 v208, v200, v201\n"
"v_cvt_pk_bf16_f32 v209, v202, v203\n"
"v_mfma_f32_16x16x32_bf16 v[192:195], a[100:103], a[52:55], v[192:195]\n"
"v_cvt_pk_bf16_f32 v210, v204, v205\n"
"v_cvt_pk_bf16_f32 v211, v206, v207\n"
"v_mfma_f32_16x16x32_bf16 v[192:195], a[104:107], a[56:59], v[192:195]\n"
"buffer_load_dwordx4 v223, s[44:47], s60 offen offset:0 nt lds\n"
"v_mfma_f32_16x16x32_bf16 v[192:195], a[108:111], a[60:63], v[192:195]\n"
"ds_read_b64_tr_b16 a[210:211], v221 offset:55296\n"
"ds_read_b64_tr_b16 a[212:213], v222 offset:51200\n"
"v_mfma_f32_16x16x32_bf16 v[196:199], a[112:115], a[48:51], v[216:219]\n"
"ds_read_b64_tr_b16 a[214:215], v222 offset:55296\n"
"ds_read_b64_tr_b16 a[216:217], v221 offset:52224\n"
"v_mfma_f32_16x16x32_bf16 v[196:199], a[116:119], a[52:55], v[196:199]\n"
"ds_read_b64_tr_b16 a[218:219], v221 offset:56320\n"
"ds_read_b64_tr_b16 a[220:221], v222 offset:52224\n"
"v_mfma_f32_16x16x32_bf16 v[196:199], a[120:123], a[56:59], v[196:199]\n"
"ds_read_b64_tr_b16 a[222:223], v222 offset:56320\n"
"v_mfma_f32_16x16x32_bf16 v[196:199], a[124:127], a[60:63], v[196:199]\n"
"buffer_load_dwordx4 v223, s[44:47], s61 offen offset:1024 nt lds\n"
"v_mfma_f32_16x16x32_bf16 v[64:67], a[160:163], v[208:211], v[64:67]\n"
"v_mfma_f32_16x16x32_bf16 v[68:71], a[164:167], v[208:211], v[68:71]\n"
"v_exp_f32 v192, v192\n"
"v_mfma_f32_16x16x32_bf16 v[72:75], a[168:171], v[208:211], v[72:75]\n"
"v_exp_f32 v193, v193\n"
"v_mfma_f32_16x16x32_bf16 v[76:79], a[172:175], v[208:211], v[76:79]\n"
"v_exp_f32 v194, v194\n"
"v_mfma_f32_16x16x32_bf16 v[80:83], a[176:179], v[208:211], v[80:83]\n"
"v_exp_f32 v195, v195\n"
"v_mfma_f32_16x16x32_bf16 v[84:87], a[180:183], v[208:211], v[84:87]\n"
"v_exp_f32 v196, v196\n"
"v_mfma_f32_16x16x32_bf16 v[88:91], a[184:187], v[208:211], v[88:91]\n"
"v_exp_f32 v197, v197\n"
"v_mfma_f32_16x16x32_bf16 v[92:95], a[188:191], v[208:211], v[92:95]\n"
"v_exp_f32 v198, v198\n"
"v_mfma_f32_16x16x32_bf16 v[200:203], a[96:99], a[64:67], v[216:219]\n"
"v_exp_f32 v199, v199\n"
"v_mfma_f32_16x16x32_bf16 v[200:203], a[100:103], a[68:71], v[200:203]\n"
"v_cvt_pk_bf16_f32 v212, v192, v193\n"
"v_cvt_pk_bf16_f32 v213, v194, v195\n"
"v_mfma_f32_16x16x32_bf16 v[200:203], a[104:107], a[72:75], v[200:203]\n"
"v_cvt_pk_bf16_f32 v214, v196, v197\n"
"v_cvt_pk_bf16_f32 v215, v198, v199\n"
"v_mfma_f32_16x16x32_bf16 v[200:203], a[108:111], a[76:79], v[200:203]\n"
"buffer_load_dwordx4 v223, s[44:47], s62 offen offset:2048 nt lds\n"
"v_mfma_f32_16x16x32_bf16 v[204:207], a[112:115], a[64:67], v[216:219]\n"
"v_mfma_f32_16x16x32_bf16 v[204:207], a[116:119], a[68:71], v[204:207]\n"
"v_mfma_f32_16x16x32_bf16 v[204:207], a[120:123], a[72:75], v[204:207]\n"
"v_mfma_f32_16x16x32_bf16 v[204:207], a[124:127], a[76:79], v[204:207]\n"
"v_smfmac_f32_16x16x64_bf16 v[228:231], v[240:243], v[208:215], v244\n"
"v_mfma_f32_16x16x32_bf16 v[96:99], a[160:163], v[212:215], v[96:99]\n"
"v_exp_f32 v200, v200\n"
"v_mfma_f32_16x16x32_bf16 v[100:103], a[164:167], v[212:215], v[100:103]\n"
"v_exp_f32 v201, v201\n"
"v_mfma_f32_16x16x32_bf16 v[104:107], a[168:171], v[212:215], v[104:107]\n"
"v_exp_f32 v202, v202\n"
"v_mfma_f32_16x16x32_bf16 v[108:111], a[172:175], v[212:215], v[108:111]\n"
"v_exp_f32 v203, v203\n"
"v_mfma_f32_16x16x32_bf16 v[112:115], a[176:179], v[212:215], v[112:115]\n"
"v_exp_f32 v204, v204\n"
"v_mfma_f32_16x16x32_bf16 v[116:119], a[180:183], v[212:215], v[116:119]\n"
"v_exp_f32 v205, v205\n"
"v_mfma_f32_16x16x32_bf16 v[120:123], a[184:187], v[212:215], v[120:123]\n"
"v_exp_f32 v206, v206\n"
"v_mfma_f32_16x16x32_bf16 v[124:127], a[188:191], v[212:215], v[124:127]\n"
"v_exp_f32 v207, v207\n"
"v_mfma_f32_16x16x32_bf16 v[192:195], a[96:99], a[80:83], v[216:219]\n"
"v_cvt_pk_bf16_f32 v208, v200, v201\n"
"v_cvt_pk_bf16_f32 v209, v202, v203\n"
"v_mfma_f32_16x16x32_bf16 v[192:195], a[100:103], a[84:87], v[192:195]\n"
"v_cvt_pk_bf16_f32 v210, v204, v205\n"
"v_cvt_pk_bf16_f32 v211, v206, v207\n"
"v_mfma_f32_16x16x32_bf16 v[192:195], a[104:107], a[88:91], v[192:195]\n"
"buffer_load_dwordx4 v223, s[44:47], 0 offen offset:3072 nt lds\n"
"v_mfma_f32_16x16x32_bf16 v[192:195], a[108:111], a[92:95], v[192:195]\n"
"v_mfma_f32_16x16x32_bf16 v[196:199], a[112:115], a[80:83], v[216:219]\n"
"v_mfma_f32_16x16x32_bf16 v[196:199], a[116:119], a[84:87], v[196:199]\n"
"v_mfma_f32_16x16x32_bf16 v[196:199], a[120:123], a[88:91], v[196:199]\n"
"v_mfma_f32_16x16x32_bf16 v[196:199], a[124:127], a[92:95], v[196:199]\n"
"v_add_u32 v223, 0x2000, v223\n"
"v_mfma_f32_16x16x32_bf16 v[128:131], a[160:163], v[208:211], v[128:131]\n"
"ds_read_b128 a[96:99], v245 offset:0\n"
"v_mfma_f32_16x16x32_bf16 v[132:135], a[164:167], v[208:211], v[132:135]\n"
"v_exp_f32 v192, v192\n"
"v_mfma_f32_16x16x32_bf16 v[136:139], a[168:171], v[208:211], v[136:139]\n"
"v_exp_f32 v193, v193\n"
"v_mfma_f32_16x16x32_bf16 v[140:143], a[172:175], v[208:211], v[140:143]\n"
"v_exp_f32 v194, v194\n"
"v_mfma_f32_16x16x32_bf16 v[144:147], a[176:179], v[208:211], v[144:147]\n"
"v_exp_f32 v195, v195\n"
"v_mfma_f32_16x16x32_bf16 v[148:151], a[180:183], v[208:211], v[148:151]\n"
"v_exp_f32 v196, v196\n"
"v_mfma_f32_16x16x32_bf16 v[152:155], a[184:187], v[208:211], v[152:155]\n"
"v_exp_f32 v197, v197\n"
"v_mfma_f32_16x16x32_bf16 v[156:159], a[188:191], v[208:211], v[156:159]\n"
"v_exp_f32 v198, v198\n"
"s_waitcnt lgkmcnt(1)\n"
"v_mfma_f32_16x16x32_bf16 v[200:203], a[128:131], a[0:3], v[216:219]\n"
"v_exp_f32 v199, v199\n"
"v_mfma_f32_16x16x32_bf16 v[200:203], a[132:135], a[4:7], v[200:203]\n"
"v_cvt_pk_bf16_f32 v212, v192, v193\n"
"v_cvt_pk_bf16_f32 v213, v194, v195\n"
"v_mfma_f32_16x16x32_bf16 v[200:203], a[136:139], a[8:11], v[200:203]\n"
"v_cvt_pk_bf16_f32 v214, v196, v197\n"
"v_cvt_pk_bf16_f32 v215, v198, v199\n"
"v_mfma_f32_16x16x32_bf16 v[200:203], a[140:143], a[12:15], v[200:203]\n"
"ds_read_b128 a[100:103], v245 offset:1024\n"
"v_mfma_f32_16x16x32_bf16 v[204:207], a[144:147], a[0:3], v[216:219]\n"
"ds_read_b128 a[104:107], v245 offset:2048\n"
"v_mfma_f32_16x16x32_bf16 v[204:207], a[148:151], a[4:7], v[204:207]\n"
"ds_read_b128 a[108:111], v245 offset:3072\n"
"v_mfma_f32_16x16x32_bf16 v[204:207], a[152:155], a[8:11], v[204:207]\n"
"v_mfma_f32_16x16x32_bf16 v[204:207], a[156:159], a[12:15], v[204:207]\n"
"s_waitcnt vmcnt(8) lgkmcnt(0)\n"
"s_barrier\n"
"s_cmp_lt_u32 s75, 4\n"
"s_cbranch_scc1 Lsl11%=\n"
"s_sleep 2\n"
"s_cmp_lt_u32 s75, 5\n"
"s_cbranch_scc1 Lsl11%=\n"
"s_sleep 2\n"
"s_cmp_lt_u32 s75, 6\n"
"s_cbranch_scc1 Lsl11%=\n"
"s_sleep 1\n"
"Lsl11%=:\n"
"v_smfmac_f32_16x16x64_bf16 v[232:235], v[240:243], v[208:215], v244\n"
"s_add_u32 m0, s70, 32768\n"
"v_mfma_f32_16x16x32_bf16 v[160:163], a[160:163], v[212:215], v[160:163]\n"
"v_exp_f32 v200, v200\n"
"v_mfma_f32_16x16x32_bf16 v[164:167], a[164:167], v[212:215], v[164:167]\n"
"v_exp_f32 v201, v201\n"
"v_mfma_f32_16x16x32_bf16 v[168:171], a[168:171], v[212:215], v[168:171]\n"
"v_exp_f32 v202, v202\n"
"v_mfma_f32_16x16x32_bf16 v[172:175], a[172:175], v[212:215], v[172:175]\n"
"v_exp_f32 v203, v203\n"
"v_mfma_f32_16x16x32_bf16 v[176:179], a[176:179], v[212:215], v[176:179]\n"
"v_exp_f32 v204, v204\n"
"v_mfma_f32_16x16x32_bf16 v[180:183], a[180:183], v[212:215], v[180:183]\n"
"v_exp_f32 v205, v205\n"
"v_mfma_f32_16x16x32_bf16 v[184:187], a[184:187], v[212:215], v[184:187]\n"
"v_exp_f32 v206, v206\n"
"v_mfma_f32_16x16x32_bf16 v[188:191], a[188:191], v[212:215], v[188:191]\n"
"v_exp_f32 v207, v207\n"
"v_mfma_f32_16x16x32_bf16 v[192:195], a[128:131], a[16:19], v[216:219]\n"
"v_cvt_pk_bf16_f32 v208, v200, v201\n"
"v_cvt_pk_bf16_f32 v209, v202, v203\n"
"v_mfma_f32_16x16x32_bf16 v[192:195], a[132:135], a[20:23], v[192:195]\n"
"v_cvt_pk_bf16_f32 v210, v204, v205\n"
"v_cvt_pk_bf16_f32 v211, v206, v207\n"
"v_mfma_f32_16x16x32_bf16 v[192:195], a[136:139], a[24:27], v[192:195]\n"
"buffer_load_dwordx4 v223, s[40:43], s60 offen offset:0 nt lds\n"
"v_mfma_f32_16x16x32_bf16 v[192:195], a[140:143], a[28:31], v[192:195]\n"
"ds_read_b128 a[112:115], v220 offset:4096\n"
"v_mfma_f32_16x16x32_bf16 v[196:199], a[144:147], a[16:19], v[216:219]\n"
"ds_read_b128 a[116:119], v220 offset:5120\n"
"v_mfma_f32_16x16x32_bf16 v[196:199], a[148:151], a[20:23], v[196:199]\n"
"ds_read_b128 a[120:123], v220 offset:6144\n"
"v_mfma_f32_16x16x32_bf16 v[196:199], a[152:155], a[24:27], v[196:199]\n"
"ds_read_b128 a[124:127], v220 offset:7168\n"
"ds_read_b64_tr_b16 a[160:161], v221 offset:0\n"
"v_mfma_f32_16x16x32_bf16 v[196:199], a[156:159], a[28:31], v[196:199]\n"
"buffer_load_dwordx4 v223, s[40:43], s61 offen offset:1024 nt lds\n"
"v_mfma_f32_16x16x32_bf16 v[0:3], a[192:195], v[208:211], v[0:3]\n"
"ds_read_b64_tr_b16 a[162:163], v221 offset:4096\n"
"ds_read_b64_tr_b16 a[164:165], v222 offset:0\n"
"v_mfma_f32_16x16x32_bf16 v[4:7], a[196:199], v[208:211], v[4:7]\n"
"v_exp_f32 v192, v192\n"
"v_mfma_f32_16x16x32_bf16 v[8:11], a[200:203], v[208:211], v[8:11]\n"
"v_exp_f32 v193, v193\n"
"v_mfma_f32_16x16x32_bf16 v[12:15], a[204:207], v[208:211], v[12:15]\n"
"v_exp_f32 v194, v194\n"
"v_mfma_f32_16x16x32_bf16 v[16:19], a[208:211], v[208:211], v[16:19]\n"
"v_exp_f32 v195, v195\n"
"v_mfma_f32_16x16x32_bf16 v[20:23], a[212:215], v[208:211], v[20:23]\n"
"v_exp_f32 v196, v196\n"
"v_mfma_f32_16x16x32_bf16 v[24:27], a[216:219], v[208:211], v[24:27]\n"
"v_exp_f32 v197, v197\n"
"v_mfma_f32_16x16x32_bf16 v[28:31], a[220:223], v[208:211], v[28:31]\n"
"v_exp_f32 v198, v198\n"
"v_mfma_f32_16x16x32_bf16 v[200:203], a[128:131], a[96:99], v[216:219]\n"
"v_exp_f32 v199, v199\n"
"v_mfma_f32_16x16x32_bf16 v[200:203], a[132:135], a[100:103], v[200:203]\n"
"v_cvt_pk_bf16_f32 v212, v192, v193\n"
"v_cvt_pk_bf16_f32 v213, v194, v195\n"
"v_mfma_f32_16x16x32_bf16 v[200:203], a[136:139], a[104:107], v[200:203]\n"
"v_cvt_pk_bf16_f32 v214, v196, v197\n"
"v_cvt_pk_bf16_f32 v215, v198, v199\n"
"v_mfma_f32_16x16x32_bf16 v[200:203], a[140:143], a[108:111], v[200:203]\n"
"buffer_load_dwordx4 v223, s[40:43], s62 offen offset:2048 nt lds\n"
"v_mfma_f32_16x16x32_bf16 v[204:207], a[144:147], a[96:99], v[216:219]\n"
"ds_read_b64_tr_b16 a[166:167], v222 offset:4096\n"
"ds_read_b64_tr_b16 a[168:169], v221 offset:1024\n"
"v_mfma_f32_16x16x32_bf16 v[204:207], a[148:151], a[100:103], v[204:207]\n"
"ds_read_b64_tr_b16 a[170:171], v221 offset:5120\n"
"ds_read_b64_tr_b16 a[172:173], v222 offset:1024\n"
"v_mfma_f32_16x16x32_bf16 v[204:207], a[152:155], a[104:107], v[204:207]\n"
"ds_read_b64_tr_b16 a[174:175], v222 offset:5120\n"
"v_mfma_f32_16x16x32_bf16 v[204:207], a[156:159], a[108:111], v[204:207]\n"
"ds_read_b128 a[96:99], v220 offset:0\n"
"v_smfmac_f32_16x16x64_bf16 v[224:227], v[240:243], v[208:215], v244\n"
"v_mfma_f32_16x16x32_bf16 v[32:35], a[192:195], v[212:215], v[32:35]\n"
"v_exp_f32 v200, v200\n"
"v_mfma_f32_16x16x32_bf16 v[36:39], a[196:199], v[212:215], v[36:39]\n"
"v_exp_f32 v201, v201\n"
"v_mfma_f32_16x16x32_bf16 v[40:43], a[200:203], v[212:215], v[40:43]\n"
"v_exp_f32 v202, v202\n"
"v_mfma_f32_16x16x32_bf16 v[44:47], a[204:207], v[212:215], v[44:47]\n"
"v_exp_f32 v203, v203\n"
"v_mfma_f32_16x16x32_bf16 v[48:51], a[208:211], v[212:215], v[48:51]\n"
"v_exp_f32 v204, v204\n"
"v_mfma_f32_16x16x32_bf16 v[52:55], a[212:215], v[212:215], v[52:55]\n"
"v_exp_f32 v205, v205\n"
"v_mfma_f32_16x16x32_bf16 v[56:59], a[216:219], v[212:215], v[56:59]\n"
"v_exp_f32 v206, v206\n"
"v_mfma_f32_16x16x32_bf16 v[60:63], a[220:223], v[212:215], v[60:63]\n"
"v_exp_f32 v207, v207\n"
"v_mfma_f32_16x16x32_bf16 v[192:195], a[128:131], a[32:35], v[216:219]\n"
"v_cvt_pk_bf16_f32 v212, v200, v201\n"
"v_cvt_pk_bf16_f32 v213, v202, v203\n"
"v_mfma_f32_16x16x32_bf16 v[192:195], a[132:135], a[36:39], v[192:195]\n"
"v_cvt_pk_bf16_f32 v214, v204, v205\n"
"v_cvt_pk_bf16_f32 v215, v206, v207\n"
"v_mfma_f32_16x16x32_bf16 v[192:195], a[136:139], a[40:43], v[192:195]\n"
"buffer_load_dwordx4 v223, s[40:43], 0 offen offset:3072 nt lds\n"
"v_mfma_f32_16x16x32_bf16 v[192:195], a[140:143], a[44:47], v[192:195]\n"
"ds_read_b128 a[100:103], v220 offset:1024\n"
"v_mfma_f32_16x16x32_bf16 v[196:199], a[144:147], a[32:35], v[216:219]\n"
"ds_read_b128 a[104:107], v220 offset:2048\n"
"v_mfma_f32_16x16x32_bf16 v[196:199], a[148:151], a[36:39], v[196:199]\n"
"ds_read_b128 a[108:111], v220 offset:3072\n"
"ds_read_b64_tr_b16 a[176:177], v221 offset:2048\n"
"v_mfma_f32_16x16x32_bf16 v[196:199], a[152:155], a[40:43], v[196:199]\n"
"ds_read_b64_tr_b16 a[178:179], v221 offset:6144\n"
"ds_read_b64_tr_b16 a[180:181], v222 offset:2048\n"
"v_mfma_f32_16x16x32_bf16 v[196:199], a[156:159], a[44:47], v[196:199]\n"
"s_add_u32 m0, s70, 40960\n"
"ds_read_b64_tr_b16 a[182:183], v222 offset:6144\n"
"v_smfmac_f32_16x16x64_bf16 v[236:239], v[240:243], v[208:215], v244\n"
"ds_read_b64_tr_b16 a[184:185], v221 offset:3072\n"
"v_mfma_f32_16x16x32_bf16 a[224:227], a[192:195], v[212:215], a[224:227]\n"
"v_exp_f32 v192, v192\n"
"v_mfma_f32_16x16x32_bf16 a[228:231], a[196:199], v[212:215], a[228:231]\n"
"v_exp_f32 v193, v193\n"
"v_mfma_f32_16x16x32_bf16 a[232:235], a[200:203], v[212:215], a[232:235]\n"
"v_exp_f32 v194, v194\n"
"v_mfma_f32_16x16x32_bf16 a[236:239], a[204:207], v[212:215], a[236:239]\n"
"v_exp_f32 v195, v195\n"
"v_mfma_f32_16x16x32_bf16 a[240:243], a[208:211], v[212:215], a[240:243]\n"
"v_exp_f32 v196, v196\n"
"v_mfma_f32_16x16x32_bf16 a[244:247], a[212:215], v[212:215], a[244:247]\n"
"v_exp_f32 v197, v197\n"
"v_mfma_f32_16x16x32_bf16 a[248:251], a[216:219], v[212:215], a[248:251]\n"
"v_exp_f32 v198, v198\n"
"v_mfma_f32_16x16x32_bf16 a[252:255], a[220:223], v[212:215], a[252:255]\n"
"v_exp_f32 v199, v199\n"
"v_mfma_f32_16x16x32_bf16 v[200:203], a[128:131], a[48:51], v[216:219]\n"
"v_cvt_pk_bf16_f32 v208, v192, v193\n"
"v_cvt_pk_bf16_f32 v209, v194, v195\n"
"v_mfma_f32_16x16x32_bf16 v[200:203], a[132:135], a[52:55], v[200:203]\n"
"v_cvt_pk_bf16_f32 v210, v196, v197\n"
"v_cvt_pk_bf16_f32 v211, v198, v199\n"
"v_mfma_f32_16x16x32_bf16 v[200:203], a[136:139], a[56:59], v[200:203]\n"
"buffer_load_dwordx4 v223, s[44:47], s60 offen offset:0 nt lds\n"
"v_mfma_f32_16x16x32_bf16 v[200:203], a[140:143], a[60:63], v[200:203]\n"
"ds_read_b64_tr_b16 a[186:187], v221 offset:7168\n"
"ds_read_b64_tr_b16 a[188:189], v222 offset:3072\n"
"v_mfma_f32_16x16x32_bf16 v[204:207], a[144:147], a[48:51], v[216:219]\n"
"ds_read_b64_tr_b16 a[190:191], v222 offset:7168\n"
"v_mfma_f32_16x16x32_bf16 v[204:207], a[148:151], a[52:55], v[204:207]\n"
"v_mfma_f32_16x16x32_bf16 v[204:207], a[152:155], a[56:59], v[204:207]\n"
"v_mfma_f32_16x16x32_bf16 v[204:207], a[156:159], a[60:63], v[204:207]\n"
"buffer_load_dwordx4 v223, s[44:47], s61 offen offset:1024 nt lds\n"
"v_mfma_f32_16x16x32_bf16 v[64:67], a[192:195], v[208:211], v[64:67]\n"
"v_mfma_f32_16x16x32_bf16 v[68:71], a[196:199], v[208:211], v[68:71]\n"
"v_exp_f32 v200, v200\n"
"v_mfma_f32_16x16x32_bf16 v[72:75], a[200:203], v[208:211], v[72:75]\n"
"v_exp_f32 v201, v201\n"
"v_mfma_f32_16x16x32_bf16 v[76:79], a[204:207], v[208:211], v[76:79]\n"
"v_exp_f32 v202, v202\n"
"v_mfma_f32_16x16x32_bf16 v[80:83], a[208:211], v[208:211], v[80:83]\n"
"v_exp_f32 v203, v203\n"
"v_mfma_f32_16x16x32_bf16 v[84:87], a[212:215], v[208:211], v[84:87]\n"
"v_exp_f32 v204, v204\n"
"v_mfma_f32_16x16x32_bf16 v[88:91], a[216:219], v[208:211], v[88:91]\n"
"v_exp_f32 v205, v205\n"
"v_mfma_f32_16x16x32_bf16 v[92:95], a[220:223], v[208:211], v[92:95]\n"
"v_exp_f32 v206, v206\n"
"v_mfma_f32_16x16x32_bf16 v[192:195], a[128:131], a[64:67], v[216:219]\n"
"v_exp_f32 v207, v207\n"
"v_mfma_f32_16x16x32_bf16 v[192:195], a[132:135], a[68:71], v[192:195]\n"
"v_cvt_pk_bf16_f32 v212, v200, v201\n"
"v_cvt_pk_bf16_f32 v213, v202, v203\n"
"v_mfma_f32_16x16x32_bf16 v[192:195], a[136:139], a[72:75], v[192:195]\n"
"v_cvt_pk_bf16_f32 v214, v204, v205\n"
"v_cvt_pk_bf16_f32 v215, v206, v207\n"
"v_mfma_f32_16x16x32_bf16 v[192:195], a[140:143], a[76:79], v[192:195]\n"
"buffer_load_dwordx4 v223, s[44:47], s62 offen offset:2048 nt lds\n"
"v_mfma_f32_16x16x32_bf16 v[196:199], a[144:147], a[64:67], v[216:219]\n"
"v_mfma_f32_16x16x32_bf16 v[196:199], a[148:151], a[68:71], v[196:199]\n"
"v_mfma_f32_16x16x32_bf16 v[196:199], a[152:155], a[72:75], v[196:199]\n"
"v_mfma_f32_16x16x32_bf16 v[196:199], a[156:159], a[76:79], v[196:199]\n"
"v_smfmac_f32_16x16x64_bf16 v[228:231], v[240:243], v[208:215], v244\n"
"v_mfma_f32_16x16x32_bf16 v[96:99], a[192:195], v[212:215], v[96:99]\n"
"v_exp_f32 v192, v192\n"
"v_mfma_f32_16x16x32_bf16 v[100:103], a[196:199], v[212:215], v[100:103]\n"
"v_exp_f32 v193, v193\n"
"v_mfma_f32_16x16x32_bf16 v[104:107], a[200:203], v[212:215], v[104:107]\n"
"v_exp_f32 v194, v194\n"
"v_mfma_f32_16x16x32_bf16 v[108:111], a[204:207], v[212:215], v[108:111]\n"
"v_exp_f32 v195, v195\n"
"v_mfma_f32_16x16x32_bf16 v[112:115], a[208:211], v[212:215], v[112:115]\n"
"v_exp_f32 v196, v196\n"
"v_mfma_f32_16x16x32_bf16 v[116:119], a[212:215], v[212:215], v[116:119]\n"
"v_exp_f32 v197, v197\n"
"v_mfma_f32_16x16x32_bf16 v[120:123], a[216:219], v[212:215], v[120:123]\n"
"v_exp_f32 v198, v198\n"
"v_mfma_f32_16x16x32_bf16 v[124:127], a[220:223], v[212:215], v[124:127]\n"
"v_exp_f32 v199, v199\n"
"v_mfma_f32_16x16x32_bf16 v[200:203], a[128:131], a[80:83], v[216:219]\n"
"v_cvt_pk_bf16_f32 v208, v192, v193\n"
"v_cvt_pk_bf16_f32 v209, v194, v195\n"
"v_mfma_f32_16x16x32_bf16 v[200:203], a[132:135], a[84:87], v[200:203]\n"
"v_cvt_pk_bf16_f32 v210, v196, v197\n"
"v_cvt_pk_bf16_f32 v211, v198, v199\n"
"v_mfma_f32_16x16x32_bf16 v[200:203], a[136:139], a[88:91], v[200:203]\n"
"buffer_load_dwordx4 v223, s[44:47], 0 offen offset:3072 nt lds\n"
"v_mfma_f32_16x16x32_bf16 v[200:203], a[140:143], a[92:95], v[200:203]\n"
"v_mfma_f32_16x16x32_bf16 v[204:207], a[144:147], a[80:83], v[216:219]\n"
"v_mfma_f32_16x16x32_bf16 v[204:207], a[148:151], a[84:87], v[204:207]\n"
"v_mfma_f32_16x16x32_bf16 v[204:207], a[152:155], a[88:91], v[204:207]\n"
"v_mfma_f32_16x16x32_bf16 v[204:207], a[156:159], a[92:95], v[204:207]\n"
"v_add_u32 v223, 0x2000, v223\n"
"v_mfma_f32_16x16x32_bf16 v[128:131], a[192:195], v[208:211], v[128:131]\n"
"v_mfma_f32_16x16x32_bf16 v[132:135], a[196:199], v[208:211], v[132:135]\n"
"v_exp_f32 v200, v200\n"
"v_mfma_f32_16x16x32_bf16 v[136:139], a[200:203], v[208:211], v[136:139]\n"
"v_exp_f32 v201, v201\n"
"v_mfma_f32_16x16x32_bf16 v[140:143], a[204:207], v[208:211], v[140:143]\n"
"v_exp_f32 v202, v202\n"
"v_mfma_f32_16x16x32_bf16 v[144:147], a[208:211], v[208:211], v[144:147]\n"
"v_exp_f32 v203, v203\n"
"v_mfma_f32_16x16x32_bf16 v[148:151], a[212:215], v[208:211], v[148:151]\n"
"v_exp_f32 v204, v204\n"
"v_mfma_f32_16x16x32_bf16 v[152:155], a[216:219], v[208:211], v[152:155]\n"
"v_exp_f32 v205, v205\n"
"v_mfma_f32_16x16x32_bf16 v[156:159], a[220:223], v[208:211], v[156:159]\n"
"v_exp_f32 v206, v206\n"
"s_waitcnt vmcnt(8) lgkmcnt(0)\n"
"s_barrier\n"
"s_cmp_lt_u32 s75, 4\n"
"s_cbranch_scc1 Lsl12%=\n"
"s_sleep 2\n"
"s_cmp_lt_u32 s75, 5\n"
"s_cbranch_scc1 Lsl12%=\n"
"s_sleep 2\n"
"s_cmp_lt_u32 s75, 6\n"
"s_cbranch_scc1 Lsl12%=\n"
"s_sleep 1\n"
"Lsl12%=:\n"
"v_mfma_f32_16x16x32_bf16 v[192:195], a[96:99], a[0:3], v[216:219]\n"
"v_exp_f32 v207, v207\n"
"v_mfma_f32_16x16x32_bf16 v[192:195], a[100:103], a[4:7], v[192:195]\n"
"v_cvt_pk_bf16_f32 v212, v200, v201\n"
"v_cvt_pk_bf16_f32 v213, v202, v203\n"
"v_mfma_f32_16x16x32_bf16 v[192:195], a[104:107], a[8:11], v[192:195]\n"
"v_cvt_pk_bf16_f32 v214, v204, v205\n"
"v_cvt_pk_bf16_f32 v215, v206, v207\n"
"v_mfma_f32_16x16x32_bf16 v[192:195], a[108:111], a[12:15], v[192:195]\n"
"s_add_u32 m0, s70, 49152\n"
"v_mfma_f32_16x16x32_bf16 v[196:199], a[112:115], a[0:3], v[216:219]\n"
"buffer_load_dwordx4 v223, s[40:43], s60 offen offset:0 nt lds\n"
"v_mfma_f32_16x16x32_bf16 v[196:199], a[116:119], a[4:7], v[196:199]\n"
"ds_read_b128 a[144:147], v220 offset:20480\n"
"v_mfma_f32_16x16x32_bf16 v[196:199], a[120:123], a[8:11], v[196:199]\n"
"ds_read_b128 a[148:151], v220 offset:21504\n"
"v_mfma_f32_16x16x32_bf16 v[196:199], a[124:127], a[12:15], v[196:199]\n"
"ds_read_b128 a[152:155], v220 offset:22528\n"
"v_smfmac_f32_16x16x64_bf16 v[232:235], v[240:243], v[208:215], v244\n"
"v_mfma_f32_16x16x32_bf16 v[160:163], a[192:195], v[212:215], v[160:163]\n"
"v_exp_f32 v192, v192\n"
"v_mfma_f32_16x16x32_bf16 v[164:167], a[196:199], v[212:215], v[164:167]\n"
"v_exp_f32 v193, v193\n"
"v_mfma_f32_16x16x32_bf16 v[168:171], a[200:203], v[212:215], v[168:171]\n"
"v_exp_f32 v194, v194\n"
"v_mfma_f32_16x16x32_bf16 v[172:175], a[204:207], v[212:215], v[172:175]\n"
"v_exp_f32 v195, v195\n"
"v_mfma_f32_16x16x32_bf16 v[176:179], a[208:211], v[212:215], v[176:179]\n"
"v_exp_f32 v196, v196\n"
"v_mfma_f32_16x16x32_bf16 v[180:183], a[212:215], v[212:215], v[180:183]\n"
"v_exp_f32 v197, v197\n"
"v_mfma_f32_16x16x32_bf16 v[184:187], a[216:219], v[212:215], v[184:187]\n"
"v_exp_f32 v198, v198\n"
"v_mfma_f32_16x16x32_bf16 v[188:191], a[220:223], v[212:215], v[188:191]\n"
"v_exp_f32 v199, v199\n"
"v_mfma_f32_16x16x32_bf16 v[200:203], a[96:99], a[16:19], v[216:219]\n"
"v_cvt_pk_bf16_f32 v208, v192, v193\n"
"v_cvt_pk_bf16_f32 v209, v194, v195\n"
"v_mfma_f32_16x16x32_bf16 v[200:203], a[100:103], a[20:23], v[200:203]\n"
"v_cvt_pk_bf16_f32 v210, v196, v197\n"
"v_cvt_pk_bf16_f32 v211, v198, v199\n"
"v_mfma_f32_16x16x32_bf16 v[200:203], a[104:107], a[24:27], v[200:203]\n"
"buffer_load_dwordx4 v223, s[40:43], s61 offen offset:1024 nt lds\n"
"v_mfma_f32_16x16x32_bf16 v[200:203], a[108:111], a[28:31], v[200:203]\n"
"ds_read_b128 a[156:159], v220 offset:23552\n"
"v_mfma_f32_16x16x32_bf16 v[204:207], a[112:115], a[16:19], v[216:219]\n"
"ds_read_b128 a[128:131], v220 offset:16384\n"
"v_mfma_f32_16x16x32_bf16 v[204:207], a[116:119], a[20:23], v[204:207]\n"
"ds_read_b128 a[132:135], v220 offset:17408\n"
"v_mfma_f32_16x16x32_bf16 v[204:207], a[120:123], a[24:27], v[204:207]\n"
"ds_read_b128 a[136:139], v220 offset:18432\n"
"v_mfma_f32_16x16x32_bf16 v[204:207], a[124:127], a[28:31], v[204:207]\n"
"buffer_load_dwordx4 v223, s[40:43], s62 offen offset:2048 nt lds\n"
"v_mfma_f32_16x16x32_bf16 v[0:3], a[160:163], v[208:211], v[0:3]\n"
"ds_read_b128 a[140:143], v220 offset:19456\n"
"ds_read_b64_tr_b16 a[192:193], v221 offset:16384\n"
"v_mfma_f32_16x16x32_bf16 v[4:7], a[164:167], v[208:211], v[4:7]\n"
"v_exp_f32 v200, v200\n"
"v_mfma_f32_16x16x32_bf16 v[8:11], a[168:171], v[208:211], v[8:11]\n"
"v_exp_f32 v201, v201\n"
"v_mfma_f32_16x16x32_bf16 v[12:15], a[172:175], v[208:211], v[12:15]\n"
"v_exp_f32 v202, v202\n"
"v_mfma_f32_16x16x32_bf16 v[16:19], a[176:179], v[208:211], v[16:19]\n"
"v_exp_f32 v203, v203\n"
"v_mfma_f32_16x16x32_bf16 v[20:23], a[180:183], v[208:211], v[20:23]\n"
"v_exp_f32 v204, v204\n"
"v_mfma_f32_16x16x32_bf16 v[24:27], a[184:187], v[208:211], v[24:27]\n"
"v_exp_f32 v205, v205\n"
"v_mfma_f32_16x16x32_bf16 v[28:31], a[188:191], v[208:211], v[28:31]\n"
"v_exp_f32 v206, v206\n"
"v_mfma_f32_16x16x32_bf16 v[192:195], a[96:99], a[32:35], v[216:219]\n"
"v_exp_f32 v207, v207\n"
"v_mfma_f32_16x16x32_bf16 v[192:195], a[100:103], a[36:39], v[192:195]\n"
"v_cvt_pk_bf16_f32 v212, v200, v201\n"
"v_cvt_pk_bf16_f32 v213, v202, v203\n"
"v_mfma_f32_16x16x32_bf16 v[192:195], a[104:107], a[40:43], v[192:195]\n"
"v_cvt_pk_bf16_f32 v214, v204, v205\n"
"v_cvt_pk_bf16_f32 v215, v206, v207\n"
"v_mfma_f32_16x16x32_bf16 v[192:195], a[108:111], a[44:47], v[192:195]\n"
"buffer_load_dwordx4 v223, s[40:43], 0 offen offset:3072 nt lds\n"
"v_mfma_f32_16x16x32_bf16 v[196:199], a[112:115], a[32:35], v[216:219]\n"
"ds_read_b64_tr_b16 a[194:195], v221 offset:20480\n"
"ds_read_b64_tr_b16 a[196:197], v222 offset:16384\n"
"v_mfma_f32_16x16x32_bf16 v[196:199], a[116:119], a[36:39], v[196:199]\n"
"ds_read_b64_tr_b16 a[198:199], v222 offset:20480\n"
"ds_read_b64_tr_b16 a[200:201], v221 offset:17408\n"
"v_mfma_f32_16x16x32_bf16 v[196:199], a[120:123], a[40:43], v[196:199]\n"
"ds_read_b64_tr_b16 a[202:203], v221 offset:21504\n"
"ds_read_b64_tr_b16 a[204:205], v222 offset:17408\n"
"v_mfma_f32_16x16x32_bf16 v[196:199], a[124:127], a[44:47], v[196:199]\n"
"ds_read_b64_tr_b16 a[206:207], v222 offset:21504\n"
"ds_read_b64_tr_b16 a[208:209], v221 offset:18432\n"
"v_smfmac_f32_16x16x64_bf16 v[224:227], v[240:243], v[208:215], v244\n"
"s_add_u32 m0, s70, 57344\n"
"v_mfma_f32_16x16x32_bf16 v[32:35], a[160:163], v[212:215], v[32:35]\n"
"v_exp_f32 v192, v192\n"
"v_mfma_f32_16x16x32_bf16 v[36:39], a[164:167], v[212:215], v[36:39]\n"
"v_exp_f32 v193, v193\n"
"v_mfma_f32_16x16x32_bf16 v[40:43], a[168:171], v[212:215], v[40:43]\n"
"v_exp_f32 v194, v194\n"
"v_mfma_f32_16x16x32_bf16 v[44:47], a[172:175], v[212:215], v[44:47]\n"
"v_exp_f32 v195, v195\n"
"v_mfma_f32_16x16x32_bf16 v[48:51], a[176:179], v[212:215], v[48:51]\n"
"v_exp_f32 v196, v196\n"
"v_mfma_f32_16x16x32_bf16 v[52:55], a[180:183], v[212:215], v[52:55]\n"
"v_exp_f32 v197, v197\n"
"v_mfma_f32_16x16x32_bf16 v[56:59], a[184:187], v[212:215], v[56:59]\n"
"v_exp_f32 v198, v198\n"
"v_mfma_f32_16x16x32_bf16 v[60:63], a[188:191], v[212:215], v[60:63]\n"
"v_exp_f32 v199, v199\n"
"v_mfma_f32_16x16x32_bf16 v[200:203], a[96:99], a[48:51], v[216:219]\n"
"v_cvt_pk_bf16_f32 v208, v192, v193\n"
"v_cvt_pk_bf16_f32 v209, v194, v195\n"
"v_mfma_f32_16x16x32_bf16 v[200:203], a[100:103], a[52:55], v[200:203]\n"
"v_cvt_pk_bf16_f32 v210, v196, v197\n"
"v_cvt_pk_bf16_f32 v211, v198, v199\n"
"v_mfma_f32_16x16x32_bf16 v[200:203], a[104:107], a[56:59], v[200:203]\n"
"buffer_load_dwordx4 v223, s[44:47], s60 offen offset:0 nt lds\n"
"v_mfma_f32_16x16x32_bf16 v[200:203], a[108:111], a[60:63], v[200:203]\n"
"ds_read_b64_tr_b16 a[210:211], v221 offset:22528\n"
"ds_read_b64_tr_b16 a[212:213], v222 offset:18432\n"
"v_mfma_f32_16x16x32_bf16 v[204:207], a[112:115], a[48:51], v[216:219]\n"
"ds_read_b64_tr_b16 a[214:215], v222 offset:22528\n"
"ds_read_b64_tr_b16 a[216:217], v221 offset:19456\n"
"v_mfma_f32_16x16x32_bf16 v[204:207], a[116:119], a[52:55], v[204:207]\n"
"ds_read_b64_tr_b16 a[218:219], v221 offset:23552\n"
"ds_read_b64_tr_b16 a[220:221], v222 offset:19456\n"
"v_mfma_f32_16x16x32_bf16 v[204:207], a[120:123], a[56:59], v[204:207]\n"
"ds_read_b64_tr_b16 a[222:223], v222 offset:23552\n"
"v_mfma_f32_16x16x32_bf16 v[204:207], a[124:127], a[60:63], v[204:207]\n"
"buffer_load_dwordx4 v223, s[44:47], s61 offen offset:1024 nt lds\n"
"v_mfma_f32_16x16x32_bf16 v[64:67], a[160:163], v[208:211], v[64:67]\n"
"v_mfma_f32_16x16x32_bf16 v[68:71], a[164:167], v[208:211], v[68:71]\n"
"v_exp_f32 v200, v200\n"
"v_mfma_f32_16x16x32_bf16 v[72:75], a[168:171], v[208:211], v[72:75]\n"
"v_exp_f32 v201, v201\n"
"v_mfma_f32_16x16x32_bf16 v[76:79], a[172:175], v[208:211], v[76:79]\n"
"v_exp_f32 v202, v202\n"
"v_mfma_f32_16x16x32_bf16 v[80:83], a[176:179], v[208:211], v[80:83]\n"
"v_exp_f32 v203, v203\n"
"v_mfma_f32_16x16x32_bf16 v[84:87], a[180:183], v[208:211], v[84:87]\n"
"v_exp_f32 v204, v204\n"
"v_mfma_f32_16x16x32_bf16 v[88:91], a[184:187], v[208:211], v[88:91]\n"
"v_exp_f32 v205, v205\n"
"v_mfma_f32_16x16x32_bf16 v[92:95], a[188:191], v[208:211], v[92:95]\n"
"v_exp_f32 v206, v206\n"
"v_mfma_f32_16x16x32_bf16 v[192:195], a[96:99], a[64:67], v[216:219]\n"
"v_exp_f32 v207, v207\n"
"v_mfma_f32_16x16x32_bf16 v[192:195], a[100:103], a[68:71], v[192:195]\n"
"v_cvt_pk_bf16_f32 v212, v200, v201\n"
"v_cvt_pk_bf16_f32 v213, v202, v203\n"
"v_mfma_f32_16x16x32_bf16 v[192:195], a[104:107], a[72:75], v[192:195]\n"
"v_cvt_pk_bf16_f32 v214, v204, v205\n"
"v_cvt_pk_bf16_f32 v215, v206, v207\n"
"v_mfma_f32_16x16x32_bf16 v[192:195], a[108:111], a[76:79], v[192:195]\n"
"buffer_load_dwordx4 v223, s[44:47], s62 offen offset:2048 nt lds\n"
"v_mfma_f32_16x16x32_bf16 v[196:199], a[112:115], a[64:67], v[216:219]\n"
"v_mfma_f32_16x16x32_bf16 v[196:199], a[116:119], a[68:71], v[196:199]\n"
"v_mfma_f32_16x16x32_bf16 v[196:199], a[120:123], a[72:75], v[196:199]\n"
"v_mfma_f32_16x16x32_bf16 v[196:199], a[124:127], a[76:79], v[196:199]\n"
"v_smfmac_f32_16x16x64_bf16 v[228:231], v[240:243], v[208:215], v244\n"
"v_mfma_f32_16x16x32_bf16 v[96:99], a[160:163], v[212:215], v[96:99]\n"
"v_exp_f32 v192, v192\n"
"v_mfma_f32_16x16x32_bf16 v[100:103], a[164:167], v[212:215], v[100:103]\n"
"v_exp_f32 v193, v193\n"
"v_mfma_f32_16x16x32_bf16 v[104:107], a[168:171], v[212:215], v[104:107]\n"
"v_exp_f32 v194, v194\n"
"v_mfma_f32_16x16x32_bf16 v[108:111], a[172:175], v[212:215], v[108:111]\n"
"v_exp_f32 v195, v195\n"
"v_mfma_f32_16x16x32_bf16 v[112:115], a[176:179], v[212:215], v[112:115]\n"
"v_exp_f32 v196, v196\n"
"v_mfma_f32_16x16x32_bf16 v[116:119], a[180:183], v[212:215], v[116:119]\n"
"v_exp_f32 v197, v197\n"
"v_mfma_f32_16x16x32_bf16 v[120:123], a[184:187], v[212:215], v[120:123]\n"
"v_exp_f32 v198, v198\n"
"v_mfma_f32_16x16x32_bf16 v[124:127], a[188:191], v[212:215], v[124:127]\n"
"v_exp_f32 v199, v199\n"
"v_mfma_f32_16x16x32_bf16 v[200:203], a[96:99], a[80:83], v[216:219]\n"
"v_cvt_pk_bf16_f32 v208, v192, v193\n"
"v_cvt_pk_bf16_f32 v209, v194, v195\n"
"v_mfma_f32_16x16x32_bf16 v[200:203], a[100:103], a[84:87], v[200:203]\n"
"v_cvt_pk_bf16_f32 v210, v196, v197\n"
"v_cvt_pk_bf16_f32 v211, v198, v199\n"
"v_mfma_f32_16x16x32_bf16 v[200:203], a[104:107], a[88:91], v[200:203]\n"
"buffer_load_dwordx4 v223, s[44:47], 0 offen offset:3072 nt lds\n"
"v_mfma_f32_16x16x32_bf16 v[200:203], a[108:111], a[92:95], v[200:203]\n"
"v_mfma_f32_16x16x32_bf16 v[204:207], a[112:115], a[80:83], v[216:219]\n"
"v_mfma_f32_16x16x32_bf16 v[204:207], a[116:119], a[84:87], v[204:207]\n"
"v_mfma_f32_16x16x32_bf16 v[204:207], a[120:123], a[88:91], v[204:207]\n"
"v_mfma_f32_16x16x32_bf16 v[204:207], a[124:127], a[92:95], v[204:207]\n"
"v_add_u32 v223, 0x2000, v223\n"
"v_mfma_f32_16x16x32_bf16 v[128:131], a[160:163], v[208:211], v[128:131]\n"
"ds_read_b128 a[96:99], v245 offset:0\n"
"v_mfma_f32_16x16x32_bf16 v[132:135], a[164:167], v[208:211], v[132:135]\n"
"v_exp_f32 v200, v200\n"
"v_mfma_f32_16x16x32_bf16 v[136:139], a[168:171], v[208:211], v[136:139]\n"
"v_exp_f32 v201, v201\n"
"v_mfma_f32_16x16x32_bf16 v[140:143], a[172:175], v[208:211], v[140:143]\n"
"v_exp_f32 v202, v202\n"
"v_mfma_f32_16x16x32_bf16 v[144:147], a[176:179], v[208:211], v[144:147]\n"
"v_exp_f32 v203, v203\n"
"v_mfma_f32_16x16x32_bf16 v[148:151], a[180:183], v[208:211], v[148:151]\n"
"v_exp_f32 v204, v204\n"
"v_mfma_f32_16x16x32_bf16 v[152:155], a[184:187], v[208:211], v[152:155]\n"
"v_exp_f32 v205, v205\n"
"v_mfma_f32_16x16x32_bf16 v[156:159], a[188:191], v[208:211], v[156:159]\n"
"v_exp_f32 v206, v206\n"
"s_waitcnt lgkmcnt(1)\n"
"v_mfma_f32_16x16x32_bf16 v[192:195], a[128:131], a[0:3], v[216:219]\n"
"v_exp_f32 v207, v207\n"
"v_mfma_f32_16x16x32_bf16 v[192:195], a[132:135], a[4:7], v[192:195]\n"
"v_cvt_pk_bf16_f32 v212, v200, v201\n"
"v_cvt_pk_bf16_f32 v213, v202, v203\n"
"v_mfma_f32_16x16x32_bf16 v[192:195], a[136:139], a[8:11], v[192:195]\n"
"v_cvt_pk_bf16_f32 v214, v204, v205\n"
"v_cvt_pk_bf16_f32 v215, v206, v207\n"
"v_mfma_f32_16x16x32_bf16 v[192:195], a[140:143], a[12:15], v[192:195]\n"
"ds_read_b128 a[100:103], v245 offset:1024\n"
"v_mfma_f32_16x16x32_bf16 v[196:199], a[144:147], a[0:3], v[216:219]\n"
"ds_read_b128 a[104:107], v245 offset:2048\n"
"v_mfma_f32_16x16x32_bf16 v[196:199], a[148:151], a[4:7], v[196:199]\n"
"ds_read_b128 a[108:111], v245 offset:3072\n"
"v_mfma_f32_16x16x32_bf16 v[196:199], a[152:155], a[8:11], v[196:199]\n"
"v_mfma_f32_16x16x32_bf16 v[196:199], a[156:159], a[12:15], v[196:199]\n"
"s_waitcnt vmcnt(8) lgkmcnt(0)\n"
"s_barrier\n"
"s_cmp_lt_u32 s75, 4\n"
"s_cbranch_scc1 Lsl13%=\n"
"s_sleep 2\n"
"s_cmp_lt_u32 s75, 5\n"
"s_cbranch_scc1 Lsl13%=\n"
"s_sleep 2\n"
"s_cmp_lt_u32 s75, 6\n"
"s_cbranch_scc1 Lsl13%=\n"
"s_sleep 1\n"
"Lsl13%=:\n"
"v_smfmac_f32_16x16x64_bf16 v[232:235], v[240:243], v[208:215], v244\n"
"s_add_u32 m0, s70, 0\n"
"v_mfma_f32_16x16x32_bf16 v[160:163], a[160:163], v[212:215], v[160:163]\n"
"v_exp_f32 v192, v192\n"
"v_mfma_f32_16x16x32_bf16 v[164:167], a[164:167], v[212:215], v[164:167]\n"
"v_exp_f32 v193, v193\n"
"v_mfma_f32_16x16x32_bf16 v[168:171], a[168:171], v[212:215], v[168:171]\n"
"v_exp_f32 v194, v194\n"
"v_mfma_f32_16x16x32_bf16 v[172:175], a[172:175], v[212:215], v[172:175]\n"
"v_exp_f32 v195, v195\n"
"v_mfma_f32_16x16x32_bf16 v[176:179], a[176:179], v[212:215], v[176:179]\n"
"v_exp_f32 v196, v196\n"
"v_mfma_f32_16x16x32_bf16 v[180:183], a[180:183], v[212:215], v[180:183]\n"
"v_exp_f32 v197, v197\n"
"v_mfma_f32_16x16x32_bf16 v[184:187], a[184:187], v[212:215], v[184:187]\n"
"v_exp_f32 v198, v198\n"
"v_mfma_f32_16x16x32_bf16 v[188:191], a[188:191], v[212:215], v[188:191]\n"
"v_exp_f32 v199, v199\n"
"v_mfma_f32_16x16x32_bf16 v[200:203], a[128:131], a[16:19], v[216:219]\n"
"v_cvt_pk_bf16_f32 v208, v192, v193\n"
"v_cvt_pk_bf16_f32 v209, v194, v195\n"
"v_mfma_f32_16x16x32_bf16 v[200:203], a[132:135], a[20:23], v[200:203]\n"
"v_cvt_pk_bf16_f32 v210, v196, v197\n"
"v_cvt_pk_bf16_f32 v211, v198, v199\n"
"v_mfma_f32_16x16x32_bf16 v[200:203], a[136:139], a[24:27], v[200:203]\n"
"buffer_load_dwordx4 v223, s[40:43], s60 offen offset:0 nt lds\n"
"v_mfma_f32_16x16x32_bf16 v[200:203], a[140:143], a[28:31], v[200:203]\n"
"ds_read_b128 a[112:115], v220 offset:36864\n"
"v_mfma_f32_16x16x32_bf16 v[204:207], a[144:147], a[16:19], v[216:219]\n"
"ds_read_b128 a[116:119], v220 offset:37888\n"
"v_mfma_f32_16x16x32_bf16 v[204:207], a[148:151], a[20:23], v[204:207]\n"
"ds_read_b128 a[120:123], v220 offset:38912\n"
"v_mfma_f32_16x16x32_bf16 v[204:207], a[152:155], a[24:27], v[204:207]\n"
"ds_read_b128 a[124:127], v220 offset:39936\n"
"ds_read_b64_tr_b16 a[160:161], v221 offset:32768\n"
"v_mfma_f32_16x16x32_bf16 v[204:207], a[156:159], a[28:31], v[204:207]\n"
"buffer_load_dwordx4 v223, s[40:43], s61 offen offset:1024 nt lds\n"
"v_mfma_f32_16x16x32_bf16 v[0:3], a[192:195], v[208:211], v[0:3]\n"
"ds_read_b64_tr_b16 a[162:163], v221 offset:36864\n"
"ds_read_b64_tr_b16 a[164:165], v222 offset:32768\n"
"v_mfma_f32_16x16x32_bf16 v[4:7], a[196:199], v[208:211], v[4:7]\n"
"v_exp_f32 v200, v200\n"
"v_mfma_f32_16x16x32_bf16 v[8:11], a[200:203], v[208:211], v[8:11]\n"
"v_exp_f32 v201, v201\n"
"v_mfma_f32_16x16x32_bf16 v[12:15], a[204:207], v[208:211], v[12:15]\n"
"v_exp_f32 v202, v202\n"
"v_mfma_f32_16x16x32_bf16 v[16:19], a[208:211], v[208:211], v[16:19]\n"
"v_exp_f32 v203, v203\n"
"v_mfma_f32_16x16x32_bf16 v[20:23], a[212:215], v[208:211], v[20:23]\n"
"v_exp_f32 v204, v204\n"
"v_mfma_f32_16x16x32_bf16 v[24:27], a[216:219], v[208:211], v[24:27]\n"
"v_exp_f32 v205, v205\n"
"v_mfma_f32_16x16x32_bf16 v[28:31], a[220:223], v[208:211], v[28:31]\n"
"v_exp_f32 v206, v206\n"
"v_mfma_f32_16x16x32_bf16 v[192:195], a[128:131], a[96:99], v[216:219]\n"
"v_exp_f32 v207, v207\n"
"v_mfma_f32_16x16x32_bf16 v[192:195], a[132:135], a[100:103], v[192:195]\n"
"v_cvt_pk_bf16_f32 v212, v200, v201\n"
"v_cvt_pk_bf16_f32 v213, v202, v203\n"
"v_mfma_f32_16x16x32_bf16 v[192:195], a[136:139], a[104:107], v[192:195]\n"
"v_cvt_pk_bf16_f32 v214, v204, v205\n"
"v_cvt_pk_bf16_f32 v215, v206, v207\n"
"v_mfma_f32_16x16x32_bf16 v[192:195], a[140:143], a[108:111], v[192:195]\n"
"buffer_load_dwordx4 v223, s[40:43], s62 offen offset:2048 nt lds\n"
"v_mfma_f32_16x16x32_bf16 v[196:199], a[144:147], a[96:99], v[216:219]\n"
"ds_read_b64_tr_b16 a[166:167], v222 offset:36864\n"
"ds_read_b64_tr_b16 a[168:169], v221 offset:33792\n"
"v_mfma_f32_16x16x32_bf16 v[196:199], a[148:151], a[100:103], v[196:199]\n"
"ds_read_b64_tr_b16 a[170:171], v221 offset:37888\n"
"ds_read_b64_tr_b16 a[172:173], v222 offset:33792\n"
"v_mfma_f32_16x16x32_bf16 v[196:199], a[152:155], a[104:107], v[196:199]\n"
"ds_read_b64_tr_b16 a[174:175], v222 offset:37888\n"
"v_mfma_f32_16x16x32_bf16 v[196:199], a[156:159], a[108:111], v[196:199]\n"
"ds_read_b128 a[96:99], v220 offset:32768\n"
"v_smfmac_f32_16x16x64_bf16 v[224:227], v[240:243], v[208:215], v244\n"
"v_mfma_f32_16x16x32_bf16 v[32:35], a[192:195], v[212:215], v[32:35]\n"
"v_exp_f32 v192, v192\n"
"v_mfma_f32_16x16x32_bf16 v[36:39], a[196:199], v[212:215], v[36:39]\n"
"v_exp_f32 v193, v193\n"
"v_mfma_f32_16x16x32_bf16 v[40:43], a[200:203], v[212:215], v[40:43]\n"
"v_exp_f32 v194, v194\n"
"v_mfma_f32_16x16x32_bf16 v[44:47], a[204:207], v[212:215], v[44:47]\n"
"v_exp_f32 v195, v195\n"
"v_mfma_f32_16x16x32_bf16 v[48:51], a[208:211], v[212:215], v[48:51]\n"
"v_exp_f32 v196, v196\n"
"v_mfma_f32_16x16x32_bf16 v[52:55], a[212:215], v[212:215], v[52:55]\n"
"v_exp_f32 v197, v197\n"
"v_mfma_f32_16x16x32_bf16 v[56:59], a[216:219], v[212:215], v[56:59]\n"
"v_exp_f32 v198, v198\n"
"v_mfma_f32_16x16x32_bf16 v[60:63], a[220:223], v[212:215], v[60:63]\n"
"v_exp_f32 v199, v199\n"
"v_mfma_f32_16x16x32_bf16 v[200:203], a[128:131], a[32:35], v[216:219]\n"
"v_cvt_pk_bf16_f32 v212, v192, v193\n"
"v_cvt_pk_bf16_f32 v213, v194, v195\n"
"v_mfma_f32_16x16x32_bf16 v[200:203], a[132:135], a[36:39], v[200:203]\n"
"v_cvt_pk_bf16_f32 v214, v196, v197\n"
"v_cvt_pk_bf16_f32 v215, v198, v199\n"
"v_mfma_f32_16x16x32_bf16 v[200:203], a[136:139], a[40:43], v[200:203]\n"
"buffer_load_dwordx4 v223, s[40:43], 0 offen offset:3072 nt lds\n"
"v_mfma_f32_16x16x32_bf16 v[200:203], a[140:143], a[44:47], v[200:203]\n"
"ds_read_b128 a[100:103], v220 offset:33792\n"
"v_mfma_f32_16x16x32_bf16 v[204:207], a[144:147], a[32:35], v[216:219]\n"
"ds_read_b128 a[104:107], v220 offset:34816\n"
"v_mfma_f32_16x16x32_bf16 v[204:207], a[148:151], a[36:39], v[204:207]\n"
"ds_read_b128 a[108:111], v220 offset:35840\n"
"ds_read_b64_tr_b16 a[176:177], v221 offset:34816\n"
"v_mfma_f32_16x16x32_bf16 v[204:207], a[152:155], a[40:43], v[204:207]\n"
"ds_read_b64_tr_b16 a[178:179], v221 offset:38912\n"
"ds_read_b64_tr_b16 a[180:181], v222 offset:34816\n"
"v_mfma_f32_16x16x32_bf16 v[204:207], a[156:159], a[44:47], v[204:207]\n"
"s_add_u32 m0, s70, 8192\n"
"ds_read_b64_tr_b16 a[182:183], v222 offset:38912\n"
"v_smfmac_f32_16x16x64_bf16 v[236:239], v[240:243], v[208:215], v244\n"
"ds_read_b64_tr_b16 a[184:185], v221 offset:35840\n"
"v_mfma_f32_16x16x32_bf16 a[224:227], a[192:195], v[212:215], a[224:227]\n"
"v_exp_f32 v200, v200\n"
"v_mfma_f32_16x16x32_bf16 a[228:231], a[196:199], v[212:215], a[228:231]\n"
"v_exp_f32 v201, v201\n"
"v_mfma_f32_16x16x32_bf16 a[232:235], a[200:203], v[212:215], a[232:235]\n"
"v_exp_f32 v202, v202\n"
"v_mfma_f32_16x16x32_bf16 a[236:239], a[204:207], v[212:215], a[236:239]\n"
"v_exp_f32 v203, v203\n"
"v_mfma_f32_16x16x32_bf16 a[240:243], a[208:211], v[212:215], a[240:243]\n"
"v_exp_f32 v204, v204\n"
"v_mfma_f32_16x16x32_bf16 a[244:247], a[212:215], v[212:215], a[244:247]\n"
"v_exp_f32 v205, v205\n"
"v_mfma_f32_16x16x32_bf16 a[248:251], a[216:219], v[212:215], a[248:251]\n"
"v_exp_f32 v206, v206\n"
"v_mfma_f32_16x16x32_bf16 a[252:255], a[220:223], v[212:215], a[252:255]\n"
"v_exp_f32 v207, v207\n"
"v_mfma_f32_16x16x32_bf16 v[192:195], a[128:131], a[48:51], v[216:219]\n"
"v_cvt_pk_bf16_f32 v208, v200, v201\n"
"v_cvt_pk_bf16_f32 v209, v202, v203\n"
"v_mfma_f32_16x16x32_bf16 v[192:195], a[132:135], a[52:55], v[192:195]\n"
"v_cvt_pk_bf16_f32 v210, v204, v205\n"
"v_cvt_pk_bf16_f32 v211, v206, v207\n"
"v_mfma_f32_16x16x32_bf16 v[192:195], a[136:139], a[56:59], v[192:195]\n"
"buffer_load_dwordx4 v223, s[44:47], s60 offen offset:0 nt lds\n"
"v_mfma_f32_16x16x32_bf16 v[192:195], a[140:143], a[60:63], v[192:195]\n"
"ds_read_b64_tr_b16 a[186:187], v221 offset:39936\n"
"ds_read_b64_tr_b16 a[188:189], v222 offset:35840\n"
"v_mfma_f32_16x16x32_bf16 v[196:199], a[144:147], a[48:51], v[216:219]\n"
"ds_read_b64_tr_b16 a[190:191], v222 offset:39936\n"
"v_mfma_f32_16x16x32_bf16 v[196:199], a[148:151], a[52:55], v[196:199]\n"
"v_mfma_f32_16x16x32_bf16 v[196:199], a[152:155], a[56:59], v[196:199]\n"
"v_mfma_f32_16x16x32_bf16 v[196:199], a[156:159], a[60:63], v[196:199]\n"
"buffer_load_dwordx4 v223, s[44:47], s61 offen offset:1024 nt lds\n"
"v_mfma_f32_16x16x32_bf16 v[64:67], a[192:195], v[208:211], v[64:67]\n"
"v_mfma_f32_16x16x32_bf16 v[68:71], a[196:199], v[208:211], v[68:71]\n"
"v_exp_f32 v192, v192\n"
"v_mfma_f32_16x16x32_bf16 v[72:75], a[200:203], v[208:211], v[72:75]\n"
"v_exp_f32 v193, v193\n"
"v_mfma_f32_16x16x32_bf16 v[76:79], a[204:207], v[208:211], v[76:79]\n"
"v_exp_f32 v194, v194\n"
"v_mfma_f32_16x16x32_bf16 v[80:83], a[208:211], v[208:211], v[80:83]\n"
"v_exp_f32 v195, v195\n"
"v_mfma_f32_16x16x32_bf16 v[84:87], a[212:215], v[208:211], v[84:87]\n"
"v_exp_f32 v196, v196\n"
"v_mfma_f32_16x16x32_bf16 v[88:91], a[216:219], v[208:211], v[88:91]\n"
"v_exp_f32 v197, v197\n"
"v_mfma_f32_16x16x32_bf16 v[92:95], a[220:223], v[208:211], v[92:95]\n"
"v_exp_f32 v198, v198\n"
"v_mfma_f32_16x16x32_bf16 v[200:203], a[128:131], a[64:67], v[216:219]\n"
"v_exp_f32 v199, v199\n"
"v_mfma_f32_16x16x32_bf16 v[200:203], a[132:135], a[68:71], v[200:203]\n"
"v_cvt_pk_bf16_f32 v212, v192, v193\n"
"v_cvt_pk_bf16_f32 v213, v194, v195\n"
"v_mfma_f32_16x16x32_bf16 v[200:203], a[136:139], a[72:75], v[200:203]\n"
"v_cvt_pk_bf16_f32 v214, v196, v197\n"
"v_cvt_pk_bf16_f32 v215, v198, v199\n"
"v_mfma_f32_16x16x32_bf16 v[200:203], a[140:143], a[76:79], v[200:203]\n"
"buffer_load_dwordx4 v223, s[44:47], s62 offen offset:2048 nt lds\n"
"v_mfma_f32_16x16x32_bf16 v[204:207], a[144:147], a[64:67], v[216:219]\n"
"v_mfma_f32_16x16x32_bf16 v[204:207], a[148:151], a[68:71], v[204:207]\n"
"v_mfma_f32_16x16x32_bf16 v[204:207], a[152:155], a[72:75], v[204:207]\n"
"v_mfma_f32_16x16x32_bf16 v[204:207], a[156:159], a[76:79], v[204:207]\n"
"v_smfmac_f32_16x16x64_bf16 v[228:231], v[240:243], v[208:215], v244\n"
"v_mfma_f32_16x16x32_bf16 v[96:99], a[192:195], v[212:215], v[96:99]\n"
"v_exp_f32 v200, v200\n"
"v_mfma_f32_16x16x32_bf16 v[100:103], a[196:199], v[212:215], v[100:103]\n"
"v_exp_f32 v201, v201\n"
"v_mfma_f32_16x16x32_bf16 v[104:107], a[200:203], v[212:215], v[104:107]\n"
"v_exp_f32 v202, v202\n"
"v_mfma_f32_16x16x32_bf16 v[108:111], a[204:207], v[212:215], v[108:111]\n"
"v_exp_f32 v203, v203\n"
"v_mfma_f32_16x16x32_bf16 v[112:115], a[208:211], v[212:215], v[112:115]\n"
"v_exp_f32 v204, v204\n"
"v_mfma_f32_16x16x32_bf16 v[116:119], a[212:215], v[212:215], v[116:119]\n"
"v_exp_f32 v205, v205\n"
"v_mfma_f32_16x16x32_bf16 v[120:123], a[216:219], v[212:215], v[120:123]\n"
"v_exp_f32 v206, v206\n"
"v_mfma_f32_16x16x32_bf16 v[124:127], a[220:223], v[212:215], v[124:127]\n"
"v_exp_f32 v207, v207\n"
"v_mfma_f32_16x16x32_bf16 v[192:195], a[128:131], a[80:83], v[216:219]\n"
"v_cvt_pk_bf16_f32 v208, v200, v201\n"
"v_cvt_pk_bf16_f32 v209, v202, v203\n"
"v_mfma_f32_16x16x32_bf16 v[192:195], a[132:135], a[84:87], v[192:195]\n"
"v_cvt_pk_bf16_f32 v210, v204, v205\n"
"v_cvt_pk_bf16_f32 v211, v206, v207\n"
"v_mfma_f32_16x16x32_bf16 v[192:195], a[136:139], a[88:91], v[192:195]\n"
"buffer_load_dwordx4 v223, s[44:47], 0 offen offset:3072 nt lds\n"
"v_mfma_f32_16x16x32_bf16 v[192:195], a[140:143], a[92:95], v[192:195]\n"
"v_mfma_f32_16x16x32_bf16 v[196:199], a[144:147], a[80:83], v[216:219]\n"
"v_mfma_f32_16x16x32_bf16 v[196:199], a[148:151], a[84:87], v[196:199]\n"
"v_mfma_f32_16x16x32_bf16 v[196:199], a[152:155], a[88:91], v[196:199]\n"
"v_mfma_f32_16x16x32_bf16 v[196:199], a[156:159], a[92:95], v[196:199]\n"
"v_add_u32 v223, 0x2000, v223\n"
"v_mfma_f32_16x16x32_bf16 v[128:131], a[192:195], v[208:211], v[128:131]\n"
"v_mfma_f32_16x16x32_bf16 v[132:135], a[196:199], v[208:211], v[132:135]\n"
"v_exp_f32 v192, v192\n"
"v_mfma_f32_16x16x32_bf16 v[136:139], a[200:203], v[208:211], v[136:139]\n"
"v_exp_f32 v193, v193\n"
"v_mfma_f32_16x16x32_bf16 v[140:143], a[204:207], v[208:211], v[140:143]\n"
"v_exp_f32 v194, v194\n"
"v_mfma_f32_16x16x32_bf16 v[144:147], a[208:211], v[208:211], v[144:147]\n"
"v_exp_f32 v195, v195\n"
"v_mfma_f32_16x16x32_bf16 v[148:151], a[212:215], v[208:211], v[148:151]\n"
"v_exp_f32 v196, v196\n"
"v_mfma_f32_16x16x32_bf16 v[152:155], a[216:219], v[208:211], v[152:155]\n"
"v_exp_f32 v197, v197\n"
"v_mfma_f32_16x16x32_bf16 v[156:159], a[220:223], v[208:211], v[156:159]\n"
"v_exp_f32 v198, v198\n"
"s_waitcnt vmcnt(8) lgkmcnt(0)\n"
"s_barrier\n"
"s_cmp_lt_u32 s75, 4\n"
"s_cbranch_scc1 Lsl14%=\n"
"s_sleep 2\n"
"s_cmp_lt_u32 s75, 5\n"
"s_cbranch_scc1 Lsl14%=\n"
"s_sleep 2\n"
"s_cmp_lt_u32 s75, 6\n"
"s_cbranch_scc1 Lsl14%=\n"
"s_sleep 1\n"
"Lsl14%=:\n"
"s_sub_u32 s75, s75, 1\n"
"s_cbranch_scc0 La1%=\n"
"v_mfma_f32_16x16x32_bf16 v[200:203], a[96:99], a[0:3], v[216:219]\n"
"v_exp_f32 v199, v199\n"
"s_mov_b64 s[48:49], %8\n"
"s_mov_b32 s50, 8388608\n"
"v_mfma_f32_16x16x32_bf16 v[200:203], a[100:103], a[4:7], v[200:203]\n"
"v_cvt_pk_bf16_f32 v212, v192, v193\n"
"v_cvt_pk_bf16_f32 v213, v194, v195\n"
"s_and_b32 s72, s78, 7\n"
"s_and_b32 s80, s76, 1\n"
"v_mfma_f32_16x16x32_bf16 v[200:203], a[104:107], a[8:11], v[200:203]\n"
"v_cvt_pk_bf16_f32 v214, v196, v197\n"
"v_cvt_pk_bf16_f32 v215, v198, v199\n"
"s_lshr_b32 s81, s76, 1\n"
"s_lshl_b32 s66, s80, 15\n"
"v_mfma_f32_16x16x32_bf16 v[200:203], a[108:111], a[12:15], v[200:203]\n"
"ds_read_b128 a[128:131], v220 offset:49152\n"
"ds_read_b128 a[132:135], v220 offset:50176\n"
"s_lshl_b32 s82, s72, 8\n"
"s_add_u32 s66, s66, s82\n"
"v_mfma_f32_16x16x32_bf16 v[204:207], a[112:115], a[0:3], v[216:219]\n"
"ds_read_b128 a[136:139], v220 offset:51200\n"
"ds_read_b128 a[140:143], v220 offset:52224\n"
"s_add_u32 s67, s66, 8192\n"
"s_add_u32 s68, s67, 8192\n"
"v_mfma_f32_16x16x32_bf16 v[204:207], a[116:119], a[4:7], v[204:207]\n"
"ds_read_b128 a[144:147], v220 offset:53248\n"
"ds_read_b128 a[148:151], v220 offset:54272\n"
"s_add_u32 s69, s68, 8192\n"
"s_mul_i32 s71, s81, 67584\n"
"v_mfma_f32_16x16x32_bf16 v[204:207], a[120:123], a[8:11], v[204:207]\n"
"ds_read_b128 a[152:155], v220 offset:55296\n"
"ds_read_b128 a[156:159], v220 offset:56320\n"
"s_mul_i32 s80, s80, 4224\n"
"s_add_u32 s71, s71, s80\n"
"v_mfma_f32_16x16x32_bf16 v[204:207], a[124:127], a[12:15], v[204:207]\n"
"s_lshl_b32 s82, s79, 18\n"
"v_lshrrev_b32 v249, 8, v255\n"
"v_lshlrev_b32 v248, 11, v249\n"
"v_smfmac_f32_16x16x64_bf16 v[232:235], v[240:243], v[208:215], v244\n"
"v_lshlrev_b32 v249, 6, v249\n"
"v_mfma_f32_16x16x32_bf16 v[160:163], a[192:195], v[212:215], v[160:163]\n"
"v_exp_f32 v200, v200\n"
"v_mfma_f32_16x16x32_bf16 v[164:167], a[196:199], v[212:215], v[164:167]\n"
"v_exp_f32 v201, v201\n"
"v_mfma_f32_16x16x32_bf16 v[168:171], a[200:203], v[212:215], v[168:171]\n"
"v_exp_f32 v202, v202\n"
"v_mfma_f32_16x16x32_bf16 v[172:175], a[204:207], v[212:215], v[172:175]\n"
"v_exp_f32 v203, v203\n"
"v_mfma_f32_16x16x32_bf16 v[176:179], a[208:211], v[212:215], v[176:179]\n"
"v_exp_f32 v204, v204\n"
"v_mfma_f32_16x16x32_bf16 v[180:183], a[212:215], v[212:215], v[180:183]\n"
"v_exp_f32 v205, v205\n"
"v_mfma_f32_16x16x32_bf16 v[184:187], a[216:219], v[212:215], v[184:187]\n"
"v_exp_f32 v206, v206\n"
"v_mfma_f32_16x16x32_bf16 v[188:191], a[220:223], v[212:215], v[188:191]\n"
"v_exp_f32 v207, v207\n"
"v_mfma_f32_16x16x32_bf16 v[192:195], a[96:99], a[16:19], v[216:219]\n"
"v_cvt_pk_bf16_f32 v208, v200, v201\n"
"v_cvt_pk_bf16_f32 v209, v202, v203\n"
"v_mfma_f32_16x16x32_bf16 v[192:195], a[100:103], a[20:23], v[192:195]\n"
"v_cvt_pk_bf16_f32 v210, v204, v205\n"
"v_cvt_pk_bf16_f32 v211, v206, v207\n"
"v_mfma_f32_16x16x32_bf16 v[192:195], a[104:107], a[24:27], v[192:195]\n"
"ds_read_b64_tr_b16 a[192:193], v221 offset:49152\n"
"ds_read_b64_tr_b16 a[194:195], v221 offset:53248\n"
"v_mfma_f32_16x16x32_bf16 v[192:195], a[108:111], a[28:31], v[192:195]\n"
"ds_read_b64_tr_b16 a[196:197], v222 offset:49152\n"
"ds_read_b64_tr_b16 a[198:199], v222 offset:53248\n"
"v_mfma_f32_16x16x32_bf16 v[196:199], a[112:115], a[16:19], v[216:219]\n"
"ds_read_b64_tr_b16 a[200:201], v221 offset:50176\n"
"ds_read_b64_tr_b16 a[202:203], v221 offset:54272\n"
"v_mfma_f32_16x16x32_bf16 v[196:199], a[116:119], a[20:23], v[196:199]\n"
"ds_read_b64_tr_b16 a[204:205], v222 offset:50176\n"
"ds_read_b64_tr_b16 a[206:207], v222 offset:54272\n"
"v_mfma_f32_16x16x32_bf16 v[196:199], a[120:123], a[24:27], v[196:199]\n"
"ds_read_b64_tr_b16 a[208:209], v221 offset:51200\n"
"ds_read_b64_tr_b16 a[210:211], v221 offset:55296\n"
"v_mfma_f32_16x16x32_bf16 v[196:199], a[124:127], a[28:31], v[196:199]\n"
"ds_read_b64_tr_b16 a[212:213], v222 offset:51200\n"
"ds_read_b64_tr_b16 a[214:215], v222 offset:55296\n"
"v_mfma_f32_16x16x32_bf16 v[0:3], a[160:163], v[208:211], v[0:3]\n"
"ds_read_b64_tr_b16 a[216:217], v221 offset:52224\n"
"ds_read_b64_tr_b16 a[218:219], v221 offset:56320\n"
"v_mfma_f32_16x16x32_bf16 v[4:7], a[164:167], v[208:211], v[4:7]\n"
"v_exp_f32 v192, v192\n"
"v_mfma_f32_16x16x32_bf16 v[8:11], a[168:171], v[208:211], v[8:11]\n"
"v_exp_f32 v193, v193\n"
"v_mfma_f32_16x16x32_bf16 v[12:15], a[172:175], v[208:211], v[12:15]\n"
"v_exp_f32 v194, v194\n"
"v_mfma_f32_16x16x32_bf16 v[16:19], a[176:179], v[208:211], v[16:19]\n"
"v_exp_f32 v195, v195\n"
"v_mfma_f32_16x16x32_bf16 v[20:23], a[180:183], v[208:211], v[20:23]\n"
"v_exp_f32 v196, v196\n"
"v_mfma_f32_16x16x32_bf16 v[24:27], a[184:187], v[208:211], v[24:27]\n"
"v_exp_f32 v197, v197\n"
"v_mfma_f32_16x16x32_bf16 v[28:31], a[188:191], v[208:211], v[28:31]\n"
"v_exp_f32 v198, v198\n"
"s_waitcnt lgkmcnt(14)\n"
"v_mfma_f32_16x16x32_bf16 v[200:203], a[128:131], a[0:3], v[216:219]\n"
"v_exp_f32 v199, v199\n"
"v_mfma_f32_16x16x32_bf16 v[200:203], a[132:135], a[4:7], v[200:203]\n"
"v_cvt_pk_bf16_f32 v212, v192, v193\n"
"v_cvt_pk_bf16_f32 v213, v194, v195\n"
"v_mfma_f32_16x16x32_bf16 v[200:203], a[136:139], a[8:11], v[200:203]\n"
"v_cvt_pk_bf16_f32 v214, v196, v197\n"
"v_cvt_pk_bf16_f32 v215, v198, v199\n"
"v_mfma_f32_16x16x32_bf16 v[200:203], a[140:143], a[12:15], v[200:203]\n"
"ds_read_b64_tr_b16 a[220:221], v222 offset:52224\n"
"ds_read_b64_tr_b16 a[222:223], v222 offset:56320\n"
"v_mfma_f32_16x16x32_bf16 v[204:207], a[144:147], a[0:3], v[216:219]\n"
"v_add_u32 v248, s82, v248\n"
"v_xor_b32 v249, v249, v255\n"
"v_mfma_f32_16x16x32_bf16 v[204:207], a[148:151], a[4:7], v[204:207]\n"
"v_and_b32 v249, 0xf0, v249\n"
"v_add_u32 v248, v248, v249\n"
"s_lshl_b32 s82, s79, 12\n"
"v_mfma_f32_16x16x32_bf16 v[204:207], a[152:155], a[8:11], v[204:207]\n"
"v_add_u32 v249, s82, v255\n"
"s_and_b32 s80, s76, 1\n"
"s_mul_i32 s82, s77, 13\n"
"v_mfma_f32_16x16x32_bf16 v[204:207], a[156:159], a[12:15], v[204:207]\n"
"ds_read_b128 a[0:3], v245 offset:0\n"
"ds_read_b128 a[4:7], v245 offset:1024\n"
"s_mul_i32 s81, s80, 6\n"
"s_add_u32 s81, s81, s82\n"
"v_smfmac_f32_16x16x64_bf16 v[224:227], v[240:243], v[208:215], v244\n"
"ds_read_b128 a[8:11], v245 offset:2048\n"
"s_mul_i32 s60, s81, 132096\n"
"s_add_u32 s61, s60, 132096\n"
"v_mfma_f32_16x16x32_bf16 v[32:35], a[160:163], v[212:215], v[32:35]\n"
"v_exp_f32 v200, v200\n"
"s_add_u32 s62, s61, 132096\n"
"s_add_u32 s63, s62, 132096\n"
"v_mfma_f32_16x16x32_bf16 v[36:39], a[164:167], v[212:215], v[36:39]\n"
"v_exp_f32 v201, v201\n"
"s_add_u32 s64, s63, 132096\n"
"s_add_u32 s65, s64, 132096\n"
"v_mfma_f32_16x16x32_bf16 v[40:43], a[168:171], v[212:215], v[40:43]\n"
"v_exp_f32 v202, v202\n"
"s_add_u32 s82, s82, 12\n"
"s_mul_i32 s70, s82, 132096\n"
"s_waitcnt vmcnt(0)\n"
"s_barrier\n"
"v_mfma_f32_16x16x32_bf16 v[44:47], a[172:175], v[212:215], v[44:47]\n"
"v_exp_f32 v203, v203\n"
"s_add_u32 m0, s71, 0\n"
"v_mfma_f32_16x16x32_bf16 v[48:51], a[176:179], v[212:215], v[48:51]\n"
"v_exp_f32 v204, v204\n"
"buffer_load_dwordx4 v248, s[48:51], s66 offen  lds\n"
"v_mfma_f32_16x16x32_bf16 v[52:55], a[180:183], v[212:215], v[52:55]\n"
"v_exp_f32 v205, v205\n"
"v_mfma_f32_16x16x32_bf16 v[56:59], a[184:187], v[212:215], v[56:59]\n"
"v_exp_f32 v206, v206\n"
"s_add_u32 m0, s71, 1056\n"
"v_mfma_f32_16x16x32_bf16 v[60:63], a[188:191], v[212:215], v[60:63]\n"
"v_exp_f32 v207, v207\n"
"buffer_load_dwordx4 v248, s[48:51], s67 offen  lds\n"
"v_mfma_f32_16x16x32_bf16 v[192:195], a[128:131], a[16:19], v[216:219]\n"
"v_cvt_pk_bf16_f32 v208, v200, v201\n"
"v_cvt_pk_bf16_f32 v209, v202, v203\n"
"v_mfma_f32_16x16x32_bf16 v[192:195], a[132:135], a[20:23], v[192:195]\n"
"v_cvt_pk_bf16_f32 v210, v204, v205\n"
"v_cvt_pk_bf16_f32 v211, v206, v207\n"
"s_add_u32 m0, s71, 2112\n"
"v_mfma_f32_16x16x32_bf16 v[192:195], a[136:139], a[24:27], v[192:195]\n"
"ds_read_b128 a[12:15], v245 offset:3072\n"
"buffer_load_dwordx4 v248, s[48:51], s68 offen  lds\n"
"v_mfma_f32_16x16x32_bf16 v[192:195], a[140:143], a[28:31], v[192:195]\n"
"v_mfma_f32_16x16x32_bf16 v[196:199], a[144:147], a[16:19], v[216:219]\n"
"s_add_u32 m0, s71, 3168\n"
"v_mfma_f32_16x16x32_bf16 v[196:199], a[148:151], a[20:23], v[196:199]\n"
"buffer_load_dwordx4 v248, s[48:51], s69 offen  lds\n"
"v_mfma_f32_16x16x32_bf16 v[196:199], a[152:155], a[24:27], v[196:199]\n"
"v_mfma_f32_16x16x32_bf16 v[196:199], a[156:159], a[28:31], v[196:199]\n"
"v_xor_b32 v248, 0x10000, v248\n"
"s_add_u32 m0, s71, 8448\n"
"s_waitcnt lgkmcnt(4)\n"
"v_mfma_f32_16x16x32_bf16 v[0:3], a[192:195], v[208:211], v[0:3]\n"
"buffer_load_dwordx4 v248, s[48:51], s66 offen  lds\n"
"v_mfma_f32_16x16x32_bf16 v[4:7], a[196:199], v[208:211], v[4:7]\n"
"v_exp_f32 v192, v192\n"
"v_mfma_f32_16x16x32_bf16 v[8:11], a[200:203], v[208:211], v[8:11]\n"
"v_exp_f32 v193, v193\n"
"s_add_u32 m0, s71, 9504\n"
"v_mfma_f32_16x16x32_bf16 v[12:15], a[204:207], v[208:211], v[12:15]\n"
"v_exp_f32 v194, v194\n"
"buffer_load_dwordx4 v248, s[48:51], s67 offen  lds\n"
"v_mfma_f32_16x16x32_bf16 v[16:19], a[208:211], v[208:211], v[16:19]\n"
"v_exp_f32 v195, v195\n"
"v_mfma_f32_16x16x32_bf16 v[20:23], a[212:215], v[208:211], v[20:23]\n"
"v_exp_f32 v196, v196\n"
"s_add_u32 m0, s71, 10560\n"
"v_mfma_f32_16x16x32_bf16 v[24:27], a[216:219], v[208:211], v[24:27]\n"
"v_exp_f32 v197, v197\n"
"buffer_load_dwordx4 v248, s[48:51], s68 offen  lds\n"
"v_mfma_f32_16x16x32_bf16 v[28:31], a[220:223], v[208:211], v[28:31]\n"
"v_exp_f32 v198, v198\n"
"s_waitcnt lgkmcnt(0)\n"
"v_mfma_f32_16x16x32_bf16 v[200:203], a[128:131], a[0:3], v[216:219]\n"
"v_exp_f32 v199, v199\n"
"s_add_u32 m0, s71, 11616\n"
"v_mfma_f32_16x16x32_bf16 v[200:203], a[132:135], a[4:7], v[200:203]\n"
"v_cvt_pk_bf16_f32 v212, v192, v193\n"
"v_cvt_pk_bf16_f32 v213, v194, v195\n"
"buffer_load_dwordx4 v248, s[48:51], s69 offen  lds\n"
"v_mfma_f32_16x16x32_bf16 v[200:203], a[136:139], a[8:11], v[200:203]\n"
"v_cvt_pk_bf16_f32 v214, v196, v197\n"
"v_cvt_pk_bf16_f32 v215, v198, v199\n"
"v_mfma_f32_16x16x32_bf16 v[200:203], a[140:143], a[12:15], v[200:203]\n"
"v_xor_b32 v248, 0x30000, v248\n"
"s_add_u32 m0, s71, 16896\n"
"v_mfma_f32_16x16x32_bf16 v[204:207], a[144:147], a[0:3], v[216:219]\n"
"buffer_load_dwordx4 v248, s[48:51], s66 offen  lds\n"
"v_mfma_f32_16x16x32_bf16 v[204:207], a[148:151], a[4:7], v[204:207]\n"
"v_mfma_f32_16x16x32_bf16 v[204:207], a[152:155], a[8:11], v[204:207]\n"
"s_add_u32 m0, s71, 17952\n"
"v_mfma_f32_16x16x32_bf16 v[204:207], a[156:159], a[12:15], v[204:207]\n"
"buffer_load_dwordx4 v248, s[48:51], s67 offen  lds\n"
"v_smfmac_f32_16x16x64_bf16 v[224:227], v[240:243], v[208:215], v244\n"
"v_mfma_f32_16x16x32_bf16 v[32:35], a[192:195], v[212:215], v[32:35]\n"
"v_exp_f32 v200, v200\n"
"s_add_u32 m0, s71, 19008\n"
"v_mfma_f32_16x16x32_bf16 v[36:39], a[196:199], v[212:215], v[36:39]\n"
"v_exp_f32 v201, v201\n"
"buffer_load_dwordx4 v248, s[48:51], s68 offen  lds\n"
"v_mfma_f32_16x16x32_bf16 v[40:43], a[200:203], v[212:215], v[40:43]\n"
"v_exp_f32 v202, v202\n"
"v_mfma_f32_16x16x32_bf16 v[44:47], a[204:207], v[212:215], v[44:47]\n"
"v_exp_f32 v203, v203\n"
"s_add_u32 m0, s71, 20064\n"
"v_add_f32 v225, v224, v225\n"
"v_mfma_f32_16x16x32_bf16 v[48:51], a[208:211], v[212:215], v[48:51]\n"
"v_exp_f32 v204, v204\n"
"buffer_load_dwordx4 v248, s[48:51], s69 offen  lds\n"
"v_add_f32 v227, v226, v227\n"
"v_mfma_f32_16x16x32_bf16 v[52:55], a[212:215], v[212:215], v[52:55]\n"
"v_exp_f32 v205, v205\n"
"v_mfma_f32_16x16x32_bf16 v[56:59], a[216:219], v[212:215], v[56:59]\n"
"v_exp_f32 v206, v206\n"
"v_mfma_f32_16x16x32_bf16 v[60:63], a[220:223], v[212:215], v[60:63]\n"
"v_exp_f32 v207, v207\n"
"v_mfma_f32_16x16x32_bf16 v[192:195], a[96:99], a[32:35], v[216:219]\n"
"v_cvt_pk_bf16_f32 v212, v200, v201\n"
"v_cvt_pk_bf16_f32 v213, v202, v203\n"
"v_mfma_f32_16x16x32_bf16 v[192:195], a[100:103], a[36:39], v[192:195]\n"
"v_cvt_pk_bf16_f32 v214, v204, v205\n"
"v_cvt_pk_bf16_f32 v215, v206, v207\n"
"v_mfma_f32_16x16x32_bf16 v[192:195], a[104:107], a[40:43], v[192:195]\n"
"v_xor_b32 v248, 0x10000, v248\n"
"s_add_u32 m0, s71, 25344\n"
"v_rcp_f32 v224, v225\n"
"v_mfma_f32_16x16x32_bf16 v[192:195], a[108:111], a[44:47], v[192:195]\n"
"buffer_load_dwordx4 v248, s[48:51], s66 offen  lds\n"
"v_rcp_f32 v226, v227\n"
"v_mfma_f32_16x16x32_bf16 v[196:199], a[112:115], a[32:35], v[216:219]\n"
"v_mul_f32 v0, v224, v0\n"
"v_mul_f32 v1, v224, v1\n"
"v_mul_f32 v2, v224, v2\n"
"v_mfma_f32_16x16x32_bf16 v[196:199], a[116:119], a[36:39], v[196:199]\n"
"s_add_u32 m0, s71, 26400\n"
"v_mul_f32 v3, v224, v3\n"
"v_mul_f32 v4, v224, v4\n"
"v_mul_f32 v5, v224, v5\n"
"v_mfma_f32_16x16x32_bf16 v[196:199], a[120:123], a[40:43], v[196:199]\n"
"buffer_load_dwordx4 v248, s[48:51], s67 offen  lds\n"
"v_mul_f32 v6, v224, v6\n"
"v_mul_f32 v7, v224, v7\n"
"v_cvt_pk_bf16_f32 v0, v0, v1\n"
"v_mfma_f32_16x16x32_bf16 v[196:199], a[124:127], a[44:47], v[196:199]\n"
"v_cvt_pk_bf16_f32 v1, v2, v3\n"
"v_cvt_pk_bf16_f32 v2, v4, v5\n"
"v_cvt_pk_bf16_f32 v3, v6, v7\n"
"v_smfmac_f32_16x16x64_bf16 v[236:239], v[240:243], v[208:215], v244\n"
"s_add_u32 m0, s71, 27456\n"
"buffer_store_dwordx4 v[0:3], v249, s[56:59], s60 offen offset:0 sc1\n"
"v_mul_f32 v8, v224, v8\n"
"v_mfma_f32_16x16x32_bf16 a[224:227], a[192:195], v[212:215], a[224:227]\n"
"v_exp_f32 v192, v192\n"
"buffer_load_dwordx4 v248, s[48:51], s68 offen  lds\n"
"v_mul_f32 v9, v224, v9\n"
"v_mfma_f32_16x16x32_bf16 a[228:231], a[196:199], v[212:215], a[228:231]\n"
"v_exp_f32 v193, v193\n"
"v_mul_f32 v10, v224, v10\n"
"v_mfma_f32_16x16x32_bf16 a[232:235], a[200:203], v[212:215], a[232:235]\n"
"v_exp_f32 v194, v194\n"
"s_add_u32 m0, s71, 28512\n"
"v_mul_f32 v11, v224, v11\n"
"v_mfma_f32_16x16x32_bf16 a[236:239], a[204:207], v[212:215], a[236:239]\n"
"v_exp_f32 v195, v195\n"
"buffer_load_dwordx4 v248, s[48:51], s69 offen  lds\n"
"v_mul_f32 v12, v224, v12\n"
"v_mfma_f32_16x16x32_bf16 a[240:243], a[208:211], v[212:215], a[240:243]\n"
"v_exp_f32 v196, v196\n"
"v_mul_f32 v13, v224, v13\n"
"v_mfma_f32_16x16x32_bf16 a[244:247], a[212:215], v[212:215], a[244:247]\n"
"v_exp_f32 v197, v197\n"
"s_sub_u32 s73, 0, s72\n"
"s_cmp_eq_u32 s72, 0\n"
"v_mul_f32 v14, v224, v14\n"
"v_mfma_f32_16x16x32_bf16 a[248:251], a[216:219], v[212:215], a[248:251]\n"
"v_exp_f32 v198, v198\n"
"s_cselect_b32 s73, 1, s73\n"
"v_mul_f32 v15, v224, v15\n"
"v_mfma_f32_16x16x32_bf16 a[252:255], a[220:223], v[212:215], a[252:255]\n"
"v_exp_f32 v199, v199\n"
"v_cvt_pk_bf16_f32 v8, v8, v9\n"
"v_mfma_f32_16x16x32_bf16 v[200:203], a[96:99], a[48:51], v[216:219]\n"
"v_cvt_pk_bf16_f32 v208, v192, v193\n"
"v_cvt_pk_bf16_f32 v209, v194, v195\n"
"v_cvt_pk_bf16_f32 v9, v10, v11\n"
"v_mfma_f32_16x16x32_bf16 v[200:203], a[100:103], a[52:55], v[200:203]\n"
"v_cvt_pk_bf16_f32 v210, v196, v197\n"
"v_cvt_pk_bf16_f32 v211, v198, v199\n"
"v_cvt_pk_bf16_f32 v10, v12, v13\n"
"v_mfma_f32_16x16x32_bf16 v[200:203], a[104:107], a[56:59], v[200:203]\n"
"v_lshl_add_u32 v248, s73, 19, v248\n"
"s_add_u32 s73, s77, s78\n"
"s_and_b32 s73, s73, 3\n"
"v_cvt_pk_bf16_f32 v11, v14, v15\n"
"v_mul_f32 v16, v224, v16\n"
"v_mfma_f32_16x16x32_bf16 v[200:203], a[108:111], a[60:63], v[200:203]\n"
"s_xor_b32 s73, s73, 3\n"
"s_lshl_b32 s73, s73, 16\n"
"v_xor_b32 v248, s73, v248\n"
"buffer_store_dwordx4 v[8:11], v249, s[56:59], s60 offen offset:1024 sc1\n"
"v_mul_f32 v17, v224, v17\n"
"v_mfma_f32_16x16x32_bf16 v[204:207], a[112:115], a[48:51], v[216:219]\n"
"v_mul_f32 v18, v224, v18\n"
"v_mul_f32 v19, v224, v19\n"
"v_mul_f32 v20, v224, v20\n"
"v_mfma_f32_16x16x32_bf16 v[204:207], a[116:119], a[52:55], v[204:207]\n"
"v_mul_f32 v21, v224, v21\n"
"v_mul_f32 v22, v224, v22\n"
"v_mul_f32 v23, v224, v23\n"
"v_mfma_f32_16x16x32_bf16 v[204:207], a[120:123], a[56:59], v[204:207]\n"
"v_cvt_pk_bf16_f32 v16, v16, v17\n"
"v_cvt_pk_bf16_f32 v17, v18, v19\n"
"v_cvt_pk_bf16_f32 v18, v20, v21\n"
"v_mfma_f32_16x16x32_bf16 v[204:207], a[124:127], a[60:63], v[204:207]\n"
"v_cvt_pk_bf16_f32 v19, v22, v23\n"
"v_mul_f32 v24, v224, v24\n"
"v_mul_f32 v25, v224, v25\n"
"v_mfma_f32_16x16x32_bf16 v[64:67], a[160:163], v[208:211], v[64:67]\n"
"v_mul_f32 v26, v224, v26\n"
"v_mul_f32 v27, v224, v27\n"
"v_mul_f32 v28, v224, v28\n"
"v_mfma_f32_16x16x32_bf16 v[68:71], a[164:167], v[208:211], v[68:71]\n"
"v_exp_f32 v200, v200\n"
"v_mul_f32 v29, v224, v29\n"
"v_mfma_f32_16x16x32_bf16 v[72:75], a[168:171], v[208:211], v[72:75]\n"
"v_exp_f32 v201, v201\n"
"v_mul_f32 v30, v224, v30\n"
"v_mfma_f32_16x16x32_bf16 v[76:79], a[172:175], v[208:211], v[76:79]\n"
"v_exp_f32 v202, v202\n"
"v_mul_f32 v31, v224, v31\n"
"v_mfma_f32_16x16x32_bf16 v[80:83], a[176:179], v[208:211], v[80:83]\n"
"v_exp_f32 v203, v203\n"
"v_cvt_pk_bf16_f32 v24, v24, v25\n"
"v_mfma_f32_16x16x32_bf16 v[84:87], a[180:183], v[208:211], v[84:87]\n"
"v_exp_f32 v204, v204\n"
"v_cvt_pk_bf16_f32 v25, v26, v27\n"
"v_mfma_f32_16x16x32_bf16 v[88:91], a[184:187], v[208:211], v[88:91]\n"
"v_exp_f32 v205, v205\n"
"v_cvt_pk_bf16_f32 v26, v28, v29\n"
"v_mfma_f32_16x16x32_bf16 v[92:95], a[188:191], v[208:211], v[92:95]\n"
"v_exp_f32 v206, v206\n"
"buffer_store_dwordx4 v[16:19], v249, s[56:59], s60 offen offset:2048 sc1\n"
"v_mfma_f32_16x16x32_bf16 v[192:195], a[128:131], a[32:35], v[216:219]\n"
"v_exp_f32 v207, v207\n"
"v_cvt_pk_bf16_f32 v27, v30, v31\n"
"v_mfma_f32_16x16x32_bf16 v[192:195], a[132:135], a[36:39], v[192:195]\n"
"v_cvt_pk_bf16_f32 v212, v200, v201\n"
"v_cvt_pk_bf16_f32 v213, v202, v203\n"
"v_mul_f32 v32, v226, v32\n"
"v_mfma_f32_16x16x32_bf16 v[192:195], a[136:139], a[40:43], v[192:195]\n"
"v_cvt_pk_bf16_f32 v214, v204, v205\n"
"v_cvt_pk_bf16_f32 v215, v206, v207\n"
"v_mul_f32 v33, v226, v33\n"
"v_mfma_f32_16x16x32_bf16 v[192:195], a[140:143], a[44:47], v[192:195]\n"
"v_mul_f32 v34, v226, v34\n"
"v_mul_f32 v35, v226, v35\n"
"v_mul_f32 v36, v226, v36\n"
"v_mfma_f32_16x16x32_bf16 v[196:199], a[144:147], a[32:35], v[216:219]\n"
"v_mul_f32 v37, v226, v37\n"
"v_mul_f32 v38, v226, v38\n"
"v_mul_f32 v39, v226, v39\n"
"v_mfma_f32_16x16x32_bf16 v[196:199], a[148:151], a[36:39], v[196:199]\n"
"v_cvt_pk_bf16_f32 v32, v32, v33\n"
"v_cvt_pk_bf16_f32 v33, v34, v35\n"
"v_cvt_pk_bf16_f32 v34, v36, v37\n"
"v_mfma_f32_16x16x32_bf16 v[196:199], a[152:155], a[40:43], v[196:199]\n"
"v_cvt_pk_bf16_f32 v35, v38, v39\n"
"v_mul_f32 v40, v226, v40\n"
"v_mul_f32 v41, v226, v41\n"
"v_mfma_f32_16x16x32_bf16 v[196:199], a[156:159], a[44:47], v[196:199]\n"
"v_mul_f32 v42, v226, v42\n"
"v_mul_f32 v43, v226, v43\n"
"v_mul_f32 v44, v226, v44\n"
"v_smfmac_f32_16x16x64_bf16 v[228:231], v[240:243], v[208:215], v244\n"
"v_mul_f32 v45, v226, v45\n"
"v_mul_f32 v46, v226, v46\n"
"v_mfma_f32_16x16x32_bf16 v[96:99], a[160:163], v[212:215], v[96:99]\n"
"v_exp_f32 v192, v192\n"
"v_mul_f32 v47, v226, v47\n"
"v_mfma_f32_16x16x32_bf16 v[100:103], a[164:167], v[212:215], v[100:103]\n"
"v_exp_f32 v193, v193\n"
"v_cvt_pk_bf16_f32 v40, v40, v41\n"
"v_mfma_f32_16x16x32_bf16 v[104:107], a[168:171], v[212:215], v[104:107]\n"
"v_exp_f32 v194, v194\n"
"buffer_store_dwordx4 v[24:27], v249, s[56:59], s60 offen offset:3072 sc1\n"
"v_mfma_f32_16x16x32_bf16 v[108:111], a[172:175], v[212:215], v[108:111]\n"
"v_exp_f32 v195, v195\n"
"v_cvt_pk_bf16_f32 v41, v42, v43\n"
"v_mfma_f32_16x16x32_bf16 v[112:115], a[176:179], v[212:215], v[112:115]\n"
"v_exp_f32 v196, v196\n"
"v_cvt_pk_bf16_f32 v42, v44, v45\n"
"v_mfma_f32_16x16x32_bf16 v[116:119], a[180:183], v[212:215], v[116:119]\n"
"v_exp_f32 v197, v197\n"
"v_cvt_pk_bf16_f32 v43, v46, v47\n"
"v_mfma_f32_16x16x32_bf16 v[120:123], a[184:187], v[212:215], v[120:123]\n"
"v_exp_f32 v198, v198\n"
"v_mul_f32 v48, v226, v48\n"
"v_mfma_f32_16x16x32_bf16 v[124:127], a[188:191], v[212:215], v[124:127]\n"
"v_exp_f32 v199, v199\n"
"v_mul_f32 v49, v226, v49\n"
"v_mfma_f32_16x16x32_bf16 v[200:203], a[128:131], a[48:51], v[216:219]\n"
"v_cvt_pk_bf16_f32 v208, v192, v193\n"
"v_cvt_pk_bf16_f32 v209, v194, v195\n"
"v_mul_f32 v50, v226, v50\n"
"v_mfma_f32_16x16x32_bf16 v[200:203], a[132:135], a[52:55], v[200:203]\n"
"v_cvt_pk_bf16_f32 v210, v196, v197\n"
"v_cvt_pk_bf16_f32 v211, v198, v199\n"
"v_mul_f32 v51, v226, v51\n"
"v_mfma_f32_16x16x32_bf16 v[200:203], a[136:139], a[56:59], v[200:203]\n"
"v_mul_f32 v52, v226, v52\n"
"v_mul_f32 v53, v226, v53\n"
"v_mul_f32 v54, v226, v54\n"
"v_mfma_f32_16x16x32_bf16 v[200:203], a[140:143], a[60:63], v[200:203]\n"
"v_mul_f32 v55, v226, v55\n"
"v_cvt_pk_bf16_f32 v48, v48, v49\n"
"v_cvt_pk_bf16_f32 v49, v50, v51\n"
"v_mfma_f32_16x16x32_bf16 v[204:207], a[144:147], a[48:51], v[216:219]\n"
"v_cvt_pk_bf16_f32 v50, v52, v53\n"
"v_cvt_pk_bf16_f32 v51, v54, v55\n"
"v_mul_f32 v56, v226, v56\n"
"v_mfma_f32_16x16x32_bf16 v[204:207], a[148:151], a[52:55], v[204:207]\n"
"v_mul_f32 v57, v226, v57\n"
"v_mul_f32 v58, v226, v58\n"
"v_mul_f32 v59, v226, v59\n"
"v_mfma_f32_16x16x32_bf16 v[204:207], a[152:155], a[56:59], v[204:207]\n"
"buffer_store_dwordx4 v[32:35], v249, s[56:59], s61 offen offset:0 sc1\n"
"v_mul_f32 v60, v226, v60\n"
"v_mul_f32 v61, v226, v61\n"
"v_mfma_f32_16x16x32_bf16 v[204:207], a[156:159], a[60:63], v[204:207]\n"
"v_mul_f32 v62, v226, v62\n"
"v_mul_f32 v63, v226, v63\n"
"v_cvt_pk_bf16_f32 v56, v56, v57\n"
"v_mfma_f32_16x16x32_bf16 v[64:67], a[192:195], v[208:211], v[64:67]\n"
"v_cvt_pk_bf16_f32 v57, v58, v59\n"
"v_cvt_pk_bf16_f32 v58, v60, v61\n"
"v_cvt_pk_bf16_f32 v59, v62, v63\n"
"v_mfma_f32_16x16x32_bf16 v[68:71], a[196:199], v[208:211], v[68:71]\n"
"v_exp_f32 v200, v200\n"
"v_mfma_f32_16x16x32_bf16 v[72:75], a[200:203], v[208:211], v[72:75]\n"
"v_exp_f32 v201, v201\n"
"v_mfma_f32_16x16x32_bf16 v[76:79], a[204:207], v[208:211], v[76:79]\n"
"v_exp_f32 v202, v202\n"
"v_mfma_f32_16x16x32_bf16 v[80:83], a[208:211], v[208:211], v[80:83]\n"
"v_exp_f32 v203, v203\n"
"v_mfma_f32_16x16x32_bf16 v[84:87], a[212:215], v[208:211], v[84:87]\n"
"v_exp_f32 v204, v204\n"
"v_mfma_f32_16x16x32_bf16 v[88:91], a[216:219], v[208:211], v[88:91]\n"
"v_exp_f32 v205, v205\n"
"v_mfma_f32_16x16x32_bf16 v[92:95], a[220:223], v[208:211], v[92:95]\n"
"v_exp_f32 v206, v206\n"
"v_mfma_f32_16x16x32_bf16 v[192:195], a[96:99], a[64:67], v[216:219]\n"
"v_exp_f32 v207, v207\n"
"v_mfma_f32_16x16x32_bf16 v[192:195], a[100:103], a[68:71], v[192:195]\n"
"v_cvt_pk_bf16_f32 v212, v200, v201\n"
"v_cvt_pk_bf16_f32 v213, v202, v203\n"
"v_mfma_f32_16x16x32_bf16 v[192:195], a[104:107], a[72:75], v[192:195]\n"
"v_cvt_pk_bf16_f32 v214, v204, v205\n"
"v_cvt_pk_bf16_f32 v215, v206, v207\n"
"buffer_store_dwordx4 v[40:43], v249, s[56:59], s61 offen offset:1024 sc1\n"
"v_mfma_f32_16x16x32_bf16 v[192:195], a[108:111], a[76:79], v[192:195]\n"
"v_mfma_f32_16x16x32_bf16 v[196:199], a[112:115], a[64:67], v[216:219]\n"
"v_mfma_f32_16x16x32_bf16 v[196:199], a[116:119], a[68:71], v[196:199]\n"
"v_mfma_f32_16x16x32_bf16 v[196:199], a[120:123], a[72:75], v[196:199]\n"
"v_mfma_f32_16x16x32_bf16 v[196:199], a[124:127], a[76:79], v[196:199]\n"
"v_smfmac_f32_16x16x64_bf16 v[228:231], v[240:243], v[208:215], v244\n"
"v_mfma_f32_16x16x32_bf16 v[96:99], a[192:195], v[212:215], v[96:99]\n"
"v_exp_f32 v192, v192\n"
"v_mfma_f32_16x16x32_bf16 v[100:103], a[196:199], v[212:215], v[100:103]\n"
"v_exp_f32 v193, v193\n"
"v_mfma_f32_16x16x32_bf16 v[104:107], a[200:203], v[212:215], v[104:107]\n"
"v_exp_f32 v194, v194\n"
"v_mfma_f32_16x16x32_bf16 v[108:111], a[204:207], v[212:215], v[108:111]\n"
"v_exp_f32 v195, v195\n"
"v_add_f32 v229, v228, v229\n"
"v_mfma_f32_16x16x32_bf16 v[112:115], a[208:211], v[212:215], v[112:115]\n"
"v_exp_f32 v196, v196\n"
"v_add_f32 v231, v230, v231\n"
"v_mfma_f32_16x16x32_bf16 v[116:119], a[212:215], v[212:215], v[116:119]\n"
"v_exp_f32 v197, v197\n"
"buffer_store_dwordx4 v[48:51], v249, s[56:59], s61 offen offset:2048 sc1\n"
"v_mfma_f32_16x16x32_bf16 v[120:123], a[216:219], v[212:215], v[120:123]\n"
"v_exp_f32 v198, v198\n"
"v_mfma_f32_16x16x32_bf16 v[124:127], a[220:223], v[212:215], v[124:127]\n"
"v_exp_f32 v199, v199\n"
"v_mfma_f32_16x16x32_bf16 v[200:203], a[96:99], a[80:83], v[216:219]\n"
"v_cvt_pk_bf16_f32 v208, v192, v193\n"
"v_cvt_pk_bf16_f32 v209, v194, v195\n"
"v_mfma_f32_16x16x32_bf16 v[200:203], a[100:103], a[84:87], v[200:203]\n"
"v_cvt_pk_bf16_f32 v210, v196, v197\n"
"v_cvt_pk_bf16_f32 v211, v198, v199\n"
"v_mfma_f32_16x16x32_bf16 v[200:203], a[104:107], a[88:91], v[200:203]\n"
"v_rcp_f32 v228, v229\n"
"v_mfma_f32_16x16x32_bf16 v[200:203], a[108:111], a[92:95], v[200:203]\n"
"v_rcp_f32 v230, v231\n"
"v_mfma_f32_16x16x32_bf16 v[204:207], a[112:115], a[80:83], v[216:219]\n"
"v_mul_f32 v64, v228, v64\n"
"v_mul_f32 v65, v228, v65\n"
"v_mul_f32 v66, v228, v66\n"
"v_mfma_f32_16x16x32_bf16 v[204:207], a[116:119], a[84:87], v[204:207]\n"
"v_mul_f32 v67, v228, v67\n"
"v_mul_f32 v68, v228, v68\n"
"v_mul_f32 v69, v228, v69\n"
"v_mfma_f32_16x16x32_bf16 v[204:207], a[120:123], a[88:91], v[204:207]\n"
"v_mul_f32 v70, v228, v70\n"
"v_mul_f32 v71, v228, v71\n"
"v_cvt_pk_bf16_f32 v64, v64, v65\n"
"v_mfma_f32_16x16x32_bf16 v[204:207], a[124:127], a[92:95], v[204:207]\n"
"v_cvt_pk_bf16_f32 v65, v66, v67\n"
"v_cvt_pk_bf16_f32 v66, v68, v69\n"
"v_cvt_pk_bf16_f32 v67, v70, v71\n"
"v_mfma_f32_16x16x32_bf16 v[128:131], a[160:163], v[208:211], v[128:131]\n"
"v_mul_f32 v72, v228, v72\n"
"v_mul_f32 v73, v228, v73\n"
"v_mul_f32 v74, v228, v74\n"
"v_mfma_f32_16x16x32_bf16 v[132:135], a[164:167], v[208:211], v[132:135]\n"
"v_exp_f32 v200, v200\n"
"buffer_store_dwordx4 v[56:59], v249, s[56:59], s61 offen offset:3072 sc1\n"
"v_mfma_f32_16x16x32_bf16 v[136:139], a[168:171], v[208:211], v[136:139]\n"
"v_exp_f32 v201, v201\n"
"v_mul_f32 v75, v228, v75\n"
"v_mfma_f32_16x16x32_bf16 v[140:143], a[172:175], v[208:211], v[140:143]\n"
"v_exp_f32 v202, v202\n"
"v_mul_f32 v76, v228, v76\n"
"v_mfma_f32_16x16x32_bf16 v[144:147], a[176:179], v[208:211], v[144:147]\n"
"v_exp_f32 v203, v203\n"
"v_mul_f32 v77, v228, v77\n"
"v_mfma_f32_16x16x32_bf16 v[148:151], a[180:183], v[208:211], v[148:151]\n"
"v_exp_f32 v204, v204\n"
"v_mul_f32 v78, v228, v78\n"
"v_mfma_f32_16x16x32_bf16 v[152:155], a[184:187], v[208:211], v[152:155]\n"
"v_exp_f32 v205, v205\n"
"v_mul_f32 v79, v228, v79\n"
"v_mfma_f32_16x16x32_bf16 v[156:159], a[188:191], v[208:211], v[156:159]\n"
"v_exp_f32 v206, v206\n"
"v_cvt_pk_bf16_f32 v72, v72, v73\n"
"v_mfma_f32_16x16x32_bf16 v[192:195], a[128:131], a[64:67], v[216:219]\n"
"v_exp_f32 v207, v207\n"
"v_cvt_pk_bf16_f32 v73, v74, v75\n"
"v_mfma_f32_16x16x32_bf16 v[192:195], a[132:135], a[68:71], v[192:195]\n"
"v_cvt_pk_bf16_f32 v212, v200, v201\n"
"v_cvt_pk_bf16_f32 v213, v202, v203\n"
"v_cvt_pk_bf16_f32 v74, v76, v77\n"
"v_mfma_f32_16x16x32_bf16 v[192:195], a[136:139], a[72:75], v[192:195]\n"
"v_cvt_pk_bf16_f32 v214, v204, v205\n"
"v_cvt_pk_bf16_f32 v215, v206, v207\n"
"v_cvt_pk_bf16_f32 v75, v78, v79\n"
"v_mfma_f32_16x16x32_bf16 v[192:195], a[140:143], a[76:79], v[192:195]\n"
"v_mul_f32 v80, v228, v80\n"
"v_mul_f32 v81, v228, v81\n"
"v_mul_f32 v82, v228, v82\n"
"v_mfma_f32_16x16x32_bf16 v[196:199], a[144:147], a[64:67], v[216:219]\n"
"v_mul_f32 v83, v228, v83\n"
"v_mul_f32 v84, v228, v84\n"
"v_mul_f32 v85, v228, v85\n"
"v_mfma_f32_16x16x32_bf16 v[196:199], a[148:151], a[68:71], v[196:199]\n"
"buffer_store_dwordx4 v[64:67], v249, s[56:59], s62 offen offset:0 sc1\n"
"v_mul_f32 v86, v228, v86\n"
"v_mul_f32 v87, v228, v87\n"
"v_mfma_f32_16x16x32_bf16 v[196:199], a[152:155], a[72:75], v[196:199]\n"
"v_cvt_pk_bf16_f32 v80, v80, v81\n"
"v_cvt_pk_bf16_f32 v81, v82, v83\n"
"v_cvt_pk_bf16_f32 v82, v84, v85\n"
"v_mfma_f32_16x16x32_bf16 v[196:199], a[156:159], a[76:79], v[196:199]\n"
"v_cvt_pk_bf16_f32 v83, v86, v87\n"
"v_smfmac_f32_16x16x64_bf16 v[232:235], v[240:243], v[208:215], v244\n"
"v_mfma_f32_16x16x32_bf16 v[160:163], a[160:163], v[212:215], v[160:163]\n"
"v_exp_f32 v192, v192\n"
"v_mfma_f32_16x16x32_bf16 v[164:167], a[164:167], v[212:215], v[164:167]\n"
"v_exp_f32 v193, v193\n"
"v_mfma_f32_16x16x32_bf16 v[168:171], a[168:171], v[212:215], v[168:171]\n"
"v_exp_f32 v194, v194\n"
"v_mfma_f32_16x16x32_bf16 v[172:175], a[172:175], v[212:215], v[172:175]\n"
"v_exp_f32 v195, v195\n"
"v_mfma_f32_16x16x32_bf16 v[176:179], a[176:179], v[212:215], v[176:179]\n"
"v_exp_f32 v196, v196\n"
"v_mfma_f32_16x16x32_bf16 v[180:183], a[180:183], v[212:215], v[180:183]\n"
"v_exp_f32 v197, v197\n"
"v_mfma_f32_16x16x32_bf16 v[184:187], a[184:187], v[212:215], v[184:187]\n"
"v_exp_f32 v198, v198\n"
"v_mfma_f32_16x16x32_bf16 v[188:191], a[188:191], v[212:215], v[188:191]\n"
"v_exp_f32 v199, v199\n"
"v_mfma_f32_16x16x32_bf16 v[200:203], a[128:131], a[80:83], v[216:219]\n"
"v_cvt_pk_bf16_f32 v208, v192, v193\n"
"v_cvt_pk_bf16_f32 v209, v194, v195\n"
"buffer_store_dwordx4 v[72:75], v249, s[56:59], s62 offen offset:1024 sc1\n"
"v_mfma_f32_16x16x32_bf16 v[200:203], a[132:135], a[84:87], v[200:203]\n"
"v_cvt_pk_bf16_f32 v210, v196, v197\n"
"v_cvt_pk_bf16_f32 v211, v198, v199\n"
"v_mfma_f32_16x16x32_bf16 v[200:203], a[136:139], a[88:91], v[200:203]\n"
"v_mfma_f32_16x16x32_bf16 v[200:203], a[140:143], a[92:95], v[200:203]\n"
"v_mfma_f32_16x16x32_bf16 v[204:207], a[144:147], a[80:83], v[216:219]\n"
"v_mfma_f32_16x16x32_bf16 v[204:207], a[148:151], a[84:87], v[204:207]\n"
"v_mfma_f32_16x16x32_bf16 v[204:207], a[152:155], a[88:91], v[204:207]\n"
"v_mfma_f32_16x16x32_bf16 v[204:207], a[156:159], a[92:95], v[204:207]\n"
"v_mfma_f32_16x16x32_bf16 v[128:131], a[192:195], v[208:211], v[128:131]\n"
"v_mfma_f32_16x16x32_bf16 v[132:135], a[196:199], v[208:211], v[132:135]\n"
"v_exp_f32 v200, v200\n"
"v_mfma_f32_16x16x32_bf16 v[136:139], a[200:203], v[208:211], v[136:139]\n"
"v_exp_f32 v201, v201\n"
"v_mfma_f32_16x16x32_bf16 v[140:143], a[204:207], v[208:211], v[140:143]\n"
"v_exp_f32 v202, v202\n"
"v_mfma_f32_16x16x32_bf16 v[144:147], a[208:211], v[208:211], v[144:147]\n"
"v_exp_f32 v203, v203\n"
"buffer_store_dwordx4 v[80:83], v249, s[56:59], s62 offen offset:2048 sc1\n"
"v_mfma_f32_16x16x32_bf16 v[148:151], a[212:215], v[208:211], v[148:151]\n"
"v_exp_f32 v204, v204\n"
"v_mfma_f32_16x16x32_bf16 v[152:155], a[216:219], v[208:211], v[152:155]\n"
"v_exp_f32 v205, v205\n"
"v_mfma_f32_16x16x32_bf16 v[156:159], a[220:223], v[208:211], v[156:159]\n"
"v_exp_f32 v206, v206\n"
"v_exp_f32 v207, v207\n"
"v_cvt_pk_bf16_f32 v212, v200, v201\n"
"v_cvt_pk_bf16_f32 v213, v202, v203\n"
"v_cvt_pk_bf16_f32 v214, v204, v205\n"
"v_cvt_pk_bf16_f32 v215, v206, v207\n"
"s_nop 1\n"
"v_smfmac_f32_16x16x64_bf16 v[232:235], v[240:243], v[208:215], v244\n"
"v_mfma_f32_16x16x32_bf16 v[160:163], a[192:195], v[212:215], v[160:163]\n"
"v_mfma_f32_16x16x32_bf16 v[164:167], a[196:199], v[212:215], v[164:167]\n"
"v_mfma_f32_16x16x32_bf16 v[168:171], a[200:203], v[212:215], v[168:171]\n"
"v_mfma_f32_16x16x32_bf16 v[172:175], a[204:207], v[212:215], v[172:175]\n"
"v_mfma_f32_16x16x32_bf16 v[176:179], a[208:211], v[212:215], v[176:179]\n"
"v_mfma_f32_16x16x32_bf16 v[180:183], a[212:215], v[212:215], v[180:183]\n"
"v_mfma_f32_16x16x32_bf16 v[184:187], a[216:219], v[212:215], v[184:187]\n"
"v_mfma_f32_16x16x32_bf16 v[188:191], a[220:223], v[212:215], v[188:191]\n"
"s_mul_i32 s80, s76, 5120\n"
"s_add_u32 s80, s80, 139264\n"
"v_add_u32 v253, s80, v255\n"
"s_xor_b32 s81, s76, 1\n"
"s_mul_i32 s81, s81, 5120\n"
"s_add_u32 s81, s81, 139264\n"
"v_add_u32 v254, s81, v255\n"
"v_lshrrev_b32 v252, 1, v255\n"
"v_and_b32 v252, 0x78, v252\n"
"s_lshl_b32 s47, s77, 4\n"
"s_add_u32 s47, s47, s78\n"
"s_lshl_b32 s47, s47, 7\n"
"s_add_u32 s47, s47, 33685504\n"
"buffer_load_dwordx2 v[250:251], v252, s[56:59], s47 offen sc1\n"
"v_add_f32 v237, v238, v239\n"
"v_add_f32 v233, v232, v233\n"
"v_add_f32 v235, v234, v235\n"
"v_rcp_f32 v232, v233\n"
"v_rcp_f32 v234, v235\n"
"ds_write_b128 v253, a[224:227] offset:0\n"
"ds_write_b128 v253, a[228:231] offset:1024\n"
"ds_write_b128 v253, a[232:235] offset:2048\n"
"ds_write_b128 v253, a[236:239] offset:3072\n"
"ds_write_b32 v253, v237 offset:4096\n"
"v_pk_mul_f32 v[88:89], v[88:89], v[228:229] op_sel_hi:[1,0]\n"
"v_pk_mul_f32 v[90:91], v[90:91], v[228:229] op_sel_hi:[1,0]\n"
"v_pk_mul_f32 v[92:93], v[92:93], v[228:229] op_sel_hi:[1,0]\n"
"v_pk_mul_f32 v[94:95], v[94:95], v[228:229] op_sel_hi:[1,0]\n"
"v_cvt_pk_bf16_f32 v88, v88, v89\n"
"v_cvt_pk_bf16_f32 v89, v90, v91\n"
"v_cvt_pk_bf16_f32 v90, v92, v93\n"
"v_cvt_pk_bf16_f32 v91, v94, v95\n"
"buffer_store_dwordx4 v[88:91], v249, s[56:59], s62 offen offset:3072 sc1\n"
"s_waitcnt lgkmcnt(0)\n"
"s_barrier\n"
"ds_read_b128 v[208:211], v254 offset:0\n"
"ds_read_b128 v[212:215], v254 offset:1024\n"
"ds_read_b128 v[216:219], v254 offset:2048\n"
"ds_read_b128 v[220:223], v254 offset:3072\n"
"ds_read_b32 v252, v254 offset:4096\n"
"s_lshl_b32 s44, s78, 3\n"
"s_add_u32 s44, s44, 163328\n"
"v_mov_b32 v7, s44\n"
"ds_read_b64 v[238:239], v7\n"
"v_pk_mul_f32 v[96:97], v[96:97], v[230:231] op_sel_hi:[1,0]\n"
"v_pk_mul_f32 v[98:99], v[98:99], v[230:231] op_sel_hi:[1,0]\n"
"v_pk_mul_f32 v[100:101], v[100:101], v[230:231] op_sel_hi:[1,0]\n"
"v_pk_mul_f32 v[102:103], v[102:103], v[230:231] op_sel_hi:[1,0]\n"
"v_cvt_pk_bf16_f32 v96, v96, v97\n"
"v_cvt_pk_bf16_f32 v97, v98, v99\n"
"v_cvt_pk_bf16_f32 v98, v100, v101\n"
"v_cvt_pk_bf16_f32 v99, v102, v103\n"
"buffer_store_dwordx4 v[96:99], v249, s[56:59], s63 offen offset:0 sc1\n"
"v_pk_mul_f32 v[104:105], v[104:105], v[230:231] op_sel_hi:[1,0]\n"
"v_pk_mul_f32 v[106:107], v[106:107], v[230:231] op_sel_hi:[1,0]\n"
"v_pk_mul_f32 v[108:109], v[108:109], v[230:231] op_sel_hi:[1,0]\n"
"v_pk_mul_f32 v[110:111], v[110:111], v[230:231] op_sel_hi:[1,0]\n"
"v_cvt_pk_bf16_f32 v104, v104, v105\n"
"v_cvt_pk_bf16_f32 v105, v106, v107\n"
"v_cvt_pk_bf16_f32 v106, v108, v109\n"
"v_cvt_pk_bf16_f32 v107, v110, v111\n"
"buffer_store_dwordx4 v[104:107], v249, s[56:59], s63 offen offset:1024 sc1\n"
"v_pk_mul_f32 v[112:113], v[112:113], v[230:231] op_sel_hi:[1,0]\n"
"v_pk_mul_f32 v[114:115], v[114:115], v[230:231] op_sel_hi:[1,0]\n"
"v_pk_mul_f32 v[116:117], v[116:117], v[230:231] op_sel_hi:[1,0]\n"
"v_pk_mul_f32 v[118:119], v[118:119], v[230:231] op_sel_hi:[1,0]\n"
"v_cvt_pk_bf16_f32 v112, v112, v113\n"
"v_cvt_pk_bf16_f32 v113, v114, v115\n"
"v_cvt_pk_bf16_f32 v114, v116, v117\n"
"v_cvt_pk_bf16_f32 v115, v118, v119\n"
"buffer_store_dwordx4 v[112:115], v249, s[56:59], s63 offen offset:2048 sc1\n"
"v_pk_mul_f32 v[120:121], v[120:121], v[230:231] op_sel_hi:[1,0]\n"
"v_pk_mul_f32 v[122:123], v[122:123], v[230:231] op_sel_hi:[1,0]\n"
"v_pk_mul_f32 v[124:125], v[124:125], v[230:231] op_sel_hi:[1,0]\n"
"v_pk_mul_f32 v[126:127], v[126:127], v[230:231] op_sel_hi:[1,0]\n"
"v_cvt_pk_bf16_f32 v120, v120, v121\n"
"v_cvt_pk_bf16_f32 v121, v122, v123\n"
"v_cvt_pk_bf16_f32 v122, v124, v125\n"
"v_cvt_pk_bf16_f32 v123, v126, v127\n"
"buffer_store_dwordx4 v[120:123], v249, s[56:59], s63 offen offset:3072 sc1\n"
"v_pk_mul_f32 v[128:129], v[128:129], v[232:233] op_sel_hi:[1,0]\n"
"v_pk_mul_f32 v[130:131], v[130:131], v[232:233] op_sel_hi:[1,0]\n"
"v_pk_mul_f32 v[132:133], v[132:133], v[232:233] op_sel_hi:[1,0]\n"
"v_pk_mul_f32 v[134:135], v[134:135], v[232:233] op_sel_hi:[1,0]\n"
"v_cvt_pk_bf16_f32 v128, v128, v129\n"
"v_cvt_pk_bf16_f32 v129, v130, v131\n"
"v_cvt_pk_bf16_f32 v130, v132, v133\n"
"v_cvt_pk_bf16_f32 v131, v134, v135\n"
"buffer_store_dwordx4 v[128:131], v249, s[56:59], s64 offen offset:0 sc1\n"
"v_pk_mul_f32 v[136:137], v[136:137], v[232:233] op_sel_hi:[1,0]\n"
"v_pk_mul_f32 v[138:139], v[138:139], v[232:233] op_sel_hi:[1,0]\n"
"v_pk_mul_f32 v[140:141], v[140:141], v[232:233] op_sel_hi:[1,0]\n"
"v_pk_mul_f32 v[142:143], v[142:143], v[232:233] op_sel_hi:[1,0]\n"
"v_cvt_pk_bf16_f32 v136, v136, v137\n"
"v_cvt_pk_bf16_f32 v137, v138, v139\n"
"v_cvt_pk_bf16_f32 v138, v140, v141\n"
"v_cvt_pk_bf16_f32 v139, v142, v143\n"
"buffer_store_dwordx4 v[136:139], v249, s[56:59], s64 offen offset:1024 sc1\n"
"v_pk_mul_f32 v[144:145], v[144:145], v[232:233] op_sel_hi:[1,0]\n"
"v_pk_mul_f32 v[146:147], v[146:147], v[232:233] op_sel_hi:[1,0]\n"
"v_pk_mul_f32 v[148:149], v[148:149], v[232:233] op_sel_hi:[1,0]\n"
"v_pk_mul_f32 v[150:151], v[150:151], v[232:233] op_sel_hi:[1,0]\n"
"v_cvt_pk_bf16_f32 v144, v144, v145\n"
"v_cvt_pk_bf16_f32 v145, v146, v147\n"
"v_cvt_pk_bf16_f32 v146, v148, v149\n"
"v_cvt_pk_bf16_f32 v147, v150, v151\n"
"buffer_store_dwordx4 v[144:147], v249, s[56:59], s64 offen offset:2048 sc1\n"
"v_pk_mul_f32 v[152:153], v[152:153], v[232:233] op_sel_hi:[1,0]\n"
"v_pk_mul_f32 v[154:155], v[154:155], v[232:233] op_sel_hi:[1,0]\n"
"v_pk_mul_f32 v[156:157], v[156:157], v[232:233] op_sel_hi:[1,0]\n"
"v_pk_mul_f32 v[158:159], v[158:159], v[232:233] op_sel_hi:[1,0]\n"
"v_cvt_pk_bf16_f32 v152, v152, v153\n"
"v_cvt_pk_bf16_f32 v153, v154, v155\n"
"v_cvt_pk_bf16_f32 v154, v156, v157\n"
"v_cvt_pk_bf16_f32 v155, v158, v159\n"
"buffer_store_dwordx4 v[152:155], v249, s[56:59], s64 offen offset:3072 sc1\n"
"v_accvgpr_read_b32 v192, a240\n"
"v_accvgpr_read_b32 v193, a241\n"
"v_accvgpr_read_b32 v194, a242\n"
"v_accvgpr_read_b32 v195, a243\n"
"v_accvgpr_read_b32 v196, a244\n"
"v_accvgpr_read_b32 v197, a245\n"
"v_accvgpr_read_b32 v198, a246\n"
"v_accvgpr_read_b32 v199, a247\n"
"v_accvgpr_read_b32 v200, a248\n"
"v_accvgpr_read_b32 v201, a249\n"
"v_accvgpr_read_b32 v202, a250\n"
"v_accvgpr_read_b32 v203, a251\n"
"v_accvgpr_read_b32 v204, a252\n"
"v_accvgpr_read_b32 v205, a253\n"
"v_accvgpr_read_b32 v206, a254\n"
"v_accvgpr_read_b32 v207, a255\n"
"s_waitcnt lgkmcnt(1)\n"
"v_add_f32 v237, v237, v252\n"
"v_rcp_f32 v236, v237\n"
"v_pk_add_f32 v[208:209], v[208:209], v[192:193]\n"
"v_pk_add_f32 v[210:211], v[210:211], v[194:195]\n"
"v_pk_add_f32 v[212:213], v[212:213], v[196:197]\n"
"v_pk_add_f32 v[214:215], v[214:215], v[198:199]\n"
"v_pk_add_f32 v[216:217], v[216:217], v[200:201]\n"
"v_pk_add_f32 v[218:219], v[218:219], v[202:203]\n"
"v_pk_add_f32 v[220:221], v[220:221], v[204:205]\n"
"v_pk_add_f32 v[222:223], v[222:223], v[206:207]\n"
"v_pk_mul_f32 v[208:209], v[208:209], v[236:237] op_sel_hi:[1,0]\n"
"v_pk_mul_f32 v[210:211], v[210:211], v[236:237] op_sel_hi:[1,0]\n"
"v_pk_mul_f32 v[212:213], v[212:213], v[236:237] op_sel_hi:[1,0]\n"
"v_pk_mul_f32 v[214:215], v[214:215], v[236:237] op_sel_hi:[1,0]\n"
"v_cvt_pk_bf16_f32 v200, v208, v209\n"
"v_cvt_pk_bf16_f32 v201, v210, v211\n"
"v_cvt_pk_bf16_f32 v202, v212, v213\n"
"v_cvt_pk_bf16_f32 v203, v214, v215\n"
"buffer_store_dwordx4 v[200:203], v249, s[56:59], s70 offen offset:2048 sc1\n"
"ds_write_b128 v254, v[200:203] offset:0\n"
"v_pk_mul_f32 v[216:217], v[216:217], v[236:237] op_sel_hi:[1,0]\n"
"v_pk_mul_f32 v[218:219], v[218:219], v[236:237] op_sel_hi:[1,0]\n"
"v_pk_mul_f32 v[220:221], v[220:221], v[236:237] op_sel_hi:[1,0]\n"
"v_pk_mul_f32 v[222:223], v[222:223], v[236:237] op_sel_hi:[1,0]\n"
"v_cvt_pk_bf16_f32 v204, v216, v217\n"
"v_cvt_pk_bf16_f32 v205, v218, v219\n"
"v_cvt_pk_bf16_f32 v206, v220, v221\n"
"v_cvt_pk_bf16_f32 v207, v222, v223\n"
"buffer_store_dwordx4 v[204:207], v249, s[56:59], s70 offen offset:3072 sc1\n"
"ds_write_b128 v254, v[204:207] offset:1024\n"
"v_pk_mul_f32 v[160:161], v[160:161], v[234:235] op_sel_hi:[1,0]\n"
"v_pk_mul_f32 v[162:163], v[162:163], v[234:235] op_sel_hi:[1,0]\n"
"v_pk_mul_f32 v[164:165], v[164:165], v[234:235] op_sel_hi:[1,0]\n"
"v_pk_mul_f32 v[166:167], v[166:167], v[234:235] op_sel_hi:[1,0]\n"
"v_cvt_pk_bf16_f32 v160, v160, v161\n"
"v_cvt_pk_bf16_f32 v161, v162, v163\n"
"v_cvt_pk_bf16_f32 v162, v164, v165\n"
"v_cvt_pk_bf16_f32 v163, v166, v167\n"
"buffer_store_dwordx4 v[160:163], v249, s[56:59], s65 offen offset:0 sc1\n"
"v_pk_mul_f32 v[168:169], v[168:169], v[234:235] op_sel_hi:[1,0]\n"
"v_pk_mul_f32 v[170:171], v[170:171], v[234:235] op_sel_hi:[1,0]\n"
"v_pk_mul_f32 v[172:173], v[172:173], v[234:235] op_sel_hi:[1,0]\n"
"v_pk_mul_f32 v[174:175], v[174:175], v[234:235] op_sel_hi:[1,0]\n"
"v_cvt_pk_bf16_f32 v168, v168, v169\n"
"v_cvt_pk_bf16_f32 v169, v170, v171\n"
"v_cvt_pk_bf16_f32 v170, v172, v173\n"
"v_cvt_pk_bf16_f32 v171, v174, v175\n"
"buffer_store_dwordx4 v[168:171], v249, s[56:59], s65 offen offset:1024 sc1\n"
"v_pk_mul_f32 v[176:177], v[176:177], v[234:235] op_sel_hi:[1,0]\n"
"v_pk_mul_f32 v[178:179], v[178:179], v[234:235] op_sel_hi:[1,0]\n"
"v_pk_mul_f32 v[180:181], v[180:181], v[234:235] op_sel_hi:[1,0]\n"
"v_pk_mul_f32 v[182:183], v[182:183], v[234:235] op_sel_hi:[1,0]\n"
"v_cvt_pk_bf16_f32 v176, v176, v177\n"
"v_cvt_pk_bf16_f32 v177, v178, v179\n"
"v_cvt_pk_bf16_f32 v178, v180, v181\n"
"v_cvt_pk_bf16_f32 v179, v182, v183\n"
"buffer_store_dwordx4 v[176:179], v249, s[56:59], s65 offen offset:2048 sc1\n"
"v_pk_mul_f32 v[184:185], v[184:185], v[234:235] op_sel_hi:[1,0]\n"
"v_pk_mul_f32 v[186:187], v[186:187], v[234:235] op_sel_hi:[1,0]\n"
"v_pk_mul_f32 v[188:189], v[188:189], v[234:235] op_sel_hi:[1,0]\n"
"v_pk_mul_f32 v[190:191], v[190:191], v[234:235] op_sel_hi:[1,0]\n"
"v_cvt_pk_bf16_f32 v184, v184, v185\n"
"v_cvt_pk_bf16_f32 v185, v186, v187\n"
"v_cvt_pk_bf16_f32 v186, v188, v189\n"
"v_cvt_pk_bf16_f32 v187, v190, v191\n"
"buffer_store_dwordx4 v[184:187], v249, s[56:59], s65 offen offset:3072 sc1\n"
"v_subrev_u32 v225, 796917760, v225\n"
"v_subrev_u32 v227, 796917760, v227\n"
"v_subrev_u32 v229, 796917760, v229\n"
"v_subrev_u32 v231, 796917760, v231\n"
"v_subrev_u32 v233, 796917760, v233\n"
"v_subrev_u32 v235, 796917760, v235\n"
"v_subrev_u32 v237, 796917760, v237\n"
"v_max3_u32 v225, v225, v227, v229\n"
"v_max3_u32 v231, v231, v233, v235\n"
"v_max3_u32 v225, v225, v231, v237\n"
"v_cmp_lt_u32 vcc, 536870912, v225\n"
"s_nop 1\n"
"s_or_b32 s85, vcc_lo, vcc_hi\n"
"s_waitcnt vmcnt(15) lgkmcnt(2)\n"
"v_cmp_ne_u64 vcc, v[250:251], v[238:239]\n"
"s_nop 1\n"
"s_or_b32 s44, vcc_lo, vcc_hi\n"
"s_cmp_eq_u32 s76, 0\n"
"s_cselect_b32 s44, s44, 0\n"
"s_and_b32 s80, s76, 1\n"
"s_lshr_b32 s81, s76, 1\n"
"s_lshr_b32 s82, s78, 3\n"
"v_lshrrev_b32 v12, 4, v255\n"
"v_lshrrev_b32 v13, 2, v12\n"
"v_lshlrev_b32 v14, 8, v13\n"
"v_and_b32 v13, 3, v13\n"
"v_lshlrev_b32 v13, 6, v13\n"
"v_lshrrev_b32 v15, 4, v12\n"
"v_lshl_add_u32 v14, v15, 5, v14\n"
"v_and_b32 v15, 3, v12\n"
"v_lshl_add_u32 v14, v15, 3, v14\n"
"s_mul_i32 s83, s81, 67584\n"
"v_add_u32 v14, s83, v14\n"
"s_xor_b32 s83, s82, 1\n"
"s_lshl_b32 s83, s83, 1\n"
"s_add_u32 s83, s83, s81\n"
"s_lshl_b32 s83, s83, 1\n"
"s_add_u32 s83, s83, s80\n"
"s_lshl_b32 s83, s83, 5\n"
"v_xor_b32 v13, s83, v13\n"
"v_xor_b32 v240, 0, v13\n"
"v_add_u32 v240, v240, v14\n"
"v_xor_b32 v241, 32, v13\n"
"v_add_u32 v241, v241, v14\n"
"v_xor_b32 v242, 64, v13\n"
"v_add_u32 v242, v242, v14\n"
"v_xor_b32 v243, 96, v13\n"
"v_add_u32 v243, v243, v14\n"
"v_xor_b32 v244, 128, v13\n"
"v_add_u32 v244, v244, v14\n"
"v_xor_b32 v245, 160, v13\n"
"v_add_u32 v245, v245, v14\n"
"v_xor_b32 v246, 192, v13\n"
"v_add_u32 v246, v246, v14\n"
"v_xor_b32 v247, 224, v13\n"
"v_add_u32 v247, v247, v14\n"
"v_and_b32 v13, 7, v12\n"
"s_lshl_b32 s83, s82, 3\n"
"v_add_u32 v13, s83, v13\n"
"v_lshlrev_b32 v254, 7, v13\n"
"v_lshlrev_b32 v13, 3, v13\n"
"v_add_u32 v13, 163328, v13\n"
"ds_read_b64 v[250:251], v13\n"
"s_lshl_b32 s46, s78, 7\n"
"s_lshl_b32 s83, s78, 3\n"
"s_add_u32 s83, s83, 163328\n"
"v_mov_b32 v15, s83\n"
"ds_read_b64 v[4:5], v15\n"
"s_lshl_b32 s45, s77, 11\n"
"s_add_u32 s45, s45, 33554432\n"
"s_add_u32 s46, s46, s45\n"
"v_or_b32 v6, s76, v255\n"
"v_cmp_eq_u32 vcc, 0, v6\n"
"v_mov_b32 v6, 0x40000000\n"
"s_nop 0\n"
"v_cndmask_b32 v6, v6, v255, vcc\n"
"s_mov_b64 s[40:41], %9\n"
"s_mov_b32 s42, 6815744\n"
"s_mov_b32 s43, 0x00020000\n"
"s_cmp_lg_u32 s85, 0\n"
"s_cbranch_scc1 Lx%=\n"
".Lmha_j1:\n"
"s_waitcnt lgkmcnt(0)\n"
"s_barrier\n"
"ds_read_b128 v[192:195], v253 offset:0\n"
"ds_read_b128 v[196:199], v253 offset:1024\n"
"Lk2%=:\n"
"ds_read_b64_tr_b16 a[208:209], v240 offset:0\n"
"ds_read_b64_tr_b16 a[210:211], v240 offset:4224\n"
"ds_read_b64_tr_b16 a[212:213], v241 offset:0\n"
"ds_read_b64_tr_b16 a[214:215], v241 offset:4224\n"
"ds_read_b64_tr_b16 a[216:217], v242 offset:0\n"
"ds_read_b64_tr_b16 a[218:219], v242 offset:4224\n"
"ds_read_b64_tr_b16 a[220:221], v243 offset:0\n"
"ds_read_b64_tr_b16 a[222:223], v243 offset:4224\n"
"ds_read_b64_tr_b16 a[224:225], v244 offset:0\n"
"ds_read_b64_tr_b16 a[226:227], v244 offset:4224\n"
"ds_read_b64_tr_b16 a[228:229], v245 offset:0\n"
"ds_read_b64_tr_b16 a[230:231], v245 offset:4224\n"
"ds_read_b64_tr_b16 a[232:233], v246 offset:0\n"
"ds_read_b64_tr_b16 a[234:235], v246 offset:4224\n"
"ds_read_b64_tr_b16 a[236:237], v247 offset:0\n"
"ds_read_b64_tr_b16 a[238:239], v247 offset:4224\n"
"ds_read_b64_tr_b16 a[240:241], v240 offset:8448\n"
"ds_read_b64_tr_b16 a[242:243], v240 offset:12672\n"
"ds_read_b64_tr_b16 a[244:245], v241 offset:8448\n"
"ds_read_b64_tr_b16 a[246:247], v241 offset:12672\n"
"ds_read_b64_tr_b16 a[248:249], v242 offset:8448\n"
"ds_read_b64_tr_b16 a[250:251], v242 offset:12672\n"
"ds_read_b64_tr_b16 a[252:253], v243 offset:8448\n"
"ds_read_b64_tr_b16 a[254:255], v243 offset:12672\n"
"ds_read_b64_tr_b16 v[224:225], v244 offset:8448\n"
"ds_read_b64_tr_b16 v[226:227], v244 offset:12672\n"
"ds_read_b64_tr_b16 v[228:229], v245 offset:8448\n"
"ds_read_b64_tr_b16 v[230:231], v245 offset:12672\n"
"ds_read_b64_tr_b16 v[232:233], v246 offset:8448\n"
"ds_read_b64_tr_b16 v[234:235], v246 offset:12672\n"
"ds_read_b64_tr_b16 v[236:237], v247 offset:8448\n"
"ds_read_b64_tr_b16 v[238:239], v247 offset:12672\n"
"s_waitcnt lgkmcnt(0)\n"
"v_mfma_f32_16x16x32_bf16 v[20:23], a[208:211], v[0:3], 0\n"
"v_mfma_f32_16x16x32_bf16 v[20:23], a[240:243], v[8:11], v[20:23]\n"
"v_mfma_f32_16x16x32_bf16 v[28:31], a[208:211], v[32:35], 0\n"
"s_sub_u32 s74, 0, s72\n"
"s_cmp_eq_u32 s72, 0\n"
"s_cselect_b32 s74, 1, s74\n"
"v_lshl_add_u32 v249, s74, 13, v249\n"
"s_add_u32 s74, s77, s78\n"
"s_and_b32 s74, s74, 3\n"
"s_lshl_b32 s74, s74, 10\n"
"v_xor_b32 v249, s74, v249\n"
"v_mfma_f32_16x16x32_bf16 v[28:31], a[240:243], v[40:43], v[28:31]\n"
"v_mfma_f32_16x16x32_bf16 v[36:39], a[208:211], v[64:67], 0\n"
"v_mfma_f32_16x16x32_bf16 v[36:39], a[240:243], v[72:75], v[36:39]\n"
"v_mfma_f32_16x16x32_bf16 v[44:47], a[208:211], v[96:99], 0\n"
"v_mfma_f32_16x16x32_bf16 v[44:47], a[240:243], v[104:107], v[44:47]\n"
"v_mfma_f32_16x16x32_bf16 v[52:55], a[208:211], v[128:131], 0\n"
"v_mfma_f32_16x16x32_bf16 v[52:55], a[240:243], v[136:139], v[52:55]\n"
"v_mfma_f32_16x16x32_bf16 v[60:63], a[208:211], v[160:163], 0\n"
"s_waitcnt vmcnt(0)\n"
"s_barrier\n"
"s_cmp_eq_u32 s44, 0\n"
"s_cbranch_scc0 Lak%=\n"
"Lakr%=:\n"
"buffer_store_dwordx2 v[4:5], v6, s[56:59], s46 offen sc1\n"
"v_mfma_f32_16x16x32_bf16 v[60:63], a[240:243], v[168:171], v[60:63]\n"
"v_mfma_f32_16x16x32_bf16 v[216:219], a[208:211], v[192:195], 0\n"
"v_mfma_f32_16x16x32_bf16 v[216:219], a[240:243], v[196:199], v[216:219]\n"
"s_add_u32 m0, s71, 33792\n"
"ds_read_b64_tr_b16 a[208:209], v240 offset:16896\n"
"v_mfma_f32_16x16x32_bf16 v[68:71], a[212:215], v[0:3], 0\n"
"buffer_load_dwordx4 v248, s[48:51], s66 offen  lds\n"
"s_add_u32 m0, s71, 34848\n"
"v_mfma_f32_16x16x32_bf16 v[68:71], a[244:247], v[8:11], v[68:71]\n"
"buffer_load_dwordx4 v248, s[48:51], s67 offen  lds\n"
"v_mfma_f32_16x16x32_bf16 v[76:79], a[212:215], v[32:35], 0\n"
"s_add_u32 m0, s71, 35904\n"
"ds_read_b64_tr_b16 a[210:211], v240 offset:21120\n"
"v_mfma_f32_16x16x32_bf16 v[76:79], a[244:247], v[40:43], v[76:79]\n"
"buffer_load_dwordx4 v248, s[48:51], s68 offen  lds\n"
"v_mfma_f32_16x16x32_bf16 v[84:87], a[212:215], v[64:67], 0\n"
"s_add_u32 m0, s71, 36960\n"
"ds_read_b64_tr_b16 a[240:241], v240 offset:25344\n"
"v_mfma_f32_16x16x32_bf16 v[84:87], a[244:247], v[72:75], v[84:87]\n"
"buffer_load_dwordx4 v248, s[48:51], s69 offen  lds\n"
"v_xor_b32 v248, 0x10000, v248\n"
"s_add_u32 m0, s71, 42240\n"
"v_mfma_f32_16x16x32_bf16 v[92:95], a[212:215], v[96:99], 0\n"
"buffer_load_dwordx4 v248, s[48:51], s66 offen  lds\n"
"v_mfma_f32_16x16x32_bf16 v[92:95], a[244:247], v[104:107], v[92:95]\n"
"s_add_u32 m0, s71, 43296\n"
"ds_read_b64_tr_b16 a[242:243], v240 offset:29568\n"
"v_mfma_f32_16x16x32_bf16 v[100:103], a[212:215], v[128:131], 0\n"
"buffer_load_dwordx4 v248, s[48:51], s67 offen  lds\n"
"v_mfma_f32_16x16x32_bf16 v[100:103], a[244:247], v[136:139], v[100:103]\n"
"s_add_u32 m0, s71, 44352\n"
"v_mfma_f32_16x16x32_bf16 v[108:111], a[212:215], v[160:163], 0\n"
"buffer_load_dwordx4 v248, s[48:51], s68 offen  lds\n"
"v_mfma_f32_16x16x32_bf16 v[108:111], a[244:247], v[168:171], v[108:111]\n"
"s_add_u32 m0, s71, 45408\n"
"ds_read_b64_tr_b16 a[212:213], v241 offset:16896\n"
"v_mfma_f32_16x16x32_bf16 a[8:11], a[216:219], v[0:3], 0\n"
"buffer_load_dwordx4 v248, s[48:51], s69 offen  lds\n"
"v_xor_b32 v248, 0x30000, v248\n"
"s_add_u32 m0, s71, 50688\n"
"v_mfma_f32_16x16x32_bf16 a[8:11], a[248:251], v[8:11], a[8:11]\n"
"buffer_load_dwordx4 v248, s[48:51], s66 offen  lds\n"
"v_mfma_f32_16x16x32_bf16 a[40:43], a[216:219], v[32:35], 0\n"
"s_add_u32 m0, s71, 51744\n"
"ds_read_b64_tr_b16 a[214:215], v241 offset:21120\n"
"v_mfma_f32_16x16x32_bf16 a[40:43], a[248:251], v[40:43], a[40:43]\n"
"buffer_load_dwordx4 v248, s[48:51], s67 offen  lds\n"
"v_mfma_f32_16x16x32_bf16 a[72:75], a[216:219], v[64:67], 0\n"
"s_add_u32 m0, s71, 52800\n"
"ds_read_b64_tr_b16 a[244:245], v241 offset:25344\n"
"v_mfma_f32_16x16x32_bf16 a[72:75], a[248:251], v[72:75], a[72:75]\n"
"buffer_load_dwordx4 v248, s[48:51], s68 offen  lds\n"
"v_mfma_f32_16x16x32_bf16 a[104:107], a[216:219], v[96:99], 0\n"
"s_add_u32 m0, s71, 53856\n"
"ds_read_b64_tr_b16 a[246:247], v241 offset:29568\n"
"v_mfma_f32_16x16x32_bf16 a[104:107], a[248:251], v[104:107], a[104:107]\n"
"buffer_load_dwordx4 v248, s[48:51], s69 offen  lds\n"
"v_xor_b32 v248, 0x10000, v248\n"
"s_add_u32 m0, s71, 59136\n"
"v_mfma_f32_16x16x32_bf16 a[136:139], a[216:219], v[128:131], 0\n"
"buffer_load_dwordx4 v248, s[48:51], s66 offen  lds\n"
"v_mfma_f32_16x16x32_bf16 a[136:139], a[248:251], v[136:139], a[136:139]\n"
"s_add_u32 m0, s71, 60192\n"
"v_mfma_f32_16x16x32_bf16 a[168:171], a[216:219], v[160:163], 0\n"
"buffer_load_dwordx4 v248, s[48:51], s67 offen  lds\n"
"v_mfma_f32_16x16x32_bf16 a[168:171], a[248:251], v[168:171], a[168:171]\n"
"s_add_u32 m0, s71, 61248\n"
"v_mfma_f32_16x16x32_bf16 a[196:199], a[216:219], v[192:195], 0\n"
"buffer_load_dwordx4 v248, s[48:51], s68 offen  lds\n"
"v_mfma_f32_16x16x32_bf16 a[196:199], a[248:251], v[196:199], a[196:199]\n"
"s_add_u32 m0, s71, 62304\n"
"ds_read_b64_tr_b16 a[216:217], v242 offset:16896\n"
"v_mfma_f32_16x16x32_bf16 a[12:15], a[220:223], v[0:3], 0\n"
"buffer_load_dwordx4 v248, s[48:51], s69 offen  lds\n"
"s_cmp_eq_u32 s72, 1\n"
"s_cselect_b32 s73, 2, 1\n"
"v_lshl_add_u32 v248, s73, 19, v248\n"
"v_xor_b32 v248, 0x30000, v248\n"
"v_mfma_f32_16x16x32_bf16 a[12:15], a[252:255], v[8:11], a[12:15]\n"
"ds_read_b64_tr_b16 a[218:219], v242 offset:21120\n"
"v_mfma_f32_16x16x32_bf16 a[44:47], a[220:223], v[32:35], 0\n"
"ds_read_b64_tr_b16 a[248:249], v242 offset:25344\n"
"v_mfma_f32_16x16x32_bf16 a[44:47], a[252:255], v[40:43], a[44:47]\n"
"ds_read_b64_tr_b16 a[250:251], v242 offset:29568\n"
"v_mfma_f32_16x16x32_bf16 a[76:79], a[220:223], v[64:67], 0\n"
"buffer_load_dwordx2 v[252:253], v254, s[56:59], s45 offen sc1\n"
"v_mfma_f32_16x16x32_bf16 a[76:79], a[252:255], v[72:75], a[76:79]\n"
"v_mfma_f32_16x16x32_bf16 a[108:111], a[220:223], v[96:99], 0\n"
"v_mfma_f32_16x16x32_bf16 a[108:111], a[252:255], v[104:107], a[108:111]\n"
"v_mfma_f32_16x16x32_bf16 a[140:143], a[220:223], v[128:131], 0\n"
"v_mfma_f32_16x16x32_bf16 a[140:143], a[252:255], v[136:139], a[140:143]\n"
"v_mfma_f32_16x16x32_bf16 a[172:175], a[220:223], v[160:163], 0\n"
"s_add_u32 m0, s71, 0\n"
"v_mfma_f32_16x16x32_bf16 a[172:175], a[252:255], v[168:171], a[172:175]\n"
"buffer_load_dwordx4 v248, s[48:51], s66 offen  lds\n"
"v_mfma_f32_16x16x32_bf16 v[116:119], a[224:227], v[0:3], 0\n"
"ds_read_b64_tr_b16 a[220:221], v243 offset:16896\n"
"v_mfma_f32_16x16x32_bf16 v[116:119], v[224:227], v[8:11], v[116:119]\n"
"ds_read_b64_tr_b16 a[222:223], v243 offset:21120\n"
"v_mfma_f32_16x16x32_bf16 v[124:127], a[224:227], v[32:35], 0\n"
"ds_read_b64_tr_b16 a[252:253], v243 offset:25344\n"
"v_mfma_f32_16x16x32_bf16 v[124:127], v[224:227], v[40:43], v[124:127]\n"
"ds_read_b64_tr_b16 a[254:255], v243 offset:29568\n"
"v_mfma_f32_16x16x32_bf16 v[132:135], a[224:227], v[64:67], 0\n"
"v_mfma_f32_16x16x32_bf16 v[132:135], v[224:227], v[72:75], v[132:135]\n"
"s_add_u32 m0, s71, 1056\n"
"v_mfma_f32_16x16x32_bf16 v[140:143], a[224:227], v[96:99], 0\n"
"buffer_load_dwordx4 v248, s[48:51], s67 offen  lds\n"
"v_mfma_f32_16x16x32_bf16 v[140:143], v[224:227], v[104:107], v[140:143]\n"
"v_mfma_f32_16x16x32_bf16 v[148:151], a[224:227], v[128:131], 0\n"
"v_mfma_f32_16x16x32_bf16 v[148:151], v[224:227], v[136:139], v[148:151]\n"
"v_mfma_f32_16x16x32_bf16 v[156:159], a[224:227], v[160:163], 0\n"
"v_mfma_f32_16x16x32_bf16 v[156:159], v[224:227], v[168:171], v[156:159]\n"
"v_mfma_f32_16x16x32_bf16 v[220:223], a[224:227], v[192:195], 0\n"
"s_add_u32 m0, s71, 2112\n"
"v_mfma_f32_16x16x32_bf16 v[220:223], v[224:227], v[196:199], v[220:223]\n"
"buffer_load_dwordx4 v248, s[48:51], s68 offen  lds\n"
"v_mfma_f32_16x16x32_bf16 v[164:167], a[228:231], v[0:3], 0\n"
"ds_read_b64_tr_b16 a[224:225], v244 offset:16896\n"
"v_mfma_f32_16x16x32_bf16 v[164:167], v[228:231], v[8:11], v[164:167]\n"
"ds_read_b64_tr_b16 a[226:227], v244 offset:21120\n"
"v_mfma_f32_16x16x32_bf16 v[172:175], a[228:231], v[32:35], 0\n"
"ds_read_b64_tr_b16 v[224:225], v244 offset:25344\n"
"v_mfma_f32_16x16x32_bf16 v[172:175], v[228:231], v[40:43], v[172:175]\n"
"ds_read_b64_tr_b16 v[226:227], v244 offset:29568\n"
"v_mfma_f32_16x16x32_bf16 v[180:183], a[228:231], v[64:67], 0\n"
"v_mfma_f32_16x16x32_bf16 v[180:183], v[228:231], v[72:75], v[180:183]\n"
"s_add_u32 m0, s71, 3168\n"
"v_mfma_f32_16x16x32_bf16 v[188:191], a[228:231], v[96:99], 0\n"
"buffer_load_dwordx4 v248, s[48:51], s69 offen  lds\n"
"v_xor_b32 v248, 0x10000, v248\n"
"v_mfma_f32_16x16x32_bf16 v[188:191], v[228:231], v[104:107], v[188:191]\n"
"v_mfma_f32_16x16x32_bf16 v[208:211], a[228:231], v[128:131], 0\n"
"v_mfma_f32_16x16x32_bf16 v[208:211], v[228:231], v[136:139], v[208:211]\n"
"v_mfma_f32_16x16x32_bf16 v[212:215], a[228:231], v[160:163], 0\n"
"v_mfma_f32_16x16x32_bf16 v[212:215], v[228:231], v[168:171], v[212:215]\n"
"ds_read_b64_tr_b16 a[228:229], v245 offset:16896\n"
"v_mfma_f32_16x16x32_bf16 a[24:27], a[232:235], v[0:3], 0\n"
"s_add_u32 m0, s71, 8448\n"
"ds_read_b64_tr_b16 a[230:231], v245 offset:21120\n"
"v_mfma_f32_16x16x32_bf16 a[24:27], v[232:235], v[8:11], a[24:27]\n"
"buffer_load_dwordx4 v248, s[48:51], s66 offen  lds\n"
"v_mfma_f32_16x16x32_bf16 a[56:59], a[232:235], v[32:35], 0\n"
"ds_read_b64_tr_b16 v[228:229], v245 offset:25344\n"
"v_mfma_f32_16x16x32_bf16 a[56:59], v[232:235], v[40:43], a[56:59]\n"
"ds_read_b64_tr_b16 v[230:231], v245 offset:29568\n"
"v_mfma_f32_16x16x32_bf16 a[88:91], a[232:235], v[64:67], 0\n"
"v_mfma_f32_16x16x32_bf16 a[88:91], v[232:235], v[72:75], a[88:91]\n"
"v_mfma_f32_16x16x32_bf16 a[120:123], a[232:235], v[96:99], 0\n"
"v_mfma_f32_16x16x32_bf16 a[120:123], v[232:235], v[104:107], a[120:123]\n"
"s_add_u32 m0, s71, 9504\n"
"v_mfma_f32_16x16x32_bf16 a[152:155], a[232:235], v[128:131], 0\n"
"buffer_load_dwordx4 v248, s[48:51], s67 offen  lds\n"
"v_mfma_f32_16x16x32_bf16 a[152:155], v[232:235], v[136:139], a[152:155]\n"
"v_mfma_f32_16x16x32_bf16 a[184:187], a[232:235], v[160:163], 0\n"
"v_mfma_f32_16x16x32_bf16 a[184:187], v[232:235], v[168:171], a[184:187]\n"
"v_mfma_f32_16x16x32_bf16 a[204:207], a[232:235], v[192:195], 0\n"
"v_mfma_f32_16x16x32_bf16 a[204:207], v[232:235], v[196:199], a[204:207]\n"
"ds_read_b64_tr_b16 a[232:233], v246 offset:16896\n"
"v_mfma_f32_16x16x32_bf16 a[28:31], a[236:239], v[0:3], 0\n"
"s_add_u32 m0, s71, 10560\n"
"ds_read_b64_tr_b16 a[234:235], v246 offset:21120\n"
"v_mfma_f32_16x16x32_bf16 a[28:31], v[236:239], v[8:11], a[28:31]\n"
"buffer_load_dwordx4 v248, s[48:51], s68 offen  lds\n"
"v_mfma_f32_16x16x32_bf16 a[60:63], a[236:239], v[32:35], 0\n"
"ds_read_b64_tr_b16 v[232:233], v246 offset:25344\n"
"v_mfma_f32_16x16x32_bf16 a[60:63], v[236:239], v[40:43], a[60:63]\n"
"ds_read_b64_tr_b16 v[234:235], v246 offset:29568\n"
"v_mfma_f32_16x16x32_bf16 a[92:95], a[236:239], v[64:67], 0\n"
"v_mfma_f32_16x16x32_bf16 a[92:95], v[236:239], v[72:75], a[92:95]\n"
"v_mfma_f32_16x16x32_bf16 a[124:127], a[236:239], v[96:99], 0\n"
"v_mfma_f32_16x16x32_bf16 a[124:127], v[236:239], v[104:107], a[124:127]\n"
"s_add_u32 m0, s71, 11616\n"
"v_mfma_f32_16x16x32_bf16 a[156:159], a[236:239], v[128:131], 0\n"
"buffer_load_dwordx4 v248, s[48:51], s69 offen  lds\n"
"v_xor_b32 v248, 0x30000, v248\n"
"v_mfma_f32_16x16x32_bf16 a[156:159], v[236:239], v[136:139], a[156:159]\n"
"v_mfma_f32_16x16x32_bf16 a[188:191], a[236:239], v[160:163], 0\n"
"v_mfma_f32_16x16x32_bf16 a[188:191], v[236:239], v[168:171], a[188:191]\n"
"s_waitcnt lgkmcnt(15)\n"
"s_waitcnt vmcnt(8)\n"
"v_cmp_ne_u64 vcc, v[252:253], v[250:251]\n"
"s_nop 1\n"
"s_cmp_eq_u64 vcc, 0\n"
"s_cbranch_scc0 Lpf%=\n"
"Lpfr%=:\n"
"v_mfma_f32_16x16x32_bf16 v[20:23], a[208:211], v[16:19], v[20:23]\n"
"ds_read_b64_tr_b16 a[236:237], v247 offset:16896\n"
"v_mfma_f32_16x16x32_bf16 v[20:23], a[240:243], v[24:27], v[20:23]\n"
"ds_read_b64_tr_b16 a[238:239], v247 offset:21120\n"
"buffer_load_dwordx4 v[4:7], v249, s[56:59], s60 offen offset:0 sc1\n"
"v_mfma_f32_16x16x32_bf16 v[28:31], a[208:211], v[48:51], v[28:31]\n"
"ds_read_b64_tr_b16 v[236:237], v247 offset:25344\n"
"v_mfma_f32_16x16x32_bf16 v[28:31], a[240:243], v[56:59], v[28:31]\n"
"ds_read_b64_tr_b16 v[238:239], v247 offset:29568\n"
"v_mfma_f32_16x16x32_bf16 v[36:39], a[208:211], v[80:83], v[36:39]\n"
"buffer_load_dwordx4 a[64:67], v249, s[56:59], s61 offen offset:0 sc1\n"
"v_mfma_f32_16x16x32_bf16 v[36:39], a[240:243], v[88:91], v[36:39]\n"
"v_mfma_f32_16x16x32_bf16 v[44:47], a[208:211], v[112:115], v[44:47]\n"
"v_mfma_f32_16x16x32_bf16 v[44:47], a[240:243], v[120:123], v[44:47]\n"
"buffer_load_dwordx4 a[4:7], v249, s[56:59], s62 offen offset:0 sc1\n"
"v_mfma_f32_16x16x32_bf16 v[52:55], a[208:211], v[144:147], v[52:55]\n"
"v_mfma_f32_16x16x32_bf16 v[52:55], a[240:243], v[152:155], v[52:55]\n"
"v_mfma_f32_16x16x32_bf16 v[60:63], a[208:211], v[176:179], v[60:63]\n"
"buffer_load_dwordx4 a[132:135], v249, s[56:59], s63 offen offset:0 sc1\n"
"v_mfma_f32_16x16x32_bf16 v[60:63], a[240:243], v[184:187], v[60:63]\n"
"v_mfma_f32_16x16x32_bf16 v[216:219], a[208:211], v[200:203], v[216:219]\n"
"v_mfma_f32_16x16x32_bf16 v[216:219], a[240:243], v[204:207], v[216:219]\n"
"s_waitcnt lgkmcnt(0)\n"
"s_barrier\n"
"buffer_load_dwordx4 a[80:83], v249, s[56:59], s64 offen offset:0 sc1\n"
"v_mfma_f32_16x16x32_bf16 v[68:71], a[212:215], v[16:19], v[68:71]\n"
"ds_read_b64_tr_b16 a[208:209], v240 offset:33792\n"
"v_mfma_f32_16x16x32_bf16 v[68:71], a[244:247], v[24:27], v[68:71]\n"
"ds_read_b64_tr_b16 a[210:211], v240 offset:38016\n"
"v_mfma_f32_16x16x32_bf16 v[76:79], a[212:215], v[48:51], v[76:79]\n"
"buffer_load_dwordx4 a[20:23], v249, s[56:59], s65 offen offset:0 sc1\n"
"v_mfma_f32_16x16x32_bf16 v[76:79], a[244:247], v[56:59], v[76:79]\n"
"ds_read_b64_tr_b16 a[240:241], v240 offset:42240\n"
"v_mfma_f32_16x16x32_bf16 v[84:87], a[212:215], v[80:83], v[84:87]\n"
"ds_read_b64_tr_b16 a[242:243], v240 offset:46464\n"
"v_mfma_f32_16x16x32_bf16 v[84:87], a[244:247], v[88:91], v[84:87]\n"
"buffer_load_dwordx4 a[148:151], v249, s[56:59], s70 offen offset:0 sc1\n"
"v_xor_b32 v249, 0x400, v249\n"
"v_mfma_f32_16x16x32_bf16 v[92:95], a[212:215], v[112:115], v[92:95]\n"
"v_mfma_f32_16x16x32_bf16 v[92:95], a[244:247], v[120:123], v[92:95]\n"
"v_mfma_f32_16x16x32_bf16 v[100:103], a[212:215], v[144:147], v[100:103]\n"
"buffer_load_dwordx4 v[12:15], v249, s[56:59], s60 offen offset:0 sc1\n"
"v_mfma_f32_16x16x32_bf16 v[100:103], a[244:247], v[152:155], v[100:103]\n"
"v_mfma_f32_16x16x32_bf16 v[108:111], a[212:215], v[176:179], v[108:111]\n"
"v_mfma_f32_16x16x32_bf16 v[108:111], a[244:247], v[184:187], v[108:111]\n"
"buffer_load_dwordx4 a[96:99], v249, s[56:59], s61 offen offset:0 sc1\n"
"v_mfma_f32_16x16x32_bf16 a[8:11], a[216:219], v[16:19], a[8:11]\n"
"ds_read_b64_tr_b16 a[212:213], v241 offset:33792\n"
"v_mfma_f32_16x16x32_bf16 a[8:11], a[248:251], v[24:27], a[8:11]\n"
"ds_read_b64_tr_b16 a[214:215], v241 offset:38016\n"
"v_mfma_f32_16x16x32_bf16 a[40:43], a[216:219], v[48:51], a[40:43]\n"
"buffer_load_dwordx4 a[36:39], v249, s[56:59], s62 offen offset:0 sc1\n"
"v_mfma_f32_16x16x32_bf16 a[40:43], a[248:251], v[56:59], a[40:43]\n"
"ds_read_b64_tr_b16 a[244:245], v241 offset:42240\n"
"v_mfma_f32_16x16x32_bf16 a[72:75], a[216:219], v[80:83], a[72:75]\n"
"ds_read_b64_tr_b16 a[246:247], v241 offset:46464\n"
"v_mfma_f32_16x16x32_bf16 a[72:75], a[248:251], v[88:91], a[72:75]\n"
"buffer_load_dwordx4 a[164:167], v249, s[56:59], s63 offen offset:0 sc1\n"
"v_mfma_f32_16x16x32_bf16 a[104:107], a[216:219], v[112:115], a[104:107]\n"
"v_mfma_f32_16x16x32_bf16 a[104:107], a[248:251], v[120:123], a[104:107]\n"
"v_mfma_f32_16x16x32_bf16 a[136:139], a[216:219], v[144:147], a[136:139]\n"
"buffer_load_dwordx4 a[112:115], v249, s[56:59], s64 offen offset:0 sc1\n"
"v_mfma_f32_16x16x32_bf16 a[136:139], a[248:251], v[152:155], a[136:139]\n"
"v_mfma_f32_16x16x32_bf16 a[168:171], a[216:219], v[176:179], a[168:171]\n"
"v_mfma_f32_16x16x32_bf16 a[168:171], a[248:251], v[184:187], a[168:171]\n"
"buffer_load_dwordx4 a[52:55], v249, s[56:59], s65 offen offset:0 sc1\n"
"v_mfma_f32_16x16x32_bf16 a[196:199], a[216:219], v[200:203], a[196:199]\n"
"v_mfma_f32_16x16x32_bf16 a[196:199], a[248:251], v[204:207], a[196:199]\n"
"ds_read_b64_tr_b16 a[216:217], v242 offset:33792\n"
"v_mfma_f32_16x16x32_bf16 a[12:15], a[220:223], v[16:19], a[12:15]\n"
"buffer_load_dwordx4 a[180:183], v249, s[56:59], s70 offen offset:0 sc1\n"
"v_xor_b32 v249, 0xc00, v249\n"
"v_mfma_f32_16x16x32_bf16 a[12:15], a[252:255], v[24:27], a[12:15]\n"
"ds_read_b64_tr_b16 a[218:219], v242 offset:38016\n"
"v_mfma_f32_16x16x32_bf16 a[44:47], a[220:223], v[48:51], a[44:47]\n"
"buffer_load_dwordx4 a[0:3], v249, s[56:59], s60 offen offset:0 sc1\n"
"v_mfma_f32_16x16x32_bf16 a[44:47], a[252:255], v[56:59], a[44:47]\n"
"s_add_u32 m0, s71, 16896\n"
"ds_read_b64_tr_b16 a[248:249], v242 offset:42240\n"
"v_mfma_f32_16x16x32_bf16 a[76:79], a[220:223], v[80:83], a[76:79]\n"
"buffer_load_dwordx4 v248, s[48:51], s66 offen  lds\n"
"v_mfma_f32_16x16x32_bf16 a[76:79], a[252:255], v[88:91], a[76:79]\n"
"ds_read_b64_tr_b16 a[250:251], v242 offset:46464\n"
"v_mfma_f32_16x16x32_bf16 a[108:111], a[220:223], v[112:115], a[108:111]\n"
"v_mfma_f32_16x16x32_bf16 a[108:111], a[252:255], v[120:123], a[108:111]\n"
"buffer_load_dwordx4 a[128:131], v249, s[56:59], s61 offen offset:0 sc1\n"
"v_mfma_f32_16x16x32_bf16 a[140:143], a[220:223], v[144:147], a[140:143]\n"
"v_mfma_f32_16x16x32_bf16 a[140:143], a[252:255], v[152:155], a[140:143]\n"
"s_add_u32 m0, s71, 17952\n"
"v_mfma_f32_16x16x32_bf16 a[172:175], a[220:223], v[176:179], a[172:175]\n"
"buffer_load_dwordx4 v248, s[48:51], s67 offen  lds\n"
"v_mfma_f32_16x16x32_bf16 a[172:175], a[252:255], v[184:187], a[172:175]\n"
"ds_read_b64_tr_b16 a[220:221], v243 offset:33792\n"
"v_mfma_f32_16x16x32_bf16 v[116:119], a[224:227], v[16:19], v[116:119]\n"
"ds_read_b64_tr_b16 a[222:223], v243 offset:38016\n"
"v_mfma_f32_16x16x32_bf16 v[116:119], v[224:227], v[24:27], v[116:119]\n"
"buffer_load_dwordx4 a[68:71], v249, s[56:59], s62 offen offset:0 sc1\n"
"v_mfma_f32_16x16x32_bf16 v[124:127], a[224:227], v[48:51], v[124:127]\n"
"ds_read_b64_tr_b16 a[252:253], v243 offset:42240\n"
"v_mfma_f32_16x16x32_bf16 v[124:127], v[224:227], v[56:59], v[124:127]\n"
"buffer_load_dwordx4 a[16:19], v249, s[56:59], s63 offen offset:0 sc1\n"
"v_mfma_f32_16x16x32_bf16 v[132:135], a[224:227], v[80:83], v[132:135]\n"
"ds_read_b64_tr_b16 a[254:255], v243 offset:46464\n"
"v_mfma_f32_16x16x32_bf16 v[132:135], v[224:227], v[88:91], v[132:135]\n"
"s_add_u32 m0, s71, 19008\n"
"v_mfma_f32_16x16x32_bf16 v[140:143], a[224:227], v[112:115], v[140:143]\n"
"buffer_load_dwordx4 v248, s[48:51], s68 offen  lds\n"
"v_mfma_f32_16x16x32_bf16 v[140:143], v[224:227], v[120:123], v[140:143]\n"
"v_mfma_f32_16x16x32_bf16 v[148:151], a[224:227], v[144:147], v[148:151]\n"
"v_mfma_f32_16x16x32_bf16 v[148:151], v[224:227], v[152:155], v[148:151]\n"
"buffer_load_dwordx4 a[144:147], v249, s[56:59], s64 offen offset:0 sc1\n"
"v_mfma_f32_16x16x32_bf16 v[156:159], a[224:227], v[176:179], v[156:159]\n"
"v_mfma_f32_16x16x32_bf16 v[156:159], v[224:227], v[184:187], v[156:159]\n"
"v_mfma_f32_16x16x32_bf16 v[220:223], a[224:227], v[200:203], v[220:223]\n"
"buffer_load_dwordx4 a[84:87], v249, s[56:59], s65 offen offset:0 sc1\n"
"v_mfma_f32_16x16x32_bf16 v[220:223], v[224:227], v[204:207], v[220:223]\n"
"s_add_u32 m0, s71, 20064\n"
"ds_read_b64_tr_b16 a[224:225], v244 offset:33792\n"
"v_mfma_f32_16x16x32_bf16 v[164:167], a[228:231], v[16:19], v[164:167]\n"
"buffer_load_dwordx4 v248, s[48:51], s69 offen  lds\n"
"v_xor_b32 v248, 0x10000, v248\n"
"v_mfma_f32_16x16x32_bf16 v[164:167], v[228:231], v[24:27], v[164:167]\n"
"ds_read_b64_tr_b16 a[226:227], v244 offset:38016\n"
"v_mfma_f32_16x16x32_bf16 v[172:175], a[228:231], v[48:51], v[172:175]\n"
"ds_read_b64_tr_b16 v[224:225], v244 offset:42240\n"
"v_mfma_f32_16x16x32_bf16 v[172:175], v[228:231], v[56:59], v[172:175]\n"
"buffer_load_dwordx4 a[192:195], v249, s[56:59], s70 offen offset:0 sc1\n"
"v_xor_b32 v249, 0x400, v249\n"
"v_mfma_f32_16x16x32_bf16 v[180:183], a[228:231], v[80:83], v[180:183]\n"
"ds_read_b64_tr_b16 v[226:227], v244 offset:46464\n"
"v_mfma_f32_16x16x32_bf16 v[180:183], v[228:231], v[88:91], v[180:183]\n"
"v_mfma_f32_16x16x32_bf16 v[188:191], a[228:231], v[112:115], v[188:191]\n"
"buffer_load_dwordx4 a[32:35], v249, s[56:59], s60 offen offset:0 sc1\n"
"v_mfma_f32_16x16x32_bf16 v[188:191], v[228:231], v[120:123], v[188:191]\n"
"v_mfma_f32_16x16x32_bf16 v[208:211], a[228:231], v[144:147], v[208:211]\n"
"s_add_u32 m0, s71, 25344\n"
"v_mfma_f32_16x16x32_bf16 v[208:211], v[228:231], v[152:155], v[208:211]\n"
"buffer_load_dwordx4 v248, s[48:51], s66 offen  lds\n"
"v_mfma_f32_16x16x32_bf16 v[212:215], a[228:231], v[176:179], v[212:215]\n"
"v_mfma_f32_16x16x32_bf16 v[212:215], v[228:231], v[184:187], v[212:215]\n"
"buffer_load_dwordx4 a[160:163], v249, s[56:59], s61 offen offset:0 sc1\n"
"v_mfma_f32_16x16x32_bf16 a[24:27], a[232:235], v[16:19], a[24:27]\n"
"ds_read_b64_tr_b16 a[228:229], v245 offset:33792\n"
"v_mfma_f32_16x16x32_bf16 a[24:27], v[232:235], v[24:27], a[24:27]\n"
"s_add_u32 m0, s71, 26400\n"
"ds_read_b64_tr_b16 a[230:231], v245 offset:38016\n"
"v_mfma_f32_16x16x32_bf16 a[56:59], a[232:235], v[48:51], a[56:59]\n"
"buffer_load_dwordx4 v248, s[48:51], s67 offen  lds\n"
"v_mfma_f32_16x16x32_bf16 a[56:59], v[232:235], v[56:59], a[56:59]\n"
"ds_read_b64_tr_b16 v[228:229], v245 offset:42240\n"
"v_mfma_f32_16x16x32_bf16 a[88:91], a[232:235], v[80:83], a[88:91]\n"
"ds_read_b64_tr_b16 v[230:231], v245 offset:46464\n"
"v_mfma_f32_16x16x32_bf16 a[88:91], v[232:235], v[88:91], a[88:91]\n"
"buffer_load_dwordx4 a[100:103], v249, s[56:59], s62 offen offset:0 sc1\n"
"v_mfma_f32_16x16x32_bf16 a[120:123], a[232:235], v[112:115], a[120:123]\n"
"v_mfma_f32_16x16x32_bf16 a[120:123], v[232:235], v[120:123], a[120:123]\n"
"v_mfma_f32_16x16x32_bf16 a[152:155], a[232:235], v[144:147], a[152:155]\n"
"buffer_load_dwordx4 a[48:51], v249, s[56:59], s63 offen offset:0 sc1\n"
"v_mfma_f32_16x16x32_bf16 a[152:155], v[232:235], v[152:155], a[152:155]\n"
"s_add_u32 m0, s71, 27456\n"
"v_mfma_f32_16x16x32_bf16 a[184:187], a[232:235], v[176:179], a[184:187]\n"
"buffer_load_dwordx4 v248, s[48:51], s68 offen  lds\n"
"v_mfma_f32_16x16x32_bf16 a[184:187], v[232:235], v[184:187], a[184:187]\n"
"v_mfma_f32_16x16x32_bf16 a[204:207], a[232:235], v[200:203], a[204:207]\n"
"v_mfma_f32_16x16x32_bf16 a[204:207], v[232:235], v[204:207], a[204:207]\n"
"buffer_load_dwordx4 a[176:179], v249, s[56:59], s64 offen offset:0 sc1\n"
"v_mfma_f32_16x16x32_bf16 a[28:31], a[236:239], v[16:19], a[28:31]\n"
"ds_read_b64_tr_b16 a[232:233], v246 offset:33792\n"
"v_mfma_f32_16x16x32_bf16 a[28:31], v[236:239], v[24:27], a[28:31]\n"
"ds_read_b64_tr_b16 a[234:235], v246 offset:38016\n"
"v_mfma_f32_16x16x32_bf16 a[60:63], a[236:239], v[48:51], a[60:63]\n"
"buffer_load_dwordx4 a[116:119], v249, s[56:59], s65 offen offset:0 sc1\n"
"v_mfma_f32_16x16x32_bf16 a[60:63], v[236:239], v[56:59], a[60:63]\n"
"ds_read_b64_tr_b16 v[232:233], v246 offset:42240\n"
"v_mfma_f32_16x16x32_bf16 a[92:95], a[236:239], v[80:83], a[92:95]\n"
"s_add_u32 m0, s71, 28512\n"
"ds_read_b64_tr_b16 v[234:235], v246 offset:46464\n"
"v_mfma_f32_16x16x32_bf16 a[92:95], v[236:239], v[88:91], a[92:95]\n"
"buffer_load_dwordx4 v248, s[48:51], s69 offen  lds\n"
"s_cmp_eq_u32 s72, 2\n"
"s_cselect_b32 s73, 2, 1\n"
"v_lshl_add_u32 v248, s73, 19, v248\n"
"v_xor_b32 v248, 0x30000, v248\n"
"v_mfma_f32_16x16x32_bf16 a[124:127], a[236:239], v[112:115], a[124:127]\n"
"v_mfma_f32_16x16x32_bf16 a[124:127], v[236:239], v[120:123], a[124:127]\n"
"v_mfma_f32_16x16x32_bf16 a[156:159], a[236:239], v[144:147], a[156:159]\n"
"buffer_load_dwordx4 a[200:203], v249, s[56:59], s70 offen offset:0 sc1\n"
"v_xor_b32 v249, 0xc00, v249\n"
"s_cmp_eq_u32 s72, 1\n"
"s_cselect_b32 s74, 2, 1\n"
"v_lshl_add_u32 v249, s74, 13, v249\n"
"v_mfma_f32_16x16x32_bf16 a[156:159], v[236:239], v[152:155], a[156:159]\n"
"v_mfma_f32_16x16x32_bf16 a[188:191], a[236:239], v[176:179], a[188:191]\n"
"v_mfma_f32_16x16x32_bf16 a[188:191], v[236:239], v[184:187], a[188:191]\n"
"s_waitcnt vmcnt(22) lgkmcnt(15)\n"
"v_mfma_f32_16x16x32_bf16 v[20:23], a[208:211], v[4:7], v[20:23]\n"
"ds_read_b64_tr_b16 a[236:237], v247 offset:33792\n"
"v_mfma_f32_16x16x32_bf16 v[20:23], a[240:243], v[12:15], v[20:23]\n"
"ds_read_b64_tr_b16 a[238:239], v247 offset:38016\n"
"buffer_load_dwordx4 v[0:3], v249, s[56:59], s60 offen offset:0 sc1\n"
"v_mfma_f32_16x16x32_bf16 v[28:31], a[208:211], a[64:67], v[28:31]\n"
"ds_read_b64_tr_b16 v[236:237], v247 offset:42240\n"
"v_mfma_f32_16x16x32_bf16 v[28:31], a[240:243], a[96:99], v[28:31]\n"
"ds_read_b64_tr_b16 v[238:239], v247 offset:46464\n"
"v_mfma_f32_16x16x32_bf16 v[36:39], a[208:211], a[4:7], v[36:39]\n"
"buffer_load_dwordx4 v[32:35], v249, s[56:59], s61 offen offset:0 sc1\n"
"v_mfma_f32_16x16x32_bf16 v[36:39], a[240:243], a[36:39], v[36:39]\n"
"v_mfma_f32_16x16x32_bf16 v[44:47], a[208:211], a[132:135], v[44:47]\n"
"v_mfma_f32_16x16x32_bf16 v[44:47], a[240:243], a[164:167], v[44:47]\n"
"buffer_load_dwordx4 v[64:67], v249, s[56:59], s62 offen offset:0 sc1\n"
"v_mfma_f32_16x16x32_bf16 v[52:55], a[208:211], a[80:83], v[52:55]\n"
"v_mfma_f32_16x16x32_bf16 v[52:55], a[240:243], a[112:115], v[52:55]\n"
"v_mfma_f32_16x16x32_bf16 v[60:63], a[208:211], a[20:23], v[60:63]\n"
"v_mfma_f32_16x16x32_bf16 v[60:63], a[240:243], a[52:55], v[60:63]\n"
"buffer_load_dwordx4 v[96:99], v249, s[56:59], s63 offen offset:0 sc1\n"
"v_mfma_f32_16x16x32_bf16 v[216:219], a[208:211], a[148:151], v[216:219]\n"
"v_mfma_f32_16x16x32_bf16 v[216:219], a[240:243], a[180:183], v[216:219]\n"
"s_waitcnt lgkmcnt(0)\n"
"s_barrier\n"
"v_mfma_f32_16x16x32_bf16 v[68:71], a[212:215], v[4:7], v[68:71]\n"
"ds_read_b64_tr_b16 a[208:209], v240 offset:50688\n"
"v_mfma_f32_16x16x32_bf16 v[68:71], a[244:247], v[12:15], v[68:71]\n"
"buffer_load_dwordx4 v[128:131], v249, s[56:59], s64 offen offset:0 sc1\n"
"v_mfma_f32_16x16x32_bf16 v[76:79], a[212:215], a[64:67], v[76:79]\n"
"ds_read_b64_tr_b16 a[210:211], v240 offset:54912\n"
"v_mfma_f32_16x16x32_bf16 v[76:79], a[244:247], a[96:99], v[76:79]\n"
"ds_read_b64_tr_b16 a[240:241], v240 offset:59136\n"
"v_mfma_f32_16x16x32_bf16 v[84:87], a[212:215], a[4:7], v[84:87]\n"
"ds_read_b64_tr_b16 a[242:243], v240 offset:63360\n"
"v_mfma_f32_16x16x32_bf16 v[84:87], a[244:247], a[36:39], v[84:87]\n"
"s_add_u32 m0, s71, 33792\n"
"v_mfma_f32_16x16x32_bf16 v[92:95], a[212:215], a[132:135], v[92:95]\n"
"buffer_load_dwordx4 v248, s[48:51], s66 offen  lds\n"
"v_mfma_f32_16x16x32_bf16 v[92:95], a[244:247], a[164:167], v[92:95]\n"
"v_mfma_f32_16x16x32_bf16 v[100:103], a[212:215], a[80:83], v[100:103]\n"
"v_mfma_f32_16x16x32_bf16 v[100:103], a[244:247], a[112:115], v[100:103]\n"
"v_mfma_f32_16x16x32_bf16 v[108:111], a[212:215], a[20:23], v[108:111]\n"
"v_mfma_f32_16x16x32_bf16 v[108:111], a[244:247], a[52:55], v[108:111]\n"
"buffer_load_dwordx4 v[160:163], v249, s[56:59], s65 offen offset:0 sc1\n"
"v_mfma_f32_16x16x32_bf16 a[8:11], a[216:219], v[4:7], a[8:11]\n"
"ds_read_b64_tr_b16 a[212:213], v241 offset:50688\n"
"v_mfma_f32_16x16x32_bf16 a[8:11], a[248:251], v[12:15], a[8:11]\n"
"ds_read_b64_tr_b16 a[214:215], v241 offset:54912\n"
"v_mfma_f32_16x16x32_bf16 a[40:43], a[216:219], a[64:67], a[40:43]\n"
"ds_read_b64_tr_b16 a[244:245], v241 offset:59136\n"
"v_mfma_f32_16x16x32_bf16 a[40:43], a[248:251], a[96:99], a[40:43]\n"
"s_add_u32 m0, s71, 34848\n"
"ds_read_b64_tr_b16 a[246:247], v241 offset:63360\n"
"v_mfma_f32_16x16x32_bf16 a[72:75], a[216:219], a[4:7], a[72:75]\n"
"buffer_load_dwordx4 v248, s[48:51], s67 offen  lds\n"
"v_mfma_f32_16x16x32_bf16 a[72:75], a[248:251], a[36:39], a[72:75]\n"
"v_mfma_f32_16x16x32_bf16 a[104:107], a[216:219], a[132:135], a[104:107]\n"
"v_mfma_f32_16x16x32_bf16 a[104:107], a[248:251], a[164:167], a[104:107]\n"
"v_mfma_f32_16x16x32_bf16 a[136:139], a[216:219], a[80:83], a[136:139]\n"
"v_mfma_f32_16x16x32_bf16 a[136:139], a[248:251], a[112:115], a[136:139]\n"
"buffer_load_dwordx4 v[192:195], v249, s[56:59], s70 offen offset:0 sc1\n"
"v_xor_b32 v249, 0x400, v249\n"
"v_mfma_f32_16x16x32_bf16 a[168:171], a[216:219], a[20:23], a[168:171]\n"
"v_mfma_f32_16x16x32_bf16 a[168:171], a[248:251], a[52:55], a[168:171]\n"
"v_mfma_f32_16x16x32_bf16 a[196:199], a[216:219], a[148:151], a[196:199]\n"
"v_mfma_f32_16x16x32_bf16 a[196:199], a[248:251], a[180:183], a[196:199]\n"
"s_add_u32 m0, s71, 35904\n"
"ds_read_b64_tr_b16 a[216:217], v242 offset:50688\n"
"v_mfma_f32_16x16x32_bf16 a[12:15], a[220:223], v[4:7], a[12:15]\n"
"buffer_load_dwordx4 v248, s[48:51], s68 offen  lds\n"
"v_mfma_f32_16x16x32_bf16 a[12:15], a[252:255], v[12:15], a[12:15]\n"
"ds_read_b64_tr_b16 a[218:219], v242 offset:54912\n"
"v_mfma_f32_16x16x32_bf16 a[44:47], a[220:223], a[64:67], a[44:47]\n"
"ds_read_b64_tr_b16 a[248:249], v242 offset:59136\n"
"v_mfma_f32_16x16x32_bf16 a[44:47], a[252:255], a[96:99], a[44:47]\n"
"ds_read_b64_tr_b16 a[250:251], v242 offset:63360\n"
"v_mfma_f32_16x16x32_bf16 a[76:79], a[220:223], a[4:7], a[76:79]\n"
"v_mfma_f32_16x16x32_bf16 a[76:79], a[252:255], a[36:39], a[76:79]\n"
"buffer_load_dwordx4 v[8:11], v249, s[56:59], s60 offen offset:0 sc1\n"
"v_mfma_f32_16x16x32_bf16 a[108:111], a[220:223], a[132:135], a[108:111]\n"
"v_mfma_f32_16x16x32_bf16 a[108:111], a[252:255], a[164:167], a[108:111]\n"
"v_mfma_f32_16x16x32_bf16 a[140:143], a[220:223], a[80:83], a[140:143]\n"
"v_mfma_f32_16x16x32_bf16 a[140:143], a[252:255], a[112:115], a[140:143]\n"
"s_add_u32 m0, s71, 36960\n"
"v_mfma_f32_16x16x32_bf16 a[172:175], a[220:223], a[20:23], a[172:175]\n"
"buffer_load_dwordx4 v248, s[48:51], s69 offen  lds\n"
"v_xor_b32 v248, 0x10000, v248\n"
"v_mfma_f32_16x16x32_bf16 a[172:175], a[252:255], a[52:55], a[172:175]\n"
"ds_read_b64_tr_b16 a[220:221], v243 offset:50688\n"
"v_mfma_f32_16x16x32_bf16 v[116:119], a[224:227], v[4:7], v[116:119]\n"
"ds_read_b64_tr_b16 a[222:223], v243 offset:54912\n"
"v_mfma_f32_16x16x32_bf16 v[116:119], v[224:227], v[12:15], v[116:119]\n"
"ds_read_b64_tr_b16 a[252:253], v243 offset:59136\n"
"v_mfma_f32_16x16x32_bf16 v[124:127], a[224:227], a[64:67], v[124:127]\n"
"ds_read_b64_tr_b16 a[254:255], v243 offset:63360\n"
"v_mfma_f32_16x16x32_bf16 v[124:127], v[224:227], a[96:99], v[124:127]\n"
"buffer_load_dwordx4 v[40:43], v249, s[56:59], s61 offen offset:0 sc1\n"
"v_mfma_f32_16x16x32_bf16 v[132:135], a[224:227], a[4:7], v[132:135]\n"
"v_mfma_f32_16x16x32_bf16 v[132:135], v[224:227], a[36:39], v[132:135]\n"
"v_mfma_f32_16x16x32_bf16 v[140:143], a[224:227], a[132:135], v[140:143]\n"
"v_mfma_f32_16x16x32_bf16 v[140:143], v[224:227], a[164:167], v[140:143]\n"
"v_mfma_f32_16x16x32_bf16 v[148:151], a[224:227], a[80:83], v[148:151]\n"
"buffer_load_dwordx4 v[72:75], v249, s[56:59], s62 offen offset:0 sc1\n"
"v_mfma_f32_16x16x32_bf16 v[148:151], v[224:227], a[112:115], v[148:151]\n"
"v_mfma_f32_16x16x32_bf16 v[156:159], a[224:227], a[20:23], v[156:159]\n"
"v_mfma_f32_16x16x32_bf16 v[156:159], v[224:227], a[52:55], v[156:159]\n"
"v_mfma_f32_16x16x32_bf16 v[220:223], a[224:227], a[148:151], v[220:223]\n"
"s_add_u32 m0, s71, 42240\n"
"v_mfma_f32_16x16x32_bf16 v[220:223], v[224:227], a[180:183], v[220:223]\n"
"buffer_load_dwordx4 v248, s[48:51], s66 offen  lds\n"
"v_mfma_f32_16x16x32_bf16 v[164:167], a[228:231], v[4:7], v[164:167]\n"
"ds_read_b64_tr_b16 a[224:225], v244 offset:50688\n"
"v_mfma_f32_16x16x32_bf16 v[164:167], v[228:231], v[12:15], v[164:167]\n"
"ds_read_b64_tr_b16 a[226:227], v244 offset:54912\n"
"v_mfma_f32_16x16x32_bf16 v[172:175], a[228:231], a[64:67], v[172:175]\n"
"ds_read_b64_tr_b16 v[224:225], v244 offset:59136\n"
"v_mfma_f32_16x16x32_bf16 v[172:175], v[228:231], a[96:99], v[172:175]\n"
"ds_read_b64_tr_b16 v[226:227], v244 offset:63360\n"
"v_mfma_f32_16x16x32_bf16 v[180:183], a[228:231], a[4:7], v[180:183]\n"
"buffer_load_dwordx4 v[104:107], v249, s[56:59], s63 offen offset:0 sc1\n"
"v_mfma_f32_16x16x32_bf16 v[180:183], v[228:231], a[36:39], v[180:183]\n"
"v_mfma_f32_16x16x32_bf16 v[188:191], a[228:231], a[132:135], v[188:191]\n"
"v_mfma_f32_16x16x32_bf16 v[188:191], v[228:231], a[164:167], v[188:191]\n"
"v_mfma_f32_16x16x32_bf16 v[208:211], a[228:231], a[80:83], v[208:211]\n"
"s_add_u32 m0, s71, 43296\n"
"v_mfma_f32_16x16x32_bf16 v[208:211], v[228:231], a[112:115], v[208:211]\n"
"buffer_load_dwordx4 v248, s[48:51], s67 offen  lds\n"
"v_mfma_f32_16x16x32_bf16 v[212:215], a[228:231], a[20:23], v[212:215]\n"
"v_mfma_f32_16x16x32_bf16 v[212:215], v[228:231], a[52:55], v[212:215]\n"
"ds_read_b64_tr_b16 a[228:229], v245 offset:50688\n"
"v_mfma_f32_16x16x32_bf16 a[24:27], a[232:235], v[4:7], a[24:27]\n"
"ds_read_b64_tr_b16 a[230:231], v245 offset:54912\n"
"v_mfma_f32_16x16x32_bf16 a[24:27], v[232:235], v[12:15], a[24:27]\n"
"ds_read_b64_tr_b16 v[228:229], v245 offset:59136\n"
"v_mfma_f32_16x16x32_bf16 a[56:59], a[232:235], a[64:67], a[56:59]\n"
"buffer_load_dwordx4 v[136:139], v249, s[56:59], s64 offen offset:0 sc1\n"
"v_mfma_f32_16x16x32_bf16 a[56:59], v[232:235], a[96:99], a[56:59]\n"
"ds_read_b64_tr_b16 v[230:231], v245 offset:63360\n"
"v_mfma_f32_16x16x32_bf16 a[88:91], a[232:235], a[4:7], a[88:91]\n"
"v_mfma_f32_16x16x32_bf16 a[88:91], v[232:235], a[36:39], a[88:91]\n"
"v_mfma_f32_16x16x32_bf16 a[120:123], a[232:235], a[132:135], a[120:123]\n"
"s_add_u32 m0, s71, 44352\n"
"v_mfma_f32_16x16x32_bf16 a[120:123], v[232:235], a[164:167], a[120:123]\n"
"buffer_load_dwordx4 v248, s[48:51], s68 offen  lds\n"
"v_mfma_f32_16x16x32_bf16 a[152:155], a[232:235], a[80:83], a[152:155]\n"
"v_mfma_f32_16x16x32_bf16 a[152:155], v[232:235], a[112:115], a[152:155]\n"
"v_mfma_f32_16x16x32_bf16 a[184:187], a[232:235], a[20:23], a[184:187]\n"
"v_mfma_f32_16x16x32_bf16 a[184:187], v[232:235], a[52:55], a[184:187]\n"
"v_mfma_f32_16x16x32_bf16 a[204:207], a[232:235], a[148:151], a[204:207]\n"
"buffer_load_dwordx4 v[168:171], v249, s[56:59], s65 offen offset:0 sc1\n"
"v_mfma_f32_16x16x32_bf16 a[204:207], v[232:235], a[180:183], a[204:207]\n"
"ds_read_b64_tr_b16 a[232:233], v246 offset:50688\n"
"v_mfma_f32_16x16x32_bf16 a[28:31], a[236:239], v[4:7], a[28:31]\n"
"ds_read_b64_tr_b16 a[234:235], v246 offset:54912\n"
"v_mfma_f32_16x16x32_bf16 a[28:31], v[236:239], v[12:15], a[28:31]\n"
"ds_read_b64_tr_b16 v[232:233], v246 offset:59136\n"
"v_mfma_f32_16x16x32_bf16 a[60:63], a[236:239], a[64:67], a[60:63]\n"
"s_add_u32 m0, s71, 45408\n"
"ds_read_b64_tr_b16 v[234:235], v246 offset:63360\n"
"v_mfma_f32_16x16x32_bf16 a[60:63], v[236:239], a[96:99], a[60:63]\n"
"buffer_load_dwordx4 v248, s[48:51], s69 offen  lds\n"
"v_xor_b32 v248, 0x30000, v248\n"
"v_mfma_f32_16x16x32_bf16 a[92:95], a[236:239], a[4:7], a[92:95]\n"
"v_mfma_f32_16x16x32_bf16 a[92:95], v[236:239], a[36:39], a[92:95]\n"
"v_mfma_f32_16x16x32_bf16 a[124:127], a[236:239], a[132:135], a[124:127]\n"
"v_mfma_f32_16x16x32_bf16 a[124:127], v[236:239], a[164:167], a[124:127]\n"
"v_mfma_f32_16x16x32_bf16 a[156:159], a[236:239], a[80:83], a[156:159]\n"
"buffer_load_dwordx4 v[196:199], v249, s[56:59], s70 offen offset:0 sc1\n"
"v_xor_b32 v249, 0xc00, v249\n"
"v_mfma_f32_16x16x32_bf16 a[156:159], v[236:239], a[112:115], a[156:159]\n"
"v_mfma_f32_16x16x32_bf16 a[188:191], a[236:239], a[20:23], a[188:191]\n"
"v_mfma_f32_16x16x32_bf16 a[188:191], v[236:239], a[52:55], a[188:191]\n"
"s_waitcnt vmcnt(22) lgkmcnt(15)\n"
"v_mfma_f32_16x16x32_bf16 v[20:23], a[208:211], a[0:3], v[20:23]\n"
"ds_read_b64_tr_b16 a[236:237], v247 offset:50688\n"
"v_mfma_f32_16x16x32_bf16 v[20:23], a[240:243], a[32:35], v[20:23]\n"
"ds_read_b64_tr_b16 a[238:239], v247 offset:54912\n"
"buffer_load_dwordx4 v[16:19], v249, s[56:59], s60 offen offset:0 sc1\n"
"v_mfma_f32_16x16x32_bf16 v[28:31], a[208:211], a[128:131], v[28:31]\n"
"ds_read_b64_tr_b16 v[236:237], v247 offset:59136\n"
"v_mfma_f32_16x16x32_bf16 v[28:31], a[240:243], a[160:163], v[28:31]\n"
"ds_read_b64_tr_b16 v[238:239], v247 offset:63360\n"
"v_mfma_f32_16x16x32_bf16 v[36:39], a[208:211], a[68:71], v[36:39]\n"
"buffer_load_dwordx4 v[48:51], v249, s[56:59], s61 offen offset:0 sc1\n"
"v_mfma_f32_16x16x32_bf16 v[36:39], a[240:243], a[100:103], v[36:39]\n"
"v_mfma_f32_16x16x32_bf16 v[44:47], a[208:211], a[16:19], v[44:47]\n"
"v_mfma_f32_16x16x32_bf16 v[44:47], a[240:243], a[48:51], v[44:47]\n"
"buffer_load_dwordx4 v[80:83], v249, s[56:59], s62 offen offset:0 sc1\n"
"v_mfma_f32_16x16x32_bf16 v[52:55], a[208:211], a[144:147], v[52:55]\n"
"v_mfma_f32_16x16x32_bf16 v[52:55], a[240:243], a[176:179], v[52:55]\n"
"v_mfma_f32_16x16x32_bf16 v[60:63], a[208:211], a[84:87], v[60:63]\n"
"v_mfma_f32_16x16x32_bf16 v[60:63], a[240:243], a[116:119], v[60:63]\n"
"buffer_load_dwordx4 v[112:115], v249, s[56:59], s63 offen offset:0 sc1\n"
"v_mfma_f32_16x16x32_bf16 v[216:219], a[208:211], a[192:195], v[216:219]\n"
"v_mfma_f32_16x16x32_bf16 v[216:219], a[240:243], a[200:203], v[216:219]\n"
"s_waitcnt lgkmcnt(0)\n"
"s_barrier\n"
"v_mfma_f32_16x16x32_bf16 v[68:71], a[212:215], a[0:3], v[68:71]\n"
"ds_read_b64_tr_b16 a[208:209], v240 offset:0\n"
"v_mfma_f32_16x16x32_bf16 v[68:71], a[244:247], a[32:35], v[68:71]\n"
"buffer_load_dwordx4 v[144:147], v249, s[56:59], s64 offen offset:0 sc1\n"
"v_mfma_f32_16x16x32_bf16 v[76:79], a[212:215], a[128:131], v[76:79]\n"
"ds_read_b64_tr_b16 a[210:211], v240 offset:4224\n"
"v_mfma_f32_16x16x32_bf16 v[76:79], a[244:247], a[160:163], v[76:79]\n"
"ds_read_b64_tr_b16 a[240:241], v240 offset:8448\n"
"v_mfma_f32_16x16x32_bf16 v[84:87], a[212:215], a[68:71], v[84:87]\n"
"ds_read_b64_tr_b16 a[242:243], v240 offset:12672\n"
"v_mfma_f32_16x16x32_bf16 v[84:87], a[244:247], a[100:103], v[84:87]\n"
"s_add_u32 m0, s71, 50688\n"
"v_mfma_f32_16x16x32_bf16 v[92:95], a[212:215], a[16:19], v[92:95]\n"
"buffer_load_dwordx4 v248, s[48:51], s66 offen  lds\n"
"v_mfma_f32_16x16x32_bf16 v[92:95], a[244:247], a[48:51], v[92:95]\n"
"v_mfma_f32_16x16x32_bf16 v[100:103], a[212:215], a[144:147], v[100:103]\n"
"v_mfma_f32_16x16x32_bf16 v[100:103], a[244:247], a[176:179], v[100:103]\n"
"v_mfma_f32_16x16x32_bf16 v[108:111], a[212:215], a[84:87], v[108:111]\n"
"v_mfma_f32_16x16x32_bf16 v[108:111], a[244:247], a[116:119], v[108:111]\n"
"buffer_load_dwordx4 v[176:179], v249, s[56:59], s65 offen offset:0 sc1\n"
"v_mfma_f32_16x16x32_bf16 a[8:11], a[216:219], a[0:3], a[8:11]\n"
"ds_read_b64_tr_b16 a[212:213], v241 offset:0\n"
"v_mfma_f32_16x16x32_bf16 a[8:11], a[248:251], a[32:35], a[8:11]\n"
"ds_read_b64_tr_b16 a[214:215], v241 offset:4224\n"
"v_mfma_f32_16x16x32_bf16 a[40:43], a[216:219], a[128:131], a[40:43]\n"
"ds_read_b64_tr_b16 a[244:245], v241 offset:8448\n"
"v_mfma_f32_16x16x32_bf16 a[40:43], a[248:251], a[160:163], a[40:43]\n"
"s_add_u32 m0, s71, 51744\n"
"ds_read_b64_tr_b16 a[246:247], v241 offset:12672\n"
"v_mfma_f32_16x16x32_bf16 a[72:75], a[216:219], a[68:71], a[72:75]\n"
"buffer_load_dwordx4 v248, s[48:51], s67 offen  lds\n"
"v_mfma_f32_16x16x32_bf16 a[72:75], a[248:251], a[100:103], a[72:75]\n"
"v_mfma_f32_16x16x32_bf16 a[104:107], a[216:219], a[16:19], a[104:107]\n"
"v_mfma_f32_16x16x32_bf16 a[104:107], a[248:251], a[48:51], a[104:107]\n"
"v_mfma_f32_16x16x32_bf16 a[136:139], a[216:219], a[144:147], a[136:139]\n"
"v_mfma_f32_16x16x32_bf16 a[136:139], a[248:251], a[176:179], a[136:139]\n"
"buffer_load_dwordx4 v[200:203], v249, s[56:59], s70 offen offset:0 sc1\n"
"v_xor_b32 v249, 0x400, v249\n"
"v_mfma_f32_16x16x32_bf16 a[168:171], a[216:219], a[84:87], a[168:171]\n"
"v_mfma_f32_16x16x32_bf16 a[168:171], a[248:251], a[116:119], a[168:171]\n"
"v_mfma_f32_16x16x32_bf16 a[196:199], a[216:219], a[192:195], a[196:199]\n"
"v_mfma_f32_16x16x32_bf16 a[196:199], a[248:251], a[200:203], a[196:199]\n"
"s_add_u32 m0, s71, 52800\n"
"ds_read_b64_tr_b16 a[216:217], v242 offset:0\n"
"v_mfma_f32_16x16x32_bf16 a[12:15], a[220:223], a[0:3], a[12:15]\n"
"buffer_load_dwordx4 v248, s[48:51], s68 offen  lds\n"
"v_mfma_f32_16x16x32_bf16 a[12:15], a[252:255], a[32:35], a[12:15]\n"
"ds_read_b64_tr_b16 a[218:219], v242 offset:4224\n"
"v_mfma_f32_16x16x32_bf16 a[44:47], a[220:223], a[128:131], a[44:47]\n"
"ds_read_b64_tr_b16 a[248:249], v242 offset:8448\n"
"v_mfma_f32_16x16x32_bf16 a[44:47], a[252:255], a[160:163], a[44:47]\n"
"ds_read_b64_tr_b16 a[250:251], v242 offset:12672\n"
"v_mfma_f32_16x16x32_bf16 a[76:79], a[220:223], a[68:71], a[76:79]\n"
"v_mfma_f32_16x16x32_bf16 a[76:79], a[252:255], a[100:103], a[76:79]\n"
"buffer_load_dwordx4 v[24:27], v249, s[56:59], s60 offen offset:0 sc1\n"
"v_mfma_f32_16x16x32_bf16 a[108:111], a[220:223], a[16:19], a[108:111]\n"
"v_mfma_f32_16x16x32_bf16 a[108:111], a[252:255], a[48:51], a[108:111]\n"
"v_mfma_f32_16x16x32_bf16 a[140:143], a[220:223], a[144:147], a[140:143]\n"
"v_mfma_f32_16x16x32_bf16 a[140:143], a[252:255], a[176:179], a[140:143]\n"
"s_add_u32 m0, s71, 53856\n"
"v_mfma_f32_16x16x32_bf16 a[172:175], a[220:223], a[84:87], a[172:175]\n"
"buffer_load_dwordx4 v248, s[48:51], s69 offen  lds\n"
"v_xor_b32 v248, 0x10000, v248\n"
"v_mfma_f32_16x16x32_bf16 a[172:175], a[252:255], a[116:119], a[172:175]\n"
"ds_read_b64_tr_b16 a[220:221], v243 offset:0\n"
"v_mfma_f32_16x16x32_bf16 v[116:119], a[224:227], a[0:3], v[116:119]\n"
"ds_read_b64_tr_b16 a[222:223], v243 offset:4224\n"
"v_mfma_f32_16x16x32_bf16 v[116:119], v[224:227], a[32:35], v[116:119]\n"
"ds_read_b64_tr_b16 a[252:253], v243 offset:8448\n"
"v_mfma_f32_16x16x32_bf16 v[124:127], a[224:227], a[128:131], v[124:127]\n"
"ds_read_b64_tr_b16 a[254:255], v243 offset:12672\n"
"v_mfma_f32_16x16x32_bf16 v[124:127], v[224:227], a[160:163], v[124:127]\n"
"buffer_load_dwordx4 v[56:59], v249, s[56:59], s61 offen offset:0 sc1\n"
"v_mfma_f32_16x16x32_bf16 v[132:135], a[224:227], a[68:71], v[132:135]\n"
"v_mfma_f32_16x16x32_bf16 v[132:135], v[224:227], a[100:103], v[132:135]\n"
"v_mfma_f32_16x16x32_bf16 v[140:143], a[224:227], a[16:19], v[140:143]\n"
"v_mfma_f32_16x16x32_bf16 v[140:143], v[224:227], a[48:51], v[140:143]\n"
"v_mfma_f32_16x16x32_bf16 v[148:151], a[224:227], a[144:147], v[148:151]\n"
"buffer_load_dwordx4 v[88:91], v249, s[56:59], s62 offen offset:0 sc1\n"
"v_mfma_f32_16x16x32_bf16 v[148:151], v[224:227], a[176:179], v[148:151]\n"
"v_mfma_f32_16x16x32_bf16 v[156:159], a[224:227], a[84:87], v[156:159]\n"
"v_mfma_f32_16x16x32_bf16 v[156:159], v[224:227], a[116:119], v[156:159]\n"
"v_mfma_f32_16x16x32_bf16 v[220:223], a[224:227], a[192:195], v[220:223]\n"
"s_add_u32 m0, s71, 59136\n"
"v_mfma_f32_16x16x32_bf16 v[220:223], v[224:227], a[200:203], v[220:223]\n"
"buffer_load_dwordx4 v248, s[48:51], s66 offen  lds\n"
"v_mfma_f32_16x16x32_bf16 v[164:167], a[228:231], a[0:3], v[164:167]\n"
"ds_read_b64_tr_b16 a[224:225], v244 offset:0\n"
"v_mfma_f32_16x16x32_bf16 v[164:167], v[228:231], a[32:35], v[164:167]\n"
"ds_read_b64_tr_b16 a[226:227], v244 offset:4224\n"
"v_mfma_f32_16x16x32_bf16 v[172:175], a[228:231], a[128:131], v[172:175]\n"
"ds_read_b64_tr_b16 v[224:225], v244 offset:8448\n"
"v_mfma_f32_16x16x32_bf16 v[172:175], v[228:231], a[160:163], v[172:175]\n"
"ds_read_b64_tr_b16 v[226:227], v244 offset:12672\n"
"v_mfma_f32_16x16x32_bf16 v[180:183], a[228:231], a[68:71], v[180:183]\n"
"buffer_load_dwordx4 v[120:123], v249, s[56:59], s63 offen offset:0 sc1\n"
"v_mfma_f32_16x16x32_bf16 v[180:183], v[228:231], a[100:103], v[180:183]\n"
"v_mfma_f32_16x16x32_bf16 v[188:191], a[228:231], a[16:19], v[188:191]\n"
"v_mfma_f32_16x16x32_bf16 v[188:191], v[228:231], a[48:51], v[188:191]\n"
"v_mfma_f32_16x16x32_bf16 v[208:211], a[228:231], a[144:147], v[208:211]\n"
"s_add_u32 m0, s71, 60192\n"
"v_mfma_f32_16x16x32_bf16 v[208:211], v[228:231], a[176:179], v[208:211]\n"
"buffer_load_dwordx4 v248, s[48:51], s67 offen  lds\n"
"v_mfma_f32_16x16x32_bf16 v[212:215], a[228:231], a[84:87], v[212:215]\n"
"v_mfma_f32_16x16x32_bf16 v[212:215], v[228:231], a[116:119], v[212:215]\n"
"ds_read_b64_tr_b16 a[228:229], v245 offset:0\n"
"v_mfma_f32_16x16x32_bf16 a[24:27], a[232:235], a[0:3], a[24:27]\n"
"ds_read_b64_tr_b16 a[230:231], v245 offset:4224\n"
"v_mfma_f32_16x16x32_bf16 a[24:27], v[232:235], a[32:35], a[24:27]\n"
"ds_read_b64_tr_b16 v[228:229], v245 offset:8448\n"
"v_mfma_f32_16x16x32_bf16 a[56:59], a[232:235], a[128:131], a[56:59]\n"
"buffer_load_dwordx4 v[152:155], v249, s[56:59], s64 offen offset:0 sc1\n"
"v_mfma_f32_16x16x32_bf16 a[56:59], v[232:235], a[160:163], a[56:59]\n"
"ds_read_b64_tr_b16 v[230:231], v245 offset:12672\n"
"v_mfma_f32_16x16x32_bf16 a[88:91], a[232:235], a[68:71], a[88:91]\n"
"v_mfma_f32_16x16x32_bf16 a[88:91], v[232:235], a[100:103], a[88:91]\n"
"v_mfma_f32_16x16x32_bf16 a[120:123], a[232:235], a[16:19], a[120:123]\n"
"s_add_u32 m0, s71, 61248\n"
"v_mfma_f32_16x16x32_bf16 a[120:123], v[232:235], a[48:51], a[120:123]\n"
"buffer_load_dwordx4 v248, s[48:51], s68 offen  lds\n"
"v_mfma_f32_16x16x32_bf16 a[152:155], a[232:235], a[144:147], a[152:155]\n"
"v_mfma_f32_16x16x32_bf16 a[152:155], v[232:235], a[176:179], a[152:155]\n"
"v_mfma_f32_16x16x32_bf16 a[184:187], a[232:235], a[84:87], a[184:187]\n"
"v_mfma_f32_16x16x32_bf16 a[184:187], v[232:235], a[116:119], a[184:187]\n"
"v_mfma_f32_16x16x32_bf16 a[204:207], a[232:235], a[192:195], a[204:207]\n"
"buffer_load_dwordx4 v[184:187], v249, s[56:59], s65 offen offset:0 sc1\n"
"v_mfma_f32_16x16x32_bf16 a[204:207], v[232:235], a[200:203], a[204:207]\n"
"ds_read_b64_tr_b16 a[232:233], v246 offset:0\n"
"v_mfma_f32_16x16x32_bf16 a[28:31], a[236:239], a[0:3], a[28:31]\n"
"ds_read_b64_tr_b16 a[234:235], v246 offset:4224\n"
"v_mfma_f32_16x16x32_bf16 a[28:31], v[236:239], a[32:35], a[28:31]\n"
"ds_read_b64_tr_b16 v[232:233], v246 offset:8448\n"
"v_mfma_f32_16x16x32_bf16 a[60:63], a[236:239], a[128:131], a[60:63]\n"
"s_add_u32 m0, s71, 62304\n"
"ds_read_b64_tr_b16 v[234:235], v246 offset:12672\n"
"v_mfma_f32_16x16x32_bf16 a[60:63], v[236:239], a[160:163], a[60:63]\n"
"buffer_load_dwordx4 v248, s[48:51], s69 offen  lds\n"
"s_cmp_eq_u32 s72, 3\n"
"s_cselect_b32 s73, 2, 1\n"
"v_lshl_add_u32 v248, s73, 19, v248\n"
"v_xor_b32 v248, 0x30000, v248\n"
"v_mfma_f32_16x16x32_bf16 a[92:95], a[236:239], a[68:71], a[92:95]\n"
"v_mfma_f32_16x16x32_bf16 a[92:95], v[236:239], a[100:103], a[92:95]\n"
"v_mfma_f32_16x16x32_bf16 a[124:127], a[236:239], a[16:19], a[124:127]\n"
"v_mfma_f32_16x16x32_bf16 a[124:127], v[236:239], a[48:51], a[124:127]\n"
"v_mfma_f32_16x16x32_bf16 a[156:159], a[236:239], a[144:147], a[156:159]\n"
"buffer_load_dwordx4 v[204:207], v249, s[56:59], s70 offen offset:0 sc1\n"
"v_xor_b32 v249, 0xc00, v249\n"
"s_cmp_eq_u32 s72, 2\n"
"s_cselect_b32 s74, 2, 1\n"
"v_lshl_add_u32 v249, s74, 13, v249\n"
"v_mfma_f32_16x16x32_bf16 a[156:159], v[236:239], a[176:179], a[156:159]\n"
"v_mfma_f32_16x16x32_bf16 a[188:191], a[236:239], a[84:87], a[188:191]\n"
"v_mfma_f32_16x16x32_bf16 a[188:191], v[236:239], a[116:119], a[188:191]\n"
"s_waitcnt vmcnt(22) lgkmcnt(15)\n"
"v_mfma_f32_16x16x32_bf16 v[20:23], a[208:211], v[0:3], v[20:23]\n"
"ds_read_b64_tr_b16 a[236:237], v247 offset:0\n"
"v_mfma_f32_16x16x32_bf16 v[20:23], a[240:243], v[8:11], v[20:23]\n"
"ds_read_b64_tr_b16 a[238:239], v247 offset:4224\n"
"buffer_load_dwordx4 v[4:7], v249, s[56:59], s60 offen offset:0 sc1\n"
"v_mfma_f32_16x16x32_bf16 v[28:31], a[208:211], v[32:35], v[28:31]\n"
"ds_read_b64_tr_b16 v[236:237], v247 offset:8448\n"
"v_mfma_f32_16x16x32_bf16 v[28:31], a[240:243], v[40:43], v[28:31]\n"
"ds_read_b64_tr_b16 v[238:239], v247 offset:12672\n"
"v_mfma_f32_16x16x32_bf16 v[36:39], a[208:211], v[64:67], v[36:39]\n"
"buffer_load_dwordx4 a[64:67], v249, s[56:59], s61 offen offset:0 sc1\n"
"v_mfma_f32_16x16x32_bf16 v[36:39], a[240:243], v[72:75], v[36:39]\n"
"v_mfma_f32_16x16x32_bf16 v[44:47], a[208:211], v[96:99], v[44:47]\n"
"v_mfma_f32_16x16x32_bf16 v[44:47], a[240:243], v[104:107], v[44:47]\n"
"buffer_load_dwordx4 a[4:7], v249, s[56:59], s62 offen offset:0 sc1\n"
"v_mfma_f32_16x16x32_bf16 v[52:55], a[208:211], v[128:131], v[52:55]\n"
"v_mfma_f32_16x16x32_bf16 v[52:55], a[240:243], v[136:139], v[52:55]\n"
"v_mfma_f32_16x16x32_bf16 v[60:63], a[208:211], v[160:163], v[60:63]\n"
"v_mfma_f32_16x16x32_bf16 v[60:63], a[240:243], v[168:171], v[60:63]\n"
"buffer_load_dwordx4 a[132:135], v249, s[56:59], s63 offen offset:0 sc1\n"
"v_mfma_f32_16x16x32_bf16 v[216:219], a[208:211], v[192:195], v[216:219]\n"
"v_mfma_f32_16x16x32_bf16 v[216:219], a[240:243], v[196:199], v[216:219]\n"
"s_waitcnt lgkmcnt(0)\n"
"s_barrier\n"
"v_mfma_f32_16x16x32_bf16 v[68:71], a[212:215], v[0:3], v[68:71]\n"
"ds_read_b64_tr_b16 a[208:209], v240 offset:16896\n"
"v_mfma_f32_16x16x32_bf16 v[68:71], a[244:247], v[8:11], v[68:71]\n"
"buffer_load_dwordx4 a[80:83], v249, s[56:59], s64 offen offset:0 sc1\n"
"v_mfma_f32_16x16x32_bf16 v[76:79], a[212:215], v[32:35], v[76:79]\n"
"ds_read_b64_tr_b16 a[210:211], v240 offset:21120\n"
"v_mfma_f32_16x16x32_bf16 v[76:79], a[244:247], v[40:43], v[76:79]\n"
"ds_read_b64_tr_b16 a[240:241], v240 offset:25344\n"
"v_mfma_f32_16x16x32_bf16 v[84:87], a[212:215], v[64:67], v[84:87]\n"
"ds_read_b64_tr_b16 a[242:243], v240 offset:29568\n"
"v_mfma_f32_16x16x32_bf16 v[84:87], a[244:247], v[72:75], v[84:87]\n"
"s_add_u32 m0, s71, 0\n"
"v_mfma_f32_16x16x32_bf16 v[92:95], a[212:215], v[96:99], v[92:95]\n"
"buffer_load_dwordx4 v248, s[48:51], s66 offen  lds\n"
"v_mfma_f32_16x16x32_bf16 v[92:95], a[244:247], v[104:107], v[92:95]\n"
"v_mfma_f32_16x16x32_bf16 v[100:103], a[212:215], v[128:131], v[100:103]\n"
"v_mfma_f32_16x16x32_bf16 v[100:103], a[244:247], v[136:139], v[100:103]\n"
"v_mfma_f32_16x16x32_bf16 v[108:111], a[212:215], v[160:163], v[108:111]\n"
"v_mfma_f32_16x16x32_bf16 v[108:111], a[244:247], v[168:171], v[108:111]\n"
"buffer_load_dwordx4 a[20:23], v249, s[56:59], s65 offen offset:0 sc1\n"
"v_mfma_f32_16x16x32_bf16 a[8:11], a[216:219], v[0:3], a[8:11]\n"
"ds_read_b64_tr_b16 a[212:213], v241 offset:16896\n"
"v_mfma_f32_16x16x32_bf16 a[8:11], a[248:251], v[8:11], a[8:11]\n"
"ds_read_b64_tr_b16 a[214:215], v241 offset:21120\n"
"v_mfma_f32_16x16x32_bf16 a[40:43], a[216:219], v[32:35], a[40:43]\n"
"ds_read_b64_tr_b16 a[244:245], v241 offset:25344\n"
"v_mfma_f32_16x16x32_bf16 a[40:43], a[248:251], v[40:43], a[40:43]\n"
"s_add_u32 m0, s71, 1056\n"
"ds_read_b64_tr_b16 a[246:247], v241 offset:29568\n"
"v_mfma_f32_16x16x32_bf16 a[72:75], a[216:219], v[64:67], a[72:75]\n"
"buffer_load_dwordx4 v248, s[48:51], s67 offen  lds\n"
"v_mfma_f32_16x16x32_bf16 a[72:75], a[248:251], v[72:75], a[72:75]\n"
"v_mfma_f32_16x16x32_bf16 a[104:107], a[216:219], v[96:99], a[104:107]\n"
"v_mfma_f32_16x16x32_bf16 a[104:107], a[248:251], v[104:107], a[104:107]\n"
"v_mfma_f32_16x16x32_bf16 a[136:139], a[216:219], v[128:131], a[136:139]\n"
"v_mfma_f32_16x16x32_bf16 a[136:139], a[248:251], v[136:139], a[136:139]\n"
"buffer_load_dwordx4 a[148:151], v249, s[56:59], s70 offen offset:0 sc1\n"
"v_xor_b32 v249, 0x400, v249\n"
"v_mfma_f32_16x16x32_bf16 a[168:171], a[216:219], v[160:163], a[168:171]\n"
"v_mfma_f32_16x16x32_bf16 a[168:171], a[248:251], v[168:171], a[168:171]\n"
"v_mfma_f32_16x16x32_bf16 a[196:199], a[216:219], v[192:195], a[196:199]\n"
"v_mfma_f32_16x16x32_bf16 a[196:199], a[248:251], v[196:199], a[196:199]\n"
"s_add_u32 m0, s71, 2112\n"
"ds_read_b64_tr_b16 a[216:217], v242 offset:16896\n"
"v_mfma_f32_16x16x32_bf16 a[12:15], a[220:223], v[0:3], a[12:15]\n"
"buffer_load_dwordx4 v248, s[48:51], s68 offen  lds\n"
"v_mfma_f32_16x16x32_bf16 a[12:15], a[252:255], v[8:11], a[12:15]\n"
"ds_read_b64_tr_b16 a[218:219], v242 offset:21120\n"
"v_mfma_f32_16x16x32_bf16 a[44:47], a[220:223], v[32:35], a[44:47]\n"
"ds_read_b64_tr_b16 a[248:249], v242 offset:25344\n"
"v_mfma_f32_16x16x32_bf16 a[44:47], a[252:255], v[40:43], a[44:47]\n"
"ds_read_b64_tr_b16 a[250:251], v242 offset:29568\n"
"v_mfma_f32_16x16x32_bf16 a[76:79], a[220:223], v[64:67], a[76:79]\n"
"v_mfma_f32_16x16x32_bf16 a[76:79], a[252:255], v[72:75], a[76:79]\n"
"buffer_load_dwordx4 v[12:15], v249, s[56:59], s60 offen offset:0 sc1\n"
"v_mfma_f32_16x16x32_bf16 a[108:111], a[220:223], v[96:99], a[108:111]\n"
"v_mfma_f32_16x16x32_bf16 a[108:111], a[252:255], v[104:107], a[108:111]\n"
"v_mfma_f32_16x16x32_bf16 a[140:143], a[220:223], v[128:131], a[140:143]\n"
"v_mfma_f32_16x16x32_bf16 a[140:143], a[252:255], v[136:139], a[140:143]\n"
"s_add_u32 m0, s71, 3168\n"
"v_mfma_f32_16x16x32_bf16 a[172:175], a[220:223], v[160:163], a[172:175]\n"
"buffer_load_dwordx4 v248, s[48:51], s69 offen  lds\n"
"v_xor_b32 v248, 0x10000, v248\n"
"v_mfma_f32_16x16x32_bf16 a[172:175], a[252:255], v[168:171], a[172:175]\n"
"ds_read_b64_tr_b16 a[220:221], v243 offset:16896\n"
"v_mfma_f32_16x16x32_bf16 v[116:119], a[224:227], v[0:3], v[116:119]\n"
"ds_read_b64_tr_b16 a[222:223], v243 offset:21120\n"
"v_mfma_f32_16x16x32_bf16 v[116:119], v[224:227], v[8:11], v[116:119]\n"
"ds_read_b64_tr_b16 a[252:253], v243 offset:25344\n"
"v_mfma_f32_16x16x32_bf16 v[124:127], a[224:227], v[32:35], v[124:127]\n"
"ds_read_b64_tr_b16 a[254:255], v243 offset:29568\n"
"v_mfma_f32_16x16x32_bf16 v[124:127], v[224:227], v[40:43], v[124:127]\n"
"buffer_load_dwordx4 a[96:99], v249, s[56:59], s61 offen offset:0 sc1\n"
"v_mfma_f32_16x16x32_bf16 v[132:135], a[224:227], v[64:67], v[132:135]\n"
"v_mfma_f32_16x16x32_bf16 v[132:135], v[224:227], v[72:75], v[132:135]\n"
"v_mfma_f32_16x16x32_bf16 v[140:143], a[224:227], v[96:99], v[140:143]\n"
"v_mfma_f32_16x16x32_bf16 v[140:143], v[224:227], v[104:107], v[140:143]\n"
"v_mfma_f32_16x16x32_bf16 v[148:151], a[224:227], v[128:131], v[148:151]\n"
"buffer_load_dwordx4 a[36:39], v249, s[56:59], s62 offen offset:0 sc1\n"
"v_mfma_f32_16x16x32_bf16 v[148:151], v[224:227], v[136:139], v[148:151]\n"
"v_mfma_f32_16x16x32_bf16 v[156:159], a[224:227], v[160:163], v[156:159]\n"
"v_mfma_f32_16x16x32_bf16 v[156:159], v[224:227], v[168:171], v[156:159]\n"
"v_mfma_f32_16x16x32_bf16 v[220:223], a[224:227], v[192:195], v[220:223]\n"
"s_add_u32 m0, s71, 8448\n"
"v_mfma_f32_16x16x32_bf16 v[220:223], v[224:227], v[196:199], v[220:223]\n"
"buffer_load_dwordx4 v248, s[48:51], s66 offen  lds\n"
"v_mfma_f32_16x16x32_bf16 v[164:167], a[228:231], v[0:3], v[164:167]\n"
"ds_read_b64_tr_b16 a[224:225], v244 offset:16896\n"
"v_mfma_f32_16x16x32_bf16 v[164:167], v[228:231], v[8:11], v[164:167]\n"
"ds_read_b64_tr_b16 a[226:227], v244 offset:21120\n"
"v_mfma_f32_16x16x32_bf16 v[172:175], a[228:231], v[32:35], v[172:175]\n"
"ds_read_b64_tr_b16 v[224:225], v244 offset:25344\n"
"v_mfma_f32_16x16x32_bf16 v[172:175], v[228:231], v[40:43], v[172:175]\n"
"ds_read_b64_tr_b16 v[226:227], v244 offset:29568\n"
"v_mfma_f32_16x16x32_bf16 v[180:183], a[228:231], v[64:67], v[180:183]\n"
"buffer_load_dwordx4 a[164:167], v249, s[56:59], s63 offen offset:0 sc1\n"
"v_mfma_f32_16x16x32_bf16 v[180:183], v[228:231], v[72:75], v[180:183]\n"
"v_mfma_f32_16x16x32_bf16 v[188:191], a[228:231], v[96:99], v[188:191]\n"
"v_mfma_f32_16x16x32_bf16 v[188:191], v[228:231], v[104:107], v[188:191]\n"
"v_mfma_f32_16x16x32_bf16 v[208:211], a[228:231], v[128:131], v[208:211]\n"
"s_add_u32 m0, s71, 9504\n"
"v_mfma_f32_16x16x32_bf16 v[208:211], v[228:231], v[136:139], v[208:211]\n"
"buffer_load_dwordx4 v248, s[48:51], s67 offen  lds\n"
"v_mfma_f32_16x16x32_bf16 v[212:215], a[228:231], v[160:163], v[212:215]\n"
"v_mfma_f32_16x16x32_bf16 v[212:215], v[228:231], v[168:171], v[212:215]\n"
"ds_read_b64_tr_b16 a[228:229], v245 offset:16896\n"
"v_mfma_f32_16x16x32_bf16 a[24:27], a[232:235], v[0:3], a[24:27]\n"
"ds_read_b64_tr_b16 a[230:231], v245 offset:21120\n"
"v_mfma_f32_16x16x32_bf16 a[24:27], v[232:235], v[8:11], a[24:27]\n"
"ds_read_b64_tr_b16 v[228:229], v245 offset:25344\n"
"v_mfma_f32_16x16x32_bf16 a[56:59], a[232:235], v[32:35], a[56:59]\n"
"buffer_load_dwordx4 a[112:115], v249, s[56:59], s64 offen offset:0 sc1\n"
"v_mfma_f32_16x16x32_bf16 a[56:59], v[232:235], v[40:43], a[56:59]\n"
"ds_read_b64_tr_b16 v[230:231], v245 offset:29568\n"
"v_mfma_f32_16x16x32_bf16 a[88:91], a[232:235], v[64:67], a[88:91]\n"
"v_mfma_f32_16x16x32_bf16 a[88:91], v[232:235], v[72:75], a[88:91]\n"
"v_mfma_f32_16x16x32_bf16 a[120:123], a[232:235], v[96:99], a[120:123]\n"
"s_add_u32 m0, s71, 10560\n"
"v_mfma_f32_16x16x32_bf16 a[120:123], v[232:235], v[104:107], a[120:123]\n"
"buffer_load_dwordx4 v248, s[48:51], s68 offen  lds\n"
"v_mfma_f32_16x16x32_bf16 a[152:155], a[232:235], v[128:131], a[152:155]\n"
"v_mfma_f32_16x16x32_bf16 a[152:155], v[232:235], v[136:139], a[152:155]\n"
"v_mfma_f32_16x16x32_bf16 a[184:187], a[232:235], v[160:163], a[184:187]\n"
"v_mfma_f32_16x16x32_bf16 a[184:187], v[232:235], v[168:171], a[184:187]\n"
"v_mfma_f32_16x16x32_bf16 a[204:207], a[232:235], v[192:195], a[204:207]\n"
"buffer_load_dwordx4 a[52:55], v249, s[56:59], s65 offen offset:0 sc1\n"
"v_mfma_f32_16x16x32_bf16 a[204:207], v[232:235], v[196:199], a[204:207]\n"
"ds_read_b64_tr_b16 a[232:233], v246 offset:16896\n"
"v_mfma_f32_16x16x32_bf16 a[28:31], a[236:239], v[0:3], a[28:31]\n"
"ds_read_b64_tr_b16 a[234:235], v246 offset:21120\n"
"v_mfma_f32_16x16x32_bf16 a[28:31], v[236:239], v[8:11], a[28:31]\n"
"ds_read_b64_tr_b16 v[232:233], v246 offset:25344\n"
"v_mfma_f32_16x16x32_bf16 a[60:63], a[236:239], v[32:35], a[60:63]\n"
"s_add_u32 m0, s71, 11616\n"
"ds_read_b64_tr_b16 v[234:235], v246 offset:29568\n"
"v_mfma_f32_16x16x32_bf16 a[60:63], v[236:239], v[40:43], a[60:63]\n"
"buffer_load_dwordx4 v248, s[48:51], s69 offen  lds\n"
"v_xor_b32 v248, 0x30000, v248\n"
"v_mfma_f32_16x16x32_bf16 a[92:95], a[236:239], v[64:67], a[92:95]\n"
"v_mfma_f32_16x16x32_bf16 a[92:95], v[236:239], v[72:75], a[92:95]\n"
"v_mfma_f32_16x16x32_bf16 a[124:127], a[236:239], v[96:99], a[124:127]\n"
"v_mfma_f32_16x16x32_bf16 a[124:127], v[236:239], v[104:107], a[124:127]\n"
"v_mfma_f32_16x16x32_bf16 a[156:159], a[236:239], v[128:131], a[156:159]\n"
"buffer_load_dwordx4 a[180:183], v249, s[56:59], s70 offen offset:0 sc1\n"
"v_xor_b32 v249, 0xc00, v249\n"
"v_mfma_f32_16x16x32_bf16 a[156:159], v[236:239], v[136:139], a[156:159]\n"
"v_mfma_f32_16x16x32_bf16 a[188:191], a[236:239], v[160:163], a[188:191]\n"
"v_mfma_f32_16x16x32_bf16 a[188:191], v[236:239], v[168:171], a[188:191]\n"
"s_waitcnt vmcnt(22) lgkmcnt(15)\n"
"v_mfma_f32_16x16x32_bf16 v[20:23], a[208:211], v[16:19], v[20:23]\n"
"ds_read_b64_tr_b16 a[236:237], v247 offset:16896\n"
"v_mfma_f32_16x16x32_bf16 v[20:23], a[240:243], v[24:27], v[20:23]\n"
"ds_read_b64_tr_b16 a[238:239], v247 offset:21120\n"
"buffer_load_dwordx4 a[0:3], v249, s[56:59], s60 offen offset:0 sc1\n"
"v_mfma_f32_16x16x32_bf16 v[28:31], a[208:211], v[48:51], v[28:31]\n"
"ds_read_b64_tr_b16 v[236:237], v247 offset:25344\n"
"v_mfma_f32_16x16x32_bf16 v[28:31], a[240:243], v[56:59], v[28:31]\n"
"ds_read_b64_tr_b16 v[238:239], v247 offset:29568\n"
"v_mfma_f32_16x16x32_bf16 v[36:39], a[208:211], v[80:83], v[36:39]\n"
"buffer_load_dwordx4 a[128:131], v249, s[56:59], s61 offen offset:0 sc1\n"
"v_mfma_f32_16x16x32_bf16 v[36:39], a[240:243], v[88:91], v[36:39]\n"
"v_mfma_f32_16x16x32_bf16 v[44:47], a[208:211], v[112:115], v[44:47]\n"
"v_mfma_f32_16x16x32_bf16 v[44:47], a[240:243], v[120:123], v[44:47]\n"
"buffer_load_dwordx4 a[68:71], v249, s[56:59], s62 offen offset:0 sc1\n"
"v_mfma_f32_16x16x32_bf16 v[52:55], a[208:211], v[144:147], v[52:55]\n"
"v_mfma_f32_16x16x32_bf16 v[52:55], a[240:243], v[152:155], v[52:55]\n"
"v_mfma_f32_16x16x32_bf16 v[60:63], a[208:211], v[176:179], v[60:63]\n"
"v_mfma_f32_16x16x32_bf16 v[60:63], a[240:243], v[184:187], v[60:63]\n"
"buffer_load_dwordx4 a[16:19], v249, s[56:59], s63 offen offset:0 sc1\n"
"v_mfma_f32_16x16x32_bf16 v[216:219], a[208:211], v[200:203], v[216:219]\n"
"v_mfma_f32_16x16x32_bf16 v[216:219], a[240:243], v[204:207], v[216:219]\n"
"s_waitcnt lgkmcnt(0)\n"
"s_barrier\n"
"v_mfma_f32_16x16x32_bf16 v[68:71], a[212:215], v[16:19], v[68:71]\n"
"ds_read_b64_tr_b16 a[208:209], v240 offset:33792\n"
"v_mfma_f32_16x16x32_bf16 v[68:71], a[244:247], v[24:27], v[68:71]\n"
"buffer_load_dwordx4 a[144:147], v249, s[56:59], s64 offen offset:0 sc1\n"
"v_mfma_f32_16x16x32_bf16 v[76:79], a[212:215], v[48:51], v[76:79]\n"
"ds_read_b64_tr_b16 a[210:211], v240 offset:38016\n"
"v_mfma_f32_16x16x32_bf16 v[76:79], a[244:247], v[56:59], v[76:79]\n"
"ds_read_b64_tr_b16 a[240:241], v240 offset:42240\n"
"v_mfma_f32_16x16x32_bf16 v[84:87], a[212:215], v[80:83], v[84:87]\n"
"ds_read_b64_tr_b16 a[242:243], v240 offset:46464\n"
"v_mfma_f32_16x16x32_bf16 v[84:87], a[244:247], v[88:91], v[84:87]\n"
"s_add_u32 m0, s71, 16896\n"
"v_mfma_f32_16x16x32_bf16 v[92:95], a[212:215], v[112:115], v[92:95]\n"
"buffer_load_dwordx4 v248, s[48:51], s66 offen  lds\n"
"v_mfma_f32_16x16x32_bf16 v[92:95], a[244:247], v[120:123], v[92:95]\n"
"v_mfma_f32_16x16x32_bf16 v[100:103], a[212:215], v[144:147], v[100:103]\n"
"v_mfma_f32_16x16x32_bf16 v[100:103], a[244:247], v[152:155], v[100:103]\n"
"v_mfma_f32_16x16x32_bf16 v[108:111], a[212:215], v[176:179], v[108:111]\n"
"v_mfma_f32_16x16x32_bf16 v[108:111], a[244:247], v[184:187], v[108:111]\n"
"buffer_load_dwordx4 a[84:87], v249, s[56:59], s65 offen offset:0 sc1\n"
"v_mfma_f32_16x16x32_bf16 a[8:11], a[216:219], v[16:19], a[8:11]\n"
"ds_read_b64_tr_b16 a[212:213], v241 offset:33792\n"
"v_mfma_f32_16x16x32_bf16 a[8:11], a[248:251], v[24:27], a[8:11]\n"
"ds_read_b64_tr_b16 a[214:215], v241 offset:38016\n"
"v_mfma_f32_16x16x32_bf16 a[40:43], a[216:219], v[48:51], a[40:43]\n"
"ds_read_b64_tr_b16 a[244:245], v241 offset:42240\n"
"v_mfma_f32_16x16x32_bf16 a[40:43], a[248:251], v[56:59], a[40:43]\n"
"s_add_u32 m0, s71, 17952\n"
"ds_read_b64_tr_b16 a[246:247], v241 offset:46464\n"
"v_mfma_f32_16x16x32_bf16 a[72:75], a[216:219], v[80:83], a[72:75]\n"
"buffer_load_dwordx4 v248, s[48:51], s67 offen  lds\n"
"v_mfma_f32_16x16x32_bf16 a[72:75], a[248:251], v[88:91], a[72:75]\n"
"v_mfma_f32_16x16x32_bf16 a[104:107], a[216:219], v[112:115], a[104:107]\n"
"v_mfma_f32_16x16x32_bf16 a[104:107], a[248:251], v[120:123], a[104:107]\n"
"v_mfma_f32_16x16x32_bf16 a[136:139], a[216:219], v[144:147], a[136:139]\n"
"v_mfma_f32_16x16x32_bf16 a[136:139], a[248:251], v[152:155], a[136:139]\n"
"buffer_load_dwordx4 a[192:195], v249, s[56:59], s70 offen offset:0 sc1\n"
"v_xor_b32 v249, 0x400, v249\n"
"v_mfma_f32_16x16x32_bf16 a[168:171], a[216:219], v[176:179], a[168:171]\n"
"v_mfma_f32_16x16x32_bf16 a[168:171], a[248:251], v[184:187], a[168:171]\n"
"v_mfma_f32_16x16x32_bf16 a[196:199], a[216:219], v[200:203], a[196:199]\n"
"v_mfma_f32_16x16x32_bf16 a[196:199], a[248:251], v[204:207], a[196:199]\n"
"s_add_u32 m0, s71, 19008\n"
"ds_read_b64_tr_b16 a[216:217], v242 offset:33792\n"
"v_mfma_f32_16x16x32_bf16 a[12:15], a[220:223], v[16:19], a[12:15]\n"
"buffer_load_dwordx4 v248, s[48:51], s68 offen  lds\n"
"v_mfma_f32_16x16x32_bf16 a[12:15], a[252:255], v[24:27], a[12:15]\n"
"ds_read_b64_tr_b16 a[218:219], v242 offset:38016\n"
"v_mfma_f32_16x16x32_bf16 a[44:47], a[220:223], v[48:51], a[44:47]\n"
"ds_read_b64_tr_b16 a[248:249], v242 offset:42240\n"
"v_mfma_f32_16x16x32_bf16 a[44:47], a[252:255], v[56:59], a[44:47]\n"
"ds_read_b64_tr_b16 a[250:251], v242 offset:46464\n"
"v_mfma_f32_16x16x32_bf16 a[76:79], a[220:223], v[80:83], a[76:79]\n"
"v_mfma_f32_16x16x32_bf16 a[76:79], a[252:255], v[88:91], a[76:79]\n"
"buffer_load_dwordx4 a[32:35], v249, s[56:59], s60 offen offset:0 sc1\n"
"v_mfma_f32_16x16x32_bf16 a[108:111], a[220:223], v[112:115], a[108:111]\n"
"v_mfma_f32_16x16x32_bf16 a[108:111], a[252:255], v[120:123], a[108:111]\n"
"v_mfma_f32_16x16x32_bf16 a[140:143], a[220:223], v[144:147], a[140:143]\n"
"v_mfma_f32_16x16x32_bf16 a[140:143], a[252:255], v[152:155], a[140:143]\n"
"s_add_u32 m0, s71, 20064\n"
"v_mfma_f32_16x16x32_bf16 a[172:175], a[220:223], v[176:179], a[172:175]\n"
"buffer_load_dwordx4 v248, s[48:51], s69 offen  lds\n"
"v_xor_b32 v248, 0x10000, v248\n"
"v_mfma_f32_16x16x32_bf16 a[172:175], a[252:255], v[184:187], a[172:175]\n"
"ds_read_b64_tr_b16 a[220:221], v243 offset:33792\n"
"v_mfma_f32_16x16x32_bf16 v[116:119], a[224:227], v[16:19], v[116:119]\n"
"ds_read_b64_tr_b16 a[222:223], v243 offset:38016\n"
"v_mfma_f32_16x16x32_bf16 v[116:119], v[224:227], v[24:27], v[116:119]\n"
"ds_read_b64_tr_b16 a[252:253], v243 offset:42240\n"
"v_mfma_f32_16x16x32_bf16 v[124:127], a[224:227], v[48:51], v[124:127]\n"
"ds_read_b64_tr_b16 a[254:255], v243 offset:46464\n"
"v_mfma_f32_16x16x32_bf16 v[124:127], v[224:227], v[56:59], v[124:127]\n"
"buffer_load_dwordx4 a[160:163], v249, s[56:59], s61 offen offset:0 sc1\n"
"v_mfma_f32_16x16x32_bf16 v[132:135], a[224:227], v[80:83], v[132:135]\n"
"v_mfma_f32_16x16x32_bf16 v[132:135], v[224:227], v[88:91], v[132:135]\n"
"v_mfma_f32_16x16x32_bf16 v[140:143], a[224:227], v[112:115], v[140:143]\n"
"v_mfma_f32_16x16x32_bf16 v[140:143], v[224:227], v[120:123], v[140:143]\n"
"v_mfma_f32_16x16x32_bf16 v[148:151], a[224:227], v[144:147], v[148:151]\n"
"buffer_load_dwordx4 a[100:103], v249, s[56:59], s62 offen offset:0 sc1\n"
"v_mfma_f32_16x16x32_bf16 v[148:151], v[224:227], v[152:155], v[148:151]\n"
"v_mfma_f32_16x16x32_bf16 v[156:159], a[224:227], v[176:179], v[156:159]\n"
"v_mfma_f32_16x16x32_bf16 v[156:159], v[224:227], v[184:187], v[156:159]\n"
"v_mfma_f32_16x16x32_bf16 v[220:223], a[224:227], v[200:203], v[220:223]\n"
"s_add_u32 m0, s71, 25344\n"
"v_mfma_f32_16x16x32_bf16 v[220:223], v[224:227], v[204:207], v[220:223]\n"
"buffer_load_dwordx4 v248, s[48:51], s66 offen  lds\n"
"v_mfma_f32_16x16x32_bf16 v[164:167], a[228:231], v[16:19], v[164:167]\n"
"ds_read_b64_tr_b16 a[224:225], v244 offset:33792\n"
"v_mfma_f32_16x16x32_bf16 v[164:167], v[228:231], v[24:27], v[164:167]\n"
"ds_read_b64_tr_b16 a[226:227], v244 offset:38016\n"
"v_mfma_f32_16x16x32_bf16 v[172:175], a[228:231], v[48:51], v[172:175]\n"
"ds_read_b64_tr_b16 v[224:225], v244 offset:42240\n"
"v_mfma_f32_16x16x32_bf16 v[172:175], v[228:231], v[56:59], v[172:175]\n"
"ds_read_b64_tr_b16 v[226:227], v244 offset:46464\n"
"v_mfma_f32_16x16x32_bf16 v[180:183], a[228:231], v[80:83], v[180:183]\n"
"buffer_load_dwordx4 a[48:51], v249, s[56:59], s63 offen offset:0 sc1\n"
"v_mfma_f32_16x16x32_bf16 v[180:183], v[228:231], v[88:91], v[180:183]\n"
"v_mfma_f32_16x16x32_bf16 v[188:191], a[228:231], v[112:115], v[188:191]\n"
"v_mfma_f32_16x16x32_bf16 v[188:191], v[228:231], v[120:123], v[188:191]\n"
"v_mfma_f32_16x16x32_bf16 v[208:211], a[228:231], v[144:147], v[208:211]\n"
"s_add_u32 m0, s71, 26400\n"
"v_mfma_f32_16x16x32_bf16 v[208:211], v[228:231], v[152:155], v[208:211]\n"
"buffer_load_dwordx4 v248, s[48:51], s67 offen  lds\n"
"v_mfma_f32_16x16x32_bf16 v[212:215], a[228:231], v[176:179], v[212:215]\n"
"v_mfma_f32_16x16x32_bf16 v[212:215], v[228:231], v[184:187], v[212:215]\n"
"ds_read_b64_tr_b16 a[228:229], v245 offset:33792\n"
"v_mfma_f32_16x16x32_bf16 a[24:27], a[232:235], v[16:19], a[24:27]\n"
"ds_read_b64_tr_b16 a[230:231], v245 offset:38016\n"
"v_mfma_f32_16x16x32_bf16 a[24:27], v[232:235], v[24:27], a[24:27]\n"
"ds_read_b64_tr_b16 v[228:229], v245 offset:42240\n"
"v_mfma_f32_16x16x32_bf16 a[56:59], a[232:235], v[48:51], a[56:59]\n"
"buffer_load_dwordx4 a[176:179], v249, s[56:59], s64 offen offset:0 sc1\n"
"v_mfma_f32_16x16x32_bf16 a[56:59], v[232:235], v[56:59], a[56:59]\n"
"ds_read_b64_tr_b16 v[230:231], v245 offset:46464\n"
"v_mfma_f32_16x16x32_bf16 a[88:91], a[232:235], v[80:83], a[88:91]\n"
"v_mfma_f32_16x16x32_bf16 a[88:91], v[232:235], v[88:91], a[88:91]\n"
"v_mfma_f32_16x16x32_bf16 a[120:123], a[232:235], v[112:115], a[120:123]\n"
"s_add_u32 m0, s71, 27456\n"
"v_mfma_f32_16x16x32_bf16 a[120:123], v[232:235], v[120:123], a[120:123]\n"
"buffer_load_dwordx4 v248, s[48:51], s68 offen  lds\n"
"v_mfma_f32_16x16x32_bf16 a[152:155], a[232:235], v[144:147], a[152:155]\n"
"v_mfma_f32_16x16x32_bf16 a[152:155], v[232:235], v[152:155], a[152:155]\n"
"v_mfma_f32_16x16x32_bf16 a[184:187], a[232:235], v[176:179], a[184:187]\n"
"v_mfma_f32_16x16x32_bf16 a[184:187], v[232:235], v[184:187], a[184:187]\n"
"v_mfma_f32_16x16x32_bf16 a[204:207], a[232:235], v[200:203], a[204:207]\n"
"buffer_load_dwordx4 a[116:119], v249, s[56:59], s65 offen offset:0 sc1\n"
"v_mfma_f32_16x16x32_bf16 a[204:207], v[232:235], v[204:207], a[204:207]\n"
"ds_read_b64_tr_b16 a[232:233], v246 offset:33792\n"
"v_mfma_f32_16x16x32_bf16 a[28:31], a[236:239], v[16:19], a[28:31]\n"
"ds_read_b64_tr_b16 a[234:235], v246 offset:38016\n"
"v_mfma_f32_16x16x32_bf16 a[28:31], v[236:239], v[24:27], a[28:31]\n"
"ds_read_b64_tr_b16 v[232:233], v246 offset:42240\n"
"v_mfma_f32_16x16x32_bf16 a[60:63], a[236:239], v[48:51], a[60:63]\n"
"s_add_u32 m0, s71, 28512\n"
"ds_read_b64_tr_b16 v[234:235], v246 offset:46464\n"
"v_mfma_f32_16x16x32_bf16 a[60:63], v[236:239], v[56:59], a[60:63]\n"
"buffer_load_dwordx4 v248, s[48:51], s69 offen  lds\n"
"s_cmp_eq_u32 s72, 4\n"
"s_cselect_b32 s73, 2, 1\n"
"v_lshl_add_u32 v248, s73, 19, v248\n"
"v_xor_b32 v248, 0x30000, v248\n"
"v_mfma_f32_16x16x32_bf16 a[92:95], a[236:239], v[80:83], a[92:95]\n"
"v_mfma_f32_16x16x32_bf16 a[92:95], v[236:239], v[88:91], a[92:95]\n"
"v_mfma_f32_16x16x32_bf16 a[124:127], a[236:239], v[112:115], a[124:127]\n"
"v_mfma_f32_16x16x32_bf16 a[124:127], v[236:239], v[120:123], a[124:127]\n"
"v_mfma_f32_16x16x32_bf16 a[156:159], a[236:239], v[144:147], a[156:159]\n"
"buffer_load_dwordx4 a[200:203], v249, s[56:59], s70 offen offset:0 sc1\n"
"v_xor_b32 v249, 0xc00, v249\n"
"s_cmp_eq_u32 s72, 3\n"
"s_cselect_b32 s74, 2, 1\n"
"v_lshl_add_u32 v249, s74, 13, v249\n"
"v_mfma_f32_16x16x32_bf16 a[156:159], v[236:239], v[152:155], a[156:159]\n"
"v_mfma_f32_16x16x32_bf16 a[188:191], a[236:239], v[176:179], a[188:191]\n"
"v_mfma_f32_16x16x32_bf16 a[188:191], v[236:239], v[184:187], a[188:191]\n"
"s_waitcnt vmcnt(22) lgkmcnt(15)\n"
"v_mfma_f32_16x16x32_bf16 v[20:23], a[208:211], v[4:7], v[20:23]\n"
"ds_read_b64_tr_b16 a[236:237], v247 offset:33792\n"
"v_mfma_f32_16x16x32_bf16 v[20:23], a[240:243], v[12:15], v[20:23]\n"
"ds_read_b64_tr_b16 a[238:239], v247 offset:38016\n"
"buffer_load_dwordx4 v[0:3], v249, s[56:59], s60 offen offset:0 sc1\n"
"v_mfma_f32_16x16x32_bf16 v[28:31], a[208:211], a[64:67], v[28:31]\n"
"ds_read_b64_tr_b16 v[236:237], v247 offset:42240\n"
"v_mfma_f32_16x16x32_bf16 v[28:31], a[240:243], a[96:99], v[28:31]\n"
"ds_read_b64_tr_b16 v[238:239], v247 offset:46464\n"
"v_mfma_f32_16x16x32_bf16 v[36:39], a[208:211], a[4:7], v[36:39]\n"
"buffer_load_dwordx4 v[32:35], v249, s[56:59], s61 offen offset:0 sc1\n"
"v_mfma_f32_16x16x32_bf16 v[36:39], a[240:243], a[36:39], v[36:39]\n"
"v_mfma_f32_16x16x32_bf16 v[44:47], a[208:211], a[132:135], v[44:47]\n"
"v_mfma_f32_16x16x32_bf16 v[44:47], a[240:243], a[164:167], v[44:47]\n"
"buffer_load_dwordx4 v[64:67], v249, s[56:59], s62 offen offset:0 sc1\n"
"v_mfma_f32_16x16x32_bf16 v[52:55], a[208:211], a[80:83], v[52:55]\n"
"v_mfma_f32_16x16x32_bf16 v[52:55], a[240:243], a[112:115], v[52:55]\n"
"v_mfma_f32_16x16x32_bf16 v[60:63], a[208:211], a[20:23], v[60:63]\n"
"v_mfma_f32_16x16x32_bf16 v[60:63], a[240:243], a[52:55], v[60:63]\n"
"buffer_load_dwordx4 v[96:99], v249, s[56:59], s63 offen offset:0 sc1\n"
"v_mfma_f32_16x16x32_bf16 v[216:219], a[208:211], a[148:151], v[216:219]\n"
"v_mfma_f32_16x16x32_bf16 v[216:219], a[240:243], a[180:183], v[216:219]\n"
"s_waitcnt lgkmcnt(0)\n"
"s_barrier\n"
"v_mfma_f32_16x16x32_bf16 v[68:71], a[212:215], v[4:7], v[68:71]\n"
"ds_read_b64_tr_b16 a[208:209], v240 offset:50688\n"
"v_mfma_f32_16x16x32_bf16 v[68:71], a[244:247], v[12:15], v[68:71]\n"
"buffer_load_dwordx4 v[128:131], v249, s[56:59], s64 offen offset:0 sc1\n"
"v_mfma_f32_16x16x32_bf16 v[76:79], a[212:215], a[64:67], v[76:79]\n"
"ds_read_b64_tr_b16 a[210:211], v240 offset:54912\n"
"v_mfma_f32_16x16x32_bf16 v[76:79], a[244:247], a[96:99], v[76:79]\n"
"ds_read_b64_tr_b16 a[240:241], v240 offset:59136\n"
"v_mfma_f32_16x16x32_bf16 v[84:87], a[212:215], a[4:7], v[84:87]\n"
"ds_read_b64_tr_b16 a[242:243], v240 offset:63360\n"
"v_mfma_f32_16x16x32_bf16 v[84:87], a[244:247], a[36:39], v[84:87]\n"
"s_add_u32 m0, s71, 33792\n"
"v_mfma_f32_16x16x32_bf16 v[92:95], a[212:215], a[132:135], v[92:95]\n"
"buffer_load_dwordx4 v248, s[48:51], s66 offen  lds\n"
"v_mfma_f32_16x16x32_bf16 v[92:95], a[244:247], a[164:167], v[92:95]\n"
"v_mfma_f32_16x16x32_bf16 v[100:103], a[212:215], a[80:83], v[100:103]\n"
"v_mfma_f32_16x16x32_bf16 v[100:103], a[244:247], a[112:115], v[100:103]\n"
"v_mfma_f32_16x16x32_bf16 v[108:111], a[212:215], a[20:23], v[108:111]\n"
"v_mfma_f32_16x16x32_bf16 v[108:111], a[244:247], a[52:55], v[108:111]\n"
"buffer_load_dwordx4 v[160:163], v249, s[56:59], s65 offen offset:0 sc1\n"
"v_mfma_f32_16x16x32_bf16 a[8:11], a[216:219], v[4:7], a[8:11]\n"
"ds_read_b64_tr_b16 a[212:213], v241 offset:50688\n"
"v_mfma_f32_16x16x32_bf16 a[8:11], a[248:251], v[12:15], a[8:11]\n"
"ds_read_b64_tr_b16 a[214:215], v241 offset:54912\n"
"v_mfma_f32_16x16x32_bf16 a[40:43], a[216:219], a[64:67], a[40:43]\n"
"ds_read_b64_tr_b16 a[244:245], v241 offset:59136\n"
"v_mfma_f32_16x16x32_bf16 a[40:43], a[248:251], a[96:99], a[40:43]\n"
"s_add_u32 m0, s71, 34848\n"
"ds_read_b64_tr_b16 a[246:247], v241 offset:63360\n"
"v_mfma_f32_16x16x32_bf16 a[72:75], a[216:219], a[4:7], a[72:75]\n"
"buffer_load_dwordx4 v248, s[48:51], s67 offen  lds\n"
"v_mfma_f32_16x16x32_bf16 a[72:75], a[248:251], a[36:39], a[72:75]\n"
"v_mfma_f32_16x16x32_bf16 a[104:107], a[216:219], a[132:135], a[104:107]\n"
"v_mfma_f32_16x16x32_bf16 a[104:107], a[248:251], a[164:167], a[104:107]\n"
"v_mfma_f32_16x16x32_bf16 a[136:139], a[216:219], a[80:83], a[136:139]\n"
"v_mfma_f32_16x16x32_bf16 a[136:139], a[248:251], a[112:115], a[136:139]\n"
"buffer_load_dwordx4 v[192:195], v249, s[56:59], s70 offen offset:0 sc1\n"
"v_xor_b32 v249, 0x400, v249\n"
"v_mfma_f32_16x16x32_bf16 a[168:171], a[216:219], a[20:23], a[168:171]\n"
"v_mfma_f32_16x16x32_bf16 a[168:171], a[248:251], a[52:55], a[168:171]\n"
"v_mfma_f32_16x16x32_bf16 a[196:199], a[216:219], a[148:151], a[196:199]\n"
"v_mfma_f32_16x16x32_bf16 a[196:199], a[248:251], a[180:183], a[196:199]\n"
"s_add_u32 m0, s71, 35904\n"
"ds_read_b64_tr_b16 a[216:217], v242 offset:50688\n"
"v_mfma_f32_16x16x32_bf16 a[12:15], a[220:223], v[4:7], a[12:15]\n"
"buffer_load_dwordx4 v248, s[48:51], s68 offen  lds\n"
"v_mfma_f32_16x16x32_bf16 a[12:15], a[252:255], v[12:15], a[12:15]\n"
"ds_read_b64_tr_b16 a[218:219], v242 offset:54912\n"
"v_mfma_f32_16x16x32_bf16 a[44:47], a[220:223], a[64:67], a[44:47]\n"
"ds_read_b64_tr_b16 a[248:249], v242 offset:59136\n"
"v_mfma_f32_16x16x32_bf16 a[44:47], a[252:255], a[96:99], a[44:47]\n"
"ds_read_b64_tr_b16 a[250:251], v242 offset:63360\n"
"v_mfma_f32_16x16x32_bf16 a[76:79], a[220:223], a[4:7], a[76:79]\n"
"v_mfma_f32_16x16x32_bf16 a[76:79], a[252:255], a[36:39], a[76:79]\n"
"buffer_load_dwordx4 v[8:11], v249, s[56:59], s60 offen offset:0 sc1\n"
"v_mfma_f32_16x16x32_bf16 a[108:111], a[220:223], a[132:135], a[108:111]\n"
"v_mfma_f32_16x16x32_bf16 a[108:111], a[252:255], a[164:167], a[108:111]\n"
"v_mfma_f32_16x16x32_bf16 a[140:143], a[220:223], a[80:83], a[140:143]\n"
"v_mfma_f32_16x16x32_bf16 a[140:143], a[252:255], a[112:115], a[140:143]\n"
"s_add_u32 m0, s71, 36960\n"
"v_mfma_f32_16x16x32_bf16 a[172:175], a[220:223], a[20:23], a[172:175]\n"
"buffer_load_dwordx4 v248, s[48:51], s69 offen  lds\n"
"v_xor_b32 v248, 0x10000, v248\n"
"v_mfma_f32_16x16x32_bf16 a[172:175], a[252:255], a[52:55], a[172:175]\n"
"ds_read_b64_tr_b16 a[220:221], v243 offset:50688\n"
"v_mfma_f32_16x16x32_bf16 v[116:119], a[224:227], v[4:7], v[116:119]\n"
"ds_read_b64_tr_b16 a[222:223], v243 offset:54912\n"
"v_mfma_f32_16x16x32_bf16 v[116:119], v[224:227], v[12:15], v[116:119]\n"
"ds_read_b64_tr_b16 a[252:253], v243 offset:59136\n"
"v_mfma_f32_16x16x32_bf16 v[124:127], a[224:227], a[64:67], v[124:127]\n"
"ds_read_b64_tr_b16 a[254:255], v243 offset:63360\n"
"v_mfma_f32_16x16x32_bf16 v[124:127], v[224:227], a[96:99], v[124:127]\n"
"buffer_load_dwordx4 v[40:43], v249, s[56:59], s61 offen offset:0 sc1\n"
"v_mfma_f32_16x16x32_bf16 v[132:135], a[224:227], a[4:7], v[132:135]\n"
"v_mfma_f32_16x16x32_bf16 v[132:135], v[224:227], a[36:39], v[132:135]\n"
"v_mfma_f32_16x16x32_bf16 v[140:143], a[224:227], a[132:135], v[140:143]\n"
"v_mfma_f32_16x16x32_bf16 v[140:143], v[224:227], a[164:167], v[140:143]\n"
"v_mfma_f32_16x16x32_bf16 v[148:151], a[224:227], a[80:83], v[148:151]\n"
"buffer_load_dwordx4 v[72:75], v249, s[56:59], s62 offen offset:0 sc1\n"
"v_mfma_f32_16x16x32_bf16 v[148:151], v[224:227], a[112:115], v[148:151]\n"
"v_mfma_f32_16x16x32_bf16 v[156:159], a[224:227], a[20:23], v[156:159]\n"
"v_mfma_f32_16x16x32_bf16 v[156:159], v[224:227], a[52:55], v[156:159]\n"
"v_mfma_f32_16x16x32_bf16 v[220:223], a[224:227], a[148:151], v[220:223]\n"
"s_add_u32 m0, s71, 42240\n"
"v_mfma_f32_16x16x32_bf16 v[220:223], v[224:227], a[180:183], v[220:223]\n"
"buffer_load_dwordx4 v248, s[48:51], s66 offen  lds\n"
"v_mfma_f32_16x16x32_bf16 v[164:167], a[228:231], v[4:7], v[164:167]\n"
"ds_read_b64_tr_b16 a[224:225], v244 offset:50688\n"
"v_mfma_f32_16x16x32_bf16 v[164:167], v[228:231], v[12:15], v[164:167]\n"
"ds_read_b64_tr_b16 a[226:227], v244 offset:54912\n"
"v_mfma_f32_16x16x32_bf16 v[172:175], a[228:231], a[64:67], v[172:175]\n"
"ds_read_b64_tr_b16 v[224:225], v244 offset:59136\n"
"v_mfma_f32_16x16x32_bf16 v[172:175], v[228:231], a[96:99], v[172:175]\n"
"ds_read_b64_tr_b16 v[226:227], v244 offset:63360\n"
"v_mfma_f32_16x16x32_bf16 v[180:183], a[228:231], a[4:7], v[180:183]\n"
"buffer_load_dwordx4 v[104:107], v249, s[56:59], s63 offen offset:0 sc1\n"
"v_mfma_f32_16x16x32_bf16 v[180:183], v[228:231], a[36:39], v[180:183]\n"
"v_mfma_f32_16x16x32_bf16 v[188:191], a[228:231], a[132:135], v[188:191]\n"
"v_mfma_f32_16x16x32_bf16 v[188:191], v[228:231], a[164:167], v[188:191]\n"
"v_mfma_f32_16x16x32_bf16 v[208:211], a[228:231], a[80:83], v[208:211]\n"
"s_add_u32 m0, s71, 43296\n"
"v_mfma_f32_16x16x32_bf16 v[208:211], v[228:231], a[112:115], v[208:211]\n"
"buffer_load_dwordx4 v248, s[48:51], s67 offen  lds\n"
"v_mfma_f32_16x16x32_bf16 v[212:215], a[228:231], a[20:23], v[212:215]\n"
"v_mfma_f32_16x16x32_bf16 v[212:215], v[228:231], a[52:55], v[212:215]\n"
"ds_read_b64_tr_b16 a[228:229], v245 offset:50688\n"
"v_mfma_f32_16x16x32_bf16 a[24:27], a[232:235], v[4:7], a[24:27]\n"
"ds_read_b64_tr_b16 a[230:231], v245 offset:54912\n"
"v_mfma_f32_16x16x32_bf16 a[24:27], v[232:235], v[12:15], a[24:27]\n"
"ds_read_b64_tr_b16 v[228:229], v245 offset:59136\n"
"v_mfma_f32_16x16x32_bf16 a[56:59], a[232:235], a[64:67], a[56:59]\n"
"buffer_load_dwordx4 v[136:139], v249, s[56:59], s64 offen offset:0 sc1\n"
"v_mfma_f32_16x16x32_bf16 a[56:59], v[232:235], a[96:99], a[56:59]\n"
"ds_read_b64_tr_b16 v[230:231], v245 offset:63360\n"
"v_mfma_f32_16x16x32_bf16 a[88:91], a[232:235], a[4:7], a[88:91]\n"
"v_mfma_f32_16x16x32_bf16 a[88:91], v[232:235], a[36:39], a[88:91]\n"
"v_mfma_f32_16x16x32_bf16 a[120:123], a[232:235], a[132:135], a[120:123]\n"
"s_add_u32 m0, s71, 44352\n"
"v_mfma_f32_16x16x32_bf16 a[120:123], v[232:235], a[164:167], a[120:123]\n"
"buffer_load_dwordx4 v248, s[48:51], s68 offen  lds\n"
"v_mfma_f32_16x16x32_bf16 a[152:155], a[232:235], a[80:83], a[152:155]\n"
"v_mfma_f32_16x16x32_bf16 a[152:155], v[232:235], a[112:115], a[152:155]\n"
"v_mfma_f32_16x16x32_bf16 a[184:187], a[232:235], a[20:23], a[184:187]\n"
"v_mfma_f32_16x16x32_bf16 a[184:187], v[232:235], a[52:55], a[184:187]\n"
"v_mfma_f32_16x16x32_bf16 a[204:207], a[232:235], a[148:151], a[204:207]\n"
"buffer_load_dwordx4 v[168:171], v249, s[56:59], s65 offen offset:0 sc1\n"
"v_mfma_f32_16x16x32_bf16 a[204:207], v[232:235], a[180:183], a[204:207]\n"
"ds_read_b64_tr_b16 a[232:233], v246 offset:50688\n"
"v_mfma_f32_16x16x32_bf16 a[28:31], a[236:239], v[4:7], a[28:31]\n"
"ds_read_b64_tr_b16 a[234:235], v246 offset:54912\n"
"v_mfma_f32_16x16x32_bf16 a[28:31], v[236:239], v[12:15], a[28:31]\n"
"ds_read_b64_tr_b16 v[232:233], v246 offset:59136\n"
"v_mfma_f32_16x16x32_bf16 a[60:63], a[236:239], a[64:67], a[60:63]\n"
"s_add_u32 m0, s71, 45408\n"
"ds_read_b64_tr_b16 v[234:235], v246 offset:63360\n"
"v_mfma_f32_16x16x32_bf16 a[60:63], v[236:239], a[96:99], a[60:63]\n"
"buffer_load_dwordx4 v248, s[48:51], s69 offen  lds\n"
"v_xor_b32 v248, 0x30000, v248\n"
"v_mfma_f32_16x16x32_bf16 a[92:95], a[236:239], a[4:7], a[92:95]\n"
"v_mfma_f32_16x16x32_bf16 a[92:95], v[236:239], a[36:39], a[92:95]\n"
"v_mfma_f32_16x16x32_bf16 a[124:127], a[236:239], a[132:135], a[124:127]\n"
"v_mfma_f32_16x16x32_bf16 a[124:127], v[236:239], a[164:167], a[124:127]\n"
"v_mfma_f32_16x16x32_bf16 a[156:159], a[236:239], a[80:83], a[156:159]\n"
"buffer_load_dwordx4 v[196:199], v249, s[56:59], s70 offen offset:0 sc1\n"
"v_xor_b32 v249, 0xc00, v249\n"
"v_mfma_f32_16x16x32_bf16 a[156:159], v[236:239], a[112:115], a[156:159]\n"
"v_mfma_f32_16x16x32_bf16 a[188:191], a[236:239], a[20:23], a[188:191]\n"
"v_mfma_f32_16x16x32_bf16 a[188:191], v[236:239], a[52:55], a[188:191]\n"
"s_waitcnt vmcnt(22) lgkmcnt(15)\n"
"v_mfma_f32_16x16x32_bf16 v[20:23], a[208:211], a[0:3], v[20:23]\n"
"ds_read_b64_tr_b16 a[236:237], v247 offset:50688\n"
"v_mfma_f32_16x16x32_bf16 v[20:23], a[240:243], a[32:35], v[20:23]\n"
"ds_read_b64_tr_b16 a[238:239], v247 offset:54912\n"
"buffer_load_dwordx4 v[16:19], v249, s[56:59], s60 offen offset:0 sc1\n"
"v_mfma_f32_16x16x32_bf16 v[28:31], a[208:211], a[128:131], v[28:31]\n"
"ds_read_b64_tr_b16 v[236:237], v247 offset:59136\n"
"v_mfma_f32_16x16x32_bf16 v[28:31], a[240:243], a[160:163], v[28:31]\n"
"ds_read_b64_tr_b16 v[238:239], v247 offset:63360\n"
"v_mfma_f32_16x16x32_bf16 v[36:39], a[208:211], a[68:71], v[36:39]\n"
"buffer_load_dwordx4 v[48:51], v249, s[56:59], s61 offen offset:0 sc1\n"
"v_mfma_f32_16x16x32_bf16 v[36:39], a[240:243], a[100:103], v[36:39]\n"
"v_mfma_f32_16x16x32_bf16 v[44:47], a[208:211], a[16:19], v[44:47]\n"
"v_mfma_f32_16x16x32_bf16 v[44:47], a[240:243], a[48:51], v[44:47]\n"
"buffer_load_dwordx4 v[80:83], v249, s[56:59], s62 offen offset:0 sc1\n"
"v_mfma_f32_16x16x32_bf16 v[52:55], a[208:211], a[144:147], v[52:55]\n"
"v_mfma_f32_16x16x32_bf16 v[52:55], a[240:243], a[176:179], v[52:55]\n"
"v_mfma_f32_16x16x32_bf16 v[60:63], a[208:211], a[84:87], v[60:63]\n"
"v_mfma_f32_16x16x32_bf16 v[60:63], a[240:243], a[116:119], v[60:63]\n"
"buffer_load_dwordx4 v[112:115], v249, s[56:59], s63 offen offset:0 sc1\n"
"v_mfma_f32_16x16x32_bf16 v[216:219], a[208:211], a[192:195], v[216:219]\n"
"v_mfma_f32_16x16x32_bf16 v[216:219], a[240:243], a[200:203], v[216:219]\n"
"s_waitcnt lgkmcnt(0)\n"
"s_barrier\n"
"v_mfma_f32_16x16x32_bf16 v[68:71], a[212:215], a[0:3], v[68:71]\n"
"ds_read_b64_tr_b16 a[208:209], v240 offset:0\n"
"v_mfma_f32_16x16x32_bf16 v[68:71], a[244:247], a[32:35], v[68:71]\n"
"buffer_load_dwordx4 v[144:147], v249, s[56:59], s64 offen offset:0 sc1\n"
"v_mfma_f32_16x16x32_bf16 v[76:79], a[212:215], a[128:131], v[76:79]\n"
"ds_read_b64_tr_b16 a[210:211], v240 offset:4224\n"
"v_mfma_f32_16x16x32_bf16 v[76:79], a[244:247], a[160:163], v[76:79]\n"
"ds_read_b64_tr_b16 a[240:241], v240 offset:8448\n"
"v_mfma_f32_16x16x32_bf16 v[84:87], a[212:215], a[68:71], v[84:87]\n"
"ds_read_b64_tr_b16 a[242:243], v240 offset:12672\n"
"v_mfma_f32_16x16x32_bf16 v[84:87], a[244:247], a[100:103], v[84:87]\n"
"s_add_u32 m0, s71, 50688\n"
"v_mfma_f32_16x16x32_bf16 v[92:95], a[212:215], a[16:19], v[92:95]\n"
"buffer_load_dwordx4 v248, s[48:51], s66 offen  lds\n"
"v_mfma_f32_16x16x32_bf16 v[92:95], a[244:247], a[48:51], v[92:95]\n"
"v_mfma_f32_16x16x32_bf16 v[100:103], a[212:215], a[144:147], v[100:103]\n"
"v_mfma_f32_16x16x32_bf16 v[100:103], a[244:247], a[176:179], v[100:103]\n"
"v_mfma_f32_16x16x32_bf16 v[108:111], a[212:215], a[84:87], v[108:111]\n"
"v_mfma_f32_16x16x32_bf16 v[108:111], a[244:247], a[116:119], v[108:111]\n"
"buffer_load_dwordx4 v[176:179], v249, s[56:59], s65 offen offset:0 sc1\n"
"v_mfma_f32_16x16x32_bf16 a[8:11], a[216:219], a[0:3], a[8:11]\n"
"ds_read_b64_tr_b16 a[212:213], v241 offset:0\n"
"v_mfma_f32_16x16x32_bf16 a[8:11], a[248:251], a[32:35], a[8:11]\n"
"ds_read_b64_tr_b16 a[214:215], v241 offset:4224\n"
"v_mfma_f32_16x16x32_bf16 a[40:43], a[216:219], a[128:131], a[40:43]\n"
"ds_read_b64_tr_b16 a[244:245], v241 offset:8448\n"
"v_mfma_f32_16x16x32_bf16 a[40:43], a[248:251], a[160:163], a[40:43]\n"
"s_add_u32 m0, s71, 51744\n"
"ds_read_b64_tr_b16 a[246:247], v241 offset:12672\n"
"v_mfma_f32_16x16x32_bf16 a[72:75], a[216:219], a[68:71], a[72:75]\n"
"buffer_load_dwordx4 v248, s[48:51], s67 offen  lds\n"
"v_mfma_f32_16x16x32_bf16 a[72:75], a[248:251], a[100:103], a[72:75]\n"
"v_mfma_f32_16x16x32_bf16 a[104:107], a[216:219], a[16:19], a[104:107]\n"
"v_mfma_f32_16x16x32_bf16 a[104:107], a[248:251], a[48:51], a[104:107]\n"
"v_mfma_f32_16x16x32_bf16 a[136:139], a[216:219], a[144:147], a[136:139]\n"
"v_mfma_f32_16x16x32_bf16 a[136:139], a[248:251], a[176:179], a[136:139]\n"
"buffer_load_dwordx4 v[200:203], v249, s[56:59], s70 offen offset:0 sc1\n"
"v_xor_b32 v249, 0x400, v249\n"
"v_mfma_f32_16x16x32_bf16 a[168:171], a[216:219], a[84:87], a[168:171]\n"
"v_mfma_f32_16x16x32_bf16 a[168:171], a[248:251], a[116:119], a[168:171]\n"
"v_mfma_f32_16x16x32_bf16 a[196:199], a[216:219], a[192:195], a[196:199]\n"
"v_mfma_f32_16x16x32_bf16 a[196:199], a[248:251], a[200:203], a[196:199]\n"
"s_add_u32 m0, s71, 52800\n"
"ds_read_b64_tr_b16 a[216:217], v242 offset:0\n"
"v_mfma_f32_16x16x32_bf16 a[12:15], a[220:223], a[0:3], a[12:15]\n"
"buffer_load_dwordx4 v248, s[48:51], s68 offen  lds\n"
"v_mfma_f32_16x16x32_bf16 a[12:15], a[252:255], a[32:35], a[12:15]\n"
"ds_read_b64_tr_b16 a[218:219], v242 offset:4224\n"
"v_mfma_f32_16x16x32_bf16 a[44:47], a[220:223], a[128:131], a[44:47]\n"
"ds_read_b64_tr_b16 a[248:249], v242 offset:8448\n"
"v_mfma_f32_16x16x32_bf16 a[44:47], a[252:255], a[160:163], a[44:47]\n"
"ds_read_b64_tr_b16 a[250:251], v242 offset:12672\n"
"v_mfma_f32_16x16x32_bf16 a[76:79], a[220:223], a[68:71], a[76:79]\n"
"v_mfma_f32_16x16x32_bf16 a[76:79], a[252:255], a[100:103], a[76:79]\n"
"buffer_load_dwordx4 v[24:27], v249, s[56:59], s60 offen offset:0 sc1\n"
"v_mfma_f32_16x16x32_bf16 a[108:111], a[220:223], a[16:19], a[108:111]\n"
"v_mfma_f32_16x16x32_bf16 a[108:111], a[252:255], a[48:51], a[108:111]\n"
"v_mfma_f32_16x16x32_bf16 a[140:143], a[220:223], a[144:147], a[140:143]\n"
"v_mfma_f32_16x16x32_bf16 a[140:143], a[252:255], a[176:179], a[140:143]\n"
"s_add_u32 m0, s71, 53856\n"
"v_mfma_f32_16x16x32_bf16 a[172:175], a[220:223], a[84:87], a[172:175]\n"
"buffer_load_dwordx4 v248, s[48:51], s69 offen  lds\n"
"v_xor_b32 v248, 0x10000, v248\n"
"v_mfma_f32_16x16x32_bf16 a[172:175], a[252:255], a[116:119], a[172:175]\n"
"ds_read_b64_tr_b16 a[220:221], v243 offset:0\n"
"v_mfma_f32_16x16x32_bf16 v[116:119], a[224:227], a[0:3], v[116:119]\n"
"ds_read_b64_tr_b16 a[222:223], v243 offset:4224\n"
"v_mfma_f32_16x16x32_bf16 v[116:119], v[224:227], a[32:35], v[116:119]\n"
"ds_read_b64_tr_b16 a[252:253], v243 offset:8448\n"
"v_mfma_f32_16x16x32_bf16 v[124:127], a[224:227], a[128:131], v[124:127]\n"
"ds_read_b64_tr_b16 a[254:255], v243 offset:12672\n"
"v_mfma_f32_16x16x32_bf16 v[124:127], v[224:227], a[160:163], v[124:127]\n"
"buffer_load_dwordx4 v[56:59], v249, s[56:59], s61 offen offset:0 sc1\n"
"v_mfma_f32_16x16x32_bf16 v[132:135], a[224:227], a[68:71], v[132:135]\n"
"v_mfma_f32_16x16x32_bf16 v[132:135], v[224:227], a[100:103], v[132:135]\n"
"v_mfma_f32_16x16x32_bf16 v[140:143], a[224:227], a[16:19], v[140:143]\n"
"v_mfma_f32_16x16x32_bf16 v[140:143], v[224:227], a[48:51], v[140:143]\n"
"v_mfma_f32_16x16x32_bf16 v[148:151], a[224:227], a[144:147], v[148:151]\n"
"buffer_load_dwordx4 v[88:91], v249, s[56:59], s62 offen offset:0 sc1\n"
"v_mfma_f32_16x16x32_bf16 v[148:151], v[224:227], a[176:179], v[148:151]\n"
"v_mfma_f32_16x16x32_bf16 v[156:159], a[224:227], a[84:87], v[156:159]\n"
"v_mfma_f32_16x16x32_bf16 v[156:159], v[224:227], a[116:119], v[156:159]\n"
"v_mfma_f32_16x16x32_bf16 v[220:223], a[224:227], a[192:195], v[220:223]\n"
"s_add_u32 m0, s71, 59136\n"
"v_mfma_f32_16x16x32_bf16 v[220:223], v[224:227], a[200:203], v[220:223]\n"
"buffer_load_dwordx4 v248, s[48:51], s66 offen  lds\n"
"v_mfma_f32_16x16x32_bf16 v[164:167], a[228:231], a[0:3], v[164:167]\n"
"ds_read_b64_tr_b16 a[224:225], v244 offset:0\n"
"v_mfma_f32_16x16x32_bf16 v[164:167], v[228:231], a[32:35], v[164:167]\n"
"ds_read_b64_tr_b16 a[226:227], v244 offset:4224\n"
"v_mfma_f32_16x16x32_bf16 v[172:175], a[228:231], a[128:131], v[172:175]\n"
"ds_read_b64_tr_b16 v[224:225], v244 offset:8448\n"
"v_mfma_f32_16x16x32_bf16 v[172:175], v[228:231], a[160:163], v[172:175]\n"
"ds_read_b64_tr_b16 v[226:227], v244 offset:12672\n"
"v_mfma_f32_16x16x32_bf16 v[180:183], a[228:231], a[68:71], v[180:183]\n"
"buffer_load_dwordx4 v[120:123], v249, s[56:59], s63 offen offset:0 sc1\n"
"v_mfma_f32_16x16x32_bf16 v[180:183], v[228:231], a[100:103], v[180:183]\n"
"v_mfma_f32_16x16x32_bf16 v[188:191], a[228:231], a[16:19], v[188:191]\n"
"v_mfma_f32_16x16x32_bf16 v[188:191], v[228:231], a[48:51], v[188:191]\n"
"v_mfma_f32_16x16x32_bf16 v[208:211], a[228:231], a[144:147], v[208:211]\n"
"s_add_u32 m0, s71, 60192\n"
"v_mfma_f32_16x16x32_bf16 v[208:211], v[228:231], a[176:179], v[208:211]\n"
"buffer_load_dwordx4 v248, s[48:51], s67 offen  lds\n"
"v_mfma_f32_16x16x32_bf16 v[212:215], a[228:231], a[84:87], v[212:215]\n"
"v_mfma_f32_16x16x32_bf16 v[212:215], v[228:231], a[116:119], v[212:215]\n"
"ds_read_b64_tr_b16 a[228:229], v245 offset:0\n"
"v_mfma_f32_16x16x32_bf16 a[24:27], a[232:235], a[0:3], a[24:27]\n"
"ds_read_b64_tr_b16 a[230:231], v245 offset:4224\n"
"v_mfma_f32_16x16x32_bf16 a[24:27], v[232:235], a[32:35], a[24:27]\n"
"ds_read_b64_tr_b16 v[228:229], v245 offset:8448\n"
"v_mfma_f32_16x16x32_bf16 a[56:59], a[232:235], a[128:131], a[56:59]\n"
"buffer_load_dwordx4 v[152:155], v249, s[56:59], s64 offen offset:0 sc1\n"
"v_mfma_f32_16x16x32_bf16 a[56:59], v[232:235], a[160:163], a[56:59]\n"
"ds_read_b64_tr_b16 v[230:231], v245 offset:12672\n"
"v_mfma_f32_16x16x32_bf16 a[88:91], a[232:235], a[68:71], a[88:91]\n"
"v_mfma_f32_16x16x32_bf16 a[88:91], v[232:235], a[100:103], a[88:91]\n"
"v_mfma_f32_16x16x32_bf16 a[120:123], a[232:235], a[16:19], a[120:123]\n"
"s_add_u32 m0, s71, 61248\n"
"v_mfma_f32_16x16x32_bf16 a[120:123], v[232:235], a[48:51], a[120:123]\n"
"buffer_load_dwordx4 v248, s[48:51], s68 offen  lds\n"
"v_mfma_f32_16x16x32_bf16 a[152:155], a[232:235], a[144:147], a[152:155]\n"
"v_mfma_f32_16x16x32_bf16 a[152:155], v[232:235], a[176:179], a[152:155]\n"
"v_mfma_f32_16x16x32_bf16 a[184:187], a[232:235], a[84:87], a[184:187]\n"
"v_mfma_f32_16x16x32_bf16 a[184:187], v[232:235], a[116:119], a[184:187]\n"
"v_mfma_f32_16x16x32_bf16 a[204:207], a[232:235], a[192:195], a[204:207]\n"
"buffer_load_dwordx4 v[184:187], v249, s[56:59], s65 offen offset:0 sc1\n"
"v_mfma_f32_16x16x32_bf16 a[204:207], v[232:235], a[200:203], a[204:207]\n"
"ds_read_b64_tr_b16 a[232:233], v246 offset:0\n"
"v_mfma_f32_16x16x32_bf16 a[28:31], a[236:239], a[0:3], a[28:31]\n"
"ds_read_b64_tr_b16 a[234:235], v246 offset:4224\n"
"v_mfma_f32_16x16x32_bf16 a[28:31], v[236:239], a[32:35], a[28:31]\n"
"ds_read_b64_tr_b16 v[232:233], v246 offset:8448\n"
"v_mfma_f32_16x16x32_bf16 a[60:63], a[236:239], a[128:131], a[60:63]\n"
"s_add_u32 m0, s71, 62304\n"
"ds_read_b64_tr_b16 v[234:235], v246 offset:12672\n"
"v_mfma_f32_16x16x32_bf16 a[60:63], v[236:239], a[160:163], a[60:63]\n"
"buffer_load_dwordx4 v248, s[48:51], s69 offen  lds\n"
"s_cmp_eq_u32 s72, 5\n"
"s_cselect_b32 s73, 2, 1\n"
"v_lshl_add_u32 v248, s73, 19, v248\n"
"v_xor_b32 v248, 0x30000, v248\n"
"v_mfma_f32_16x16x32_bf16 a[92:95], a[236:239], a[68:71], a[92:95]\n"
"v_mfma_f32_16x16x32_bf16 a[92:95], v[236:239], a[100:103], a[92:95]\n"
"v_mfma_f32_16x16x32_bf16 a[124:127], a[236:239], a[16:19], a[124:127]\n"
"v_mfma_f32_16x16x32_bf16 a[124:127], v[236:239], a[48:51], a[124:127]\n"
"v_mfma_f32_16x16x32_bf16 a[156:159], a[236:239], a[144:147], a[156:159]\n"
"buffer_load_dwordx4 v[204:207], v249, s[56:59], s70 offen offset:0 sc1\n"
"v_xor_b32 v249, 0xc00, v249\n"
"s_cmp_eq_u32 s72, 4\n"
"s_cselect_b32 s74, 2, 1\n"
"v_lshl_add_u32 v249, s74, 13, v249\n"
"v_mfma_f32_16x16x32_bf16 a[156:159], v[236:239], a[176:179], a[156:159]\n"
"v_mfma_f32_16x16x32_bf16 a[188:191], a[236:239], a[84:87], a[188:191]\n"
"v_mfma_f32_16x16x32_bf16 a[188:191], v[236:239], a[116:119], a[188:191]\n"
"s_waitcnt vmcnt(22) lgkmcnt(15)\n"
"v_mfma_f32_16x16x32_bf16 v[20:23], a[208:211], v[0:3], v[20:23]\n"
"ds_read_b64_tr_b16 a[236:237], v247 offset:0\n"
"v_mfma_f32_16x16x32_bf16 v[20:23], a[240:243], v[8:11], v[20:23]\n"
"ds_read_b64_tr_b16 a[238:239], v247 offset:4224\n"
"buffer_load_dwordx4 v[4:7], v249, s[56:59], s60 offen offset:0 sc1\n"
"v_mfma_f32_16x16x32_bf16 v[28:31], a[208:211], v[32:35], v[28:31]\n"
"ds_read_b64_tr_b16 v[236:237], v247 offset:8448\n"
"v_mfma_f32_16x16x32_bf16 v[28:31], a[240:243], v[40:43], v[28:31]\n"
"ds_read_b64_tr_b16 v[238:239], v247 offset:12672\n"
"v_mfma_f32_16x16x32_bf16 v[36:39], a[208:211], v[64:67], v[36:39]\n"
"buffer_load_dwordx4 a[64:67], v249, s[56:59], s61 offen offset:0 sc1\n"
"v_mfma_f32_16x16x32_bf16 v[36:39], a[240:243], v[72:75], v[36:39]\n"
"v_mfma_f32_16x16x32_bf16 v[44:47], a[208:211], v[96:99], v[44:47]\n"
"v_mfma_f32_16x16x32_bf16 v[44:47], a[240:243], v[104:107], v[44:47]\n"
"buffer_load_dwordx4 a[4:7], v249, s[56:59], s62 offen offset:0 sc1\n"
"v_mfma_f32_16x16x32_bf16 v[52:55], a[208:211], v[128:131], v[52:55]\n"
"v_mfma_f32_16x16x32_bf16 v[52:55], a[240:243], v[136:139], v[52:55]\n"
"v_mfma_f32_16x16x32_bf16 v[60:63], a[208:211], v[160:163], v[60:63]\n"
"v_mfma_f32_16x16x32_bf16 v[60:63], a[240:243], v[168:171], v[60:63]\n"
"buffer_load_dwordx4 a[132:135], v249, s[56:59], s63 offen offset:0 sc1\n"
"v_mfma_f32_16x16x32_bf16 v[216:219], a[208:211], v[192:195], v[216:219]\n"
"v_mfma_f32_16x16x32_bf16 v[216:219], a[240:243], v[196:199], v[216:219]\n"
"s_waitcnt lgkmcnt(0)\n"
"s_barrier\n"
"v_mfma_f32_16x16x32_bf16 v[68:71], a[212:215], v[0:3], v[68:71]\n"
"ds_read_b64_tr_b16 a[208:209], v240 offset:16896\n"
"v_mfma_f32_16x16x32_bf16 v[68:71], a[244:247], v[8:11], v[68:71]\n"
"buffer_load_dwordx4 a[80:83], v249, s[56:59], s64 offen offset:0 sc1\n"
"v_mfma_f32_16x16x32_bf16 v[76:79], a[212:215], v[32:35], v[76:79]\n"
"ds_read_b64_tr_b16 a[210:211], v240 offset:21120\n"
"v_mfma_f32_16x16x32_bf16 v[76:79], a[244:247], v[40:43], v[76:79]\n"
"ds_read_b64_tr_b16 a[240:241], v240 offset:25344\n"
"v_mfma_f32_16x16x32_bf16 v[84:87], a[212:215], v[64:67], v[84:87]\n"
"ds_read_b64_tr_b16 a[242:243], v240 offset:29568\n"
"v_mfma_f32_16x16x32_bf16 v[84:87], a[244:247], v[72:75], v[84:87]\n"
"s_add_u32 m0, s71, 0\n"
"v_mfma_f32_16x16x32_bf16 v[92:95], a[212:215], v[96:99], v[92:95]\n"
"buffer_load_dwordx4 v248, s[48:51], s66 offen  lds\n"
"v_mfma_f32_16x16x32_bf16 v[92:95], a[244:247], v[104:107], v[92:95]\n"
"v_mfma_f32_16x16x32_bf16 v[100:103], a[212:215], v[128:131], v[100:103]\n"
"v_mfma_f32_16x16x32_bf16 v[100:103], a[244:247], v[136:139], v[100:103]\n"
"v_mfma_f32_16x16x32_bf16 v[108:111], a[212:215], v[160:163], v[108:111]\n"
"v_mfma_f32_16x16x32_bf16 v[108:111], a[244:247], v[168:171], v[108:111]\n"
"buffer_load_dwordx4 a[20:23], v249, s[56:59], s65 offen offset:0 sc1\n"
"v_mfma_f32_16x16x32_bf16 a[8:11], a[216:219], v[0:3], a[8:11]\n"
"ds_read_b64_tr_b16 a[212:213], v241 offset:16896\n"
"v_mfma_f32_16x16x32_bf16 a[8:11], a[248:251], v[8:11], a[8:11]\n"
"ds_read_b64_tr_b16 a[214:215], v241 offset:21120\n"
"v_mfma_f32_16x16x32_bf16 a[40:43], a[216:219], v[32:35], a[40:43]\n"
"ds_read_b64_tr_b16 a[244:245], v241 offset:25344\n"
"v_mfma_f32_16x16x32_bf16 a[40:43], a[248:251], v[40:43], a[40:43]\n"
"s_add_u32 m0, s71, 1056\n"
"ds_read_b64_tr_b16 a[246:247], v241 offset:29568\n"
"v_mfma_f32_16x16x32_bf16 a[72:75], a[216:219], v[64:67], a[72:75]\n"
"buffer_load_dwordx4 v248, s[48:51], s67 offen  lds\n"
"v_mfma_f32_16x16x32_bf16 a[72:75], a[248:251], v[72:75], a[72:75]\n"
"v_mfma_f32_16x16x32_bf16 a[104:107], a[216:219], v[96:99], a[104:107]\n"
"v_mfma_f32_16x16x32_bf16 a[104:107], a[248:251], v[104:107], a[104:107]\n"
"v_mfma_f32_16x16x32_bf16 a[136:139], a[216:219], v[128:131], a[136:139]\n"
"v_mfma_f32_16x16x32_bf16 a[136:139], a[248:251], v[136:139], a[136:139]\n"
"buffer_load_dwordx4 a[148:151], v249, s[56:59], s70 offen offset:0 sc1\n"
"v_xor_b32 v249, 0x400, v249\n"
"v_mfma_f32_16x16x32_bf16 a[168:171], a[216:219], v[160:163], a[168:171]\n"
"v_mfma_f32_16x16x32_bf16 a[168:171], a[248:251], v[168:171], a[168:171]\n"
"v_mfma_f32_16x16x32_bf16 a[196:199], a[216:219], v[192:195], a[196:199]\n"
"v_mfma_f32_16x16x32_bf16 a[196:199], a[248:251], v[196:199], a[196:199]\n"
"s_add_u32 m0, s71, 2112\n"
"ds_read_b64_tr_b16 a[216:217], v242 offset:16896\n"
"v_mfma_f32_16x16x32_bf16 a[12:15], a[220:223], v[0:3], a[12:15]\n"
"buffer_load_dwordx4 v248, s[48:51], s68 offen  lds\n"
"v_mfma_f32_16x16x32_bf16 a[12:15], a[252:255], v[8:11], a[12:15]\n"
"ds_read_b64_tr_b16 a[218:219], v242 offset:21120\n"
"v_mfma_f32_16x16x32_bf16 a[44:47], a[220:223], v[32:35], a[44:47]\n"
"ds_read_b64_tr_b16 a[248:249], v242 offset:25344\n"
"v_mfma_f32_16x16x32_bf16 a[44:47], a[252:255], v[40:43], a[44:47]\n"
"ds_read_b64_tr_b16 a[250:251], v242 offset:29568\n"
"v_mfma_f32_16x16x32_bf16 a[76:79], a[220:223], v[64:67], a[76:79]\n"
"v_mfma_f32_16x16x32_bf16 a[76:79], a[252:255], v[72:75], a[76:79]\n"
"buffer_load_dwordx4 v[12:15], v249, s[56:59], s60 offen offset:0 sc1\n"
"v_mfma_f32_16x16x32_bf16 a[108:111], a[220:223], v[96:99], a[108:111]\n"
"v_mfma_f32_16x16x32_bf16 a[108:111], a[252:255], v[104:107], a[108:111]\n"
"v_mfma_f32_16x16x32_bf16 a[140:143], a[220:223], v[128:131], a[140:143]\n"
"v_mfma_f32_16x16x32_bf16 a[140:143], a[252:255], v[136:139], a[140:143]\n"
"s_add_u32 m0, s71, 3168\n"
"v_mfma_f32_16x16x32_bf16 a[172:175], a[220:223], v[160:163], a[172:175]\n"
"buffer_load_dwordx4 v248, s[48:51], s69 offen  lds\n"
"v_xor_b32 v248, 0x10000, v248\n"
"v_mfma_f32_16x16x32_bf16 a[172:175], a[252:255], v[168:171], a[172:175]\n"
"ds_read_b64_tr_b16 a[220:221], v243 offset:16896\n"
"v_mfma_f32_16x16x32_bf16 v[116:119], a[224:227], v[0:3], v[116:119]\n"
"ds_read_b64_tr_b16 a[222:223], v243 offset:21120\n"
"v_mfma_f32_16x16x32_bf16 v[116:119], v[224:227], v[8:11], v[116:119]\n"
"ds_read_b64_tr_b16 a[252:253], v243 offset:25344\n"
"v_mfma_f32_16x16x32_bf16 v[124:127], a[224:227], v[32:35], v[124:127]\n"
"ds_read_b64_tr_b16 a[254:255], v243 offset:29568\n"
"v_mfma_f32_16x16x32_bf16 v[124:127], v[224:227], v[40:43], v[124:127]\n"
"buffer_load_dwordx4 a[96:99], v249, s[56:59], s61 offen offset:0 sc1\n"
"v_mfma_f32_16x16x32_bf16 v[132:135], a[224:227], v[64:67], v[132:135]\n"
"v_mfma_f32_16x16x32_bf16 v[132:135], v[224:227], v[72:75], v[132:135]\n"
"v_mfma_f32_16x16x32_bf16 v[140:143], a[224:227], v[96:99], v[140:143]\n"
"v_mfma_f32_16x16x32_bf16 v[140:143], v[224:227], v[104:107], v[140:143]\n"
"v_mfma_f32_16x16x32_bf16 v[148:151], a[224:227], v[128:131], v[148:151]\n"
"buffer_load_dwordx4 a[36:39], v249, s[56:59], s62 offen offset:0 sc1\n"
"v_mfma_f32_16x16x32_bf16 v[148:151], v[224:227], v[136:139], v[148:151]\n"
"v_mfma_f32_16x16x32_bf16 v[156:159], a[224:227], v[160:163], v[156:159]\n"
"v_mfma_f32_16x16x32_bf16 v[156:159], v[224:227], v[168:171], v[156:159]\n"
"v_mfma_f32_16x16x32_bf16 v[220:223], a[224:227], v[192:195], v[220:223]\n"
"s_add_u32 m0, s71, 8448\n"
"v_mfma_f32_16x16x32_bf16 v[220:223], v[224:227], v[196:199], v[220:223]\n"
"buffer_load_dwordx4 v248, s[48:51], s66 offen  lds\n"
"v_mfma_f32_16x16x32_bf16 v[164:167], a[228:231], v[0:3], v[164:167]\n"
"ds_read_b64_tr_b16 a[224:225], v244 offset:16896\n"
"v_mfma_f32_16x16x32_bf16 v[164:167], v[228:231], v[8:11], v[164:167]\n"
"ds_read_b64_tr_b16 a[226:227], v244 offset:21120\n"
"v_mfma_f32_16x16x32_bf16 v[172:175], a[228:231], v[32:35], v[172:175]\n"
"ds_read_b64_tr_b16 v[224:225], v244 offset:25344\n"
"v_mfma_f32_16x16x32_bf16 v[172:175], v[228:231], v[40:43], v[172:175]\n"
"ds_read_b64_tr_b16 v[226:227], v244 offset:29568\n"
"v_mfma_f32_16x16x32_bf16 v[180:183], a[228:231], v[64:67], v[180:183]\n"
"buffer_load_dwordx4 a[164:167], v249, s[56:59], s63 offen offset:0 sc1\n"
"v_mfma_f32_16x16x32_bf16 v[180:183], v[228:231], v[72:75], v[180:183]\n"
"v_mfma_f32_16x16x32_bf16 v[188:191], a[228:231], v[96:99], v[188:191]\n"
"v_mfma_f32_16x16x32_bf16 v[188:191], v[228:231], v[104:107], v[188:191]\n"
"v_mfma_f32_16x16x32_bf16 v[208:211], a[228:231], v[128:131], v[208:211]\n"
"s_add_u32 m0, s71, 9504\n"
"v_mfma_f32_16x16x32_bf16 v[208:211], v[228:231], v[136:139], v[208:211]\n"
"buffer_load_dwordx4 v248, s[48:51], s67 offen  lds\n"
"v_mfma_f32_16x16x32_bf16 v[212:215], a[228:231], v[160:163], v[212:215]\n"
"v_mfma_f32_16x16x32_bf16 v[212:215], v[228:231], v[168:171], v[212:215]\n"
"ds_read_b64_tr_b16 a[228:229], v245 offset:16896\n"
"v_mfma_f32_16x16x32_bf16 a[24:27], a[232:235], v[0:3], a[24:27]\n"
"ds_read_b64_tr_b16 a[230:231], v245 offset:21120\n"
"v_mfma_f32_16x16x32_bf16 a[24:27], v[232:235], v[8:11], a[24:27]\n"
"ds_read_b64_tr_b16 v[228:229], v245 offset:25344\n"
"v_mfma_f32_16x16x32_bf16 a[56:59], a[232:235], v[32:35], a[56:59]\n"
"buffer_load_dwordx4 a[112:115], v249, s[56:59], s64 offen offset:0 sc1\n"
"v_mfma_f32_16x16x32_bf16 a[56:59], v[232:235], v[40:43], a[56:59]\n"
"ds_read_b64_tr_b16 v[230:231], v245 offset:29568\n"
"v_mfma_f32_16x16x32_bf16 a[88:91], a[232:235], v[64:67], a[88:91]\n"
"v_mfma_f32_16x16x32_bf16 a[88:91], v[232:235], v[72:75], a[88:91]\n"
"v_mfma_f32_16x16x32_bf16 a[120:123], a[232:235], v[96:99], a[120:123]\n"
"s_add_u32 m0, s71, 10560\n"
"v_mfma_f32_16x16x32_bf16 a[120:123], v[232:235], v[104:107], a[120:123]\n"
"buffer_load_dwordx4 v248, s[48:51], s68 offen  lds\n"
"v_mfma_f32_16x16x32_bf16 a[152:155], a[232:235], v[128:131], a[152:155]\n"
"v_mfma_f32_16x16x32_bf16 a[152:155], v[232:235], v[136:139], a[152:155]\n"
"v_mfma_f32_16x16x32_bf16 a[184:187], a[232:235], v[160:163], a[184:187]\n"
"v_mfma_f32_16x16x32_bf16 a[184:187], v[232:235], v[168:171], a[184:187]\n"
"v_mfma_f32_16x16x32_bf16 a[204:207], a[232:235], v[192:195], a[204:207]\n"
"buffer_load_dwordx4 a[52:55], v249, s[56:59], s65 offen offset:0 sc1\n"
"v_mfma_f32_16x16x32_bf16 a[204:207], v[232:235], v[196:199], a[204:207]\n"
"ds_read_b64_tr_b16 a[232:233], v246 offset:16896\n"
"v_mfma_f32_16x16x32_bf16 a[28:31], a[236:239], v[0:3], a[28:31]\n"
"ds_read_b64_tr_b16 a[234:235], v246 offset:21120\n"
"v_mfma_f32_16x16x32_bf16 a[28:31], v[236:239], v[8:11], a[28:31]\n"
"ds_read_b64_tr_b16 v[232:233], v246 offset:25344\n"
"v_mfma_f32_16x16x32_bf16 a[60:63], a[236:239], v[32:35], a[60:63]\n"
"s_add_u32 m0, s71, 11616\n"
"ds_read_b64_tr_b16 v[234:235], v246 offset:29568\n"
"v_mfma_f32_16x16x32_bf16 a[60:63], v[236:239], v[40:43], a[60:63]\n"
"buffer_load_dwordx4 v248, s[48:51], s69 offen  lds\n"
"v_xor_b32 v248, 0x30000, v248\n"
"v_mfma_f32_16x16x32_bf16 a[92:95], a[236:239], v[64:67], a[92:95]\n"
"v_mfma_f32_16x16x32_bf16 a[92:95], v[236:239], v[72:75], a[92:95]\n"
"v_mfma_f32_16x16x32_bf16 a[124:127], a[236:239], v[96:99], a[124:127]\n"
"v_mfma_f32_16x16x32_bf16 a[124:127], v[236:239], v[104:107], a[124:127]\n"
"v_mfma_f32_16x16x32_bf16 a[156:159], a[236:239], v[128:131], a[156:159]\n"
"buffer_load_dwordx4 a[180:183], v249, s[56:59], s70 offen offset:0 sc1\n"
"v_xor_b32 v249, 0xc00, v249\n"
"v_mfma_f32_16x16x32_bf16 a[156:159], v[236:239], v[136:139], a[156:159]\n"
"v_mfma_f32_16x16x32_bf16 a[188:191], a[236:239], v[160:163], a[188:191]\n"
"v_mfma_f32_16x16x32_bf16 a[188:191], v[236:239], v[168:171], a[188:191]\n"
"s_waitcnt vmcnt(22) lgkmcnt(15)\n"
"v_mfma_f32_16x16x32_bf16 v[20:23], a[208:211], v[16:19], v[20:23]\n"
"ds_read_b64_tr_b16 a[236:237], v247 offset:16896\n"
"v_mfma_f32_16x16x32_bf16 v[20:23], a[240:243], v[24:27], v[20:23]\n"
"ds_read_b64_tr_b16 a[238:239], v247 offset:21120\n"
"buffer_load_dwordx4 a[0:3], v249, s[56:59], s60 offen offset:0 sc1\n"
"v_mfma_f32_16x16x32_bf16 v[28:31], a[208:211], v[48:51], v[28:31]\n"
"ds_read_b64_tr_b16 v[236:237], v247 offset:25344\n"
"v_mfma_f32_16x16x32_bf16 v[28:31], a[240:243], v[56:59], v[28:31]\n"
"ds_read_b64_tr_b16 v[238:239], v247 offset:29568\n"
"v_mfma_f32_16x16x32_bf16 v[36:39], a[208:211], v[80:83], v[36:39]\n"
"buffer_load_dwordx4 a[128:131], v249, s[56:59], s61 offen offset:0 sc1\n"
"v_mfma_f32_16x16x32_bf16 v[36:39], a[240:243], v[88:91], v[36:39]\n"
"v_mfma_f32_16x16x32_bf16 v[44:47], a[208:211], v[112:115], v[44:47]\n"
"v_mfma_f32_16x16x32_bf16 v[44:47], a[240:243], v[120:123], v[44:47]\n"
"buffer_load_dwordx4 a[68:71], v249, s[56:59], s62 offen offset:0 sc1\n"
"v_mfma_f32_16x16x32_bf16 v[52:55], a[208:211], v[144:147], v[52:55]\n"
"v_mfma_f32_16x16x32_bf16 v[52:55], a[240:243], v[152:155], v[52:55]\n"
"v_mfma_f32_16x16x32_bf16 v[60:63], a[208:211], v[176:179], v[60:63]\n"
"v_mfma_f32_16x16x32_bf16 v[60:63], a[240:243], v[184:187], v[60:63]\n"
"buffer_load_dwordx4 a[16:19], v249, s[56:59], s63 offen offset:0 sc1\n"
"v_mfma_f32_16x16x32_bf16 v[216:219], a[208:211], v[200:203], v[216:219]\n"
"v_mfma_f32_16x16x32_bf16 v[216:219], a[240:243], v[204:207], v[216:219]\n"
"s_waitcnt lgkmcnt(0)\n"
"s_barrier\n"
"v_mfma_f32_16x16x32_bf16 v[68:71], a[212:215], v[16:19], v[68:71]\n"
"ds_read_b64_tr_b16 a[208:209], v240 offset:33792\n"
"v_mfma_f32_16x16x32_bf16 v[68:71], a[244:247], v[24:27], v[68:71]\n"
"buffer_load_dwordx4 a[144:147], v249, s[56:59], s64 offen offset:0 sc1\n"
"v_mfma_f32_16x16x32_bf16 v[76:79], a[212:215], v[48:51], v[76:79]\n"
"ds_read_b64_tr_b16 a[210:211], v240 offset:38016\n"
"v_mfma_f32_16x16x32_bf16 v[76:79], a[244:247], v[56:59], v[76:79]\n"
"ds_read_b64_tr_b16 a[240:241], v240 offset:42240\n"
"v_mfma_f32_16x16x32_bf16 v[84:87], a[212:215], v[80:83], v[84:87]\n"
"ds_read_b64_tr_b16 a[242:243], v240 offset:46464\n"
"v_mfma_f32_16x16x32_bf16 v[84:87], a[244:247], v[88:91], v[84:87]\n"
"s_add_u32 m0, s71, 16896\n"
"v_mfma_f32_16x16x32_bf16 v[92:95], a[212:215], v[112:115], v[92:95]\n"
"buffer_load_dwordx4 v248, s[48:51], s66 offen  lds\n"
"v_mfma_f32_16x16x32_bf16 v[92:95], a[244:247], v[120:123], v[92:95]\n"
"v_mfma_f32_16x16x32_bf16 v[100:103], a[212:215], v[144:147], v[100:103]\n"
"v_mfma_f32_16x16x32_bf16 v[100:103], a[244:247], v[152:155], v[100:103]\n"
"v_mfma_f32_16x16x32_bf16 v[108:111], a[212:215], v[176:179], v[108:111]\n"
"v_mfma_f32_16x16x32_bf16 v[108:111], a[244:247], v[184:187], v[108:111]\n"
"buffer_load_dwordx4 a[84:87], v249, s[56:59], s65 offen offset:0 sc1\n"
"v_mfma_f32_16x16x32_bf16 a[8:11], a[216:219], v[16:19], a[8:11]\n"
"ds_read_b64_tr_b16 a[212:213], v241 offset:33792\n"
"v_mfma_f32_16x16x32_bf16 a[8:11], a[248:251], v[24:27], a[8:11]\n"
"ds_read_b64_tr_b16 a[214:215], v241 offset:38016\n"
"v_mfma_f32_16x16x32_bf16 a[40:43], a[216:219], v[48:51], a[40:43]\n"
"ds_read_b64_tr_b16 a[244:245], v241 offset:42240\n"
"v_mfma_f32_16x16x32_bf16 a[40:43], a[248:251], v[56:59], a[40:43]\n"
"s_add_u32 m0, s71, 17952\n"
"ds_read_b64_tr_b16 a[246:247], v241 offset:46464\n"
"v_mfma_f32_16x16x32_bf16 a[72:75], a[216:219], v[80:83], a[72:75]\n"
"buffer_load_dwordx4 v248, s[48:51], s67 offen  lds\n"
"v_mfma_f32_16x16x32_bf16 a[72:75], a[248:251], v[88:91], a[72:75]\n"
"v_mfma_f32_16x16x32_bf16 a[104:107], a[216:219], v[112:115], a[104:107]\n"
"v_mfma_f32_16x16x32_bf16 a[104:107], a[248:251], v[120:123], a[104:107]\n"
"v_mfma_f32_16x16x32_bf16 a[136:139], a[216:219], v[144:147], a[136:139]\n"
"v_mfma_f32_16x16x32_bf16 a[136:139], a[248:251], v[152:155], a[136:139]\n"
"buffer_load_dwordx4 a[192:195], v249, s[56:59], s70 offen offset:0 sc1\n"
"v_xor_b32 v249, 0x400, v249\n"
"v_mfma_f32_16x16x32_bf16 a[168:171], a[216:219], v[176:179], a[168:171]\n"
"v_mfma_f32_16x16x32_bf16 a[168:171], a[248:251], v[184:187], a[168:171]\n"
"v_mfma_f32_16x16x32_bf16 a[196:199], a[216:219], v[200:203], a[196:199]\n"
"v_mfma_f32_16x16x32_bf16 a[196:199], a[248:251], v[204:207], a[196:199]\n"
"s_add_u32 m0, s71, 19008\n"
"ds_read_b64_tr_b16 a[216:217], v242 offset:33792\n"
"v_mfma_f32_16x16x32_bf16 a[12:15], a[220:223], v[16:19], a[12:15]\n"
"buffer_load_dwordx4 v248, s[48:51], s68 offen  lds\n"
"v_mfma_f32_16x16x32_bf16 a[12:15], a[252:255], v[24:27], a[12:15]\n"
"ds_read_b64_tr_b16 a[218:219], v242 offset:38016\n"
"v_mfma_f32_16x16x32_bf16 a[44:47], a[220:223], v[48:51], a[44:47]\n"
"ds_read_b64_tr_b16 a[248:249], v242 offset:42240\n"
"v_mfma_f32_16x16x32_bf16 a[44:47], a[252:255], v[56:59], a[44:47]\n"
"ds_read_b64_tr_b16 a[250:251], v242 offset:46464\n"
"v_mfma_f32_16x16x32_bf16 a[76:79], a[220:223], v[80:83], a[76:79]\n"
"v_mfma_f32_16x16x32_bf16 a[76:79], a[252:255], v[88:91], a[76:79]\n"
"buffer_load_dwordx4 a[32:35], v249, s[56:59], s60 offen offset:0 sc1\n"
"v_mfma_f32_16x16x32_bf16 a[108:111], a[220:223], v[112:115], a[108:111]\n"
"v_mfma_f32_16x16x32_bf16 a[108:111], a[252:255], v[120:123], a[108:111]\n"
"v_mfma_f32_16x16x32_bf16 a[140:143], a[220:223], v[144:147], a[140:143]\n"
"v_mfma_f32_16x16x32_bf16 a[140:143], a[252:255], v[152:155], a[140:143]\n"
"s_add_u32 m0, s71, 20064\n"
"v_mfma_f32_16x16x32_bf16 a[172:175], a[220:223], v[176:179], a[172:175]\n"
"buffer_load_dwordx4 v248, s[48:51], s69 offen  lds\n"
"v_xor_b32 v248, 0x10000, v248\n"
"v_mfma_f32_16x16x32_bf16 a[172:175], a[252:255], v[184:187], a[172:175]\n"
"ds_read_b64_tr_b16 a[220:221], v243 offset:33792\n"
"v_mfma_f32_16x16x32_bf16 v[116:119], a[224:227], v[16:19], v[116:119]\n"
"ds_read_b64_tr_b16 a[222:223], v243 offset:38016\n"
"v_mfma_f32_16x16x32_bf16 v[116:119], v[224:227], v[24:27], v[116:119]\n"
"ds_read_b64_tr_b16 a[252:253], v243 offset:42240\n"
"v_mfma_f32_16x16x32_bf16 v[124:127], a[224:227], v[48:51], v[124:127]\n"
"ds_read_b64_tr_b16 a[254:255], v243 offset:46464\n"
"v_mfma_f32_16x16x32_bf16 v[124:127], v[224:227], v[56:59], v[124:127]\n"
"buffer_load_dwordx4 a[160:163], v249, s[56:59], s61 offen offset:0 sc1\n"
"v_mfma_f32_16x16x32_bf16 v[132:135], a[224:227], v[80:83], v[132:135]\n"
"v_mfma_f32_16x16x32_bf16 v[132:135], v[224:227], v[88:91], v[132:135]\n"
"v_mfma_f32_16x16x32_bf16 v[140:143], a[224:227], v[112:115], v[140:143]\n"
"v_mfma_f32_16x16x32_bf16 v[140:143], v[224:227], v[120:123], v[140:143]\n"
"v_mfma_f32_16x16x32_bf16 v[148:151], a[224:227], v[144:147], v[148:151]\n"
"buffer_load_dwordx4 a[100:103], v249, s[56:59], s62 offen offset:0 sc1\n"
"v_mfma_f32_16x16x32_bf16 v[148:151], v[224:227], v[152:155], v[148:151]\n"
"v_mfma_f32_16x16x32_bf16 v[156:159], a[224:227], v[176:179], v[156:159]\n"
"v_mfma_f32_16x16x32_bf16 v[156:159], v[224:227], v[184:187], v[156:159]\n"
"v_mfma_f32_16x16x32_bf16 v[220:223], a[224:227], v[200:203], v[220:223]\n"
"s_add_u32 m0, s71, 25344\n"
"v_mfma_f32_16x16x32_bf16 v[220:223], v[224:227], v[204:207], v[220:223]\n"
"buffer_load_dwordx4 v248, s[48:51], s66 offen  lds\n"
"v_mfma_f32_16x16x32_bf16 v[164:167], a[228:231], v[16:19], v[164:167]\n"
"ds_read_b64_tr_b16 a[224:225], v244 offset:33792\n"
"v_mfma_f32_16x16x32_bf16 v[164:167], v[228:231], v[24:27], v[164:167]\n"
"ds_read_b64_tr_b16 a[226:227], v244 offset:38016\n"
"v_mfma_f32_16x16x32_bf16 v[172:175], a[228:231], v[48:51], v[172:175]\n"
"ds_read_b64_tr_b16 v[224:225], v244 offset:42240\n"
"v_mfma_f32_16x16x32_bf16 v[172:175], v[228:231], v[56:59], v[172:175]\n"
"ds_read_b64_tr_b16 v[226:227], v244 offset:46464\n"
"v_mfma_f32_16x16x32_bf16 v[180:183], a[228:231], v[80:83], v[180:183]\n"
"buffer_load_dwordx4 a[48:51], v249, s[56:59], s63 offen offset:0 sc1\n"
"v_mfma_f32_16x16x32_bf16 v[180:183], v[228:231], v[88:91], v[180:183]\n"
"v_mfma_f32_16x16x32_bf16 v[188:191], a[228:231], v[112:115], v[188:191]\n"
"v_mfma_f32_16x16x32_bf16 v[188:191], v[228:231], v[120:123], v[188:191]\n"
"v_mfma_f32_16x16x32_bf16 v[208:211], a[228:231], v[144:147], v[208:211]\n"
"s_add_u32 m0, s71, 26400\n"
"v_mfma_f32_16x16x32_bf16 v[208:211], v[228:231], v[152:155], v[208:211]\n"
"buffer_load_dwordx4 v248, s[48:51], s67 offen  lds\n"
"v_mfma_f32_16x16x32_bf16 v[212:215], a[228:231], v[176:179], v[212:215]\n"
"v_mfma_f32_16x16x32_bf16 v[212:215], v[228:231], v[184:187], v[212:215]\n"
"ds_read_b64_tr_b16 a[228:229], v245 offset:33792\n"
"v_mfma_f32_16x16x32_bf16 a[24:27], a[232:235], v[16:19], a[24:27]\n"
"ds_read_b64_tr_b16 a[230:231], v245 offset:38016\n"
"v_mfma_f32_16x16x32_bf16 a[24:27], v[232:235], v[24:27], a[24:27]\n"
"ds_read_b64_tr_b16 v[228:229], v245 offset:42240\n"
"v_mfma_f32_16x16x32_bf16 a[56:59], a[232:235], v[48:51], a[56:59]\n"
"buffer_load_dwordx4 a[176:179], v249, s[56:59], s64 offen offset:0 sc1\n"
"v_mfma_f32_16x16x32_bf16 a[56:59], v[232:235], v[56:59], a[56:59]\n"
"ds_read_b64_tr_b16 v[230:231], v245 offset:46464\n"
"v_mfma_f32_16x16x32_bf16 a[88:91], a[232:235], v[80:83], a[88:91]\n"
"v_mfma_f32_16x16x32_bf16 a[88:91], v[232:235], v[88:91], a[88:91]\n"
"v_mfma_f32_16x16x32_bf16 a[120:123], a[232:235], v[112:115], a[120:123]\n"
"s_add_u32 m0, s71, 27456\n"
"v_mfma_f32_16x16x32_bf16 a[120:123], v[232:235], v[120:123], a[120:123]\n"
"buffer_load_dwordx4 v248, s[48:51], s68 offen  lds\n"
"v_mfma_f32_16x16x32_bf16 a[152:155], a[232:235], v[144:147], a[152:155]\n"
"v_mfma_f32_16x16x32_bf16 a[152:155], v[232:235], v[152:155], a[152:155]\n"
"v_mfma_f32_16x16x32_bf16 a[184:187], a[232:235], v[176:179], a[184:187]\n"
"v_mfma_f32_16x16x32_bf16 a[184:187], v[232:235], v[184:187], a[184:187]\n"
"v_mfma_f32_16x16x32_bf16 a[204:207], a[232:235], v[200:203], a[204:207]\n"
"buffer_load_dwordx4 a[116:119], v249, s[56:59], s65 offen offset:0 sc1\n"
"v_mfma_f32_16x16x32_bf16 a[204:207], v[232:235], v[204:207], a[204:207]\n"
"ds_read_b64_tr_b16 a[232:233], v246 offset:33792\n"
"v_mfma_f32_16x16x32_bf16 a[28:31], a[236:239], v[16:19], a[28:31]\n"
"ds_read_b64_tr_b16 a[234:235], v246 offset:38016\n"
"v_mfma_f32_16x16x32_bf16 a[28:31], v[236:239], v[24:27], a[28:31]\n"
"ds_read_b64_tr_b16 v[232:233], v246 offset:42240\n"
"v_mfma_f32_16x16x32_bf16 a[60:63], a[236:239], v[48:51], a[60:63]\n"
"s_add_u32 m0, s71, 28512\n"
"ds_read_b64_tr_b16 v[234:235], v246 offset:46464\n"
"v_mfma_f32_16x16x32_bf16 a[60:63], v[236:239], v[56:59], a[60:63]\n"
"buffer_load_dwordx4 v248, s[48:51], s69 offen  lds\n"
"s_cmp_eq_u32 s72, 6\n"
"s_cselect_b32 s73, 2, 1\n"
"v_lshl_add_u32 v248, s73, 19, v248\n"
"v_xor_b32 v248, 0x30000, v248\n"
"v_mfma_f32_16x16x32_bf16 a[92:95], a[236:239], v[80:83], a[92:95]\n"
"v_mfma_f32_16x16x32_bf16 a[92:95], v[236:239], v[88:91], a[92:95]\n"
"v_mfma_f32_16x16x32_bf16 a[124:127], a[236:239], v[112:115], a[124:127]\n"
"v_mfma_f32_16x16x32_bf16 a[124:127], v[236:239], v[120:123], a[124:127]\n"
"v_mfma_f32_16x16x32_bf16 a[156:159], a[236:239], v[144:147], a[156:159]\n"
"buffer_load_dwordx4 a[200:203], v249, s[56:59], s70 offen offset:0 sc1\n"
"v_xor_b32 v249, 0xc00, v249\n"
"s_cmp_eq_u32 s72, 5\n"
"s_cselect_b32 s74, 2, 1\n"
"v_lshl_add_u32 v249, s74, 13, v249\n"
"v_mfma_f32_16x16x32_bf16 a[156:159], v[236:239], v[152:155], a[156:159]\n"
"v_mfma_f32_16x16x32_bf16 a[188:191], a[236:239], v[176:179], a[188:191]\n"
"v_mfma_f32_16x16x32_bf16 a[188:191], v[236:239], v[184:187], a[188:191]\n"
"s_waitcnt vmcnt(22) lgkmcnt(15)\n"
"v_mfma_f32_16x16x32_bf16 v[20:23], a[208:211], v[4:7], v[20:23]\n"
"ds_read_b64_tr_b16 a[236:237], v247 offset:33792\n"
"v_mfma_f32_16x16x32_bf16 v[20:23], a[240:243], v[12:15], v[20:23]\n"
"ds_read_b64_tr_b16 a[238:239], v247 offset:38016\n"
"buffer_load_dwordx4 v[0:3], v249, s[56:59], s60 offen offset:0 sc1\n"
"v_mfma_f32_16x16x32_bf16 v[28:31], a[208:211], a[64:67], v[28:31]\n"
"ds_read_b64_tr_b16 v[236:237], v247 offset:42240\n"
"v_mfma_f32_16x16x32_bf16 v[28:31], a[240:243], a[96:99], v[28:31]\n"
"ds_read_b64_tr_b16 v[238:239], v247 offset:46464\n"
"v_mfma_f32_16x16x32_bf16 v[36:39], a[208:211], a[4:7], v[36:39]\n"
"buffer_load_dwordx4 v[32:35], v249, s[56:59], s61 offen offset:0 sc1\n"
"v_mfma_f32_16x16x32_bf16 v[36:39], a[240:243], a[36:39], v[36:39]\n"
"v_mfma_f32_16x16x32_bf16 v[44:47], a[208:211], a[132:135], v[44:47]\n"
"v_mfma_f32_16x16x32_bf16 v[44:47], a[240:243], a[164:167], v[44:47]\n"
"buffer_load_dwordx4 v[64:67], v249, s[56:59], s62 offen offset:0 sc1\n"
"v_mfma_f32_16x16x32_bf16 v[52:55], a[208:211], a[80:83], v[52:55]\n"
"v_mfma_f32_16x16x32_bf16 v[52:55], a[240:243], a[112:115], v[52:55]\n"
"v_mfma_f32_16x16x32_bf16 v[60:63], a[208:211], a[20:23], v[60:63]\n"
"v_mfma_f32_16x16x32_bf16 v[60:63], a[240:243], a[52:55], v[60:63]\n"
"buffer_load_dwordx4 v[96:99], v249, s[56:59], s63 offen offset:0 sc1\n"
"v_mfma_f32_16x16x32_bf16 v[216:219], a[208:211], a[148:151], v[216:219]\n"
"v_mfma_f32_16x16x32_bf16 v[216:219], a[240:243], a[180:183], v[216:219]\n"
"s_waitcnt lgkmcnt(0)\n"
"s_barrier\n"
"v_mfma_f32_16x16x32_bf16 v[68:71], a[212:215], v[4:7], v[68:71]\n"
"ds_read_b64_tr_b16 a[208:209], v240 offset:50688\n"
"v_mfma_f32_16x16x32_bf16 v[68:71], a[244:247], v[12:15], v[68:71]\n"
"buffer_load_dwordx4 v[128:131], v249, s[56:59], s64 offen offset:0 sc1\n"
"v_mfma_f32_16x16x32_bf16 v[76:79], a[212:215], a[64:67], v[76:79]\n"
"ds_read_b64_tr_b16 a[210:211], v240 offset:54912\n"
"v_mfma_f32_16x16x32_bf16 v[76:79], a[244:247], a[96:99], v[76:79]\n"
"ds_read_b64_tr_b16 a[240:241], v240 offset:59136\n"
"v_mfma_f32_16x16x32_bf16 v[84:87], a[212:215], a[4:7], v[84:87]\n"
"ds_read_b64_tr_b16 a[242:243], v240 offset:63360\n"
"v_mfma_f32_16x16x32_bf16 v[84:87], a[244:247], a[36:39], v[84:87]\n"
"s_add_u32 m0, s71, 33792\n"
"v_mfma_f32_16x16x32_bf16 v[92:95], a[212:215], a[132:135], v[92:95]\n"
"buffer_load_dwordx4 v248, s[48:51], s66 offen  lds\n"
"v_mfma_f32_16x16x32_bf16 v[92:95], a[244:247], a[164:167], v[92:95]\n"
"v_mfma_f32_16x16x32_bf16 v[100:103], a[212:215], a[80:83], v[100:103]\n"
"v_mfma_f32_16x16x32_bf16 v[100:103], a[244:247], a[112:115], v[100:103]\n"
"v_mfma_f32_16x16x32_bf16 v[108:111], a[212:215], a[20:23], v[108:111]\n"
"v_mfma_f32_16x16x32_bf16 v[108:111], a[244:247], a[52:55], v[108:111]\n"
"buffer_load_dwordx4 v[160:163], v249, s[56:59], s65 offen offset:0 sc1\n"
"v_mfma_f32_16x16x32_bf16 a[8:11], a[216:219], v[4:7], a[8:11]\n"
"ds_read_b64_tr_b16 a[212:213], v241 offset:50688\n"
"v_mfma_f32_16x16x32_bf16 a[8:11], a[248:251], v[12:15], a[8:11]\n"
"ds_read_b64_tr_b16 a[214:215], v241 offset:54912\n"
"v_mfma_f32_16x16x32_bf16 a[40:43], a[216:219], a[64:67], a[40:43]\n"
"ds_read_b64_tr_b16 a[244:245], v241 offset:59136\n"
"v_mfma_f32_16x16x32_bf16 a[40:43], a[248:251], a[96:99], a[40:43]\n"
"s_add_u32 m0, s71, 34848\n"
"ds_read_b64_tr_b16 a[246:247], v241 offset:63360\n"
"v_mfma_f32_16x16x32_bf16 a[72:75], a[216:219], a[4:7], a[72:75]\n"
"buffer_load_dwordx4 v248, s[48:51], s67 offen  lds\n"
"v_mfma_f32_16x16x32_bf16 a[72:75], a[248:251], a[36:39], a[72:75]\n"
"v_mfma_f32_16x16x32_bf16 a[104:107], a[216:219], a[132:135], a[104:107]\n"
"v_mfma_f32_16x16x32_bf16 a[104:107], a[248:251], a[164:167], a[104:107]\n"
"v_mfma_f32_16x16x32_bf16 a[136:139], a[216:219], a[80:83], a[136:139]\n"
"v_mfma_f32_16x16x32_bf16 a[136:139], a[248:251], a[112:115], a[136:139]\n"
"buffer_load_dwordx4 v[192:195], v249, s[56:59], s70 offen offset:0 sc1\n"
"v_xor_b32 v249, 0x400, v249\n"
"v_mfma_f32_16x16x32_bf16 a[168:171], a[216:219], a[20:23], a[168:171]\n"
"v_mfma_f32_16x16x32_bf16 a[168:171], a[248:251], a[52:55], a[168:171]\n"
"v_mfma_f32_16x16x32_bf16 a[196:199], a[216:219], a[148:151], a[196:199]\n"
"v_mfma_f32_16x16x32_bf16 a[196:199], a[248:251], a[180:183], a[196:199]\n"
"s_add_u32 m0, s71, 35904\n"
"ds_read_b64_tr_b16 a[216:217], v242 offset:50688\n"
"v_mfma_f32_16x16x32_bf16 a[12:15], a[220:223], v[4:7], a[12:15]\n"
"buffer_load_dwordx4 v248, s[48:51], s68 offen  lds\n"
"v_mfma_f32_16x16x32_bf16 a[12:15], a[252:255], v[12:15], a[12:15]\n"
"ds_read_b64_tr_b16 a[218:219], v242 offset:54912\n"
"v_mfma_f32_16x16x32_bf16 a[44:47], a[220:223], a[64:67], a[44:47]\n"
"ds_read_b64_tr_b16 a[248:249], v242 offset:59136\n"
"v_mfma_f32_16x16x32_bf16 a[44:47], a[252:255], a[96:99], a[44:47]\n"
"ds_read_b64_tr_b16 a[250:251], v242 offset:63360\n"
"v_mfma_f32_16x16x32_bf16 a[76:79], a[220:223], a[4:7], a[76:79]\n"
"v_mfma_f32_16x16x32_bf16 a[76:79], a[252:255], a[36:39], a[76:79]\n"
"buffer_load_dwordx4 v[8:11], v249, s[56:59], s60 offen offset:0 sc1\n"
"v_mfma_f32_16x16x32_bf16 a[108:111], a[220:223], a[132:135], a[108:111]\n"
"v_mfma_f32_16x16x32_bf16 a[108:111], a[252:255], a[164:167], a[108:111]\n"
"v_mfma_f32_16x16x32_bf16 a[140:143], a[220:223], a[80:83], a[140:143]\n"
"v_mfma_f32_16x16x32_bf16 a[140:143], a[252:255], a[112:115], a[140:143]\n"
"s_add_u32 m0, s71, 36960\n"
"v_mfma_f32_16x16x32_bf16 a[172:175], a[220:223], a[20:23], a[172:175]\n"
"buffer_load_dwordx4 v248, s[48:51], s69 offen  lds\n"
"v_xor_b32 v248, 0x10000, v248\n"
"v_mfma_f32_16x16x32_bf16 a[172:175], a[252:255], a[52:55], a[172:175]\n"
"ds_read_b64_tr_b16 a[220:221], v243 offset:50688\n"
"v_mfma_f32_16x16x32_bf16 v[116:119], a[224:227], v[4:7], v[116:119]\n"
"ds_read_b64_tr_b16 a[222:223], v243 offset:54912\n"
"v_mfma_f32_16x16x32_bf16 v[116:119], v[224:227], v[12:15], v[116:119]\n"
"ds_read_b64_tr_b16 a[252:253], v243 offset:59136\n"
"v_mfma_f32_16x16x32_bf16 v[124:127], a[224:227], a[64:67], v[124:127]\n"
"ds_read_b64_tr_b16 a[254:255], v243 offset:63360\n"
"v_mfma_f32_16x16x32_bf16 v[124:127], v[224:227], a[96:99], v[124:127]\n"
"buffer_load_dwordx4 v[40:43], v249, s[56:59], s61 offen offset:0 sc1\n"
"v_mfma_f32_16x16x32_bf16 v[132:135], a[224:227], a[4:7], v[132:135]\n"
"v_mfma_f32_16x16x32_bf16 v[132:135], v[224:227], a[36:39], v[132:135]\n"
"v_mfma_f32_16x16x32_bf16 v[140:143], a[224:227], a[132:135], v[140:143]\n"
"v_mfma_f32_16x16x32_bf16 v[140:143], v[224:227], a[164:167], v[140:143]\n"
"v_mfma_f32_16x16x32_bf16 v[148:151], a[224:227], a[80:83], v[148:151]\n"
"buffer_load_dwordx4 v[72:75], v249, s[56:59], s62 offen offset:0 sc1\n"
"v_mfma_f32_16x16x32_bf16 v[148:151], v[224:227], a[112:115], v[148:151]\n"
"v_mfma_f32_16x16x32_bf16 v[156:159], a[224:227], a[20:23], v[156:159]\n"
"v_mfma_f32_16x16x32_bf16 v[156:159], v[224:227], a[52:55], v[156:159]\n"
"v_mfma_f32_16x16x32_bf16 v[220:223], a[224:227], a[148:151], v[220:223]\n"
"s_add_u32 m0, s71, 42240\n"
"v_mfma_f32_16x16x32_bf16 v[220:223], v[224:227], a[180:183], v[220:223]\n"
"buffer_load_dwordx4 v248, s[48:51], s66 offen  lds\n"
"v_mfma_f32_16x16x32_bf16 v[164:167], a[228:231], v[4:7], v[164:167]\n"
"ds_read_b64_tr_b16 a[224:225], v244 offset:50688\n"
"v_mfma_f32_16x16x32_bf16 v[164:167], v[228:231], v[12:15], v[164:167]\n"
"ds_read_b64_tr_b16 a[226:227], v244 offset:54912\n"
"v_mfma_f32_16x16x32_bf16 v[172:175], a[228:231], a[64:67], v[172:175]\n"
"ds_read_b64_tr_b16 v[224:225], v244 offset:59136\n"
"v_mfma_f32_16x16x32_bf16 v[172:175], v[228:231], a[96:99], v[172:175]\n"
"ds_read_b64_tr_b16 v[226:227], v244 offset:63360\n"
"v_mfma_f32_16x16x32_bf16 v[180:183], a[228:231], a[4:7], v[180:183]\n"
"buffer_load_dwordx4 v[104:107], v249, s[56:59], s63 offen offset:0 sc1\n"
"v_mfma_f32_16x16x32_bf16 v[180:183], v[228:231], a[36:39], v[180:183]\n"
"v_mfma_f32_16x16x32_bf16 v[188:191], a[228:231], a[132:135], v[188:191]\n"
"v_mfma_f32_16x16x32_bf16 v[188:191], v[228:231], a[164:167], v[188:191]\n"
"v_mfma_f32_16x16x32_bf16 v[208:211], a[228:231], a[80:83], v[208:211]\n"
"s_add_u32 m0, s71, 43296\n"
"v_mfma_f32_16x16x32_bf16 v[208:211], v[228:231], a[112:115], v[208:211]\n"
"buffer_load_dwordx4 v248, s[48:51], s67 offen  lds\n"
"v_mfma_f32_16x16x32_bf16 v[212:215], a[228:231], a[20:23], v[212:215]\n"
"v_mfma_f32_16x16x32_bf16 v[212:215], v[228:231], a[52:55], v[212:215]\n"
"ds_read_b64_tr_b16 a[228:229], v245 offset:50688\n"
"v_mfma_f32_16x16x32_bf16 a[24:27], a[232:235], v[4:7], a[24:27]\n"
"ds_read_b64_tr_b16 a[230:231], v245 offset:54912\n"
"v_mfma_f32_16x16x32_bf16 a[24:27], v[232:235], v[12:15], a[24:27]\n"
"ds_read_b64_tr_b16 v[228:229], v245 offset:59136\n"
"v_mfma_f32_16x16x32_bf16 a[56:59], a[232:235], a[64:67], a[56:59]\n"
"buffer_load_dwordx4 v[136:139], v249, s[56:59], s64 offen offset:0 sc1\n"
"v_mfma_f32_16x16x32_bf16 a[56:59], v[232:235], a[96:99], a[56:59]\n"
"ds_read_b64_tr_b16 v[230:231], v245 offset:63360\n"
"v_mfma_f32_16x16x32_bf16 a[88:91], a[232:235], a[4:7], a[88:91]\n"
"v_mfma_f32_16x16x32_bf16 a[88:91], v[232:235], a[36:39], a[88:91]\n"
"v_mfma_f32_16x16x32_bf16 a[120:123], a[232:235], a[132:135], a[120:123]\n"
"s_add_u32 m0, s71, 44352\n"
"v_mfma_f32_16x16x32_bf16 a[120:123], v[232:235], a[164:167], a[120:123]\n"
"buffer_load_dwordx4 v248, s[48:51], s68 offen  lds\n"
"v_mfma_f32_16x16x32_bf16 a[152:155], a[232:235], a[80:83], a[152:155]\n"
"v_mfma_f32_16x16x32_bf16 a[152:155], v[232:235], a[112:115], a[152:155]\n"
"v_mfma_f32_16x16x32_bf16 a[184:187], a[232:235], a[20:23], a[184:187]\n"
"v_mfma_f32_16x16x32_bf16 a[184:187], v[232:235], a[52:55], a[184:187]\n"
"v_mfma_f32_16x16x32_bf16 a[204:207], a[232:235], a[148:151], a[204:207]\n"
"buffer_load_dwordx4 v[168:171], v249, s[56:59], s65 offen offset:0 sc1\n"
"v_mfma_f32_16x16x32_bf16 a[204:207], v[232:235], a[180:183], a[204:207]\n"
"ds_read_b64_tr_b16 a[232:233], v246 offset:50688\n"
"v_mfma_f32_16x16x32_bf16 a[28:31], a[236:239], v[4:7], a[28:31]\n"
"ds_read_b64_tr_b16 a[234:235], v246 offset:54912\n"
"v_mfma_f32_16x16x32_bf16 a[28:31], v[236:239], v[12:15], a[28:31]\n"
"ds_read_b64_tr_b16 v[232:233], v246 offset:59136\n"
"v_mfma_f32_16x16x32_bf16 a[60:63], a[236:239], a[64:67], a[60:63]\n"
"s_add_u32 m0, s71, 45408\n"
"ds_read_b64_tr_b16 v[234:235], v246 offset:63360\n"
"v_mfma_f32_16x16x32_bf16 a[60:63], v[236:239], a[96:99], a[60:63]\n"
"buffer_load_dwordx4 v248, s[48:51], s69 offen  lds\n"
"v_xor_b32 v248, 0x30000, v248\n"
"v_mfma_f32_16x16x32_bf16 a[92:95], a[236:239], a[4:7], a[92:95]\n"
"v_mfma_f32_16x16x32_bf16 a[92:95], v[236:239], a[36:39], a[92:95]\n"
"v_mfma_f32_16x16x32_bf16 a[124:127], a[236:239], a[132:135], a[124:127]\n"
"v_mfma_f32_16x16x32_bf16 a[124:127], v[236:239], a[164:167], a[124:127]\n"
"v_mfma_f32_16x16x32_bf16 a[156:159], a[236:239], a[80:83], a[156:159]\n"
"buffer_load_dwordx4 v[196:199], v249, s[56:59], s70 offen offset:0 sc1\n"
"v_xor_b32 v249, 0xc00, v249\n"
"v_mfma_f32_16x16x32_bf16 a[156:159], v[236:239], a[112:115], a[156:159]\n"
"v_mfma_f32_16x16x32_bf16 a[188:191], a[236:239], a[20:23], a[188:191]\n"
"v_mfma_f32_16x16x32_bf16 a[188:191], v[236:239], a[52:55], a[188:191]\n"
"s_waitcnt vmcnt(22) lgkmcnt(15)\n"
"v_mfma_f32_16x16x32_bf16 v[20:23], a[208:211], a[0:3], v[20:23]\n"
"ds_read_b64_tr_b16 a[236:237], v247 offset:50688\n"
"v_mfma_f32_16x16x32_bf16 v[20:23], a[240:243], a[32:35], v[20:23]\n"
"ds_read_b64_tr_b16 a[238:239], v247 offset:54912\n"
"buffer_load_dwordx4 v[16:19], v249, s[56:59], s60 offen offset:0 sc1\n"
"v_mfma_f32_16x16x32_bf16 v[28:31], a[208:211], a[128:131], v[28:31]\n"
"ds_read_b64_tr_b16 v[236:237], v247 offset:59136\n"
"v_mfma_f32_16x16x32_bf16 v[28:31], a[240:243], a[160:163], v[28:31]\n"
"ds_read_b64_tr_b16 v[238:239], v247 offset:63360\n"
"v_mfma_f32_16x16x32_bf16 v[36:39], a[208:211], a[68:71], v[36:39]\n"
"buffer_load_dwordx4 v[48:51], v249, s[56:59], s61 offen offset:0 sc1\n"
"v_mfma_f32_16x16x32_bf16 v[36:39], a[240:243], a[100:103], v[36:39]\n"
"v_mfma_f32_16x16x32_bf16 v[44:47], a[208:211], a[16:19], v[44:47]\n"
"v_mfma_f32_16x16x32_bf16 v[44:47], a[240:243], a[48:51], v[44:47]\n"
"buffer_load_dwordx4 v[80:83], v249, s[56:59], s62 offen offset:0 sc1\n"
"v_mfma_f32_16x16x32_bf16 v[52:55], a[208:211], a[144:147], v[52:55]\n"
"v_mfma_f32_16x16x32_bf16 v[52:55], a[240:243], a[176:179], v[52:55]\n"
"v_mfma_f32_16x16x32_bf16 v[60:63], a[208:211], a[84:87], v[60:63]\n"
"v_mfma_f32_16x16x32_bf16 v[60:63], a[240:243], a[116:119], v[60:63]\n"
"buffer_load_dwordx4 v[112:115], v249, s[56:59], s63 offen offset:0 sc1\n"
"v_mfma_f32_16x16x32_bf16 v[216:219], a[208:211], a[192:195], v[216:219]\n"
"v_mfma_f32_16x16x32_bf16 v[216:219], a[240:243], a[200:203], v[216:219]\n"
"s_waitcnt lgkmcnt(0)\n"
"s_barrier\n"
"v_mfma_f32_16x16x32_bf16 v[68:71], a[212:215], a[0:3], v[68:71]\n"
"ds_read_b64_tr_b16 a[208:209], v240 offset:0\n"
"v_mfma_f32_16x16x32_bf16 v[68:71], a[244:247], a[32:35], v[68:71]\n"
"buffer_load_dwordx4 v[144:147], v249, s[56:59], s64 offen offset:0 sc1\n"
"v_mfma_f32_16x16x32_bf16 v[76:79], a[212:215], a[128:131], v[76:79]\n"
"ds_read_b64_tr_b16 a[210:211], v240 offset:4224\n"
"v_mfma_f32_16x16x32_bf16 v[76:79], a[244:247], a[160:163], v[76:79]\n"
"ds_read_b64_tr_b16 a[240:241], v240 offset:8448\n"
"v_mfma_f32_16x16x32_bf16 v[84:87], a[212:215], a[68:71], v[84:87]\n"
"ds_read_b64_tr_b16 a[242:243], v240 offset:12672\n"
"v_mfma_f32_16x16x32_bf16 v[84:87], a[244:247], a[100:103], v[84:87]\n"
"s_add_u32 m0, s71, 50688\n"
"v_mfma_f32_16x16x32_bf16 v[92:95], a[212:215], a[16:19], v[92:95]\n"
"buffer_load_dwordx4 v248, s[48:51], s66 offen  lds\n"
"v_mfma_f32_16x16x32_bf16 v[92:95], a[244:247], a[48:51], v[92:95]\n"
"v_mfma_f32_16x16x32_bf16 v[100:103], a[212:215], a[144:147], v[100:103]\n"
"v_mfma_f32_16x16x32_bf16 v[100:103], a[244:247], a[176:179], v[100:103]\n"
"v_mfma_f32_16x16x32_bf16 v[108:111], a[212:215], a[84:87], v[108:111]\n"
"v_mfma_f32_16x16x32_bf16 v[108:111], a[244:247], a[116:119], v[108:111]\n"
"buffer_load_dwordx4 v[176:179], v249, s[56:59], s65 offen offset:0 sc1\n"
"v_mfma_f32_16x16x32_bf16 a[8:11], a[216:219], a[0:3], a[8:11]\n"
"ds_read_b64_tr_b16 a[212:213], v241 offset:0\n"
"v_mfma_f32_16x16x32_bf16 a[8:11], a[248:251], a[32:35], a[8:11]\n"
"ds_read_b64_tr_b16 a[214:215], v241 offset:4224\n"
"v_mfma_f32_16x16x32_bf16 a[40:43], a[216:219], a[128:131], a[40:43]\n"
"ds_read_b64_tr_b16 a[244:245], v241 offset:8448\n"
"v_mfma_f32_16x16x32_bf16 a[40:43], a[248:251], a[160:163], a[40:43]\n"
"s_add_u32 m0, s71, 51744\n"
"ds_read_b64_tr_b16 a[246:247], v241 offset:12672\n"
"v_mfma_f32_16x16x32_bf16 a[72:75], a[216:219], a[68:71], a[72:75]\n"
"buffer_load_dwordx4 v248, s[48:51], s67 offen  lds\n"
"v_mfma_f32_16x16x32_bf16 a[72:75], a[248:251], a[100:103], a[72:75]\n"
"v_mfma_f32_16x16x32_bf16 a[104:107], a[216:219], a[16:19], a[104:107]\n"
"v_mfma_f32_16x16x32_bf16 a[104:107], a[248:251], a[48:51], a[104:107]\n"
"v_mfma_f32_16x16x32_bf16 a[136:139], a[216:219], a[144:147], a[136:139]\n"
"v_mfma_f32_16x16x32_bf16 a[136:139], a[248:251], a[176:179], a[136:139]\n"
"buffer_load_dwordx4 v[200:203], v249, s[56:59], s70 offen offset:0 sc1\n"
"v_xor_b32 v249, 0x400, v249\n"
"v_mfma_f32_16x16x32_bf16 a[168:171], a[216:219], a[84:87], a[168:171]\n"
"v_mfma_f32_16x16x32_bf16 a[168:171], a[248:251], a[116:119], a[168:171]\n"
"v_mfma_f32_16x16x32_bf16 a[196:199], a[216:219], a[192:195], a[196:199]\n"
"v_mfma_f32_16x16x32_bf16 a[196:199], a[248:251], a[200:203], a[196:199]\n"
"s_add_u32 m0, s71, 52800\n"
"ds_read_b64_tr_b16 a[216:217], v242 offset:0\n"
"v_mfma_f32_16x16x32_bf16 a[12:15], a[220:223], a[0:3], a[12:15]\n"
"buffer_load_dwordx4 v248, s[48:51], s68 offen  lds\n"
"v_mfma_f32_16x16x32_bf16 a[12:15], a[252:255], a[32:35], a[12:15]\n"
"ds_read_b64_tr_b16 a[218:219], v242 offset:4224\n"
"v_mfma_f32_16x16x32_bf16 a[44:47], a[220:223], a[128:131], a[44:47]\n"
"ds_read_b64_tr_b16 a[248:249], v242 offset:8448\n"
"v_mfma_f32_16x16x32_bf16 a[44:47], a[252:255], a[160:163], a[44:47]\n"
"ds_read_b64_tr_b16 a[250:251], v242 offset:12672\n"
"v_mfma_f32_16x16x32_bf16 a[76:79], a[220:223], a[68:71], a[76:79]\n"
"v_mfma_f32_16x16x32_bf16 a[76:79], a[252:255], a[100:103], a[76:79]\n"
"buffer_load_dwordx4 v[24:27], v249, s[56:59], s60 offen offset:0 sc1\n"
"v_mfma_f32_16x16x32_bf16 a[108:111], a[220:223], a[16:19], a[108:111]\n"
"v_mfma_f32_16x16x32_bf16 a[108:111], a[252:255], a[48:51], a[108:111]\n"
"v_mfma_f32_16x16x32_bf16 a[140:143], a[220:223], a[144:147], a[140:143]\n"
"v_mfma_f32_16x16x32_bf16 a[140:143], a[252:255], a[176:179], a[140:143]\n"
"s_add_u32 m0, s71, 53856\n"
"v_mfma_f32_16x16x32_bf16 a[172:175], a[220:223], a[84:87], a[172:175]\n"
"buffer_load_dwordx4 v248, s[48:51], s69 offen  lds\n"
"v_xor_b32 v248, 0x10000, v248\n"
"v_mfma_f32_16x16x32_bf16 a[172:175], a[252:255], a[116:119], a[172:175]\n"
"ds_read_b64_tr_b16 a[220:221], v243 offset:0\n"
"v_mfma_f32_16x16x32_bf16 v[116:119], a[224:227], a[0:3], v[116:119]\n"
"ds_read_b64_tr_b16 a[222:223], v243 offset:4224\n"
"v_mfma_f32_16x16x32_bf16 v[116:119], v[224:227], a[32:35], v[116:119]\n"
"ds_read_b64_tr_b16 a[252:253], v243 offset:8448\n"
"v_mfma_f32_16x16x32_bf16 v[124:127], a[224:227], a[128:131], v[124:127]\n"
"ds_read_b64_tr_b16 a[254:255], v243 offset:12672\n"
"v_mfma_f32_16x16x32_bf16 v[124:127], v[224:227], a[160:163], v[124:127]\n"
"buffer_load_dwordx4 v[56:59], v249, s[56:59], s61 offen offset:0 sc1\n"
"v_mfma_f32_16x16x32_bf16 v[132:135], a[224:227], a[68:71], v[132:135]\n"
"v_mfma_f32_16x16x32_bf16 v[132:135], v[224:227], a[100:103], v[132:135]\n"
"v_mfma_f32_16x16x32_bf16 v[140:143], a[224:227], a[16:19], v[140:143]\n"
"v_mfma_f32_16x16x32_bf16 v[140:143], v[224:227], a[48:51], v[140:143]\n"
"v_mfma_f32_16x16x32_bf16 v[148:151], a[224:227], a[144:147], v[148:151]\n"
"buffer_load_dwordx4 v[88:91], v249, s[56:59], s62 offen offset:0 sc1\n"
"v_mfma_f32_16x16x32_bf16 v[148:151], v[224:227], a[176:179], v[148:151]\n"
"v_mfma_f32_16x16x32_bf16 v[156:159], a[224:227], a[84:87], v[156:159]\n"
"v_mfma_f32_16x16x32_bf16 v[156:159], v[224:227], a[116:119], v[156:159]\n"
"v_mfma_f32_16x16x32_bf16 v[220:223], a[224:227], a[192:195], v[220:223]\n"
"s_add_u32 m0, s71, 59136\n"
"v_mfma_f32_16x16x32_bf16 v[220:223], v[224:227], a[200:203], v[220:223]\n"
"buffer_load_dwordx4 v248, s[48:51], s66 offen  lds\n"
"v_mfma_f32_16x16x32_bf16 v[164:167], a[228:231], a[0:3], v[164:167]\n"
"ds_read_b64_tr_b16 a[224:225], v244 offset:0\n"
"v_mfma_f32_16x16x32_bf16 v[164:167], v[228:231], a[32:35], v[164:167]\n"
"ds_read_b64_tr_b16 a[226:227], v244 offset:4224\n"
"v_mfma_f32_16x16x32_bf16 v[172:175], a[228:231], a[128:131], v[172:175]\n"
"ds_read_b64_tr_b16 v[224:225], v244 offset:8448\n"
"v_mfma_f32_16x16x32_bf16 v[172:175], v[228:231], a[160:163], v[172:175]\n"
"ds_read_b64_tr_b16 v[226:227], v244 offset:12672\n"
"v_mfma_f32_16x16x32_bf16 v[180:183], a[228:231], a[68:71], v[180:183]\n"
"buffer_load_dwordx4 v[120:123], v249, s[56:59], s63 offen offset:0 sc1\n"
"v_mfma_f32_16x16x32_bf16 v[180:183], v[228:231], a[100:103], v[180:183]\n"
"v_mfma_f32_16x16x32_bf16 v[188:191], a[228:231], a[16:19], v[188:191]\n"
"v_mfma_f32_16x16x32_bf16 v[188:191], v[228:231], a[48:51], v[188:191]\n"
"v_mfma_f32_16x16x32_bf16 v[208:211], a[228:231], a[144:147], v[208:211]\n"
"s_add_u32 m0, s71, 60192\n"
"v_mfma_f32_16x16x32_bf16 v[208:211], v[228:231], a[176:179], v[208:211]\n"
"buffer_load_dwordx4 v248, s[48:51], s67 offen  lds\n"
"v_mfma_f32_16x16x32_bf16 v[212:215], a[228:231], a[84:87], v[212:215]\n"
"v_mfma_f32_16x16x32_bf16 v[212:215], v[228:231], a[116:119], v[212:215]\n"
"ds_read_b64_tr_b16 a[228:229], v245 offset:0\n"
"v_mfma_f32_16x16x32_bf16 a[24:27], a[232:235], a[0:3], a[24:27]\n"
"ds_read_b64_tr_b16 a[230:231], v245 offset:4224\n"
"v_mfma_f32_16x16x32_bf16 a[24:27], v[232:235], a[32:35], a[24:27]\n"
"ds_read_b64_tr_b16 v[228:229], v245 offset:8448\n"
"v_mfma_f32_16x16x32_bf16 a[56:59], a[232:235], a[128:131], a[56:59]\n"
"buffer_load_dwordx4 v[152:155], v249, s[56:59], s64 offen offset:0 sc1\n"
"v_mfma_f32_16x16x32_bf16 a[56:59], v[232:235], a[160:163], a[56:59]\n"
"ds_read_b64_tr_b16 v[230:231], v245 offset:12672\n"
"v_mfma_f32_16x16x32_bf16 a[88:91], a[232:235], a[68:71], a[88:91]\n"
"v_mfma_f32_16x16x32_bf16 a[88:91], v[232:235], a[100:103], a[88:91]\n"
"v_mfma_f32_16x16x32_bf16 a[120:123], a[232:235], a[16:19], a[120:123]\n"
"s_add_u32 m0, s71, 61248\n"
"v_mfma_f32_16x16x32_bf16 a[120:123], v[232:235], a[48:51], a[120:123]\n"
"buffer_load_dwordx4 v248, s[48:51], s68 offen  lds\n"
"v_mfma_f32_16x16x32_bf16 a[152:155], a[232:235], a[144:147], a[152:155]\n"
"v_mfma_f32_16x16x32_bf16 a[152:155], v[232:235], a[176:179], a[152:155]\n"
"v_mfma_f32_16x16x32_bf16 a[184:187], a[232:235], a[84:87], a[184:187]\n"
"v_mfma_f32_16x16x32_bf16 a[184:187], v[232:235], a[116:119], a[184:187]\n"
"v_mfma_f32_16x16x32_bf16 a[204:207], a[232:235], a[192:195], a[204:207]\n"
"buffer_load_dwordx4 v[184:187], v249, s[56:59], s65 offen offset:0 sc1\n"
"v_mfma_f32_16x16x32_bf16 a[204:207], v[232:235], a[200:203], a[204:207]\n"
"ds_read_b64_tr_b16 a[232:233], v246 offset:0\n"
"v_mfma_f32_16x16x32_bf16 a[28:31], a[236:239], a[0:3], a[28:31]\n"
"ds_read_b64_tr_b16 a[234:235], v246 offset:4224\n"
"v_mfma_f32_16x16x32_bf16 a[28:31], v[236:239], a[32:35], a[28:31]\n"
"ds_read_b64_tr_b16 v[232:233], v246 offset:8448\n"
"v_mfma_f32_16x16x32_bf16 a[60:63], a[236:239], a[128:131], a[60:63]\n"
"s_add_u32 m0, s71, 62304\n"
"ds_read_b64_tr_b16 v[234:235], v246 offset:12672\n"
"v_mfma_f32_16x16x32_bf16 a[60:63], v[236:239], a[160:163], a[60:63]\n"
"buffer_load_dwordx4 v248, s[48:51], s69 offen  lds\n"
"v_mfma_f32_16x16x32_bf16 a[92:95], a[236:239], a[68:71], a[92:95]\n"
"v_mfma_f32_16x16x32_bf16 a[92:95], v[236:239], a[100:103], a[92:95]\n"
"v_mfma_f32_16x16x32_bf16 a[124:127], a[236:239], a[16:19], a[124:127]\n"
"v_mfma_f32_16x16x32_bf16 a[124:127], v[236:239], a[48:51], a[124:127]\n"
"v_mfma_f32_16x16x32_bf16 a[156:159], a[236:239], a[144:147], a[156:159]\n"
"buffer_load_dwordx4 v[204:207], v249, s[56:59], s70 offen offset:0 sc1\n"
"v_xor_b32 v249, 0xc00, v249\n"
"s_cmp_eq_u32 s72, 6\n"
"s_cselect_b32 s74, 2, 1\n"
"v_lshl_add_u32 v249, s74, 13, v249\n"
"v_mfma_f32_16x16x32_bf16 a[156:159], v[236:239], a[176:179], a[156:159]\n"
"v_mfma_f32_16x16x32_bf16 a[188:191], a[236:239], a[84:87], a[188:191]\n"
"v_mfma_f32_16x16x32_bf16 a[188:191], v[236:239], a[116:119], a[188:191]\n"
"s_waitcnt vmcnt(22) lgkmcnt(15)\n"
"v_mfma_f32_16x16x32_bf16 v[20:23], a[208:211], v[0:3], v[20:23]\n"
"ds_read_b64_tr_b16 a[236:237], v247 offset:0\n"
"v_mfma_f32_16x16x32_bf16 v[20:23], a[240:243], v[8:11], v[20:23]\n"
"ds_read_b64_tr_b16 a[238:239], v247 offset:4224\n"
"buffer_load_dwordx4 v[4:7], v249, s[56:59], s60 offen offset:0 sc1\n"
"v_mfma_f32_16x16x32_bf16 v[28:31], a[208:211], v[32:35], v[28:31]\n"
"ds_read_b64_tr_b16 v[236:237], v247 offset:8448\n"
"v_mfma_f32_16x16x32_bf16 v[28:31], a[240:243], v[40:43], v[28:31]\n"
"ds_read_b64_tr_b16 v[238:239], v247 offset:12672\n"
"v_mfma_f32_16x16x32_bf16 v[36:39], a[208:211], v[64:67], v[36:39]\n"
"buffer_load_dwordx4 a[64:67], v249, s[56:59], s61 offen offset:0 sc1\n"
"v_mfma_f32_16x16x32_bf16 v[36:39], a[240:243], v[72:75], v[36:39]\n"
"v_mfma_f32_16x16x32_bf16 v[44:47], a[208:211], v[96:99], v[44:47]\n"
"v_mfma_f32_16x16x32_bf16 v[44:47], a[240:243], v[104:107], v[44:47]\n"
"buffer_load_dwordx4 a[4:7], v249, s[56:59], s62 offen offset:0 sc1\n"
"v_mfma_f32_16x16x32_bf16 v[52:55], a[208:211], v[128:131], v[52:55]\n"
"v_mfma_f32_16x16x32_bf16 v[52:55], a[240:243], v[136:139], v[52:55]\n"
"v_mfma_f32_16x16x32_bf16 v[60:63], a[208:211], v[160:163], v[60:63]\n"
"v_mfma_f32_16x16x32_bf16 v[60:63], a[240:243], v[168:171], v[60:63]\n"
"buffer_load_dwordx4 a[132:135], v249, s[56:59], s63 offen offset:0 sc1\n"
"v_mfma_f32_16x16x32_bf16 v[216:219], a[208:211], v[192:195], v[216:219]\n"
"v_mfma_f32_16x16x32_bf16 v[216:219], a[240:243], v[196:199], v[216:219]\n"
"s_waitcnt lgkmcnt(0)\n"
"s_barrier\n"
"v_mfma_f32_16x16x32_bf16 v[68:71], a[212:215], v[0:3], v[68:71]\n"
"ds_read_b64_tr_b16 a[208:209], v240 offset:16896\n"
"v_mfma_f32_16x16x32_bf16 v[68:71], a[244:247], v[8:11], v[68:71]\n"
"buffer_load_dwordx4 a[80:83], v249, s[56:59], s64 offen offset:0 sc1\n"
"v_mfma_f32_16x16x32_bf16 v[76:79], a[212:215], v[32:35], v[76:79]\n"
"ds_read_b64_tr_b16 a[210:211], v240 offset:21120\n"
"v_mfma_f32_16x16x32_bf16 v[76:79], a[244:247], v[40:43], v[76:79]\n"
"ds_read_b64_tr_b16 a[240:241], v240 offset:25344\n"
"v_mfma_f32_16x16x32_bf16 v[84:87], a[212:215], v[64:67], v[84:87]\n"
"ds_read_b64_tr_b16 a[242:243], v240 offset:29568\n"
"v_mfma_f32_16x16x32_bf16 v[84:87], a[244:247], v[72:75], v[84:87]\n"
"v_mfma_f32_16x16x32_bf16 v[92:95], a[212:215], v[96:99], v[92:95]\n"
"v_mfma_f32_16x16x32_bf16 v[92:95], a[244:247], v[104:107], v[92:95]\n"
"v_mfma_f32_16x16x32_bf16 v[100:103], a[212:215], v[128:131], v[100:103]\n"
"v_mfma_f32_16x16x32_bf16 v[100:103], a[244:247], v[136:139], v[100:103]\n"
"v_mfma_f32_16x16x32_bf16 v[108:111], a[212:215], v[160:163], v[108:111]\n"
"buffer_load_dwordx4 a[20:23], v249, s[56:59], s65 offen offset:0 sc1\n"
"v_mfma_f32_16x16x32_bf16 v[108:111], a[244:247], v[168:171], v[108:111]\n"
"ds_read_b64_tr_b16 a[212:213], v241 offset:16896\n"
"v_mfma_f32_16x16x32_bf16 a[8:11], a[216:219], v[0:3], a[8:11]\n"
"ds_read_b64_tr_b16 a[214:215], v241 offset:21120\n"
"v_mfma_f32_16x16x32_bf16 a[8:11], a[248:251], v[8:11], a[8:11]\n"
"ds_read_b64_tr_b16 a[244:245], v241 offset:25344\n"
"v_mfma_f32_16x16x32_bf16 a[40:43], a[216:219], v[32:35], a[40:43]\n"
"ds_read_b64_tr_b16 a[246:247], v241 offset:29568\n"
"v_mfma_f32_16x16x32_bf16 a[40:43], a[248:251], v[40:43], a[40:43]\n"
"v_mfma_f32_16x16x32_bf16 a[72:75], a[216:219], v[64:67], a[72:75]\n"
"v_mfma_f32_16x16x32_bf16 a[72:75], a[248:251], v[72:75], a[72:75]\n"
"v_mfma_f32_16x16x32_bf16 a[104:107], a[216:219], v[96:99], a[104:107]\n"
"v_mfma_f32_16x16x32_bf16 a[104:107], a[248:251], v[104:107], a[104:107]\n"
"buffer_load_dwordx4 a[148:151], v249, s[56:59], s70 offen offset:0 sc1\n"
"v_xor_b32 v249, 0x400, v249\n"
"v_mfma_f32_16x16x32_bf16 a[136:139], a[216:219], v[128:131], a[136:139]\n"
"v_mfma_f32_16x16x32_bf16 a[136:139], a[248:251], v[136:139], a[136:139]\n"
"v_mfma_f32_16x16x32_bf16 a[168:171], a[216:219], v[160:163], a[168:171]\n"
"v_mfma_f32_16x16x32_bf16 a[168:171], a[248:251], v[168:171], a[168:171]\n"
"v_mfma_f32_16x16x32_bf16 a[196:199], a[216:219], v[192:195], a[196:199]\n"
"v_mfma_f32_16x16x32_bf16 a[196:199], a[248:251], v[196:199], a[196:199]\n"
"ds_read_b64_tr_b16 a[216:217], v242 offset:16896\n"
"v_mfma_f32_16x16x32_bf16 a[12:15], a[220:223], v[0:3], a[12:15]\n"
"ds_read_b64_tr_b16 a[218:219], v242 offset:21120\n"
"v_mfma_f32_16x16x32_bf16 a[12:15], a[252:255], v[8:11], a[12:15]\n"
"ds_read_b64_tr_b16 a[248:249], v242 offset:25344\n"
"v_mfma_f32_16x16x32_bf16 a[44:47], a[220:223], v[32:35], a[44:47]\n"
"ds_read_b64_tr_b16 a[250:251], v242 offset:29568\n"
"v_mfma_f32_16x16x32_bf16 a[44:47], a[252:255], v[40:43], a[44:47]\n"
"buffer_load_dwordx4 v[12:15], v249, s[56:59], s60 offen offset:0 sc1\n"
"v_mfma_f32_16x16x32_bf16 a[76:79], a[220:223], v[64:67], a[76:79]\n"
"v_mfma_f32_16x16x32_bf16 a[76:79], a[252:255], v[72:75], a[76:79]\n"
"v_mfma_f32_16x16x32_bf16 a[108:111], a[220:223], v[96:99], a[108:111]\n"
"v_mfma_f32_16x16x32_bf16 a[108:111], a[252:255], v[104:107], a[108:111]\n"
"v_mfma_f32_16x16x32_bf16 a[140:143], a[220:223], v[128:131], a[140:143]\n"
"v_mfma_f32_16x16x32_bf16 a[140:143], a[252:255], v[136:139], a[140:143]\n"
"v_mfma_f32_16x16x32_bf16 a[172:175], a[220:223], v[160:163], a[172:175]\n"
"v_mfma_f32_16x16x32_bf16 a[172:175], a[252:255], v[168:171], a[172:175]\n"
"ds_read_b64_tr_b16 a[220:221], v243 offset:16896\n"
"v_mfma_f32_16x16x32_bf16 v[116:119], a[224:227], v[0:3], v[116:119]\n"
"buffer_load_dwordx4 a[96:99], v249, s[56:59], s61 offen offset:0 sc1\n"
"v_mfma_f32_16x16x32_bf16 v[116:119], v[224:227], v[8:11], v[116:119]\n"
"ds_read_b64_tr_b16 a[222:223], v243 offset:21120\n"
"v_mfma_f32_16x16x32_bf16 v[124:127], a[224:227], v[32:35], v[124:127]\n"
"ds_read_b64_tr_b16 a[252:253], v243 offset:25344\n"
"v_mfma_f32_16x16x32_bf16 v[124:127], v[224:227], v[40:43], v[124:127]\n"
"ds_read_b64_tr_b16 a[254:255], v243 offset:29568\n"
"v_mfma_f32_16x16x32_bf16 v[132:135], a[224:227], v[64:67], v[132:135]\n"
"v_mfma_f32_16x16x32_bf16 v[132:135], v[224:227], v[72:75], v[132:135]\n"
"v_mfma_f32_16x16x32_bf16 v[140:143], a[224:227], v[96:99], v[140:143]\n"
"v_mfma_f32_16x16x32_bf16 v[140:143], v[224:227], v[104:107], v[140:143]\n"
"v_mfma_f32_16x16x32_bf16 v[148:151], a[224:227], v[128:131], v[148:151]\n"
"v_mfma_f32_16x16x32_bf16 v[148:151], v[224:227], v[136:139], v[148:151]\n"
"v_mfma_f32_16x16x32_bf16 v[156:159], a[224:227], v[160:163], v[156:159]\n"
"buffer_load_dwordx4 a[36:39], v249, s[56:59], s62 offen offset:0 sc1\n"
"v_mfma_f32_16x16x32_bf16 v[156:159], v[224:227], v[168:171], v[156:159]\n"
"v_mfma_f32_16x16x32_bf16 v[220:223], a[224:227], v[192:195], v[220:223]\n"
"v_mfma_f32_16x16x32_bf16 v[220:223], v[224:227], v[196:199], v[220:223]\n"
"ds_read_b64_tr_b16 a[224:225], v244 offset:16896\n"
"v_mfma_f32_16x16x32_bf16 v[164:167], a[228:231], v[0:3], v[164:167]\n"
"ds_read_b64_tr_b16 a[226:227], v244 offset:21120\n"
"v_mfma_f32_16x16x32_bf16 v[164:167], v[228:231], v[8:11], v[164:167]\n"
"ds_read_b64_tr_b16 v[224:225], v244 offset:25344\n"
"v_mfma_f32_16x16x32_bf16 v[172:175], a[228:231], v[32:35], v[172:175]\n"
"ds_read_b64_tr_b16 v[226:227], v244 offset:29568\n"
"v_mfma_f32_16x16x32_bf16 v[172:175], v[228:231], v[40:43], v[172:175]\n"
"v_mfma_f32_16x16x32_bf16 v[180:183], a[228:231], v[64:67], v[180:183]\n"
"v_mfma_f32_16x16x32_bf16 v[180:183], v[228:231], v[72:75], v[180:183]\n"
"buffer_load_dwordx4 a[164:167], v249, s[56:59], s63 offen offset:0 sc1\n"
"v_mfma_f32_16x16x32_bf16 v[188:191], a[228:231], v[96:99], v[188:191]\n"
"v_mfma_f32_16x16x32_bf16 v[188:191], v[228:231], v[104:107], v[188:191]\n"
"v_mfma_f32_16x16x32_bf16 v[208:211], a[228:231], v[128:131], v[208:211]\n"
"v_mfma_f32_16x16x32_bf16 v[208:211], v[228:231], v[136:139], v[208:211]\n"
"v_mfma_f32_16x16x32_bf16 v[212:215], a[228:231], v[160:163], v[212:215]\n"
"v_mfma_f32_16x16x32_bf16 v[212:215], v[228:231], v[168:171], v[212:215]\n"
"ds_read_b64_tr_b16 a[228:229], v245 offset:16896\n"
"v_mfma_f32_16x16x32_bf16 a[24:27], a[232:235], v[0:3], a[24:27]\n"
"ds_read_b64_tr_b16 a[230:231], v245 offset:21120\n"
"v_mfma_f32_16x16x32_bf16 a[24:27], v[232:235], v[8:11], a[24:27]\n"
"ds_read_b64_tr_b16 v[228:229], v245 offset:25344\n"
"v_mfma_f32_16x16x32_bf16 a[56:59], a[232:235], v[32:35], a[56:59]\n"
"ds_read_b64_tr_b16 v[230:231], v245 offset:29568\n"
"v_mfma_f32_16x16x32_bf16 a[56:59], v[232:235], v[40:43], a[56:59]\n"
"buffer_load_dwordx4 a[112:115], v249, s[56:59], s64 offen offset:0 sc1\n"
"v_mfma_f32_16x16x32_bf16 a[88:91], a[232:235], v[64:67], a[88:91]\n"
"v_mfma_f32_16x16x32_bf16 a[88:91], v[232:235], v[72:75], a[88:91]\n"
"v_mfma_f32_16x16x32_bf16 a[120:123], a[232:235], v[96:99], a[120:123]\n"
"v_mfma_f32_16x16x32_bf16 a[120:123], v[232:235], v[104:107], a[120:123]\n"
"v_mfma_f32_16x16x32_bf16 a[152:155], a[232:235], v[128:131], a[152:155]\n"
"v_mfma_f32_16x16x32_bf16 a[152:155], v[232:235], v[136:139], a[152:155]\n"
"v_mfma_f32_16x16x32_bf16 a[184:187], a[232:235], v[160:163], a[184:187]\n"
"v_mfma_f32_16x16x32_bf16 a[184:187], v[232:235], v[168:171], a[184:187]\n"
"v_mfma_f32_16x16x32_bf16 a[204:207], a[232:235], v[192:195], a[204:207]\n"
"buffer_load_dwordx4 a[52:55], v249, s[56:59], s65 offen offset:0 sc1\n"
"v_mfma_f32_16x16x32_bf16 a[204:207], v[232:235], v[196:199], a[204:207]\n"
"ds_read_b64_tr_b16 a[232:233], v246 offset:16896\n"
"v_mfma_f32_16x16x32_bf16 a[28:31], a[236:239], v[0:3], a[28:31]\n"
"ds_read_b64_tr_b16 a[234:235], v246 offset:21120\n"
"v_mfma_f32_16x16x32_bf16 a[28:31], v[236:239], v[8:11], a[28:31]\n"
"ds_read_b64_tr_b16 v[232:233], v246 offset:25344\n"
"v_mfma_f32_16x16x32_bf16 a[60:63], a[236:239], v[32:35], a[60:63]\n"
"ds_read_b64_tr_b16 v[234:235], v246 offset:29568\n"
"v_mfma_f32_16x16x32_bf16 a[60:63], v[236:239], v[40:43], a[60:63]\n"
"v_mfma_f32_16x16x32_bf16 a[92:95], a[236:239], v[64:67], a[92:95]\n"
"v_mfma_f32_16x16x32_bf16 a[92:95], v[236:239], v[72:75], a[92:95]\n"
"v_mfma_f32_16x16x32_bf16 a[124:127], a[236:239], v[96:99], a[124:127]\n"
"v_mfma_f32_16x16x32_bf16 a[124:127], v[236:239], v[104:107], a[124:127]\n"
"v_mfma_f32_16x16x32_bf16 a[156:159], a[236:239], v[128:131], a[156:159]\n"
"buffer_load_dwordx4 a[180:183], v249, s[56:59], s70 offen offset:0 sc1\n"
"v_xor_b32 v249, 0xc00, v249\n"
"v_mfma_f32_16x16x32_bf16 a[156:159], v[236:239], v[136:139], a[156:159]\n"
"v_mfma_f32_16x16x32_bf16 a[188:191], a[236:239], v[160:163], a[188:191]\n"
"v_mfma_f32_16x16x32_bf16 a[188:191], v[236:239], v[168:171], a[188:191]\n"
"s_waitcnt vmcnt(14) lgkmcnt(15)\n"
"v_mfma_f32_16x16x32_bf16 v[20:23], a[208:211], v[16:19], v[20:23]\n"
"ds_read_b64_tr_b16 a[236:237], v247 offset:16896\n"
"v_mfma_f32_16x16x32_bf16 v[20:23], a[240:243], v[24:27], v[20:23]\n"
"ds_read_b64_tr_b16 a[238:239], v247 offset:21120\n"
"buffer_load_dwordx4 a[0:3], v249, s[56:59], s60 offen offset:0 sc1\n"
"v_mfma_f32_16x16x32_bf16 v[28:31], a[208:211], v[48:51], v[28:31]\n"
"ds_read_b64_tr_b16 v[236:237], v247 offset:25344\n"
"v_mfma_f32_16x16x32_bf16 v[28:31], a[240:243], v[56:59], v[28:31]\n"
"ds_read_b64_tr_b16 v[238:239], v247 offset:29568\n"
"v_mfma_f32_16x16x32_bf16 v[36:39], a[208:211], v[80:83], v[36:39]\n"
"buffer_load_dwordx4 a[128:131], v249, s[56:59], s61 offen offset:0 sc1\n"
"v_mfma_f32_16x16x32_bf16 v[36:39], a[240:243], v[88:91], v[36:39]\n"
"v_mfma_f32_16x16x32_bf16 v[44:47], a[208:211], v[112:115], v[44:47]\n"
"v_mfma_f32_16x16x32_bf16 v[44:47], a[240:243], v[120:123], v[44:47]\n"
"buffer_load_dwordx4 a[68:71], v249, s[56:59], s62 offen offset:0 sc1\n"
"v_mfma_f32_16x16x32_bf16 v[52:55], a[208:211], v[144:147], v[52:55]\n"
"v_mfma_f32_16x16x32_bf16 v[52:55], a[240:243], v[152:155], v[52:55]\n"
"v_mfma_f32_16x16x32_bf16 v[60:63], a[208:211], v[176:179], v[60:63]\n"
"v_mfma_f32_16x16x32_bf16 v[60:63], a[240:243], v[184:187], v[60:63]\n"
"buffer_load_dwordx4 a[16:19], v249, s[56:59], s63 offen offset:0 sc1\n"
"v_mfma_f32_16x16x32_bf16 v[216:219], a[208:211], v[200:203], v[216:219]\n"
"v_mfma_f32_16x16x32_bf16 v[216:219], a[240:243], v[204:207], v[216:219]\n"
"s_waitcnt lgkmcnt(0)\n"
"s_barrier\n"
"v_mfma_f32_16x16x32_bf16 v[68:71], a[212:215], v[16:19], v[68:71]\n"
"ds_read_b64_tr_b16 a[208:209], v240 offset:33792\n"
"v_mfma_f32_16x16x32_bf16 v[68:71], a[244:247], v[24:27], v[68:71]\n"
"buffer_load_dwordx4 a[144:147], v249, s[56:59], s64 offen offset:0 sc1\n"
"v_mfma_f32_16x16x32_bf16 v[76:79], a[212:215], v[48:51], v[76:79]\n"
"ds_read_b64_tr_b16 a[210:211], v240 offset:38016\n"
"v_mfma_f32_16x16x32_bf16 v[76:79], a[244:247], v[56:59], v[76:79]\n"
"ds_read_b64_tr_b16 a[240:241], v240 offset:42240\n"
"v_mfma_f32_16x16x32_bf16 v[84:87], a[212:215], v[80:83], v[84:87]\n"
"ds_read_b64_tr_b16 a[242:243], v240 offset:46464\n"
"v_mfma_f32_16x16x32_bf16 v[84:87], a[244:247], v[88:91], v[84:87]\n"
"v_mfma_f32_16x16x32_bf16 v[92:95], a[212:215], v[112:115], v[92:95]\n"
"v_mfma_f32_16x16x32_bf16 v[92:95], a[244:247], v[120:123], v[92:95]\n"
"v_mfma_f32_16x16x32_bf16 v[100:103], a[212:215], v[144:147], v[100:103]\n"
"v_mfma_f32_16x16x32_bf16 v[100:103], a[244:247], v[152:155], v[100:103]\n"
"v_mfma_f32_16x16x32_bf16 v[108:111], a[212:215], v[176:179], v[108:111]\n"
"buffer_load_dwordx4 a[84:87], v249, s[56:59], s65 offen offset:0 sc1\n"
"v_mfma_f32_16x16x32_bf16 v[108:111], a[244:247], v[184:187], v[108:111]\n"
"ds_read_b64_tr_b16 a[212:213], v241 offset:33792\n"
"v_mfma_f32_16x16x32_bf16 a[8:11], a[216:219], v[16:19], a[8:11]\n"
"ds_read_b64_tr_b16 a[214:215], v241 offset:38016\n"
"v_mfma_f32_16x16x32_bf16 a[8:11], a[248:251], v[24:27], a[8:11]\n"
"ds_read_b64_tr_b16 a[244:245], v241 offset:42240\n"
"v_mfma_f32_16x16x32_bf16 a[40:43], a[216:219], v[48:51], a[40:43]\n"
"ds_read_b64_tr_b16 a[246:247], v241 offset:46464\n"
"v_mfma_f32_16x16x32_bf16 a[40:43], a[248:251], v[56:59], a[40:43]\n"
"v_mfma_f32_16x16x32_bf16 a[72:75], a[216:219], v[80:83], a[72:75]\n"
"v_mfma_f32_16x16x32_bf16 a[72:75], a[248:251], v[88:91], a[72:75]\n"
"v_mfma_f32_16x16x32_bf16 a[104:107], a[216:219], v[112:115], a[104:107]\n"
"v_mfma_f32_16x16x32_bf16 a[104:107], a[248:251], v[120:123], a[104:107]\n"
"buffer_load_dwordx4 a[192:195], v249, s[56:59], s70 offen offset:0 sc1\n"
"v_xor_b32 v249, 0x400, v249\n"
"v_mfma_f32_16x16x32_bf16 a[136:139], a[216:219], v[144:147], a[136:139]\n"
"v_mfma_f32_16x16x32_bf16 a[136:139], a[248:251], v[152:155], a[136:139]\n"
"v_mfma_f32_16x16x32_bf16 a[168:171], a[216:219], v[176:179], a[168:171]\n"
"v_mfma_f32_16x16x32_bf16 a[168:171], a[248:251], v[184:187], a[168:171]\n"
"v_mfma_f32_16x16x32_bf16 a[196:199], a[216:219], v[200:203], a[196:199]\n"
"v_mfma_f32_16x16x32_bf16 a[196:199], a[248:251], v[204:207], a[196:199]\n"
"ds_read_b64_tr_b16 a[216:217], v242 offset:33792\n"
"v_mfma_f32_16x16x32_bf16 a[12:15], a[220:223], v[16:19], a[12:15]\n"
"ds_read_b64_tr_b16 a[218:219], v242 offset:38016\n"
"v_mfma_f32_16x16x32_bf16 a[12:15], a[252:255], v[24:27], a[12:15]\n"
"ds_read_b64_tr_b16 a[248:249], v242 offset:42240\n"
"v_mfma_f32_16x16x32_bf16 a[44:47], a[220:223], v[48:51], a[44:47]\n"
"ds_read_b64_tr_b16 a[250:251], v242 offset:46464\n"
"v_mfma_f32_16x16x32_bf16 a[44:47], a[252:255], v[56:59], a[44:47]\n"
"buffer_load_dwordx4 a[32:35], v249, s[56:59], s60 offen offset:0 sc1\n"
"v_mfma_f32_16x16x32_bf16 a[76:79], a[220:223], v[80:83], a[76:79]\n"
"v_mfma_f32_16x16x32_bf16 a[76:79], a[252:255], v[88:91], a[76:79]\n"
"v_mfma_f32_16x16x32_bf16 a[108:111], a[220:223], v[112:115], a[108:111]\n"
"v_mfma_f32_16x16x32_bf16 a[108:111], a[252:255], v[120:123], a[108:111]\n"
"v_mfma_f32_16x16x32_bf16 a[140:143], a[220:223], v[144:147], a[140:143]\n"
"v_mfma_f32_16x16x32_bf16 a[140:143], a[252:255], v[152:155], a[140:143]\n"
"v_mfma_f32_16x16x32_bf16 a[172:175], a[220:223], v[176:179], a[172:175]\n"
"v_mfma_f32_16x16x32_bf16 a[172:175], a[252:255], v[184:187], a[172:175]\n"
"ds_read_b64_tr_b16 a[220:221], v243 offset:33792\n"
"v_mfma_f32_16x16x32_bf16 v[116:119], a[224:227], v[16:19], v[116:119]\n"
"buffer_load_dwordx4 a[160:163], v249, s[56:59], s61 offen offset:0 sc1\n"
"v_mfma_f32_16x16x32_bf16 v[116:119], v[224:227], v[24:27], v[116:119]\n"
"ds_read_b64_tr_b16 a[222:223], v243 offset:38016\n"
"v_mfma_f32_16x16x32_bf16 v[124:127], a[224:227], v[48:51], v[124:127]\n"
"ds_read_b64_tr_b16 a[252:253], v243 offset:42240\n"
"v_mfma_f32_16x16x32_bf16 v[124:127], v[224:227], v[56:59], v[124:127]\n"
"ds_read_b64_tr_b16 a[254:255], v243 offset:46464\n"
"v_mfma_f32_16x16x32_bf16 v[132:135], a[224:227], v[80:83], v[132:135]\n"
"v_mfma_f32_16x16x32_bf16 v[132:135], v[224:227], v[88:91], v[132:135]\n"
"v_mfma_f32_16x16x32_bf16 v[140:143], a[224:227], v[112:115], v[140:143]\n"
"v_mfma_f32_16x16x32_bf16 v[140:143], v[224:227], v[120:123], v[140:143]\n"
"v_mfma_f32_16x16x32_bf16 v[148:151], a[224:227], v[144:147], v[148:151]\n"
"v_mfma_f32_16x16x32_bf16 v[148:151], v[224:227], v[152:155], v[148:151]\n"
"v_mfma_f32_16x16x32_bf16 v[156:159], a[224:227], v[176:179], v[156:159]\n"
"buffer_load_dwordx4 a[100:103], v249, s[56:59], s62 offen offset:0 sc1\n"
"v_mfma_f32_16x16x32_bf16 v[156:159], v[224:227], v[184:187], v[156:159]\n"
"v_mfma_f32_16x16x32_bf16 v[220:223], a[224:227], v[200:203], v[220:223]\n"
"v_mfma_f32_16x16x32_bf16 v[220:223], v[224:227], v[204:207], v[220:223]\n"
"ds_read_b64_tr_b16 a[224:225], v240 offset:50688\n"
"v_mfma_f32_16x16x32_bf16 v[164:167], a[228:231], v[16:19], v[164:167]\n"
"ds_read_b64_tr_b16 a[226:227], v240 offset:54912\n"
"v_mfma_f32_16x16x32_bf16 v[164:167], v[228:231], v[24:27], v[164:167]\n"
"ds_read_b64_tr_b16 v[224:225], v240 offset:59136\n"
"v_mfma_f32_16x16x32_bf16 v[172:175], a[228:231], v[48:51], v[172:175]\n"
"ds_read_b64_tr_b16 v[226:227], v240 offset:63360\n"
"v_mfma_f32_16x16x32_bf16 v[172:175], v[228:231], v[56:59], v[172:175]\n"
"v_mfma_f32_16x16x32_bf16 v[180:183], a[228:231], v[80:83], v[180:183]\n"
"v_mfma_f32_16x16x32_bf16 v[180:183], v[228:231], v[88:91], v[180:183]\n"
"buffer_load_dwordx4 a[48:51], v249, s[56:59], s63 offen offset:0 sc1\n"
"v_mfma_f32_16x16x32_bf16 v[188:191], a[228:231], v[112:115], v[188:191]\n"
"v_mfma_f32_16x16x32_bf16 v[188:191], v[228:231], v[120:123], v[188:191]\n"
"v_mfma_f32_16x16x32_bf16 v[208:211], a[228:231], v[144:147], v[208:211]\n"
"v_mfma_f32_16x16x32_bf16 v[208:211], v[228:231], v[152:155], v[208:211]\n"
"v_mfma_f32_16x16x32_bf16 v[212:215], a[228:231], v[176:179], v[212:215]\n"
"v_mfma_f32_16x16x32_bf16 v[212:215], v[228:231], v[184:187], v[212:215]\n"
"ds_read_b64_tr_b16 a[228:229], v241 offset:50688\n"
"v_mfma_f32_16x16x32_bf16 a[24:27], a[232:235], v[16:19], a[24:27]\n"
"ds_read_b64_tr_b16 a[230:231], v241 offset:54912\n"
"v_mfma_f32_16x16x32_bf16 a[24:27], v[232:235], v[24:27], a[24:27]\n"
"ds_read_b64_tr_b16 v[228:229], v241 offset:59136\n"
"v_mfma_f32_16x16x32_bf16 a[56:59], a[232:235], v[48:51], a[56:59]\n"
"ds_read_b64_tr_b16 v[230:231], v241 offset:63360\n"
"v_mfma_f32_16x16x32_bf16 a[56:59], v[232:235], v[56:59], a[56:59]\n"
"buffer_load_dwordx4 a[176:179], v249, s[56:59], s64 offen offset:0 sc1\n"
"v_mfma_f32_16x16x32_bf16 a[88:91], a[232:235], v[80:83], a[88:91]\n"
"v_mfma_f32_16x16x32_bf16 a[88:91], v[232:235], v[88:91], a[88:91]\n"
"v_mfma_f32_16x16x32_bf16 a[120:123], a[232:235], v[112:115], a[120:123]\n"
"v_mfma_f32_16x16x32_bf16 a[120:123], v[232:235], v[120:123], a[120:123]\n"
"v_mfma_f32_16x16x32_bf16 a[152:155], a[232:235], v[144:147], a[152:155]\n"
"v_mfma_f32_16x16x32_bf16 a[152:155], v[232:235], v[152:155], a[152:155]\n"
"v_mfma_f32_16x16x32_bf16 a[184:187], a[232:235], v[176:179], a[184:187]\n"
"v_mfma_f32_16x16x32_bf16 a[184:187], v[232:235], v[184:187], a[184:187]\n"
"v_mfma_f32_16x16x32_bf16 a[204:207], a[232:235], v[200:203], a[204:207]\n"
"buffer_load_dwordx4 a[116:119], v249, s[56:59], s65 offen offset:0 sc1\n"
"v_mfma_f32_16x16x32_bf16 a[204:207], v[232:235], v[204:207], a[204:207]\n"
"ds_read_b64_tr_b16 a[232:233], v242 offset:50688\n"
"v_mfma_f32_16x16x32_bf16 a[28:31], a[236:239], v[16:19], a[28:31]\n"
"ds_read_b64_tr_b16 a[234:235], v242 offset:54912\n"
"v_mfma_f32_16x16x32_bf16 a[28:31], v[236:239], v[24:27], a[28:31]\n"
"ds_read_b64_tr_b16 v[232:233], v242 offset:59136\n"
"v_mfma_f32_16x16x32_bf16 a[60:63], a[236:239], v[48:51], a[60:63]\n"
"ds_read_b64_tr_b16 v[234:235], v242 offset:63360\n"
"v_mfma_f32_16x16x32_bf16 a[60:63], v[236:239], v[56:59], a[60:63]\n"
"v_mfma_f32_16x16x32_bf16 a[92:95], a[236:239], v[80:83], a[92:95]\n"
"v_mfma_f32_16x16x32_bf16 a[92:95], v[236:239], v[88:91], a[92:95]\n"
"v_mfma_f32_16x16x32_bf16 a[124:127], a[236:239], v[112:115], a[124:127]\n"
"v_mfma_f32_16x16x32_bf16 a[124:127], v[236:239], v[120:123], a[124:127]\n"
"v_mfma_f32_16x16x32_bf16 a[156:159], a[236:239], v[144:147], a[156:159]\n"
"buffer_load_dwordx4 a[200:203], v249, s[56:59], s70 offen offset:0 sc1\n"
"v_mfma_f32_16x16x32_bf16 a[156:159], v[236:239], v[152:155], a[156:159]\n"
"v_mfma_f32_16x16x32_bf16 a[188:191], a[236:239], v[176:179], a[188:191]\n"
"v_mfma_f32_16x16x32_bf16 a[188:191], v[236:239], v[184:187], a[188:191]\n"
"s_waitcnt vmcnt(14) lgkmcnt(15)\n"
"v_mfma_f32_16x16x32_bf16 v[20:23], a[208:211], v[4:7], v[20:23]\n"
"ds_read_b64_tr_b16 a[236:237], v243 offset:50688\n"
"v_mfma_f32_16x16x32_bf16 v[20:23], a[240:243], v[12:15], v[20:23]\n"
"ds_read_b64_tr_b16 a[238:239], v243 offset:54912\n"
"v_mfma_f32_16x16x32_bf16 v[28:31], a[208:211], a[64:67], v[28:31]\n"
"ds_read_b64_tr_b16 v[236:237], v243 offset:59136\n"
"v_mfma_f32_16x16x32_bf16 v[28:31], a[240:243], a[96:99], v[28:31]\n"
"ds_read_b64_tr_b16 v[238:239], v243 offset:63360\n"
"v_mfma_f32_16x16x32_bf16 v[36:39], a[208:211], a[4:7], v[36:39]\n"
"v_mfma_f32_16x16x32_bf16 v[36:39], a[240:243], a[36:39], v[36:39]\n"
"v_mfma_f32_16x16x32_bf16 v[44:47], a[208:211], a[132:135], v[44:47]\n"
"v_mfma_f32_16x16x32_bf16 v[44:47], a[240:243], a[164:167], v[44:47]\n"
"v_mfma_f32_16x16x32_bf16 v[52:55], a[208:211], a[80:83], v[52:55]\n"
"v_mfma_f32_16x16x32_bf16 v[52:55], a[240:243], a[112:115], v[52:55]\n"
"s_and_b32 s80, s76, 1\n"
"s_lshr_b32 s81, s76, 1\n"
"v_mfma_f32_16x16x32_bf16 v[60:63], a[208:211], a[20:23], v[60:63]\n"
"s_mul_i32 s82, s80, 13312\n"
"s_mul_i32 s83, s81, 67584\n"
"v_mfma_f32_16x16x32_bf16 v[60:63], a[240:243], a[52:55], v[60:63]\n"
"s_add_u32 s83, s83, s82\n"
"v_add_u32 v200, s83, v255\n"
"v_mfma_f32_16x16x32_bf16 v[216:219], a[208:211], a[148:151], v[216:219]\n"
"s_xor_b32 s83, s81, 1\n"
"s_mul_i32 s83, s83, 67584\n"
"v_mfma_f32_16x16x32_bf16 v[216:219], a[240:243], a[180:183], v[216:219]\n"
"ds_read_b64_tr_b16 a[208:209], v244 offset:33792\n"
"s_add_u32 s83, s83, s82\n"
"v_mfma_f32_16x16x32_bf16 v[68:71], a[212:215], v[4:7], v[68:71]\n"
"ds_read_b64_tr_b16 a[210:211], v244 offset:38016\n"
"v_add_u32 v201, s83, v255\n"
"v_mfma_f32_16x16x32_bf16 v[68:71], a[244:247], v[12:15], v[68:71]\n"
"ds_read_b64_tr_b16 a[240:241], v244 offset:42240\n"
"s_lshl_b32 s82, s77, 4\n"
"v_mfma_f32_16x16x32_bf16 v[76:79], a[212:215], a[64:67], v[76:79]\n"
"ds_read_b64_tr_b16 a[242:243], v244 offset:46464\n"
"s_add_u32 s82, s82, s78\n"
"v_mfma_f32_16x16x32_bf16 v[76:79], a[244:247], a[96:99], v[76:79]\n"
"s_lshl_b32 s82, s82, 2\n"
"s_add_u32 s82, s82, s76\n"
"v_mfma_f32_16x16x32_bf16 v[84:87], a[212:215], a[4:7], v[84:87]\n"
"s_xor_b32 s83, s82, 32\n"
"s_mul_i32 s46, s82, 13312\n"
"v_mfma_f32_16x16x32_bf16 v[84:87], a[244:247], a[36:39], v[84:87]\n"
"s_add_u32 s46, s46, 34603008\n"
"s_mul_i32 s47, s83, 13312\n"
"v_mfma_f32_16x16x32_bf16 v[92:95], a[212:215], a[132:135], v[92:95]\n"
"s_add_u32 s47, s47, 34603008\n"
"s_lshr_b32 s82, s82, 2\n"
"v_mfma_f32_16x16x32_bf16 v[92:95], a[244:247], a[164:167], v[92:95]\n"
"s_lshr_b32 s83, s83, 2\n"
"s_lshl_b32 s45, s82, 7\n"
"v_mfma_f32_16x16x32_bf16 v[100:103], a[212:215], a[80:83], v[100:103]\n"
"s_add_u32 s45, s45, 33816576\n"
"s_lshl_b32 s44, s83, 7\n"
"v_mfma_f32_16x16x32_bf16 v[100:103], a[244:247], a[112:115], v[100:103]\n"
"s_add_u32 s44, s44, 33816576\n"
"s_lshl_b32 s82, s78, 3\n"
"v_mfma_f32_16x16x32_bf16 v[108:111], a[212:215], a[20:23], v[108:111]\n"
"s_add_u32 s82, s82, 163328\n"
"v_mov_b32 v202, s82\n"
"v_mfma_f32_16x16x32_bf16 v[108:111], a[244:247], a[52:55], v[108:111]\n"
"ds_read_b64_tr_b16 a[212:213], v245 offset:33792\n"
"ds_read_b64 v[204:205], v202\n"
"v_mfma_f32_16x16x32_bf16 a[8:11], a[216:219], v[4:7], a[8:11]\n"
"ds_read_b64_tr_b16 a[214:215], v245 offset:38016\n"
"s_xor_b32 s82, s82, 64\n"
"v_mfma_f32_16x16x32_bf16 a[8:11], a[248:251], v[12:15], a[8:11]\n"
"ds_read_b64_tr_b16 a[244:245], v245 offset:42240\n"
"v_mov_b32 v203, s82\n"
"v_mfma_f32_16x16x32_bf16 a[40:43], a[216:219], a[64:67], a[40:43]\n"
"ds_read_b64_tr_b16 a[246:247], v245 offset:46464\n"
"ds_read_b64 v[206:207], v203\n"
"v_mfma_f32_16x16x32_bf16 a[40:43], a[248:251], a[96:99], a[40:43]\n"
"s_mul_i32 s82, s77, 208\n"
"s_mul_i32 s83, s80, 96\n"
"v_mfma_f32_16x16x32_bf16 a[72:75], a[216:219], a[4:7], a[72:75]\n"
"s_add_u32 s83, s83, s82\n"
"s_lshl_b32 s83, s83, 11\n"
"v_mfma_f32_16x16x32_bf16 a[72:75], a[248:251], a[36:39], a[72:75]\n"
"s_add_u32 s82, s82, 192\n"
"s_lshl_b32 s82, s82, 11\n"
"v_mfma_f32_16x16x32_bf16 a[104:107], a[216:219], a[132:135], a[104:107]\n"
"s_lshl_b32 s84, s72, 1\n"
"s_lshr_b32 s85, s78, 3\n"
"v_mfma_f32_16x16x32_bf16 a[104:107], a[248:251], a[164:167], a[104:107]\n"
"s_add_u32 s84, s84, s85\n"
"s_lshl_b32 s84, s84, 1\n"
"v_mfma_f32_16x16x32_bf16 a[136:139], a[216:219], a[80:83], a[136:139]\n"
"s_add_u32 s84, s84, s81\n"
"s_lshl_b32 s84, s84, 1\n"
"v_mfma_f32_16x16x32_bf16 a[136:139], a[248:251], a[112:115], a[136:139]\n"
"s_add_u32 s84, s84, s80\n"
"s_lshl_b32 s84, s84, 5\n"
"v_mfma_f32_16x16x32_bf16 a[168:171], a[216:219], a[20:23], a[168:171]\n"
"s_add_u32 s60, s83, s84\n"
"s_xor_b32 s61, s60, 32\n"
"v_mfma_f32_16x16x32_bf16 a[168:171], a[248:251], a[52:55], a[168:171]\n"
"s_add_u32 s62, s82, s84\n"
"s_waitcnt lgkmcnt(0)\n"
"v_lshrrev_b32 v202, 4, v255\n"
"v_mfma_f32_16x16x32_bf16 a[196:199], a[216:219], a[148:151], a[196:199]\n"
"v_and_b32 v203, 15, v202\n"
"v_lshrrev_b32 v202, 4, v202\n"
"v_mfma_f32_16x16x32_bf16 a[196:199], a[248:251], a[180:183], a[196:199]\n"
"ds_read_b64_tr_b16 a[216:217], v246 offset:33792\n"
"v_lshlrev_b32 v202, 3, v202\n"
"v_mfma_f32_16x16x32_bf16 a[12:15], a[220:223], v[4:7], a[12:15]\n"
"ds_read_b64_tr_b16 a[218:219], v246 offset:38016\n"
"v_lshl_add_u32 v202, v203, 11, v202\n"
"v_mfma_f32_16x16x32_bf16 a[12:15], a[252:255], v[12:15], a[12:15]\n"
"ds_read_b64_tr_b16 a[248:249], v246 offset:42240\n"
"v_or_b32 v203, s76, v255\n"
"v_mfma_f32_16x16x32_bf16 a[44:47], a[220:223], a[64:67], a[44:47]\n"
"ds_read_b64_tr_b16 a[250:251], v246 offset:46464\n"
"v_cmp_eq_u32 vcc, 0, v203\n"
"v_mfma_f32_16x16x32_bf16 a[44:47], a[252:255], a[96:99], a[44:47]\n"
"v_mov_b32 v203, 0x40000000\n"
"s_nop 0\n"
"v_mfma_f32_16x16x32_bf16 a[76:79], a[220:223], a[4:7], a[76:79]\n"
"v_cndmask_b32 v203, v203, v255, vcc\n"
"v_lshrrev_b32 v248, 1, v255\n"
"v_mfma_f32_16x16x32_bf16 a[76:79], a[252:255], a[36:39], a[76:79]\n"
"v_mfma_f32_16x16x32_bf16 a[108:111], a[220:223], a[132:135], a[108:111]\n"
"v_mfma_f32_16x16x32_bf16 a[108:111], a[252:255], a[164:167], a[108:111]\n"
"v_mfma_f32_16x16x32_bf16 a[140:143], a[220:223], a[80:83], a[140:143]\n"
"v_mfma_f32_16x16x32_bf16 a[140:143], a[252:255], a[112:115], a[140:143]\n"
"v_mfma_f32_16x16x32_bf16 a[172:175], a[220:223], a[20:23], a[172:175]\n"
"v_mfma_f32_16x16x32_bf16 a[172:175], a[252:255], a[52:55], a[172:175]\n"
"ds_read_b64_tr_b16 a[220:221], v247 offset:33792\n"
"s_waitcnt vmcnt(0)\n"
"v_mfma_f32_16x16x32_bf16 a[8:11], a[232:235], a[0:3], a[8:11]\n"
"ds_read_b64_tr_b16 a[222:223], v247 offset:38016\n"
"v_mfma_f32_16x16x32_bf16 a[8:11], v[232:235], a[32:35], a[8:11]\n"
"ds_read_b64_tr_b16 a[252:253], v247 offset:42240\n"
"v_mfma_f32_16x16x32_bf16 a[40:43], a[232:235], a[128:131], a[40:43]\n"
"ds_read_b64_tr_b16 a[254:255], v247 offset:46464\n"
"v_mfma_f32_16x16x32_bf16 a[40:43], v[232:235], a[160:163], a[40:43]\n"
"v_mfma_f32_16x16x32_bf16 a[72:75], a[232:235], a[68:71], a[72:75]\n"
"v_mfma_f32_16x16x32_bf16 a[72:75], v[232:235], a[100:103], a[72:75]\n"
"ds_write_b128 v200, a[8:11] offset:0\n"
"v_mfma_f32_16x16x32_bf16 a[104:107], a[232:235], a[16:19], a[104:107]\n"
"v_mfma_f32_16x16x32_bf16 a[104:107], v[232:235], a[48:51], a[104:107]\n"
"ds_write_b128 v200, a[40:43] offset:2048\n"
"v_mfma_f32_16x16x32_bf16 a[136:139], a[232:235], a[144:147], a[136:139]\n"
"v_mfma_f32_16x16x32_bf16 a[136:139], v[232:235], a[176:179], a[136:139]\n"
"ds_write_b128 v200, a[72:75] offset:4096\n"
"v_mfma_f32_16x16x32_bf16 a[168:171], a[232:235], a[84:87], a[168:171]\n"
"v_mfma_f32_16x16x32_bf16 a[168:171], v[232:235], a[116:119], a[168:171]\n"
"ds_write_b128 v200, a[104:107] offset:6144\n"
"v_mfma_f32_16x16x32_bf16 a[196:199], a[232:235], a[192:195], a[196:199]\n"
"v_mfma_f32_16x16x32_bf16 a[196:199], v[232:235], a[200:203], a[196:199]\n"
"ds_read_b64_tr_b16 a[232:233], v246 offset:50688\n"
"ds_write_b128 v200, a[136:139] offset:8192\n"
"v_mfma_f32_16x16x32_bf16 a[12:15], a[236:239], a[0:3], a[12:15]\n"
"ds_read_b64_tr_b16 a[234:235], v246 offset:54912\n"
"v_mfma_f32_16x16x32_bf16 a[12:15], v[236:239], a[32:35], a[12:15]\n"
"ds_read_b64_tr_b16 v[232:233], v246 offset:59136\n"
"ds_write_b128 v200, a[168:171] offset:10240\n"
"v_mfma_f32_16x16x32_bf16 a[44:47], a[236:239], a[128:131], a[44:47]\n"
"ds_read_b64_tr_b16 v[234:235], v246 offset:63360\n"
"v_mfma_f32_16x16x32_bf16 a[44:47], v[236:239], a[160:163], a[44:47]\n"
"ds_write_b128 v200, a[196:199] offset:12288\n"
"v_mfma_f32_16x16x32_bf16 a[76:79], a[236:239], a[68:71], a[76:79]\n"
"v_mfma_f32_16x16x32_bf16 a[76:79], v[236:239], a[100:103], a[76:79]\n"
"ds_write_b128 v200, a[12:15] offset:1024\n"
"v_mfma_f32_16x16x32_bf16 a[108:111], a[236:239], a[16:19], a[108:111]\n"
"v_mfma_f32_16x16x32_bf16 a[108:111], v[236:239], a[48:51], a[108:111]\n"
"ds_write_b128 v200, a[44:47] offset:3072\n"
"v_mfma_f32_16x16x32_bf16 a[140:143], a[236:239], a[144:147], a[140:143]\n"
"v_mfma_f32_16x16x32_bf16 a[140:143], v[236:239], a[176:179], a[140:143]\n"
"ds_write_b128 v200, a[76:79] offset:5120\n"
"v_mfma_f32_16x16x32_bf16 a[172:175], a[236:239], a[84:87], a[172:175]\n"
"v_mfma_f32_16x16x32_bf16 a[172:175], v[236:239], a[116:119], a[172:175]\n"
"ds_read_b64_tr_b16 a[236:237], v247 offset:50688\n"
"ds_write_b128 v200, a[108:111] offset:7168\n"
"v_mfma_f32_16x16x32_bf16 v[20:23], a[224:227], a[0:3], v[20:23]\n"
"ds_read_b64_tr_b16 a[238:239], v247 offset:54912\n"
"v_mfma_f32_16x16x32_bf16 v[20:23], v[224:227], a[32:35], v[20:23]\n"
"ds_read_b64_tr_b16 v[236:237], v247 offset:59136\n"
"ds_write_b128 v200, a[140:143] offset:9216\n"
"v_mfma_f32_16x16x32_bf16 v[28:31], a[224:227], a[128:131], v[28:31]\n"
"ds_read_b64_tr_b16 v[238:239], v247 offset:63360\n"
"v_mfma_f32_16x16x32_bf16 v[28:31], v[224:227], a[160:163], v[28:31]\n"
"ds_write_b128 v200, a[172:175] offset:11264\n"
"v_mfma_f32_16x16x32_bf16 v[36:39], a[224:227], a[68:71], v[36:39]\n"
"v_mfma_f32_16x16x32_bf16 v[36:39], v[224:227], a[100:103], v[36:39]\n"
"v_mfma_f32_16x16x32_bf16 v[44:47], a[224:227], a[16:19], v[44:47]\n"
"v_mfma_f32_16x16x32_bf16 v[44:47], v[224:227], a[48:51], v[44:47]\n"
"v_mfma_f32_16x16x32_bf16 v[52:55], a[224:227], a[144:147], v[52:55]\n"
"v_mfma_f32_16x16x32_bf16 v[52:55], v[224:227], a[176:179], v[52:55]\n"
"v_mfma_f32_16x16x32_bf16 v[60:63], a[224:227], a[84:87], v[60:63]\n"
"v_mfma_f32_16x16x32_bf16 v[60:63], v[224:227], a[116:119], v[60:63]\n"
"v_mfma_f32_16x16x32_bf16 v[216:219], a[224:227], a[192:195], v[216:219]\n"
"v_mfma_f32_16x16x32_bf16 v[216:219], v[224:227], a[200:203], v[216:219]\n"
"ds_read_b64_tr_b16 a[224:225], v244 offset:50688\n"
"v_mfma_f32_16x16x32_bf16 v[68:71], a[228:231], a[0:3], v[68:71]\n"
"ds_read_b64_tr_b16 a[226:227], v244 offset:54912\n"
"v_mfma_f32_16x16x32_bf16 v[68:71], v[228:231], a[32:35], v[68:71]\n"
"ds_read_b64_tr_b16 v[224:225], v244 offset:59136\n"
"v_mfma_f32_16x16x32_bf16 v[76:79], a[228:231], a[128:131], v[76:79]\n"
"ds_read_b64_tr_b16 v[226:227], v244 offset:63360\n"
"v_mfma_f32_16x16x32_bf16 v[76:79], v[228:231], a[160:163], v[76:79]\n"
"v_mfma_f32_16x16x32_bf16 v[84:87], a[228:231], a[68:71], v[84:87]\n"
"v_mfma_f32_16x16x32_bf16 v[84:87], v[228:231], a[100:103], v[84:87]\n"
"v_mfma_f32_16x16x32_bf16 v[92:95], a[228:231], a[16:19], v[92:95]\n"
"v_mfma_f32_16x16x32_bf16 v[92:95], v[228:231], a[48:51], v[92:95]\n"
"v_mfma_f32_16x16x32_bf16 v[100:103], a[228:231], a[144:147], v[100:103]\n"
"v_mfma_f32_16x16x32_bf16 v[100:103], v[228:231], a[176:179], v[100:103]\n"
"v_mfma_f32_16x16x32_bf16 v[108:111], a[228:231], a[84:87], v[108:111]\n"
"v_mfma_f32_16x16x32_bf16 v[108:111], v[228:231], a[116:119], v[108:111]\n"
"ds_read_b64_tr_b16 a[228:229], v245 offset:50688\n"
"v_mfma_f32_16x16x32_bf16 v[116:119], a[208:211], v[4:7], v[116:119]\n"
"ds_read_b64_tr_b16 a[230:231], v245 offset:54912\n"
"v_mfma_f32_16x16x32_bf16 v[116:119], a[240:243], v[12:15], v[116:119]\n"
"ds_read_b64_tr_b16 v[228:229], v245 offset:59136\n"
"v_mfma_f32_16x16x32_bf16 v[124:127], a[208:211], a[64:67], v[124:127]\n"
"ds_read_b64_tr_b16 v[230:231], v245 offset:63360\n"
"s_waitcnt lgkmcnt(0)\n"
"s_barrier\n"
"v_mfma_f32_16x16x32_bf16 v[124:127], a[240:243], a[96:99], v[124:127]\n"
"ds_read_b128 v[0:3], v201 offset:0\n"
"s_waitcnt lgkmcnt(0)\n"
"v_add_f32 v20, v20, v0\n"
"v_add_f32 v21, v21, v1\n"
"v_add_f32 v22, v22, v2\n"
"v_mfma_f32_16x16x32_bf16 v[132:135], a[208:211], a[4:7], v[132:135]\n"
"ds_read_b128 v[8:11], v201 offset:1024\n"
"v_add_f32 v23, v23, v3\n"
"v_cvt_pk_bf16_f32 v20, v20, v21\n"
"v_cvt_pk_bf16_f32 v21, v22, v23\n"
"s_waitcnt lgkmcnt(0)\n"
"v_mfma_f32_16x16x32_bf16 v[132:135], a[240:243], a[36:39], v[132:135]\n"
"ds_read_b128 v[16:19], v201 offset:2048\n"
"v_add_f32 v68, v68, v8\n"
"v_add_f32 v69, v69, v9\n"
"v_add_f32 v70, v70, v10\n"
"v_mfma_f32_16x16x32_bf16 v[140:143], a[208:211], a[132:135], v[140:143]\n"
"ds_read_b128 v[24:27], v201 offset:3072\n"
"v_add_f32 v71, v71, v11\n"
"v_cvt_pk_bf16_f32 v22, v68, v69\n"
"v_cvt_pk_bf16_f32 v23, v70, v71\n"
"v_mfma_f32_16x16x32_bf16 v[140:143], a[240:243], a[164:167], v[140:143]\n"
"ds_read_b128 v[32:35], v201 offset:4096\n"
"buffer_store_dwordx4 v[20:23], v255, s[56:59], s46 offen offset:0 sc1\n"
"v_mfma_f32_16x16x32_bf16 v[148:151], a[208:211], a[80:83], v[148:151]\n"
"ds_read_b128 v[40:43], v201 offset:5120\n"
"s_waitcnt lgkmcnt(3)\n"
"v_add_f32 v28, v28, v16\n"
"v_add_f32 v29, v29, v17\n"
"v_add_f32 v30, v30, v18\n"
"v_mfma_f32_16x16x32_bf16 v[148:151], a[240:243], a[112:115], v[148:151]\n"
"ds_read_b128 v[48:51], v201 offset:6144\n"
"v_add_f32 v31, v31, v19\n"
"v_cvt_pk_bf16_f32 v28, v28, v29\n"
"v_cvt_pk_bf16_f32 v29, v30, v31\n"
"s_waitcnt lgkmcnt(3)\n"
"v_mfma_f32_16x16x32_bf16 v[156:159], a[208:211], a[20:23], v[156:159]\n"
"ds_read_b128 v[56:59], v201 offset:7168\n"
"v_add_f32 v76, v76, v24\n"
"v_add_f32 v77, v77, v25\n"
"v_add_f32 v78, v78, v26\n"
"v_mfma_f32_16x16x32_bf16 v[156:159], a[240:243], a[52:55], v[156:159]\n"
"ds_read_b128 v[64:67], v201 offset:8192\n"
"v_add_f32 v79, v79, v27\n"
"v_cvt_pk_bf16_f32 v30, v76, v77\n"
"v_cvt_pk_bf16_f32 v31, v78, v79\n"
"v_mfma_f32_16x16x32_bf16 v[220:223], a[208:211], a[148:151], v[220:223]\n"
"ds_read_b128 v[72:75], v201 offset:9216\n"
"buffer_store_dwordx4 v[28:31], v255, s[56:59], s46 offen offset:1024 sc1\n"
"v_mfma_f32_16x16x32_bf16 v[220:223], a[240:243], a[180:183], v[220:223]\n"
"ds_read_b128 v[80:83], v201 offset:10240\n"
"s_waitcnt lgkmcnt(6)\n"
"v_add_f32 v36, v36, v32\n"
"v_add_f32 v37, v37, v33\n"
"v_add_f32 v38, v38, v34\n"
"v_mfma_f32_16x16x32_bf16 v[164:167], a[212:215], v[4:7], v[164:167]\n"
"ds_read_b128 v[88:91], v201 offset:11264\n"
"v_add_f32 v39, v39, v35\n"
"v_cvt_pk_bf16_f32 v36, v36, v37\n"
"v_cvt_pk_bf16_f32 v37, v38, v39\n"
"s_waitcnt lgkmcnt(6)\n"
"v_mfma_f32_16x16x32_bf16 v[164:167], a[244:247], v[12:15], v[164:167]\n"
"ds_read_b128 v[96:99], v201 offset:12288\n"
"v_add_f32 v84, v84, v40\n"
"v_add_f32 v85, v85, v41\n"
"v_add_f32 v86, v86, v42\n"
"v_mfma_f32_16x16x32_bf16 v[172:175], a[212:215], a[64:67], v[172:175]\n"
"v_add_f32 v87, v87, v43\n"
"v_cvt_pk_bf16_f32 v38, v84, v85\n"
"v_cvt_pk_bf16_f32 v39, v86, v87\n"
"v_mfma_f32_16x16x32_bf16 v[172:175], a[244:247], a[96:99], v[172:175]\n"
"buffer_store_dwordx4 v[36:39], v255, s[56:59], s46 offen offset:2048 sc1\n"
"v_mfma_f32_16x16x32_bf16 v[180:183], a[212:215], a[4:7], v[180:183]\n"
"s_waitcnt lgkmcnt(6)\n"
"v_add_f32 v44, v44, v48\n"
"v_add_f32 v45, v45, v49\n"
"v_add_f32 v46, v46, v50\n"
"v_add_f32 v47, v47, v51\n"
"v_mfma_f32_16x16x32_bf16 v[180:183], a[244:247], a[36:39], v[180:183]\n"
"v_cvt_pk_bf16_f32 v44, v44, v45\n"
"v_cvt_pk_bf16_f32 v45, v46, v47\n"
"s_waitcnt lgkmcnt(5)\n"
"v_add_f32 v92, v92, v56\n"
"v_add_f32 v93, v93, v57\n"
"v_mfma_f32_16x16x32_bf16 v[188:191], a[212:215], a[132:135], v[188:191]\n"
"v_add_f32 v94, v94, v58\n"
"v_add_f32 v95, v95, v59\n"
"v_cvt_pk_bf16_f32 v46, v92, v93\n"
"v_cvt_pk_bf16_f32 v47, v94, v95\n"
"v_mfma_f32_16x16x32_bf16 v[188:191], a[244:247], a[164:167], v[188:191]\n"
"buffer_store_dwordx4 v[44:47], v255, s[56:59], s46 offen offset:3072 sc1\n"
"v_mfma_f32_16x16x32_bf16 v[208:211], a[212:215], a[80:83], v[208:211]\n"
"s_add_u32 s46, s46, 4096\n"
"s_waitcnt lgkmcnt(4)\n"
"v_add_f32 v52, v52, v64\n"
"v_add_f32 v53, v53, v65\n"
"v_add_f32 v54, v54, v66\n"
"v_mfma_f32_16x16x32_bf16 v[208:211], a[244:247], a[112:115], v[208:211]\n"
"v_add_f32 v55, v55, v67\n"
"v_cvt_pk_bf16_f32 v52, v52, v53\n"
"v_cvt_pk_bf16_f32 v53, v54, v55\n"
"s_waitcnt lgkmcnt(3)\n"
"v_add_f32 v100, v100, v72\n"
"v_mfma_f32_16x16x32_bf16 v[212:215], a[212:215], a[20:23], v[212:215]\n"
"v_add_f32 v101, v101, v73\n"
"v_add_f32 v102, v102, v74\n"
"v_add_f32 v103, v103, v75\n"
"v_cvt_pk_bf16_f32 v54, v100, v101\n"
"v_mfma_f32_16x16x32_bf16 v[212:215], a[244:247], a[52:55], v[212:215]\n"
"v_cvt_pk_bf16_f32 v55, v102, v103\n"
"buffer_store_dwordx4 v[52:55], v255, s[56:59], s46 offen offset:0 sc1\n"
"v_mfma_f32_16x16x32_bf16 a[24:27], a[216:219], v[4:7], a[24:27]\n"
"s_waitcnt lgkmcnt(2)\n"
"v_add_f32 v60, v60, v80\n"
"v_add_f32 v61, v61, v81\n"
"v_add_f32 v62, v62, v82\n"
"v_add_f32 v63, v63, v83\n"
"v_mfma_f32_16x16x32_bf16 a[24:27], a[248:251], v[12:15], a[24:27]\n"
"v_cvt_pk_bf16_f32 v60, v60, v61\n"
"v_cvt_pk_bf16_f32 v61, v62, v63\n"
"s_waitcnt lgkmcnt(1)\n"
"v_add_f32 v108, v108, v88\n"
"v_add_f32 v109, v109, v89\n"
"v_mfma_f32_16x16x32_bf16 a[56:59], a[216:219], a[64:67], a[56:59]\n"
"v_add_f32 v110, v110, v90\n"
"v_add_f32 v111, v111, v91\n"
"v_cvt_pk_bf16_f32 v62, v108, v109\n"
"v_cvt_pk_bf16_f32 v63, v110, v111\n"
"v_mfma_f32_16x16x32_bf16 a[56:59], a[248:251], a[96:99], a[56:59]\n"
"buffer_store_dwordx4 v[60:63], v255, s[56:59], s46 offen offset:1024 sc1\n"
"v_mfma_f32_16x16x32_bf16 a[88:91], a[216:219], a[4:7], a[88:91]\n"
"s_waitcnt lgkmcnt(0)\n"
"v_add_f32 v216, v216, v96\n"
"v_add_f32 v217, v217, v97\n"
"v_add_f32 v218, v218, v98\n"
"v_add_f32 v219, v219, v99\n"
"v_mfma_f32_16x16x32_bf16 a[88:91], a[248:251], a[36:39], a[88:91]\n"
"v_cvt_pk_bf16_f32 v216, v216, v217\n"
"v_cvt_pk_bf16_f32 v217, v218, v219\n"
"buffer_store_dwordx2 v[216:217], v248, s[56:59], s46 offen offset:2048 sc1\n"
"v_mfma_f32_16x16x32_bf16 a[120:123], a[216:219], a[132:135], a[120:123]\n"
"v_mfma_f32_16x16x32_bf16 a[120:123], a[248:251], a[164:167], a[120:123]\n"
"v_mfma_f32_16x16x32_bf16 a[152:155], a[216:219], a[80:83], a[152:155]\n"
"v_mfma_f32_16x16x32_bf16 a[152:155], a[248:251], a[112:115], a[152:155]\n"
"v_mfma_f32_16x16x32_bf16 a[184:187], a[216:219], a[20:23], a[184:187]\n"
"v_mfma_f32_16x16x32_bf16 a[184:187], a[248:251], a[52:55], a[184:187]\n"
"v_mfma_f32_16x16x32_bf16 a[204:207], a[216:219], a[148:151], a[204:207]\n"
"v_mfma_f32_16x16x32_bf16 a[204:207], a[248:251], a[180:183], a[204:207]\n"
"v_mfma_f32_16x16x32_bf16 a[28:31], a[220:223], v[4:7], a[28:31]\n"
"v_mfma_f32_16x16x32_bf16 a[28:31], a[252:255], v[12:15], a[28:31]\n"
"v_mfma_f32_16x16x32_bf16 a[60:63], a[220:223], a[64:67], a[60:63]\n"
"v_mfma_f32_16x16x32_bf16 a[60:63], a[252:255], a[96:99], a[60:63]\n"
"v_mfma_f32_16x16x32_bf16 a[92:95], a[220:223], a[4:7], a[92:95]\n"
"v_mfma_f32_16x16x32_bf16 a[92:95], a[252:255], a[36:39], a[92:95]\n"
"v_mfma_f32_16x16x32_bf16 a[124:127], a[220:223], a[132:135], a[124:127]\n"
"v_mfma_f32_16x16x32_bf16 a[124:127], a[252:255], a[164:167], a[124:127]\n"
"v_mfma_f32_16x16x32_bf16 a[156:159], a[220:223], a[80:83], a[156:159]\n"
"v_mfma_f32_16x16x32_bf16 a[156:159], a[252:255], a[112:115], a[156:159]\n"
"v_mfma_f32_16x16x32_bf16 a[188:191], a[220:223], a[20:23], a[188:191]\n"
"v_mfma_f32_16x16x32_bf16 a[188:191], a[252:255], a[52:55], a[188:191]\n"
"v_mfma_f32_16x16x32_bf16 a[24:27], a[232:235], a[0:3], a[24:27]\n"
"v_mfma_f32_16x16x32_bf16 a[24:27], v[232:235], a[32:35], a[24:27]\n"
"v_mfma_f32_16x16x32_bf16 a[56:59], a[232:235], a[128:131], a[56:59]\n"
"v_mfma_f32_16x16x32_bf16 a[56:59], v[232:235], a[160:163], a[56:59]\n"
"v_mfma_f32_16x16x32_bf16 a[88:91], a[232:235], a[68:71], a[88:91]\n"
"buffer_load_dwordx2 v[252:253], off, s[56:59], s44 sc1\n"
"v_mfma_f32_16x16x32_bf16 a[88:91], v[232:235], a[100:103], a[88:91]\n"
"ds_write_b128 v201, a[24:27] offset:0\n"
"v_mfma_f32_16x16x32_bf16 a[120:123], a[232:235], a[16:19], a[120:123]\n"
"v_mfma_f32_16x16x32_bf16 a[120:123], v[232:235], a[48:51], a[120:123]\n"
"ds_write_b128 v201, a[56:59] offset:2048\n"
"v_mfma_f32_16x16x32_bf16 a[152:155], a[232:235], a[144:147], a[152:155]\n"
"v_mfma_f32_16x16x32_bf16 a[152:155], v[232:235], a[176:179], a[152:155]\n"
"ds_write_b128 v201, a[88:91] offset:4096\n"
"v_mfma_f32_16x16x32_bf16 a[184:187], a[232:235], a[84:87], a[184:187]\n"
"v_mfma_f32_16x16x32_bf16 a[184:187], v[232:235], a[116:119], a[184:187]\n"
"ds_write_b128 v201, a[120:123] offset:6144\n"
"v_mfma_f32_16x16x32_bf16 a[204:207], a[232:235], a[192:195], a[204:207]\n"
"v_mfma_f32_16x16x32_bf16 a[204:207], v[232:235], a[200:203], a[204:207]\n"
"ds_write_b128 v201, a[152:155] offset:8192\n"
"v_mfma_f32_16x16x32_bf16 a[28:31], a[236:239], a[0:3], a[28:31]\n"
"v_mfma_f32_16x16x32_bf16 a[28:31], v[236:239], a[32:35], a[28:31]\n"
"ds_write_b128 v201, a[184:187] offset:10240\n"
"v_mfma_f32_16x16x32_bf16 a[60:63], a[236:239], a[128:131], a[60:63]\n"
"v_mfma_f32_16x16x32_bf16 a[60:63], v[236:239], a[160:163], a[60:63]\n"
"ds_write_b128 v201, a[204:207] offset:12288\n"
"v_mfma_f32_16x16x32_bf16 a[92:95], a[236:239], a[68:71], a[92:95]\n"
"v_mfma_f32_16x16x32_bf16 a[92:95], v[236:239], a[100:103], a[92:95]\n"
"ds_write_b128 v201, a[28:31] offset:1024\n"
"v_mfma_f32_16x16x32_bf16 a[124:127], a[236:239], a[16:19], a[124:127]\n"
"s_waitcnt vmcnt(1)\n"
"s_barrier\n"
"buffer_store_dwordx2 v[204:205], v203, s[56:59], s45 offen sc1\n"
"v_mfma_f32_16x16x32_bf16 a[124:127], v[236:239], a[48:51], a[124:127]\n"
"ds_write_b128 v201, a[60:63] offset:3072\n"
"v_mfma_f32_16x16x32_bf16 a[156:159], a[236:239], a[144:147], a[156:159]\n"
"v_mfma_f32_16x16x32_bf16 a[156:159], v[236:239], a[176:179], a[156:159]\n"
"ds_write_b128 v201, a[92:95] offset:5120\n"
"v_mfma_f32_16x16x32_bf16 a[188:191], a[236:239], a[84:87], a[188:191]\n"
"v_mfma_f32_16x16x32_bf16 a[188:191], v[236:239], a[116:119], a[188:191]\n"
"ds_write_b128 v201, a[124:127] offset:7168\n"
"v_mfma_f32_16x16x32_bf16 v[116:119], a[224:227], a[0:3], v[116:119]\n"
"v_mfma_f32_16x16x32_bf16 v[116:119], v[224:227], a[32:35], v[116:119]\n"
"ds_write_b128 v201, a[156:159] offset:9216\n"
"v_mfma_f32_16x16x32_bf16 v[124:127], a[224:227], a[128:131], v[124:127]\n"
"v_mfma_f32_16x16x32_bf16 v[124:127], v[224:227], a[160:163], v[124:127]\n"
"ds_write_b128 v201, a[188:191] offset:11264\n"
"v_mfma_f32_16x16x32_bf16 v[132:135], a[224:227], a[68:71], v[132:135]\n"
"v_mfma_f32_16x16x32_bf16 v[132:135], v[224:227], a[100:103], v[132:135]\n"
"v_mfma_f32_16x16x32_bf16 v[140:143], a[224:227], a[16:19], v[140:143]\n"
"v_mfma_f32_16x16x32_bf16 v[140:143], v[224:227], a[48:51], v[140:143]\n"
"v_mfma_f32_16x16x32_bf16 v[148:151], a[224:227], a[144:147], v[148:151]\n"
"v_mfma_f32_16x16x32_bf16 v[148:151], v[224:227], a[176:179], v[148:151]\n"
"v_mfma_f32_16x16x32_bf16 v[156:159], a[224:227], a[84:87], v[156:159]\n"
"v_mfma_f32_16x16x32_bf16 v[156:159], v[224:227], a[116:119], v[156:159]\n"
"v_mfma_f32_16x16x32_bf16 v[220:223], a[224:227], a[192:195], v[220:223]\n"
"v_mfma_f32_16x16x32_bf16 v[220:223], v[224:227], a[200:203], v[220:223]\n"
"v_mfma_f32_16x16x32_bf16 v[164:167], a[228:231], a[0:3], v[164:167]\n"
"s_waitcnt vmcnt(1)\n"
"v_cmp_ne_u64 vcc, v[252:253], v[206:207]\n"
"s_nop 1\n"
"s_cmp_eq_u64 vcc, 0\n"
"s_cbranch_scc0 Lpg%=\n"
"Lpgr%=:\n"
"v_mfma_f32_16x16x32_bf16 v[164:167], v[228:231], a[32:35], v[164:167]\n"
"buffer_load_dwordx4 v[104:107], v255, s[56:59], s47 offen offset:0 sc1\n"
"v_mfma_f32_16x16x32_bf16 v[172:175], a[228:231], a[128:131], v[172:175]\n"
"buffer_load_dwordx4 v[112:115], v255, s[56:59], s47 offen offset:1024 sc1\n"
"v_mfma_f32_16x16x32_bf16 v[172:175], v[228:231], a[160:163], v[172:175]\n"
"buffer_load_dwordx4 v[120:123], v255, s[56:59], s47 offen offset:2048 sc1\n"
"v_mfma_f32_16x16x32_bf16 v[180:183], a[228:231], a[68:71], v[180:183]\n"
"buffer_load_dwordx4 v[128:131], v255, s[56:59], s47 offen offset:3072 sc1\n"
"v_mfma_f32_16x16x32_bf16 v[180:183], v[228:231], a[100:103], v[180:183]\n"
"s_add_u32 s47, s47, 4096\n"
"buffer_load_dwordx4 v[136:139], v255, s[56:59], s47 offen offset:0 sc1\n"
"v_mfma_f32_16x16x32_bf16 v[188:191], a[228:231], a[16:19], v[188:191]\n"
"buffer_load_dwordx4 v[144:147], v255, s[56:59], s47 offen offset:1024 sc1\n"
"v_mfma_f32_16x16x32_bf16 v[188:191], v[228:231], a[48:51], v[188:191]\n"
"buffer_load_dwordx2 v[152:153], v248, s[56:59], s47 offen offset:2048 sc1\n"
"v_mfma_f32_16x16x32_bf16 v[208:211], a[228:231], a[144:147], v[208:211]\n"
"v_mfma_f32_16x16x32_bf16 v[208:211], v[228:231], a[176:179], v[208:211]\n"
"v_mfma_f32_16x16x32_bf16 v[212:215], a[228:231], a[84:87], v[212:215]\n"
"v_mfma_f32_16x16x32_bf16 v[212:215], v[228:231], a[116:119], v[212:215]\n"
"s_waitcnt lgkmcnt(0)\n"
"s_barrier\n"
"ds_read_b128 v[0:3], v200 offset:0\n"
"ds_read_b128 v[8:11], v200 offset:1024\n"
"ds_read_b128 v[16:19], v200 offset:2048\n"
"ds_read_b128 v[24:27], v200 offset:3072\n"
"ds_read_b128 v[32:35], v200 offset:4096\n"
"ds_read_b128 v[40:43], v200 offset:5120\n"
"ds_read_b128 v[48:51], v200 offset:6144\n"
"ds_read_b128 v[56:59], v200 offset:7168\n"
"ds_read_b128 v[64:67], v200 offset:8192\n"
"ds_read_b128 v[72:75], v200 offset:9216\n"
"ds_read_b128 v[80:83], v200 offset:10240\n"
"ds_read_b128 v[88:91], v200 offset:11264\n"
"ds_read_b128 v[96:99], v200 offset:12288\n"
"s_waitcnt lgkmcnt(12)\n"
"v_pk_add_f32 v[116:117], v[116:117], v[0:1]\n"
"v_pk_add_f32 v[118:119], v[118:119], v[2:3]\n"
"s_waitcnt lgkmcnt(11)\n"
"v_pk_add_f32 v[164:165], v[164:165], v[8:9]\n"
"v_pk_add_f32 v[166:167], v[166:167], v[10:11]\n"
"s_waitcnt lgkmcnt(10)\n"
"v_pk_add_f32 v[124:125], v[124:125], v[16:17]\n"
"v_pk_add_f32 v[126:127], v[126:127], v[18:19]\n"
"s_waitcnt lgkmcnt(9)\n"
"v_pk_add_f32 v[172:173], v[172:173], v[24:25]\n"
"v_pk_add_f32 v[174:175], v[174:175], v[26:27]\n"
"s_waitcnt lgkmcnt(8)\n"
"v_pk_add_f32 v[132:133], v[132:133], v[32:33]\n"
"v_pk_add_f32 v[134:135], v[134:135], v[34:35]\n"
"s_waitcnt lgkmcnt(7)\n"
"v_pk_add_f32 v[180:181], v[180:181], v[40:41]\n"
"v_pk_add_f32 v[182:183], v[182:183], v[42:43]\n"
"s_waitcnt lgkmcnt(6)\n"
"v_pk_add_f32 v[140:141], v[140:141], v[48:49]\n"
"v_pk_add_f32 v[142:143], v[142:143], v[50:51]\n"
"s_waitcnt lgkmcnt(5)\n"
"v_pk_add_f32 v[188:189], v[188:189], v[56:57]\n"
"v_pk_add_f32 v[190:191], v[190:191], v[58:59]\n"
"s_waitcnt lgkmcnt(4)\n"
"v_pk_add_f32 v[148:149], v[148:149], v[64:65]\n"
"v_pk_add_f32 v[150:151], v[150:151], v[66:67]\n"
"s_waitcnt lgkmcnt(3)\n"
"v_pk_add_f32 v[208:209], v[208:209], v[72:73]\n"
"v_pk_add_f32 v[210:211], v[210:211], v[74:75]\n"
"s_waitcnt lgkmcnt(2)\n"
"v_pk_add_f32 v[156:157], v[156:157], v[80:81]\n"
"v_pk_add_f32 v[158:159], v[158:159], v[82:83]\n"
"s_waitcnt lgkmcnt(1)\n"
"v_pk_add_f32 v[212:213], v[212:213], v[88:89]\n"
"v_pk_add_f32 v[214:215], v[214:215], v[90:91]\n"
"s_waitcnt lgkmcnt(0)\n"
"v_pk_add_f32 v[220:221], v[220:221], v[96:97]\n"
"v_pk_add_f32 v[222:223], v[222:223], v[98:99]\n"
"v_lshrrev_b32 v200, 8, v255\n"
"s_lshl_b32 s82, s80, 1\n"
"v_xor_b32 v200, s82, v200\n"
"v_lshlrev_b32 v200, 4, v200\n"
"v_and_b32 v201, 0xfffff800, v202\n"
"v_add_u32 v200, v200, v201\n"
"s_and_b32 s60, s60, 0xffffffdf\n"
"s_waitcnt vmcnt(6)\n"
"v_lshlrev_b32 v64, 16, v104\n"
"v_and_b32 v65, 0xffff0000, v104\n"
"v_lshlrev_b32 v66, 16, v105\n"
"v_and_b32 v67, 0xffff0000, v105\n"
"v_pk_add_f32 v[116:117], v[116:117], v[64:65]\n"
"v_pk_add_f32 v[118:119], v[118:119], v[66:67]\n"
"v_cvt_pk_bf16_f32 v116, v116, v117\n"
"v_cvt_pk_bf16_f32 v117, v118, v119\n"
"v_lshlrev_b32 v72, 16, v106\n"
"v_and_b32 v73, 0xffff0000, v106\n"
"v_lshlrev_b32 v74, 16, v107\n"
"v_and_b32 v75, 0xffff0000, v107\n"
"v_pk_add_f32 v[164:165], v[164:165], v[72:73]\n"
"v_pk_add_f32 v[166:167], v[166:167], v[74:75]\n"
"v_cvt_pk_bf16_f32 v118, v164, v165\n"
"v_cvt_pk_bf16_f32 v119, v166, v167\n"
"s_nop 1\n"
"v_permlane32_swap_b32 v116, v118\n"
"v_permlane32_swap_b32 v117, v119\n"
"s_nop 0\n"
"v_permlane16_swap_b32 v116, v118\n"
"v_permlane16_swap_b32 v117, v119\n"
"buffer_store_dwordx4 v[116:119], v200, s[40:43], s60 offen sc0 sc1\n"
"s_add_u32 s60, s60, 32768\n"
"s_waitcnt vmcnt(6)\n"
"v_lshlrev_b32 v80, 16, v112\n"
"v_and_b32 v81, 0xffff0000, v112\n"
"v_lshlrev_b32 v82, 16, v113\n"
"v_and_b32 v83, 0xffff0000, v113\n"
"v_pk_add_f32 v[124:125], v[124:125], v[80:81]\n"
"v_pk_add_f32 v[126:127], v[126:127], v[82:83]\n"
"v_cvt_pk_bf16_f32 v124, v124, v125\n"
"v_cvt_pk_bf16_f32 v125, v126, v127\n"
"v_lshlrev_b32 v88, 16, v114\n"
"v_and_b32 v89, 0xffff0000, v114\n"
"v_lshlrev_b32 v90, 16, v115\n"
"v_and_b32 v91, 0xffff0000, v115\n"
"v_pk_add_f32 v[172:173], v[172:173], v[88:89]\n"
"v_pk_add_f32 v[174:175], v[174:175], v[90:91]\n"
"v_cvt_pk_bf16_f32 v126, v172, v173\n"
"v_cvt_pk_bf16_f32 v127, v174, v175\n"
"s_nop 1\n"
"v_permlane32_swap_b32 v124, v126\n"
"v_permlane32_swap_b32 v125, v127\n"
"s_nop 0\n"
"v_permlane16_swap_b32 v124, v126\n"
"v_permlane16_swap_b32 v125, v127\n"
"buffer_store_dwordx4 v[124:127], v200, s[40:43], s60 offen sc0 sc1\n"
"s_add_u32 s60, s60, 32768\n"
"s_waitcnt vmcnt(6)\n"
"v_lshlrev_b32 v64, 16, v120\n"
"v_and_b32 v65, 0xffff0000, v120\n"
"v_lshlrev_b32 v66, 16, v121\n"
"v_and_b32 v67, 0xffff0000, v121\n"
"v_pk_add_f32 v[132:133], v[132:133], v[64:65]\n"
"v_pk_add_f32 v[134:135], v[134:135], v[66:67]\n"
"v_cvt_pk_bf16_f32 v132, v132, v133\n"
"v_cvt_pk_bf16_f32 v133, v134, v135\n"
"v_lshlrev_b32 v72, 16, v122\n"
"v_and_b32 v73, 0xffff0000, v122\n"
"v_lshlrev_b32 v74, 16, v123\n"
"v_and_b32 v75, 0xffff0000, v123\n"
"v_pk_add_f32 v[180:181], v[180:181], v[72:73]\n"
"v_pk_add_f32 v[182:183], v[182:183], v[74:75]\n"
"v_cvt_pk_bf16_f32 v134, v180, v181\n"
"v_cvt_pk_bf16_f32 v135, v182, v183\n"
"s_nop 1\n"
"v_permlane32_swap_b32 v132, v134\n"
"v_permlane32_swap_b32 v133, v135\n"
"s_nop 0\n"
"v_permlane16_swap_b32 v132, v134\n"
"v_permlane16_swap_b32 v133, v135\n"
"buffer_store_dwordx4 v[132:135], v200, s[40:43], s60 offen sc0 sc1\n"
"s_add_u32 s60, s60, 32768\n"
"s_waitcnt vmcnt(6)\n"
"v_lshlrev_b32 v80, 16, v128\n"
"v_and_b32 v81, 0xffff0000, v128\n"
"v_lshlrev_b32 v82, 16, v129\n"
"v_and_b32 v83, 0xffff0000, v129\n"
"v_pk_add_f32 v[140:141], v[140:141], v[80:81]\n"
"v_pk_add_f32 v[142:143], v[142:143], v[82:83]\n"
"v_cvt_pk_bf16_f32 v140, v140, v141\n"
"v_cvt_pk_bf16_f32 v141, v142, v143\n"
"v_lshlrev_b32 v88, 16, v130\n"
"v_and_b32 v89, 0xffff0000, v130\n"
"v_lshlrev_b32 v90, 16, v131\n"
"v_and_b32 v91, 0xffff0000, v131\n"
"v_pk_add_f32 v[188:189], v[188:189], v[88:89]\n"
"v_pk_add_f32 v[190:191], v[190:191], v[90:91]\n"
"v_cvt_pk_bf16_f32 v142, v188, v189\n"
"v_cvt_pk_bf16_f32 v143, v190, v191\n"
"s_nop 1\n"
"v_permlane32_swap_b32 v140, v142\n"
"v_permlane32_swap_b32 v141, v143\n"
"s_nop 0\n"
"v_permlane16_swap_b32 v140, v142\n"
"v_permlane16_swap_b32 v141, v143\n"
"buffer_store_dwordx4 v[140:143], v200, s[40:43], s60 offen sc0 sc1\n"
"s_add_u32 s60, s60, 32768\n"
"s_waitcnt vmcnt(6)\n"
"v_lshlrev_b32 v64, 16, v136\n"
"v_and_b32 v65, 0xffff0000, v136\n"
"v_lshlrev_b32 v66, 16, v137\n"
"v_and_b32 v67, 0xffff0000, v137\n"
"v_pk_add_f32 v[148:149], v[148:149], v[64:65]\n"
"v_pk_add_f32 v[150:151], v[150:151], v[66:67]\n"
"v_cvt_pk_bf16_f32 v148, v148, v149\n"
"v_cvt_pk_bf16_f32 v149, v150, v151\n"
"v_lshlrev_b32 v72, 16, v138\n"
"v_and_b32 v73, 0xffff0000, v138\n"
"v_lshlrev_b32 v74, 16, v139\n"
"v_and_b32 v75, 0xffff0000, v139\n"
"v_pk_add_f32 v[208:209], v[208:209], v[72:73]\n"
"v_pk_add_f32 v[210:211], v[210:211], v[74:75]\n"
"v_cvt_pk_bf16_f32 v150, v208, v209\n"
"v_cvt_pk_bf16_f32 v151, v210, v211\n"
"s_nop 1\n"
"v_permlane32_swap_b32 v148, v150\n"
"v_permlane32_swap_b32 v149, v151\n"
"s_nop 0\n"
"v_permlane16_swap_b32 v148, v150\n"
"v_permlane16_swap_b32 v149, v151\n"
"buffer_store_dwordx4 v[148:151], v200, s[40:43], s60 offen sc0 sc1\n"
"s_add_u32 s60, s60, 32768\n"
"s_waitcnt vmcnt(6)\n"
"v_lshlrev_b32 v80, 16, v144\n"
"v_and_b32 v81, 0xffff0000, v144\n"
"v_lshlrev_b32 v82, 16, v145\n"
"v_and_b32 v83, 0xffff0000, v145\n"
"v_pk_add_f32 v[156:157], v[156:157], v[80:81]\n"
"v_pk_add_f32 v[158:159], v[158:159], v[82:83]\n"
"v_cvt_pk_bf16_f32 v156, v156, v157\n"
"v_cvt_pk_bf16_f32 v157, v158, v159\n"
"v_lshlrev_b32 v88, 16, v146\n"
"v_and_b32 v89, 0xffff0000, v146\n"
"v_lshlrev_b32 v90, 16, v147\n"
"v_and_b32 v91, 0xffff0000, v147\n"
"v_pk_add_f32 v[212:213], v[212:213], v[88:89]\n"
"v_pk_add_f32 v[214:215], v[214:215], v[90:91]\n"
"v_cvt_pk_bf16_f32 v158, v212, v213\n"
"v_cvt_pk_bf16_f32 v159, v214, v215\n"
"s_nop 1\n"
"v_permlane32_swap_b32 v156, v158\n"
"v_permlane32_swap_b32 v157, v159\n"
"s_nop 0\n"
"v_permlane16_swap_b32 v156, v158\n"
"v_permlane16_swap_b32 v157, v159\n"
"buffer_store_dwordx4 v[156:159], v200, s[40:43], s60 offen sc0 sc1\n"
"s_waitcnt vmcnt(6)\n"
"v_lshlrev_b32 v64, 16, v152\n"
"v_and_b32 v65, 0xffff0000, v152\n"
"v_lshlrev_b32 v66, 16, v153\n"
"v_and_b32 v67, 0xffff0000, v153\n"
"v_pk_add_f32 v[220:221], v[220:221], v[64:65]\n"
"v_pk_add_f32 v[222:223], v[222:223], v[66:67]\n"
"v_cvt_pk_bf16_f32 v220, v220, v221\n"
"v_cvt_pk_bf16_f32 v221, v222, v223\n"
"buffer_store_dwordx2 v[220:221], v202, s[40:43], s62 offen sc0 sc1\n"
"s_endpgm\n"
"Lak%=:\n"
"s_lshl_b32 s44, s78, 3\n"
"s_add_u32 s44, s44, 163328\n"
"v_mov_b32 v15, s44\n"
"ds_read_b64 v[252:253], v15\n"
"v_lshrrev_b32 v14, 1, v255\n"
"v_and_b32 v14, 0x78, v14\n"
"s_lshl_b32 s47, s77, 4\n"
"s_add_u32 s47, s47, s78\n"
"s_lshl_b32 s47, s47, 7\n"
"s_add_u32 s47, s47, 33685504\n"
"s_mov_b32 s75, 4194304\n"
"Lakl%=:\n"
"buffer_load_dwordx2 v[12:13], v14, s[56:59], s47 offen sc1\n"
"s_waitcnt vmcnt(0) lgkmcnt(0)\n"
"v_cmp_ne_u64 vcc, v[12:13], v[252:253]\n"
"s_nop 1\n"
"s_cmp_eq_u64 vcc, 0\n"
"s_cbranch_scc1 Lakr%=\n"
"s_sub_u32 s75, s75, 1\n"
"s_cbranch_scc1 Lakr%=\n"
"s_sleep 2\n"
"s_branch Lakl%=\n"
"Lpf%=:\n"
"s_mov_b32 s75, 4194304\n"
"Lpfl%=:\n"
"buffer_load_dwordx2 v[252:253], v254, s[56:59], s45 offen sc1\n"
"s_waitcnt vmcnt(0) lgkmcnt(0)\n"
"v_cmp_ne_u64 vcc, v[252:253], v[250:251]\n"
"s_nop 1\n"
"s_cmp_eq_u64 vcc, 0\n"
"s_cbranch_scc1 Lpfr%=\n"
"s_sub_u32 s75, s75, 1\n"
"s_cbranch_scc1 Lpfr%=\n"
"s_sleep 2\n"
"s_branch Lpfl%=\n"
"Lpg%=:\n"
"s_mov_b32 s75, 4194304\n"
"Lpgl%=:\n"
"buffer_load_dwordx2 v[252:253], off, s[56:59], s44 sc1\n"
"s_waitcnt vmcnt(0) lgkmcnt(0)\n"
"v_cmp_ne_u64 vcc, v[252:253], v[206:207]\n"
"s_nop 1\n"
"s_cmp_eq_u64 vcc, 0\n"
"s_cbranch_scc1 Lpgr%=\n"
"s_sub_u32 s75, s75, 1\n"
"s_cbranch_scc1 Lpgr%=\n"
"s_sleep 2\n"
"s_branch Lpgl%=\n"
"Lx%=:\n"
"s_waitcnt vmcnt(0) lgkmcnt(0)\n"
  :: "s"(0), "s"(x), "s"(kc), "s"(vc), "s"(wq), "s"(of), "s"(g), "s"(w), "s"(wo), "s"(out) : "v0", "v1", "v2", "v3", "v4", "v5", "v6", "v7", "v8", "v9", "v10", "v11", "v12", "v13", "v14", "v15", "v16", "v17", "v18", "v19", "v20", "v21", "v22", "v23", "v24", "v25", "v26", "v27", "v28", "v29", "v30", "v31", "v32", "v33", "v34", "v35", "v36", "v37", "v38", "v39", "v40", "v41", "v42", "v43", "v44", "v45", "v46", "v47", "v48", "v49", "v50", "v51", "v52", "v53", "v54", "v55", "v56", "v57", "v58", "v59", "v60", "v61", "v62", "v63", "v64", "v65", "v66", "v67", "v68", "v69", "v70", "v71", "v72", "v73", "v74", "v75", "v76", "v77", "v78", "v79", "v80", "v81", "v82", "v83", "v84", "v85", "v86", "v87", "v88", "v89", "v90", "v91", "v92", "v93", "v94", "v95", "v96", "v97", "v98", "v99", "v100", "v101", "v102", "v103", "v104", "v105", "v106", "v107", "v108", "v109", "v110", "v111", "v112", "v113", "v114", "v115", "v116", "v117", "v118", "v119", "v120", "v121", "v122", "v123", "v124", "v125", "v126", "v127", "v128", "v129", "v130", "v131", "v132", "v133", "v134", "v135", "v136", "v137", "v138", "v139", "v140", "v141", "v142", "v143", "v144", "v145", "v146", "v147", "v148", "v149", "v150", "v151", "v152", "v153", "v154", "v155", "v156", "v157", "v158", "v159", "v160", "v161", "v162", "v163", "v164", "v165", "v166", "v167", "v168", "v169", "v170", "v171", "v172", "v173", "v174", "v175", "v176", "v177", "v178", "v179", "v180", "v181", "v182", "v183", "v184", "v185", "v186", "v187", "v188", "v189", "v190", "v191", "v192", "v193", "v194", "v195", "v196", "v197", "v198", "v199", "v200", "v201", "v202", "v203", "v204", "v205", "v206", "v207", "v208", "v209", "v210", "v211", "v212", "v213", "v214", "v215", "v216", "v217", "v218", "v219", "v220", "v221", "v222", "v223", "v224", "v225", "v226", "v227", "v228", "v229", "v230", "v231", "v232", "v233", "v234", "v235", "v236", "v237", "v238", "v239", "v240", "v241", "v242", "v243", "v244", "v245", "v246", "v247", "v248", "v249", "v250", "v251", "v252", "v253", "v254", "v255", "a0", "a1", "a2", "a3", "a4", "a5", "a6", "a7", "a8", "a9", "a10", "a11", "a12", "a13", "a14", "a15", "a16", "a17", "a18", "a19", "a20", "a21", "a22", "a23", "a24", "a25", "a26", "a27", "a28", "a29", "a30", "a31", "a32", "a33", "a34", "a35", "a36", "a37", "a38", "a39", "a40", "a41", "a42", "a43", "a44", "a45", "a46", "a47", "a48", "a49", "a50", "a51", "a52", "a53", "a54", "a55", "a56", "a57", "a58", "a59", "a60", "a61", "a62", "a63", "a64", "a65", "a66", "a67", "a68", "a69", "a70", "a71", "a72", "a73", "a74", "a75", "a76", "a77", "a78", "a79", "a80", "a81", "a82", "a83", "a84", "a85", "a86", "a87", "a88", "a89", "a90", "a91", "a92", "a93", "a94", "a95", "a96", "a97", "a98", "a99", "a100", "a101", "a102", "a103", "a104", "a105", "a106", "a107", "a108", "a109", "a110", "a111", "a112", "a113", "a114", "a115", "a116", "a117", "a118", "a119", "a120", "a121", "a122", "a123", "a124", "a125", "a126", "a127", "a128", "a129", "a130", "a131", "a132", "a133", "a134", "a135", "a136", "a137", "a138", "a139", "a140", "a141", "a142", "a143", "a144", "a145", "a146", "a147", "a148", "a149", "a150", "a151", "a152", "a153", "a154", "a155", "a156", "a157", "a158", "a159", "a160", "a161", "a162", "a163", "a164", "a165", "a166", "a167", "a168", "a169", "a170", "a171", "a172", "a173", "a174", "a175", "a176", "a177", "a178", "a179", "a180", "a181", "a182", "a183", "a184", "a185", "a186", "a187", "a188", "a189", "a190", "a191", "a192", "a193", "a194", "a195", "a196", "a197", "a198", "a199", "a200", "a201", "a202", "a203", "a204", "a205", "a206", "a207", "a208", "a209", "a210", "a211", "a212", "a213", "a214", "a215", "a216", "a217", "a218", "a219", "a220", "a221", "a222", "a223", "a224", "a225", "a226", "a227", "a228", "a229", "a230", "a231", "a232", "a233", "a234", "a235", "a236", "a237", "a238", "a239", "a240", "a241", "a242", "a243", "a244", "a245", "a246", "a247", "a248", "a249", "a250", "a251", "a252", "a253", "a254", "a255", "s36", "s37", "s38", "s39", "s40", "s41", "s42", "s43", "s44", "s45", "s46", "s47", "s48", "s49", "s50", "s51", "s52", "s53", "s54", "s55", "s56", "s57", "s58", "s59", "s60", "s61", "s62", "s63", "s64", "s65", "s66", "s67", "s68", "s69", "s70", "s71", "s72", "s73", "s74", "s75", "s76", "s77", "s78", "s79", "s80", "s81", "s82", "s83", "s84", "s85", "s86", "s87", "s88", "s89", "s90", "s91", "s92", "s93", "s94", "s95", "s96", "s97", "s98", "s99", "vcc", "scc", "memory");
  unsigned lane = __builtin_amdgcn_mbcnt_hi(~0u, __builtin_amdgcn_mbcnt_lo(~0u, 0u));
  slow_wave((const k1u16*)x,(const k1u16*)kc,(const k1u16*)vc,(const k1u16*)wq,(k1u16*)of,g,w,lane);
  asm volatile("s_waitcnt vmcnt(0)\n"
"s_mov_b32 s76, %7\n"
"s_mov_b32 s99, %6\n"
"s_and_b32 s80, s99, 7\n"
"s_lshr_b32 s81, s99, 3\n"
"s_and_b32 s77, s80, 1\n"
"s_lshl_b32 s77, s77, 3\n"
"s_and_b32 s82, s81, 7\n"
"s_add_u32 s77, s77, s82\n"
"s_lshr_b32 s78, s80, 1\n"
"s_lshl_b32 s78, s78, 2\n"
"s_lshr_b32 s82, s81, 3\n"
"s_add_u32 s78, s78, s82\n"
"s_lshr_b32 s83, s76, 1\n"
"s_and_b32 s84, s76, 1\n"
"s_lshl_b32 s79, s78, 1\n"
"s_add_u32 s79, s79, s83\n"
"v_mbcnt_lo_u32_b32 v0, -1, 0\n"
"v_mbcnt_hi_u32_b32 v0, -1, v0\n"
"s_cmp_eq_u32 s84, 1\n"
"s_cbranch_scc1 Lcold1%=\n"
"Lcold0%=:\n"
"s_mov_b64 s[56:57], %5\n"
"s_mov_b32 s58, 50331648\n"
"s_mov_b32 s59, 0x00020000\n"
"s_mov_b64 s[48:49], %8\n"
"s_mov_b32 s50, 8388608\n"
"s_mov_b32 s51, 0x00020000\n"
"v_lshlrev_b32 v255, 4, v0\n"
"s_mul_i32 s80, s76, 5120\n"
"s_add_u32 s80, s80, 139264\n"
"v_add_u32 v253, s80, v255\n"
"s_xor_b32 s81, s76, 1\n"
"s_mul_i32 s81, s81, 5120\n"
"s_add_u32 s81, s81, 139264\n"
"v_add_u32 v254, s81, v255\n"
"s_and_b32 s72, s78, 7\n"
"s_and_b32 s80, s76, 1\n"
"s_lshr_b32 s81, s76, 1\n"
"s_lshl_b32 s66, s80, 15\n"
"s_lshl_b32 s82, s72, 8\n"
"s_add_u32 s66, s66, s82\n"
"s_add_u32 s67, s66, 8192\n"
"s_add_u32 s68, s67, 8192\n"
"s_add_u32 s69, s68, 8192\n"
"s_mul_i32 s71, s81, 67584\n"
"s_mul_i32 s80, s80, 4224\n"
"s_add_u32 s71, s71, s80\n"
"s_lshl_b32 s82, s79, 18\n"
"v_lshrrev_b32 v249, 8, v255\n"
"v_lshlrev_b32 v248, 11, v249\n"
"v_lshlrev_b32 v249, 6, v249\n"
"v_add_u32 v248, s82, v248\n"
"v_xor_b32 v249, v249, v255\n"
"v_and_b32 v249, 0xf0, v249\n"
"v_add_u32 v248, v248, v249\n"
"v_xor_b32 v248, 0x10000, v248\n"
"v_xor_b32 v248, 0x30000, v248\n"
"v_xor_b32 v248, 0x10000, v248\n"
"s_sub_u32 s73, 0, s72\n"
"s_cmp_eq_u32 s72, 0\n"
"s_cselect_b32 s73, 1, s73\n"
"v_lshl_add_u32 v248, s73, 19, v248\n"
"s_add_u32 s73, s77, s78\n"
"s_and_b32 s73, s73, 3\n"
"s_xor_b32 s73, s73, 3\n"
"s_lshl_b32 s73, s73, 16\n"
"v_xor_b32 v248, s73, v248\n"
"s_lshl_b32 s82, s79, 12\n"
"v_add_u32 v249, s82, v255\n"
"s_and_b32 s80, s76, 1\n"
"s_mul_i32 s82, s77, 13\n"
"s_mul_i32 s81, s80, 6\n"
"s_add_u32 s81, s81, s82\n"
"s_mul_i32 s60, s81, 132096\n"
"s_add_u32 s61, s60, 132096\n"
"s_add_u32 s62, s61, 132096\n"
"s_add_u32 s63, s62, 132096\n"
"s_add_u32 s64, s63, 132096\n"
"s_add_u32 s65, s64, 132096\n"
"s_add_u32 s82, s82, 12\n"
"s_mul_i32 s70, s82, 132096\n"
"s_nop 4\n"
"buffer_load_dwordx4 v[0:3], v249, s[56:59], s60 offen offset:0 sc1\n"
"buffer_load_dwordx4 v[8:11], v249, s[56:59], s60 offen offset:1024 sc1\n"
"buffer_load_dwordx4 v[16:19], v249, s[56:59], s60 offen offset:2048 sc1\n"
"buffer_load_dwordx4 v[24:27], v249, s[56:59], s60 offen offset:3072 sc1\n"
"buffer_load_dwordx4 v[32:35], v249, s[56:59], s61 offen offset:0 sc1\n"
"buffer_load_dwordx4 v[40:43], v249, s[56:59], s61 offen offset:1024 sc1\n"
"buffer_load_dwordx4 v[48:51], v249, s[56:59], s61 offen offset:2048 sc1\n"
"buffer_load_dwordx4 v[56:59], v249, s[56:59], s61 offen offset:3072 sc1\n"
"buffer_load_dwordx4 v[64:67], v249, s[56:59], s62 offen offset:0 sc1\n"
"buffer_load_dwordx4 v[72:75], v249, s[56:59], s62 offen offset:1024 sc1\n"
"buffer_load_dwordx4 v[80:83], v249, s[56:59], s62 offen offset:2048 sc1\n"
"buffer_load_dwordx4 v[88:91], v249, s[56:59], s62 offen offset:3072 sc1\n"
"buffer_load_dwordx4 v[96:99], v249, s[56:59], s63 offen offset:0 sc1\n"
"buffer_load_dwordx4 v[104:107], v249, s[56:59], s63 offen offset:1024 sc1\n"
"buffer_load_dwordx4 v[112:115], v249, s[56:59], s63 offen offset:2048 sc1\n"
"buffer_load_dwordx4 v[120:123], v249, s[56:59], s63 offen offset:3072 sc1\n"
"buffer_load_dwordx4 v[128:131], v249, s[56:59], s64 offen offset:0 sc1\n"
"buffer_load_dwordx4 v[136:139], v249, s[56:59], s64 offen offset:1024 sc1\n"
"buffer_load_dwordx4 v[144:147], v249, s[56:59], s64 offen offset:2048 sc1\n"
"buffer_load_dwordx4 v[152:155], v249, s[56:59], s64 offen offset:3072 sc1\n"
"buffer_load_dwordx4 v[160:163], v249, s[56:59], s65 offen offset:0 sc1\n"
"buffer_load_dwordx4 v[168:171], v249, s[56:59], s65 offen offset:1024 sc1\n"
"buffer_load_dwordx4 v[176:179], v249, s[56:59], s65 offen offset:2048 sc1\n"
"buffer_load_dwordx4 v[184:187], v249, s[56:59], s65 offen offset:3072 sc1\n"
"buffer_load_dwordx4 v[192:195], v249, s[56:59], s70 offen offset:0 sc1\n"
"buffer_load_dwordx4 v[196:199], v249, s[56:59], s70 offen offset:1024 sc1\n"
"s_waitcnt vmcnt(0)\n"
"ds_write_b128 v254, v[192:195] offset:0\n"
"ds_write_b128 v254, v[196:199] offset:1024\n"
"s_nop 1\n"
"s_and_b32 s80, s76, 1\n"
"s_lshr_b32 s81, s76, 1\n"
"s_lshr_b32 s82, s78, 3\n"
"v_lshrrev_b32 v12, 4, v255\n"
"v_lshrrev_b32 v13, 2, v12\n"
"v_lshlrev_b32 v14, 8, v13\n"
"v_and_b32 v13, 3, v13\n"
"v_lshlrev_b32 v13, 6, v13\n"
"v_lshrrev_b32 v15, 4, v12\n"
"v_lshl_add_u32 v14, v15, 5, v14\n"
"v_and_b32 v15, 3, v12\n"
"v_lshl_add_u32 v14, v15, 3, v14\n"
"s_mul_i32 s83, s81, 67584\n"
"v_add_u32 v14, s83, v14\n"
"s_xor_b32 s83, s82, 1\n"
"s_lshl_b32 s83, s83, 1\n"
"s_add_u32 s83, s83, s81\n"
"s_lshl_b32 s83, s83, 1\n"
"s_add_u32 s83, s83, s80\n"
"s_lshl_b32 s83, s83, 5\n"
"v_xor_b32 v13, s83, v13\n"
"v_xor_b32 v240, 0, v13\n"
"v_add_u32 v240, v240, v14\n"
"v_xor_b32 v241, 32, v13\n"
"v_add_u32 v241, v241, v14\n"
"v_xor_b32 v242, 64, v13\n"
"v_add_u32 v242, v242, v14\n"
"v_xor_b32 v243, 96, v13\n"
"v_add_u32 v243, v243, v14\n"
"v_xor_b32 v244, 128, v13\n"
"v_add_u32 v244, v244, v14\n"
"v_xor_b32 v245, 160, v13\n"
"v_add_u32 v245, v245, v14\n"
"v_xor_b32 v246, 192, v13\n"
"v_add_u32 v246, v246, v14\n"
"v_xor_b32 v247, 224, v13\n"
"v_add_u32 v247, v247, v14\n"
"v_and_b32 v13, 7, v12\n"
"s_lshl_b32 s83, s82, 3\n"
"v_add_u32 v13, s83, v13\n"
"v_lshlrev_b32 v254, 7, v13\n"
"v_lshlrev_b32 v13, 3, v13\n"
"v_add_u32 v13, 163328, v13\n"
"ds_read_b64 v[250:251], v13\n"
"s_lshl_b32 s46, s78, 7\n"
"s_lshl_b32 s83, s78, 3\n"
"s_add_u32 s83, s83, 163328\n"
"v_mov_b32 v15, s83\n"
"ds_read_b64 v[4:5], v15\n"
"s_lshl_b32 s45, s77, 11\n"
"s_add_u32 s45, s45, 33554432\n"
"s_add_u32 s46, s46, s45\n"
"v_or_b32 v6, s76, v255\n"
"v_cmp_eq_u32 vcc, 0, v6\n"
"v_mov_b32 v6, 0x40000000\n"
"s_nop 0\n"
"v_cndmask_b32 v6, v6, v255, vcc\n"
"s_mov_b64 s[40:41], %9\n"
"s_mov_b32 s42, 6815744\n"
"s_mov_b32 s43, 0x00020000\n"
"s_cmp_eq_u32 s76, 0\n"
"s_cselect_b32 s44, 1, 0\n"
"s_getpc_b64 s[80:81]\n"
"Lpc0%=:\n"
"s_add_u32 s80, s80, .Lmha_j0-Lpc0%=\n"
"s_addc_u32 s81, s81, -1\n"
"s_setpc_b64 s[80:81]\n"
"Lcold1%=:\n"
"s_mov_b64 s[56:57], %5\n"
"s_mov_b32 s58, 50331648\n"
"s_mov_b32 s59, 0x00020000\n"
"s_mov_b64 s[48:49], %8\n"
"s_mov_b32 s50, 8388608\n"
"s_mov_b32 s51, 0x00020000\n"
"v_lshlrev_b32 v255, 4, v0\n"
"s_mul_i32 s80, s76, 5120\n"
"s_add_u32 s80, s80, 139264\n"
"v_add_u32 v253, s80, v255\n"
"s_xor_b32 s81, s76, 1\n"
"s_mul_i32 s81, s81, 5120\n"
"s_add_u32 s81, s81, 139264\n"
"v_add_u32 v254, s81, v255\n"
"s_and_b32 s72, s78, 7\n"
"s_and_b32 s80, s76, 1\n"
"s_lshr_b32 s81, s76, 1\n"
"s_lshl_b32 s66, s80, 15\n"
"s_lshl_b32 s82, s72, 8\n"
"s_add_u32 s66, s66, s82\n"
"s_add_u32 s67, s66, 8192\n"
"s_add_u32 s68, s67, 8192\n"
"s_add_u32 s69, s68, 8192\n"
"s_mul_i32 s71, s81, 67584\n"
"s_mul_i32 s80, s80, 4224\n"
"s_add_u32 s71, s71, s80\n"
"s_lshl_b32 s82, s79, 18\n"
"v_lshrrev_b32 v249, 8, v255\n"
"v_lshlrev_b32 v248, 11, v249\n"
"v_lshlrev_b32 v249, 6, v249\n"
"v_add_u32 v248, s82, v248\n"
"v_xor_b32 v249, v249, v255\n"
"v_and_b32 v249, 0xf0, v249\n"
"v_add_u32 v248, v248, v249\n"
"v_xor_b32 v248, 0x10000, v248\n"
"v_xor_b32 v248, 0x30000, v248\n"
"v_xor_b32 v248, 0x10000, v248\n"
"s_sub_u32 s73, 0, s72\n"
"s_cmp_eq_u32 s72, 0\n"
"s_cselect_b32 s73, 1, s73\n"
"v_lshl_add_u32 v248, s73, 19, v248\n"
"s_add_u32 s73, s77, s78\n"
"s_and_b32 s73, s73, 3\n"
"s_xor_b32 s73, s73, 3\n"
"s_lshl_b32 s73, s73, 16\n"
"v_xor_b32 v248, s73, v248\n"
"s_lshl_b32 s82, s79, 12\n"
"v_add_u32 v249, s82, v255\n"
"s_and_b32 s80, s76, 1\n"
"s_mul_i32 s82, s77, 13\n"
"s_mul_i32 s81, s80, 6\n"
"s_add_u32 s81, s81, s82\n"
"s_mul_i32 s60, s81, 132096\n"
"s_add_u32 s61, s60, 132096\n"
"s_add_u32 s62, s61, 132096\n"
"s_add_u32 s63, s62, 132096\n"
"s_add_u32 s64, s63, 132096\n"
"s_add_u32 s65, s64, 132096\n"
"s_add_u32 s82, s82, 12\n"
"s_mul_i32 s70, s82, 132096\n"
"s_nop 4\n"
"buffer_load_dwordx4 v[0:3], v249, s[56:59], s60 offen offset:0 sc1\n"
"buffer_load_dwordx4 v[8:11], v249, s[56:59], s60 offen offset:1024 sc1\n"
"buffer_load_dwordx4 v[16:19], v249, s[56:59], s60 offen offset:2048 sc1\n"
"buffer_load_dwordx4 v[24:27], v249, s[56:59], s60 offen offset:3072 sc1\n"
"buffer_load_dwordx4 v[32:35], v249, s[56:59], s61 offen offset:0 sc1\n"
"buffer_load_dwordx4 v[40:43], v249, s[56:59], s61 offen offset:1024 sc1\n"
"buffer_load_dwordx4 v[48:51], v249, s[56:59], s61 offen offset:2048 sc1\n"
"buffer_load_dwordx4 v[56:59], v249, s[56:59], s61 offen offset:3072 sc1\n"
"buffer_load_dwordx4 v[64:67], v249, s[56:59], s62 offen offset:0 sc1\n"
"buffer_load_dwordx4 v[72:75], v249, s[56:59], s62 offen offset:1024 sc1\n"
"buffer_load_dwordx4 v[80:83], v249, s[56:59], s62 offen offset:2048 sc1\n"
"buffer_load_dwordx4 v[88:91], v249, s[56:59], s62 offen offset:3072 sc1\n"
"buffer_load_dwordx4 v[96:99], v249, s[56:59], s63 offen offset:0 sc1\n"
"buffer_load_dwordx4 v[104:107], v249, s[56:59], s63 offen offset:1024 sc1\n"
"buffer_load_dwordx4 v[112:115], v249, s[56:59], s63 offen offset:2048 sc1\n"
"buffer_load_dwordx4 v[120:123], v249, s[56:59], s63 offen offset:3072 sc1\n"
"buffer_load_dwordx4 v[128:131], v249, s[56:59], s64 offen offset:0 sc1\n"
"buffer_load_dwordx4 v[136:139], v249, s[56:59], s64 offen offset:1024 sc1\n"
"buffer_load_dwordx4 v[144:147], v249, s[56:59], s64 offen offset:2048 sc1\n"
"buffer_load_dwordx4 v[152:155], v249, s[56:59], s64 offen offset:3072 sc1\n"
"buffer_load_dwordx4 v[160:163], v249, s[56:59], s65 offen offset:0 sc1\n"
"buffer_load_dwordx4 v[168:171], v249, s[56:59], s65 offen offset:1024 sc1\n"
"buffer_load_dwordx4 v[176:179], v249, s[56:59], s65 offen offset:2048 sc1\n"
"buffer_load_dwordx4 v[184:187], v249, s[56:59], s65 offen offset:3072 sc1\n"
"buffer_load_dwordx4 v[200:203], v249, s[56:59], s70 offen offset:2048 sc1\n"
"buffer_load_dwordx4 v[204:207], v249, s[56:59], s70 offen offset:3072 sc1\n"
"s_waitcnt vmcnt(0)\n"
"ds_write_b128 v254, v[200:203] offset:0\n"
"ds_write_b128 v254, v[204:207] offset:1024\n"
"s_nop 1\n"
"s_and_b32 s80, s76, 1\n"
"s_lshr_b32 s81, s76, 1\n"
"s_lshr_b32 s82, s78, 3\n"
"v_lshrrev_b32 v12, 4, v255\n"
"v_lshrrev_b32 v13, 2, v12\n"
"v_lshlrev_b32 v14, 8, v13\n"
"v_and_b32 v13, 3, v13\n"
"v_lshlrev_b32 v13, 6, v13\n"
"v_lshrrev_b32 v15, 4, v12\n"
"v_lshl_add_u32 v14, v15, 5, v14\n"
"v_and_b32 v15, 3, v12\n"
"v_lshl_add_u32 v14, v15, 3, v14\n"
"s_mul_i32 s83, s81, 67584\n"
"v_add_u32 v14, s83, v14\n"
"s_xor_b32 s83, s82, 1\n"
"s_lshl_b32 s83, s83, 1\n"
"s_add_u32 s83, s83, s81\n"
"s_lshl_b32 s83, s83, 1\n"
"s_add_u32 s83, s83, s80\n"
"s_lshl_b32 s83, s83, 5\n"
"v_xor_b32 v13, s83, v13\n"
"v_xor_b32 v240, 0, v13\n"
"v_add_u32 v240, v240, v14\n"
"v_xor_b32 v241, 32, v13\n"
"v_add_u32 v241, v241, v14\n"
"v_xor_b32 v242, 64, v13\n"
"v_add_u32 v242, v242, v14\n"
"v_xor_b32 v243, 96, v13\n"
"v_add_u32 v243, v243, v14\n"
"v_xor_b32 v244, 128, v13\n"
"v_add_u32 v244, v244, v14\n"
"v_xor_b32 v245, 160, v13\n"
"v_add_u32 v245, v245, v14\n"
"v_xor_b32 v246, 192, v13\n"
"v_add_u32 v246, v246, v14\n"
"v_xor_b32 v247, 224, v13\n"
"v_add_u32 v247, v247, v14\n"
"v_and_b32 v13, 7, v12\n"
"s_lshl_b32 s83, s82, 3\n"
"v_add_u32 v13, s83, v13\n"
"v_lshlrev_b32 v254, 7, v13\n"
"v_lshlrev_b32 v13, 3, v13\n"
"v_add_u32 v13, 163328, v13\n"
"ds_read_b64 v[250:251], v13\n"
"s_lshl_b32 s46, s78, 7\n"
"s_lshl_b32 s83, s78, 3\n"
"s_add_u32 s83, s83, 163328\n"
"v_mov_b32 v15, s83\n"
"ds_read_b64 v[4:5], v15\n"
"s_lshl_b32 s45, s77, 11\n"
"s_add_u32 s45, s45, 33554432\n"
"s_add_u32 s46, s46, s45\n"
"v_or_b32 v6, s76, v255\n"
"v_cmp_eq_u32 vcc, 0, v6\n"
"v_mov_b32 v6, 0x40000000\n"
"s_nop 0\n"
"v_cndmask_b32 v6, v6, v255, vcc\n"
"s_mov_b64 s[40:41], %9\n"
"s_mov_b32 s42, 6815744\n"
"s_mov_b32 s43, 0x00020000\n"
"s_cmp_eq_u32 s76, 0\n"
"s_cselect_b32 s44, 1, 0\n"
"s_getpc_b64 s[80:81]\n"
"Lpc1%=:\n"
"s_add_u32 s80, s80, .Lmha_j1-Lpc1%=\n"
"s_addc_u32 s81, s81, -1\n"
"s_setpc_b64 s[80:81]\n"
  :: "s"(0), "s"(x), "s"(kc), "s"(vc), "s"(wq), "s"(of), "s"(g), "s"(w), "s"(wo), "s"(out) : "v0", "v1", "v2", "v3", "v4", "v5", "v6", "v7", "v8", "v9", "v10", "v11", "v12", "v13", "v14", "v15", "v16", "v17", "v18", "v19", "v20", "v21", "v22", "v23", "v24", "v25", "v26", "v27", "v28", "v29", "v30", "v31", "v32", "v33", "v34", "v35", "v36", "v37", "v38", "v39", "v40", "v41", "v42", "v43", "v44", "v45", "v46", "v47", "v48", "v49", "v50", "v51", "v52", "v53", "v54", "v55", "v56", "v57", "v58", "v59", "v60", "v61", "v62", "v63", "v64", "v65", "v66", "v67", "v68", "v69", "v70", "v71", "v72", "v73", "v74", "v75", "v76", "v77", "v78", "v79", "v80", "v81", "v82", "v83", "v84", "v85", "v86", "v87", "v88", "v89", "v90", "v91", "v92", "v93", "v94", "v95", "v96", "v97", "v98", "v99", "v100", "v101", "v102", "v103", "v104", "v105", "v106", "v107", "v108", "v109", "v110", "v111", "v112", "v113", "v114", "v115", "v116", "v117", "v118", "v119", "v120", "v121", "v122", "v123", "v124", "v125", "v126", "v127", "v128", "v129", "v130", "v131", "v132", "v133", "v134", "v135", "v136", "v137", "v138", "v139", "v140", "v141", "v142", "v143", "v144", "v145", "v146", "v147", "v148", "v149", "v150", "v151", "v152", "v153", "v154", "v155", "v156", "v157", "v158", "v159", "v160", "v161", "v162", "v163", "v164", "v165", "v166", "v167", "v168", "v169", "v170", "v171", "v172", "v173", "v174", "v175", "v176", "v177", "v178", "v179", "v180", "v181", "v182", "v183", "v184", "v185", "v186", "v187", "v188", "v189", "v190", "v191", "v192", "v193", "v194", "v195", "v196", "v197", "v198", "v199", "v200", "v201", "v202", "v203", "v204", "v205", "v206", "v207", "v208", "v209", "v210", "v211", "v212", "v213", "v214", "v215", "v216", "v217", "v218", "v219", "v220", "v221", "v222", "v223", "v224", "v225", "v226", "v227", "v228", "v229", "v230", "v231", "v232", "v233", "v234", "v235", "v236", "v237", "v238", "v239", "v240", "v241", "v242", "v243", "v244", "v245", "v246", "v247", "v248", "v249", "v250", "v251", "v252", "v253", "v254", "v255", "a0", "a1", "a2", "a3", "a4", "a5", "a6", "a7", "a8", "a9", "a10", "a11", "a12", "a13", "a14", "a15", "a16", "a17", "a18", "a19", "a20", "a21", "a22", "a23", "a24", "a25", "a26", "a27", "a28", "a29", "a30", "a31", "a32", "a33", "a34", "a35", "a36", "a37", "a38", "a39", "a40", "a41", "a42", "a43", "a44", "a45", "a46", "a47", "a48", "a49", "a50", "a51", "a52", "a53", "a54", "a55", "a56", "a57", "a58", "a59", "a60", "a61", "a62", "a63", "a64", "a65", "a66", "a67", "a68", "a69", "a70", "a71", "a72", "a73", "a74", "a75", "a76", "a77", "a78", "a79", "a80", "a81", "a82", "a83", "a84", "a85", "a86", "a87", "a88", "a89", "a90", "a91", "a92", "a93", "a94", "a95", "a96", "a97", "a98", "a99", "a100", "a101", "a102", "a103", "a104", "a105", "a106", "a107", "a108", "a109", "a110", "a111", "a112", "a113", "a114", "a115", "a116", "a117", "a118", "a119", "a120", "a121", "a122", "a123", "a124", "a125", "a126", "a127", "a128", "a129", "a130", "a131", "a132", "a133", "a134", "a135", "a136", "a137", "a138", "a139", "a140", "a141", "a142", "a143", "a144", "a145", "a146", "a147", "a148", "a149", "a150", "a151", "a152", "a153", "a154", "a155", "a156", "a157", "a158", "a159", "a160", "a161", "a162", "a163", "a164", "a165", "a166", "a167", "a168", "a169", "a170", "a171", "a172", "a173", "a174", "a175", "a176", "a177", "a178", "a179", "a180", "a181", "a182", "a183", "a184", "a185", "a186", "a187", "a188", "a189", "a190", "a191", "a192", "a193", "a194", "a195", "a196", "a197", "a198", "a199", "a200", "a201", "a202", "a203", "a204", "a205", "a206", "a207", "a208", "a209", "a210", "a211", "a212", "a213", "a214", "a215", "a216", "a217", "a218", "a219", "a220", "a221", "a222", "a223", "a224", "a225", "a226", "a227", "a228", "a229", "a230", "a231", "a232", "a233", "a234", "a235", "a236", "a237", "a238", "a239", "a240", "a241", "a242", "a243", "a244", "a245", "a246", "a247", "a248", "a249", "a250", "a251", "a252", "a253", "a254", "a255", "s36", "s37", "s38", "s39", "s40", "s41", "s42", "s43", "s44", "s45", "s46", "s47", "s48", "s49", "s50", "s51", "s52", "s53", "s54", "s55", "s56", "s57", "s58", "s59", "s60", "s61", "s62", "s63", "s64", "s65", "s66", "s67", "s68", "s69", "s70", "s71", "s72", "s73", "s74", "s75", "s76", "s77", "s78", "s79", "s80", "s81", "s82", "s83", "s84", "s85", "s86", "s87", "s88", "s89", "s90", "s91", "s92", "s93", "s94", "s95", "s96", "s97", "s98", "s99", "vcc", "scc", "memory");
}
static void run_mode(int mode, void* const* d_in, void* d_out, void* d_ws, hipStream_t stream){
  hipFuncSetAttribute((const void*)mha_kernel, hipFuncAttributeMaxDynamicSharedMemorySize, 163840);
  mha_kernel<<<256,256,163840,stream>>>(d_in[0], d_in[1], d_in[2], d_in[3], d_ws, d_in[4], d_out);
}
extern "C" void kernel_launch(void* const* d_in, const int* in_sizes, int n_in, void* d_out, int out_size, void* d_ws, size_t ws_size, hipStream_t stream){
  run_mode(0,d_in,d_out,d_ws,stream);
}
```
